```python
import torch

def _softmax(x, axis=-1):
    return torch.softmax(x, dim=axis)
def _gelu(x):
    return torch.nn.functional.gelu(x, approximate="tanh")
def _silu(x):
    return torch.nn.functional.silu(x)
def _sigmoid(x):
    return torch.sigmoid(x)
def _layernorm(x, axis=-1, eps=1e-5):
    m = x.mean(dim=axis, keepdim=True)
    v = x.var(dim=axis, keepdim=True, unbiased=False)
    return (x - m) / torch.sqrt(v + eps)
def _scatter_add(tgt, idx, src, axis=0):
    return tgt.clone().index_add_(axis, idx, src)
def _segment_sum(src, seg, n):
    out = torch.zeros((n,) + src.shape[1:], dtype=src.dtype)
    return out.index_add_(0, seg, src)
def _pad(x, pw):
    flat = tuple(v for pair in reversed(pw) for v in pair)
    return torch.nn.functional.pad(x, flat)

kernel_name = 'interleave_layers__sliding_local_dilated__softmax_full_mha__prompt'

def setup_inputs():

    _w = (lambda a: (lambda t: t.to(torch.bfloat16) if t.is_floating_point() else t)(torch.as_tensor(a)))
    def _A_setup():
        B, S, D = 1, 2048, 1024
        return {
            "x": _w((lambda *s: torch.randn(*s, dtype=torch.bfloat16) * 0.05)(B, S, D)),
            "wqkv": _w((lambda *s: torch.randn(*s, dtype=torch.bfloat16) * 0.05)(D, 3 * D)),
            "wo": _w((lambda *s: torch.randn(*s, dtype=torch.bfloat16) * 0.05)(D, D)),
        }
    _A_in = _A_setup()

    def _B_setup():
        B, S, D = 1, 2048, 1024
        return {
            "x": _w((lambda *s: torch.randn(*s, dtype=torch.bfloat16) * 0.05)(B, S, D)),
            "wqkv": _w((lambda *s: torch.randn(*s, dtype=torch.bfloat16) * 0.05)(D, 3 * D)),
            "wo": _w((lambda *s: torch.randn(*s, dtype=torch.bfloat16) * 0.05)(D, D)),
        }
    _B_in = _B_setup()

    return {
        "A_x": _A_in["x"],
        "A_wqkv": _A_in["wqkv"],
        "A_wo": _A_in["wo"],
        "B_x": _B_in["x"],
        "B_wqkv": _B_in["wqkv"],
        "B_wo": _B_in["wo"],
    }

def reference(A_x, A_wqkv, A_wo, B_x, B_wqkv, B_wo):
    def _A_ref(x, wqkv, wo):
        B, S, D = x.shape
        H = 32; Dh = D // H
        W, dil = 8, 2
        qkv = (lambda x, *d: x.permute(*d))((x @ wqkv).reshape(B, S, 3, H, Dh), 2, 0, 3, 1, 4)
        q, k, v = qkv[0], qkv[1], qkv[2]
        scale = Dh ** -0.5
        pos = torch.arange(S)
        off = torch.arange(W) * dil
        idx = pos[:, None] - off[None, :]
        mask = idx >= 0
        idx_c = torch.where(mask, idx, 0).reshape(-1)
        kg = k[:, :, idx_c, :].reshape(B, H, S, W, Dh)
        vg = v[:, :, idx_c, :].reshape(B, H, S, W, Dh)
        logits = torch.einsum("bhsd,bhswd->bhsw", q, kg) * scale
        logits = torch.where(mask[None, None, :, :], logits, -1e9)
        attn = _softmax(logits, axis=-1)
        o = torch.einsum("bhsw,bhswd->bhsd", attn, vg)
        return (lambda x, *d: x.permute(*d))(o, 0, 2, 1, 3).reshape(B, S, D) @ wo

    def _B_ref(x, wqkv, wo):
        B, S, D = x.shape
        H = 32; Dh = D // H
        qkv = (lambda x, *d: x.permute(*d))((x @ wqkv).reshape(B, S, 3, H, Dh), 2, 0, 3, 1, 4)
        q, k, v = qkv[0], qkv[1], qkv[2]
        scale = Dh ** -0.5
        scores = torch.einsum("bhqd,bhkd->bhqk", q, k) * scale
        p = _softmax(scores, axis=-1)
        o = torch.einsum("bhqk,bhkd->bhqd", p, v)
        return (lambda x, *d: x.permute(*d))(o, 0, 2, 1, 3).reshape(B, S, D) @ wo

    x = A_x
    x = x + _A_ref(_layernorm(x, axis=-1), A_wqkv, A_wo)
    x = x + _B_ref(_layernorm(x, axis=-1), B_wqkv, B_wo)
    x = x + _A_ref(_layernorm(x, axis=-1), A_wqkv, A_wo)
    x = x + _B_ref(_layernorm(x, axis=-1), B_wqkv, B_wo)
    return x
```

```cpp
#include <hip/hip_runtime.h>
#include <stdint.h>

typedef unsigned short u16;
typedef unsigned long long u64;
typedef __attribute__((ext_vector_type(8))) short bf16x8;
typedef __attribute__((ext_vector_type(4))) short s16x4;
typedef __attribute__((ext_vector_type(4))) float f32x4;
typedef __attribute__((ext_vector_type(16))) float f32x16;
typedef __attribute__((ext_vector_type(4))) unsigned u32x4;
typedef __attribute__((ext_vector_type(2))) unsigned u32x2;
typedef __attribute__((ext_vector_type(2))) __bf16 bf16x2_t;
typedef __attribute__((ext_vector_type(2))) float f32x2;

#define LAS __attribute__((address_space(3)))
#define GAS __attribute__((address_space(1)))
#define DEV __device__ __forceinline__
typedef LAS char lchar;

constexpr int SEQ = 2048, DM = 1024, NH = 32;
constexpr float QSCALE = 0.17677669529663687f, LOG2E = 1.4426950408889634f;
#define POLLMAX (1 << 16)
#define EMU 1

DEV unsigned cvtpk(float lo, float hi) {
    f32x2 v = {lo, hi};
    bf16x2_t b = __builtin_convertvector(v, bf16x2_t);
    return __builtin_bit_cast(unsigned, b);
}
DEV float lo16(unsigned v) { return __uint_as_float(v << 16); }
DEV float hi16(unsigned v) { return __uint_as_float(v & 0xffff0000u); }
DEV float bfr(float f) { return lo16(cvtpk(f, f)); }
DEV u32x2 pk4(f32x4 a) { return (u32x2){cvtpk(a[0], a[1]), cvtpk(a[2], a[3])}; }
DEV bf16x8 cat(u32x2 a, u32x2 b) { return __builtin_bit_cast(bf16x8, (u32x4){a[0], a[1], b[0], b[1]}); }
DEV u32x2 sw16(unsigned a, unsigned b) { auto r = __builtin_amdgcn_permlane16_swap(a, b, false, false); return (u32x2){r[0], r[1]}; }
DEV u32x2 sw32(unsigned a, unsigned b) { auto r = __builtin_amdgcn_permlane32_swap(a, b, false, false); return (u32x2){r[0], r[1]}; }
DEV f32x2 sw16f(float a, float b) { return __builtin_bit_cast(f32x2, sw16(__float_as_uint(a), __float_as_uint(b))); }
DEV f32x2 sw32f(float a, float b) { return __builtin_bit_cast(f32x2, sw32(__float_as_uint(a), __float_as_uint(b))); }
DEV u32x4 unit16(u32x2 a, u32x2 b) {
    const u32x2 x = sw16(a[0], b[0]), y = sw16(a[1], b[1]);
    return (u32x4){x[0], y[0], x[1], y[1]};
}
template <int OFF> DEV void st16wt(const void* p, u32x4 v) {
    asm volatile("global_store_dwordx4 %0, %1, off offset:%2 sc1\n\ts_nop 1" ::"v"(p), "v"(v), "n"(OFF) : "memory");
}
DEV void st8wt(const void* p, u32x2 v) { asm volatile("global_store_dwordx2 %0, %1, off sc1\n\ts_nop 1" ::"v"(p), "v"(v) : "memory"); }
template <int CTRL> DEV float dppadd(float v) {
    return v + __builtin_bit_cast(float, __builtin_amdgcn_update_dpp(0, __builtin_bit_cast(int, v), CTRL, 0xf, 0xf, true));
}
DEV int fswz(int k) { return ((k >> 1) & 1) | (((k >> 3) & 1) << 1); }
DEV int fz(int tok) { return (-(tok >> 2)) & 3; }
DEV s16x4 ldtr4(const lchar* p) { return __builtin_bit_cast(s16x4, __builtin_amdgcn_ds_read_tr16_b64_v4i16((LAS s16x4*)p)); }
DEV bf16x8 cat8(s16x4 a, s16x4 b) { return (bf16x8){a[0], a[1], a[2], a[3], b[0], b[1], b[2], b[3]}; }
DEV bf16x8 ldk(const lchar* p) { return *(const LAS bf16x8*)p; }
DEV bf16x8 ldv(const lchar* p) { return cat8(ldtr4(p), ldtr4(p + 512)); }

#define STAMP(i)
#define STPARG
#define STPPASS(o)
#define BAR() asm volatile("s_barrier" ::: "memory")
#define BARL() asm volatile("s_waitcnt lgkmcnt(0)\n\ts_barrier" ::: "memory")
#define WAITB(n) asm volatile("s_waitcnt vmcnt(%0)\n\ts_barrier" ::"n"(n) : "memory")
#define WAITV(n) asm volatile("s_waitcnt vmcnt(%0)" ::"n"(n) : "memory")
#define SB() __builtin_amdgcn_sched_barrier(0)
#define LGKM0() __builtin_amdgcn_s_waitcnt(0xc07f)
#define MFMA32(a, b, c) __builtin_amdgcn_mfma_f32_32x32x16_bf16(a, b, c, 0, 0, 0)
#define MFMA16(a, b, c) __builtin_amdgcn_mfma_f32_16x16x32_bf16(a, b, c, 0, 0, 0)

DEV void mfma16t(f32x4& c, const bf16x8& a, const bf16x8& b) { asm volatile("v_mfma_f32_16x16x32_bf16 %0, %1, %2, %0" : "+v"(c) : "v"(a), "v"(b)); }
#define MFMA_DONE() do { SB(); asm volatile("s_nop 7\n\ts_nop 3"); SB(); } while (0)
DEV float fmx(float a, float b) { return __builtin_elementwise_maximum(a, b); }

constexpr int XS = 18432;
constexpr int WQ0 = 81920, WQS = 24576;
constexpr int WO0 = 65536;
constexpr int EXO = 147456;
constexpr int AR1 = 122880, AR2 = 32768;
constexpr int SNM = 155648, SRS = SNM + 576, SCOL = SNM + 1280, DUMP = SNM + 3072;
constexpr int LDS_BYTES = 163840;
constexpr size_t MB = 1u << 20;
constexpr size_t WS_XN = 1 * MB, WS_O = 6 * MB, WS_X = 22 * MB, WS_KV = 34 * MB, WS_ST = 50 * MB, WS_CS = 52 * MB, WS_FL = 53 * MB;
constexpr int FL_O = 0;
constexpr int FL_X = 4096;
constexpr int FL_KV = 8192;
constexpr int FL_L = 10240;
constexpr int FL_C = 11264;
constexpr int FL_EP = 12288;

#define GL_HEAD "s_mov_b32 m0, %1\n\ts_nop 4\n\tglobal_load_lds_dwordx4 %2, %0 sc1"
DEV void glds1(const void* sb, unsigned lds, unsigned v0) { asm volatile(GL_HEAD ::"s"(sb), "s"(lds), "v"(v0) : "memory"); }
DEV void glds2(const void* sb, unsigned lds, unsigned v0, unsigned v1) {
    asm volatile(GL_HEAD "\n\tglobal_load_lds_dwordx4 %3, %0 offset:1024 sc1" ::"s"(sb), "s"(lds), "v"(v0), "v"(v1) : "memory");
}
DEV void glds4(const void* sb, unsigned lds, unsigned v0, unsigned v1, unsigned v2, unsigned v3) {
    asm volatile(GL_HEAD "\n\tglobal_load_lds_dwordx4 %3, %0 offset:1024 sc1\n\tglobal_load_lds_dwordx4 %4, %0 offset:2048 sc1\n\t"
                 "global_load_lds_dwordx4 %5, %0 offset:3072 sc1" ::"s"(sb), "s"(lds), "v"(v0), "v"(v1), "v"(v2), "v"(v3) : "memory");
}
DEV u64 ldflag(const u64* p) { return __hip_atomic_load((const GAS u64*)p, __ATOMIC_RELAXED, __HIP_MEMORY_SCOPE_AGENT); }
DEV void stflag(u64* p, u64 v) { __hip_atomic_store((GAS u64*)p, v, __ATOMIC_RELAXED, __HIP_MEMORY_SCOPE_AGENT); }
template <class F> DEV unsigned poll(const u64* p, u64 tag, unsigned need, F fold) {
    unsigned m = ~0u;
    for (int it = 0; it < POLLMAX; ++it) {
        const unsigned r = fold(__ballot(ldflag(p) == tag));
        if ((r & need) == need) { m = r; break; }
        __builtin_amdgcn_s_sleep(1);
    }
    asm volatile("" ::: "memory");
    return m;
}
DEV void publish(u64* f, int lane, u64 tag) {
    WAITV(0);
    if (lane == 0) stflag(f, tag);
}
#define OPAQUE(l, a, b, c) do { asm volatile("" : "+v"(l)); asm volatile("" : "+s"(a), "+s"(b), "+s"(c)); } while (0)

template <bool LOCAL, class F>
DEV void qkv_main(lchar* smem, f32x4 (&acc)[6][4], f32x4 (&acch)[2], int lane, int wm, int wn, F tailread) {
    constexpr int XC_B = LOCAL ? 9216 : 8192, HO = LOCAL ? 1024 : 0, BROW = 384, NR = LOCAL ? 17 : 16;
    const int l15 = lane & 15, lg = lane >> 4, hs = LOCAL ? wm : 0;
    const lchar* xhb = smem + l15 * 64 + ((lg ^ fz(l15)) << 4);
    const lchar* xb = xhb + HO + wm * 4096;
    const int k0 = 8 * lg + (l15 >> 2);
    const lchar* wb0 = smem + WQ0 + k0 * BROW + (((wn * 2 + hs) ^ fswz(k0)) << 5) + (l15 & 3) * 8;
    const lchar* wb1 = smem + WQ0 + k0 * BROW + (((wn * 2 + (hs ^ 1)) ^ fswz(k0)) << 5) + (l15 & 3) * 8;
#pragma unroll
    for (int a = 0; a < 6; ++a)
#pragma unroll
        for (int b = 0; b < 4; ++b) { acc[a][b] = (f32x4){0.f, 0.f, 0.f, 0.f}; asm volatile("" : "+v"(acc[a][b])); }
    acch[0] = acch[1] = (f32x4){0.f, 0.f, 0.f, 0.f};
    if (LOCAL) asm volatile("" : "+v"(acch[0]), "+v"(acch[1]), "+v"(xhb));
    asm volatile("" : "+v"(xb), "+v"(wb0), "+v"(wb1));
    bf16x8 xf[2][5];
    s16x4 wh[2][6][2];
    auto rd = [&](int b, int j, int xo, int wo) {
        if (j < 4) xf[b][j] = ldk(xb + xo + j * 1024);
        else if (j < 16) wh[b][(j - 4) >> 1][j & 1] = ldtr4(((j & 2) ? wb1 : wb0) + wo + ((j - 4) >> 2) * 128 + (j & 1) * 4 * BROW);
        else xf[b][4] = ldk(xhb + xo);
    };
    auto kstep = [&](int b, int xo, int wo, bool last) {
        int j = 0;
#pragma unroll
        for (int ni = 0; ni < 6; ++ni) {
            const bf16x8 w = cat8(wh[b][ni][0], wh[b][ni][1]);
#pragma unroll
            for (int ti = 0; ti < 5; ++ti) {
                if (ti == 4) {
                    if (!LOCAL || (ni != 2 && ni != 4)) continue;
                    if (ni == 2) mfma16t(acch[0], w, xf[b][4]);
                    else mfma16t(acch[1], xf[b][4], w);
                } else {
                    if (LOCAL && ni >= 4) mfma16t(acc[ni][ti], xf[b][ti], w);
                    else mfma16t(acc[ni][ti], w, xf[b][ti]);
                }
                SB();
                if (last) tailread(j);
                else if (j < NR) rd(b ^ 1, j, xo, wo);
                SB();
                ++j;
            }
        }
        if (!last) { LGKM0(); SB(); }
    };
    BAR(); SB();
#pragma unroll
    for (int j = 0; j < NR; ++j) rd(0, j, 0, 2 * WQS);
    LGKM0(); SB();
#pragma unroll 1
    for (int it = 0; it < 5; ++it) {
#pragma unroll
        for (int s = 0; s < 3; ++s) {
            kstep(0, s * XS + XC_B, ((s + 2) % 3) * WQS + 32 * BROW, false);
            BAR(); SB();
            kstep(1, ((s + 1) % 3) * XS, s * WQS, false);
        }
    }
    kstep(0, XC_B, 2 * WQS + 32 * BROW, false);
    BAR(); SB();
    kstep(1, 0, 0, true);
    MFMA_DONE();
}

DEV void qa_compute(lchar* smem, bool fold, u16* O, u64* fo, u64 tag, int g, int hp, int lane, int wid STPARG) {
    OPAQUE(lane, wid, g, hp);
    const int wm = wid >> 1, wn = wid & 1, l15 = lane & 15, lg = lane >> 4;
    f32x4 acc[6][4], acch[2];
    float nm[5], rs[5], csv[2];
    f32x4 nmv[5], rsv[5], cs[4];
    const lchar* sa = smem + SNM + l15 * 4;
    const lchar* sb = smem + SNM + lg * 16;
    const lchar* sc = smem + SCOL + wn * 128;
    const int ro = 64 + wm * 256;
    f32x4 cprev, ccur;
#pragma unroll
    for (int j = 0; j < 4; ++j) {
        const int d = l15 - 4 * lg - j;
        ccur[j] = (d >= 0 && !(d & 1)) ? 0.f : -3.0e38f;
        cprev[j] = (d <= -2 && !(d & 1)) ? 0.f : -3.0e38f;
    }
    const char* optr = (const char*)O + ((size_t)(hp * 2 + wn) * SEQ + g * 128 + wm * 64 + l15) * 64 +
                       (((((lg & 1) << 1) | (lg >> 1)) ^ (wm << 1) ^ fz(l15)) << 4);
    asm volatile("" : "+v"(cprev), "+v"(ccur), "+v"(optr));
    qkv_main<true>(smem, acc, acch, lane, wm, wn, [&](int j) {
        if (j < 4) cs[j] = *(const LAS f32x4*)(sc + (j >> 1) * 256 + (((j & 1) ^ wm) << 6) + lg * 16);
        else if (j < 9) { const int o = j < 8 ? ro + (j - 4) * 64 : 0; nm[j - 4] = *(const LAS float*)(sa + o); rs[j - 4] = *(const LAS float*)(sa + o + 576); }
        else if (j < 11) csv[j - 9] = *(const LAS float*)(sc + 512 + (((j - 9) ^ wm) << 6) + l15 * 4);
        else if (j < 21) {
            const int t = (j - 11) >> 1, o = t < 4 ? ro + t * 64 : 0;
            if ((j - 11) & 1) rsv[t] = *(const LAS f32x4*)(sb + o + 576);
            else nmv[t] = *(const LAS f32x4*)(sb + o);
        }
    });
    STAMP(2);
    if (fold) {
#pragma unroll
        for (int ti = 0; ti < 4; ++ti) {
#pragma unroll
            for (int ni = 0; ni < 4; ++ni) acc[ni][ti] = (acc[ni][ti] + nm[ti] * cs[ni]) * rs[ti];
#pragma unroll
            for (int h = 0; h < 2; ++h) acc[4 + h][ti] = (acc[4 + h][ti] + nmv[ti] * csv[h]) * rsv[ti];
        }
        acch[0] = (acch[0] + nm[4] * cs[2]) * rs[4];
        acch[1] = (acch[1] + nmv[4] * csv[0]) * rsv[4];
    }
    u32x2 kp[5][2], vp[5][2];
    lchar* ex = smem + EXO + wn * 4096 + lane * 16;
    {
        const u32x2 a = pk4(acch[0]), b = pk4(acch[1]);
        *(LAS u32x4*)(ex + wm * 1024) = (u32x4){a[0], a[1], b[0], b[1]};
    }
#pragma unroll
    for (int h = 0; h < 2; ++h) { kp[4][h] = pk4(acc[2 + h][3]); vp[4][h] = pk4(acc[4 + h][3]); }
    if (wm == 0) {
#pragma unroll
        for (int h = 0; h < 2; ++h) *(LAS u32x4*)(ex + 2048 + h * 1024) = (u32x4){kp[4][h][0], kp[4][h][1], vp[4][h][0], vp[4][h][1]};
    }
    bf16x8 qf[4];
#pragma unroll
    for (int ti = 0; ti < 4; ++ti) {
        qf[ti] = cat(pk4(acc[0][ti]), pk4(acc[1][ti]));
        if (ti < 3) {
#pragma unroll
            for (int h = 0; h < 2; ++h) { kp[ti + 1][h] = pk4(acc[2 + h][ti]); vp[ti + 1][h] = pk4(acc[4 + h][ti]); }
        }
    }
    SB();
    BARL();
#pragma unroll
    for (int h = 0; h < 2; ++h) {
        const u32x4 e = *(const LAS u32x4*)(ex + wm * 2048 + ((h ^ wm) << 10));
        kp[0][h] = (u32x2){e[0], e[1]}; vp[0][h] = (u32x2){e[2], e[3]};
    }
    f32x4 cp0 = cprev;
    if (g == 0 && wm == 0) cp0 = (f32x4){-3.0e38f, -3.0e38f, -3.0e38f, -3.0e38f};
    f32x4 sv[4];
    float mx[4];
#pragma unroll
    for (int t = 0; t < 4; ++t) {
        const int ti = 3 - t;
        const f32x4 s1 = MFMA16(cat(kp[ti + 1][0], kp[ti + 1][1]), qf[ti], ccur);
        const f32x4 s0 = MFMA16(cat(kp[ti][0], kp[ti][1]), qf[ti], ti ? cprev : cp0);
        f32x4 v = {fmx(s0[0], s0[1]), fmx(s0[2], s0[3]), fmx(s1[0], s1[1]), fmx(s1[2], s1[3])};
        if (EMU) {
            const u32x2 r1 = pk4(v);
            const u32x2 r2 = pk4((f32x4){lo16(r1[0]), hi16(r1[0]), lo16(r1[1]), hi16(r1[1])} * QSCALE);
            v = (f32x4){lo16(r2[0]), hi16(r2[0]), lo16(r2[1]), hi16(r2[1])};
        }
        sv[ti] = v;
        mx[ti] = fmx(fmx(fmx(v[0], v[1]), v[2]), v[3]);
    }
    {
        const f32x2 a = sw16f(mx[0], mx[1]), b = sw16f(mx[2], mx[3]);
        const f32x2 c = sw32f(fmx(a[0], a[1]), fmx(b[0], b[1]));
        const float r = fmx(c[0], c[1]) * -(EMU ? LOG2E : QSCALE * LOG2E);
        const f32x2 d = sw32f(r, r), e = sw16f(d[0], d[0]), f = sw16f(d[1], d[1]);
        mx[0] = e[0]; mx[1] = e[1]; mx[2] = f[0]; mx[3] = f[1];
    }
    const unsigned sel0 = (lane & 1) ? 0x01000c0cu : 0x0c0c0100u, sel1 = (lane & 1) ? 0x03020c0cu : 0x0c0c0302u;
    const bf16x8 ones = {0x3f80, 0x3f80, 0x3f80, 0x3f80, 0x3f80, 0x3f80, 0x3f80, 0x3f80};
    const f32x4 z = {0.f, 0.f, 0.f, 0.f};
    u32x4 ov[4];
#pragma unroll
    for (int ti = 0; ti < 4; ++ti) {
        const f32x4 x = sv[ti] * (EMU ? LOG2E : QSCALE * LOG2E) + mx[ti];
        const unsigned c0 = cvtpk(__builtin_amdgcn_exp2f(x[0]), __builtin_amdgcn_exp2f(x[1]));
        const unsigned c1 = cvtpk(__builtin_amdgcn_exp2f(x[2]), __builtin_amdgcn_exp2f(x[3]));
        const bf16x8 p = __builtin_bit_cast(bf16x8, (u32x4){__builtin_amdgcn_perm(c0, c0, sel0), __builtin_amdgcn_perm(c0, c0, sel1),
                                                            __builtin_amdgcn_perm(c1, c1, sel0), __builtin_amdgcn_perm(c1, c1, sel1)});
        const f32x4 l = MFMA16(ones, p, z);
        const f32x4 o0 = MFMA16(cat(vp[ti][0], vp[ti + 1][0]), p, z);
        const f32x4 o1 = MFMA16(cat(vp[ti][1], vp[ti + 1][1]), p, z);
        const float inv = __builtin_amdgcn_rcpf(l[0]);
        ov[ti] = unit16(pk4(o0 * inv), pk4(o1 * inv));
    }
    st16wt<0>(optr, ov[0]); st16wt<1024>(optr, ov[1]); st16wt<2048>(optr, ov[2]); st16wt<3072>(optr, ov[3]);
    STAMP(10);
    publish(fo + (wn * 32 + g * 2 + wm) * 16 + hp, lane, tag);
    STAMP(11);
}

#define MX3(a, b, c) fmx(fmx((a), (b)), (c))
DEV float rowmax32(const f32x16& s0) {
    float ma = MX3(s0[0], s0[1], s0[2]), mb = MX3(s0[3], s0[4], s0[5]);
    ma = MX3(ma, s0[6], s0[7]); mb = MX3(mb, s0[8], s0[9]);
    ma = MX3(ma, s0[10], s0[11]); mb = MX3(mb, s0[12], s0[13]);
    ma = fmx(MX3(ma, s0[14], s0[15]), mb);
    const f32x2 b = sw32f(ma, ma);
    return fmx(b[0], b[1]);
}
#define EX(i) do { S[i] = __builtin_amdgcn_exp2f(S[i]); SB(); } while (0)
#define CV(d, i) do { d = cvtpk(S[i], S[i + 1]); SB(); } while (0)
template <class F0, class F1, class F2>
DEV void block(f32x16& S, f32x16& N, const bf16x8& kf0, const bf16x8& kf1, const bf16x8& q0, const bf16x8& q1, const f32x16& c,
               f32x16& oP, f32x4& lP, const bf16x8& vf1, u32x4& p1, f32x16& oC, f32x4& lC, const bf16x8& vf0,
               const bf16x8& sel, F0 ldK, F1 ldV1, F2 ldV0) {
    u32x4 p0;
    N = MFMA32(kf0, q0, c); SB();
    EX(0); EX(1); EX(2);
    N = MFMA32(kf1, q1, N); SB();
    ldK(); SB();
    EX(3); CV(p0[0], 0); CV(p0[1], 2); EX(4);
    oP = MFMA32(vf1, __builtin_bit_cast(bf16x8, p1), oP); SB();
    ldV1(); SB();
    EX(5); EX(6); EX(7);
    lP = MFMA16(sel, __builtin_bit_cast(bf16x8, p1), lP); SB();
    CV(p0[2], 4); CV(p0[3], 6); EX(8);
    oC = MFMA32(vf0, __builtin_bit_cast(bf16x8, p0), oC); SB();
    ldV0(); SB();
    EX(9); EX(10); EX(11);
    lC = MFMA16(sel, __builtin_bit_cast(bf16x8, p0), lC); SB();
    EX(12); CV(p1[0], 8); CV(p1[1], 10); EX(13); EX(14); EX(15); CV(p1[2], 12); CV(p1[3], 14);
    asm volatile("" : "+v"(p1)); SB();
}
DEV void attn_slow(const u16* kh, const u16* vh, const bf16x8& q0, const bf16x8& q1, f32x16& o, float& l, int lane) {
    const int r = lane & 31, hh = lane >> 5;
    f32x16 c;
#pragma unroll
    for (int i = 0; i < 16; ++i) { o[i] = 0.f; c[i] = 0.f; }
    float m = 0.f;
    l = 0.f;
#pragma unroll 1
    for (int t = 0; t < SEQ / 32; ++t) {
        u32x4 k0, k1;
        const u16* kp = kh + (t * 32 + r) * 32;
        asm volatile("global_load_dwordx4 %0, %2, off sc1\n\tglobal_load_dwordx4 %1, %3, off sc1\n\ts_waitcnt vmcnt(0)"
                     : "=&v"(k0), "=&v"(k1) : "v"(kp + ((hh ^ ((r >> 2) & 3)) << 3)), "v"(kp + (((2 + hh) ^ ((r >> 2) & 3)) << 3)) : "memory");
        bf16x8 v0, v1;
#pragma unroll
        for (int e = 0; e < 8; ++e) {
            const int key = (e & 3) + 8 * (e >> 2) + 4 * hh, pos = (((r >> 3) ^ ((key >> 1) & 3)) << 3) + (r & 7);
            v0[e] = (short)__hip_atomic_load((const GAS u16*)vh + (t * 32 + key) * 32 + pos, __ATOMIC_RELAXED, __HIP_MEMORY_SCOPE_AGENT);
            v1[e] = (short)__hip_atomic_load((const GAS u16*)vh + (t * 32 + 16 + key) * 32 + pos, __ATOMIC_RELAXED, __HIP_MEMORY_SCOPE_AGENT);
        }
        f32x16 s = MFMA32(__builtin_bit_cast(bf16x8, k0), q0, c);
        s = MFMA32(__builtin_bit_cast(bf16x8, k1), q1, s);
        const float mx = rowmax32(s);
        const float dl = t == 0 ? mx : fmx(mx, 0.f);
        const float alpha = t == 0 ? 0.f : __builtin_amdgcn_exp2f(-dl);
        m += dl;
        l *= alpha;
        unsigned p[8];
#pragma unroll
        for (int i = 0; i < 16; ++i) { o[i] *= alpha; c[i] = -m; }
#pragma unroll
        for (int i = 0; i < 8; ++i) {
            p[i] = cvtpk(__builtin_amdgcn_exp2f(s[2 * i] - dl), __builtin_amdgcn_exp2f(s[2 * i + 1] - dl));
            l += lo16(p[i]) + hi16(p[i]);
        }
        o = MFMA32(v0, __builtin_bit_cast(bf16x8, (u32x4){p[0], p[1], p[2], p[3]}), o);
        o = MFMA32(v1, __builtin_bit_cast(bf16x8, (u32x4){p[4], p[5], p[6], p[7]}), o);
    }
    const f32x2 b = sw32f(l, l);
    l = b[0] + b[1];
}

DEV void qb_compute(lchar* smem, u16* kv, u16* O, u64* fkv, u64* fo, u64 tag, int g, int hp, int lane, int wid STPARG) {
    OPAQUE(lane, wid, g, hp);
    const int wm = wid >> 1, wn = wid & 1, l15 = lane & 15, lg = lane >> 4, h = hp * 2 + wn;
    bf16x8 qa0, qa1, qb0, qb1;
    lchar* gs = smem + wn * 16384;
    {
        f32x4 acc[6][4], acch[2];
        float nm[4], rs[4];
        f32x4 cs[6];
        const lchar* sa = smem + SNM + 64 + wm * 256 + l15 * 4;
        const lchar* sc = smem + SCOL + 768 + wn * 128 + lg * 16;
        const int un = ((lg & 1) << 1) | (lg >> 1);
        const int ko = l15 * 64 + ((un ^ (l15 >> 2)) << 4), vo = l15 * 64 + ((un ^ ((l15 >> 1) & 3)) << 4);
        const char* kptr = (const char*)kv + ((size_t)h * SEQ + g * 128 + wm * 64) * 64 + ko;
        const char* vptr = (const char*)kv + ((size_t)(NH + h) * SEQ + g * 128 + wm * 64) * 64 + vo;
        lchar* kl = gs + wm * 8192 + ko;
        lchar* vl = gs + wm * 8192 + 4096 + vo;
        asm volatile("" : "+v"(kptr), "+v"(vptr));
        qkv_main<false>(smem, acc, acch, lane, wm, wn, [&](int j) {
            if (j < 6) cs[j] = *(const LAS f32x4*)(sc + (j >> 1) * 256 + (j & 1) * 64);
            else if (j < 10) { nm[j - 6] = *(const LAS float*)(sa + (j - 6) * 64); rs[j - 6] = *(const LAS float*)(sa + (j - 6) * 64 + 576); }
        });
        STAMP(2);
        auto tile = [&](int ni, int ti, float sc_) { return pk4((acc[ni][ti] + nm[ti] * cs[ni]) * sc_); };
#define KVOUT(ni, ti, gp, lp) do { const u32x4 v = unit16(tile(ni, ti, rs[ti]), tile(ni + 1, ti, rs[ti])); st16wt<ti * 1024>(gp, v); *(LAS u32x4*)(lp + ti * 1024) = v; } while (0)
        KVOUT(2, 0, kptr, kl); KVOUT(2, 1, kptr, kl); KVOUT(2, 2, kptr, kl); KVOUT(2, 3, kptr, kl);
        KVOUT(4, 0, vptr, vl); KVOUT(4, 1, vptr, vl); KVOUT(4, 2, vptr, vl); KVOUT(4, 3, vptr, vl);
#pragma unroll
        for (int ti = 0; ti < 4; ++ti) rs[ti] *= QSCALE * LOG2E;
        qa0 = __builtin_bit_cast(bf16x8, unit16(tile(0, 0, rs[0]), tile(0, 1, rs[1])));
        qa1 = __builtin_bit_cast(bf16x8, unit16(tile(1, 0, rs[0]), tile(1, 1, rs[1])));
        qb0 = __builtin_bit_cast(bf16x8, unit16(tile(0, 2, rs[2]), tile(0, 3, rs[3])));
        qb1 = __builtin_bit_cast(bf16x8, unit16(tile(1, 2, rs[2]), tile(1, 3, rs[3])));
    }
    STAMP(3);
    const int r = lane & 31, hh = lane >> 5, sw = (r >> 2) & 3;
    const int vrow = 4 * (lg >> 1) + (l15 >> 2);
    const lchar* kb0 = gs + r * 64 + ((hh ^ sw) << 4);
    const lchar* kb1 = gs + r * 64 + (((2 + hh) ^ sw) << 4);
    const lchar* vb = gs + 4096 + vrow * 64 + (((((lg & 1) << 1) | ((l15 >> 1) & 1)) ^ ((vrow >> 1) & 3)) << 4) + (l15 & 1) * 8;
    const short one = ((l15 & 1) == (lg & 1)) ? (short)0x3f80 : (short)0;
    const bf16x8 sel = {one, one, one, one, one, one, one, one};
    f32x16 oA, oB, cA, cB, sA, sB;
    f32x4 lA = {0.f, 0.f, 0.f, 0.f}, lB = {0.f, 0.f, 0.f, 0.f};
#pragma unroll
    for (int i = 0; i < 16; ++i) { oA[i] = 0.f; oB[i] = 0.f; }
    bf16x8 kf0, kf1, vf0, vf1;
    u32x4 p1 = {0u, 0u, 0u, 0u};
    auto nop = [&]() {};
    auto tilestep = [&](int cu, int nx) {
        block(sA, sB, kf0, kf1, qb0, qb1, cB, oB, lB, vf1, p1, oA, lA, vf0, sel,
              [&]() { kf0 = ldk(kb0 + nx); kf1 = ldk(kb1 + nx); }, [&]() { vf1 = ldv(vb + cu + 1024); }, nop);
        block(sB, sA, kf0, kf1, qa0, qa1, cA, oA, lA, vf1, p1, oB, lB, vf0, sel, nop, nop, [&]() { vf0 = ldv(vb + nx); });
    };
    {
        const int own = wm * 8192, oth = own ^ 8192;
        kf0 = ldk(kb0 + own); kf1 = ldk(kb1 + own);
        vf0 = ldv(vb + own); vf1 = vf0;
        sA = MFMA32(kf0, qa0, oA); sB = MFMA32(kf0, qb0, oA);
        sA = MFMA32(kf1, qa1, sA); sB = MFMA32(kf1, qb1, sB);
        const float mA = rowmax32(sA), mB = rowmax32(sB);
#pragma unroll
        for (int i = 0; i < 16; ++i) { cA[i] = -mA; cB[i] = -mB; sA[i] -= mA; }
        asm volatile("" : "+v"(cA), "+v"(cB));
        SB();
        publish(fkv + wn * 32 + g * 2 + wm, lane, tag);
        SB();
        STAMP(5);
        tilestep(own, own + 2048);
        BARL(); SB();
        tilestep(own + 2048, oth);
        tilestep(oth, oth + 2048);
        BARL(); SB();
        STAMP(6);
        tilestep(oth + 2048, AR1);
    }
#pragma unroll 1
    for (int it = 0; it < 5; ++it) {
#pragma unroll
        for (int s = 0; s < 3; ++s) {
            const int cur = s == 0 ? AR1 : s == 1 ? AR2 : 0, nxt = s == 0 ? AR2 : s == 1 ? 0 : AR1;
            tilestep(cur, cur + 2048);
            tilestep(cur + 2048, cur + 8192);
            tilestep(cur + 8192, cur + 10240);
            BARL(); SB();
            tilestep(cur + 10240, nxt);
        }
    }
    oB = MFMA32(vf1, __builtin_bit_cast(bf16x8, p1), oB);
    lB = MFMA16(sel, __builtin_bit_cast(bf16x8, p1), lB);
    STAMP(9);
    float la = (lane & 16) ? lA[1] : lA[0], lb = (lane & 16) ? lB[1] : lB[0];
    if (__any(!(la <= 1073741824.0f) || !(lb <= 1073741824.0f))) {
        const u16* kh = kv + (size_t)h * SEQ * 32;
#pragma unroll 1
        for (int ps = 0; ps < 2; ++ps) {
            oA = oB; la = lb;
            attn_slow(kh, kh + (size_t)NH * SEQ * 32, ps ? qb0 : qa0, ps ? qb1 : qa1, oB, lb, lane);
        }
    }
    const char* op = (const char*)O + ((size_t)h * SEQ + g * 128 + wm * 64 + r) * 64;
    const char* op0 = op + ((hh ^ fz(r)) << 4);
    const char* op1 = op + (((2 + hh) ^ fz(r)) << 4);
    auto fin = [&](const f32x16& o, float l, u32x4& u0, u32x4& u1) {
        const f32x16 y = o * __builtin_amdgcn_rcpf(l);
        const u32x2 a = sw32(cvtpk(y[0], y[1]), cvtpk(y[4], y[5])), b = sw32(cvtpk(y[2], y[3]), cvtpk(y[6], y[7]));
        const u32x2 c = sw32(cvtpk(y[8], y[9]), cvtpk(y[12], y[13])), d = sw32(cvtpk(y[10], y[11]), cvtpk(y[14], y[15]));
        u0 = (u32x4){a[0], b[0], a[1], b[1]};
        u1 = (u32x4){c[0], d[0], c[1], d[1]};
    };
    u32x4 u0, u1;
    fin(oA, la, u0, u1);
    st16wt<0>(op0, u0); st16wt<0>(op1, u1);
    fin(oB, lb, u0, u1);
    st16wt<2048>(op0, u0); st16wt<2048>(op1, u1);
    BARL();
    STAMP(10);
    publish(fo + (wn * 32 + g * 2 + wm) * 16 + hp, lane, tag);
    STAMP(11);
}

DEV void wo_compute(lchar* smem, const u16* R, bool rcm, u16* C, char* stats, u64* fx, u64 tag, int g, int cb, int lane, int wid STPARG) {
    OPAQUE(lane, wid, g, cb);
    const int wm = wid >> 1, wn = wid & 1, l15 = lane & 15, lg = lane >> 4;
    const int un = ((lg & 1) << 1) | (lg >> 1), row = g * 128 + wm * 64 + l15, ch = cb * 2 + wn;
    const unsigned ocm = (ch * SEQ + row) * 64 + ((un ^ fz(l15)) << 4), orm = row * 2048 + ch * 64 + un * 16;
    u32x4 rr[4];
    {
        const char* p = (const char*)R + (rcm ? ocm : orm);
        const unsigned st = rcm ? 1024 : 32768;
        asm volatile("global_load_dwordx4 %0, %4, off sc1\n\tglobal_load_dwordx4 %1, %5, off sc1\n\tglobal_load_dwordx4 %2, %6, off sc1\n\t"
                     "global_load_dwordx4 %3, %7, off sc1\n\ts_waitcnt vmcnt(0)"
                     : "=&v"(rr[0]), "=&v"(rr[1]), "=&v"(rr[2]), "=&v"(rr[3]) : "v"(p), "v"(p + st), "v"(p + 2 * st), "v"(p + 3 * st) : "memory");
    }
    f32x4 acc[2][4];
#pragma unroll
    for (int ti = 0; ti < 4; ++ti) {
        const u32x2 x = sw16(rr[ti][0], rr[ti][2]), y = sw16(rr[ti][1], rr[ti][3]);
#pragma unroll
        for (int ni = 0; ni < 2; ++ni) acc[ni][ti] = (f32x4){lo16(x[ni]), hi16(x[ni]), lo16(y[ni]), hi16(y[ni])};
    }
    const char* cp = (const char*)C + (stats ? ocm : orm);
    const char* sp = stats + (ch * SEQ + g * 128 + wm * 64 + lane) * 8;
    const lchar* xb = smem + (wm * 64 + l15) * 64 + ((lg ^ fz(l15)) << 4);
    const int k0 = 8 * lg + (l15 >> 2);
    const lchar* wb0 = smem + WO0 + k0 * 128 + (((wn * 2) ^ fswz(k0)) << 5) + (l15 & 3) * 8;
    const lchar* wb1 = smem + WO0 + k0 * 128 + (((wn * 2 + 1) ^ fswz(k0)) << 5) + (l15 & 3) * 8;
    asm volatile("" : "+v"(cp), "+v"(sp), "+v"(xb), "+v"(wb0), "+v"(wb1));
    bf16x8 xf[2][8];
    s16x4 wh[2][8];
    auto rdx = [&](int t, int j) { xf[t & 1][j] = ldk(xb + (t & 3) * 16384 + (j >> 2) * 8192 + (j & 3) * 1024); };
    auto rdw = [&](int t, int j) { wh[t & 1][j] = ldtr4(((j & 2) ? wb1 : wb0) + (t % 11) * 8192 + (j >> 2) * 4096 + (j & 1) * 512); };
    auto mm = [&](int t, int kk, int j) {
        const int ti = j >> 1, ni = j & 1;
        mfma16t(acc[ni][ti], cat8(wh[t & 1][kk * 4 + ni * 2], wh[t & 1][kk * 4 + ni * 2 + 1]), xf[t & 1][kk * 4 + ti]);
    };
    SB();
#pragma unroll
    for (int t = 0; t < 16; ++t) {
        BAR(); SB();
        if (t == 0) {
            STAMP(4);
#pragma unroll
            for (int j = 0; j < 4; ++j) { rdw(0, j); rdx(0, j); }
#pragma unroll
            for (int j = 4; j < 8; ++j) { rdw(0, j); rdx(0, j); }
            SB();
        } else {
#pragma unroll
            for (int j = 0; j < 8; ++j) { rdx(t, j); SB(); mm(t - 1, 1, j); SB(); }
        }
#pragma unroll
        for (int j = 0; j < 8; ++j) { mm(t, 0, j); SB(); if (t < 15) { rdw(t + 1, j); SB(); } }
        LGKM0(); SB();
    }
    BAR(); SB();
#pragma unroll
    for (int j = 0; j < 8; ++j) mm(15, 1, j);
    MFMA_DONE();
    STAMP(2);
    u32x4 ov[4];
#pragma unroll
    for (int ti = 0; ti < 4; ++ti) ov[ti] = unit16(pk4(acc[0][ti]), pk4(acc[1][ti]));
    if (stats) {
        st16wt<0>(cp, ov[0]); st16wt<1024>(cp, ov[1]); st16wt<2048>(cp, ov[2]); st16wt<3072>(cp, ov[3]);
        float s[4], q[4];
        const bf16x2_t one2 = __builtin_bit_cast(bf16x2_t, 0x3f803f80u);
#pragma unroll
        for (int ti = 0; ti < 4; ++ti) {
            s[ti] = 0.f; q[ti] = 0.f;
#pragma unroll
            for (int e = 0; e < 4; ++e) {
                const unsigned u = ov[ti][e];
                const bf16x2_t v = __builtin_bit_cast(bf16x2_t, u);
                s[ti] = __builtin_amdgcn_fdot2_f32_bf16(v, one2, s[ti], false);
                q[ti] = __builtin_amdgcn_fdot2_f32_bf16(v, v, q[ti], false);
            }
        }
        auto red = [&](const float (&v)[4]) {
            const f32x2 a = sw16f(v[0], v[1]), b = sw16f(v[2], v[3]);
            const f32x2 c = sw32f(a[0] + a[1], b[0] + b[1]);
            return c[0] + c[1];
        };
        st8wt(sp, (u32x2){__float_as_uint(red(s)), __float_as_uint(red(q))});
        STAMP(10);
        publish(fx + (wn * 32 + g * 2 + wm) * 16 + cb, lane, tag);
        STAMP(11);
    } else {
#pragma unroll
        for (int ti = 0; ti < 4; ++ti) *(GAS u32x4*)(cp + ti * 32768) = ov[ti];
        STAMP(10);
    }
}

DEV void ln0_rows(u32x4 (&x)[4]) {
    auto sum32 = [&](float v) {
        v = dppadd<0x128>(dppadd<0x4E>(dppadd<0xB1>(v)));
        const f32x2 a = sw16f(v, v);
        v = a[0] + a[1];
        const f32x2 b = sw32f(v, v);
        return b[0] + b[1];
    };
    f32x2 v[16];
#pragma unroll
    for (int i = 0; i < 16; ++i) v[i] = (f32x2){lo16(x[i >> 2][i & 3]), hi16(x[i >> 2][i & 3])};
    f32x2 s = v[0];
#pragma unroll
    for (int i = 1; i < 16; ++i) s += v[i];
    const float mean = sum32(s[0] + s[1]) * (1.0f / DM);
    f32x2 q = {0.f, 0.f};
#pragma unroll
    for (int i = 0; i < 16; ++i) { const f32x2 d = v[i] - mean; q += d * d; }
    const float var = sum32(q[0] + q[1]) * (1.0f / DM);
    const float mb = bfr(mean);
    const float inv = 1.0f / bfr(sqrtf(bfr(bfr(var) + 1e-5f)));
#pragma unroll
    for (int i = 0; i < 16; ++i) {
        const f32x2 e = v[i] - mb;
        const unsigned d = cvtpk(e[0], e[1]);
        const f32x2 y = (f32x2){lo16(d), hi16(d)} * inv;
        x[i >> 2][i & 3] = cvtpk(y[0], y[1]);
    }
}

struct HW { unsigned wq[16], wo[4], vo16, lds; };
DEV void wq_issue(const HW& h, const u16* W, int t, int lw) {
    const char* src = (const char*)W + (size_t)t * (64 * 6144);
    const unsigned l = h.lds + WQ0 + ((t + 2) % 3) * WQS + (lw < 2 ? lw * 4096 : 8192);
    if (lw < 3) glds4(src, l, h.wq[0], h.wq[1], h.wq[2], h.wq[3]);
    if (lw == 2) {
        glds4(src, l + 4096, h.wq[4], h.wq[5], h.wq[6], h.wq[7]);
        glds4(src, l + 8192, h.wq[8], h.wq[9], h.wq[10], h.wq[11]);
        glds4(src, l + 12288, h.wq[12], h.wq[13], h.wq[14], h.wq[15]);
    }
}
DEV void wo_issue(const HW& h, const u16* W, int t0, int t1) {
#pragma unroll 1
    for (int i = 2 * t0; i < 2 * t1; ++i) glds4((const char*)W + (size_t)i * (32 * 2048), h.lds + WO0 + i * 4096, h.wo[0], h.wo[1], h.wo[2], h.wo[3]);
}
DEV void bars(int n) {
#pragma unroll 1
    for (int i = 0; i < n; ++i) BARL();
}

template <bool LOCAL>
DEV void qkv_helper(lchar* smem, const HW& h, int layer, const u16* X, const u16* W, const u16* W2, const u16* Wo, const char* stats, float* csum,
                    u64* fl, u64 tag, int g, int hp, int lane, int lw STPARG) {
    OPAQUE(lane, lw, g, hp);
    constexpr int NX = LOCAL ? 9 : 8, HO = LOCAL ? 16 : 0;
    const int m0 = g * 128;
    const bool halo = LOCAL && g > 0;
    const u64* fx = fl + FL_X + (layer - 1) * 1024 + (g * 2 - (halo ? 1 : 0)) * 16 + (lane < (halo ? 48 : 32) ? lane : 0);
    auto fold = [&](u64 m) { return (unsigned)(m & (m >> 16) & (halo ? m >> 32 : m)); };
    if (lw < 2) {
        const unsigned vo0 = (LOCAL && g == 0) ? h.vo16 + 1024 : h.vo16;
        const char* xsrc = (const char*)X + ((size_t)lw * SEQ + m0 - HO) * 64;
        const unsigned ldx = h.lds + lw * (NX * 1024);
        unsigned rdy = 0u;
        STAMP(0);
        if (LOCAL && layer == 0) {
            const u64* p = fl + FL_L + g * 64 - (halo ? 8 : 0) + (lane < (halo ? 36 : 32) ? lane * 2 : 0);
            for (int it = 0; it < POLLMAX; ++it) {
                u32x4 f;
                asm volatile("global_load_dwordx4 %0, %1, off sc1\n\ts_waitcnt vmcnt(0)" : "=&v"(f) : "v"(p) : "memory");
                const unsigned tl = (unsigned)tag, th = (unsigned)(tag >> 32);
                if (__all(f[0] == tl && f[1] == th && f[2] == tl && f[3] == th)) break;
                __builtin_amdgcn_s_sleep(1);
            }
            rdy = ~0u;
        }
        auto issue = [&](int t, int so) {
            if (!((rdy >> t) & 1)) rdy = poll(fx + lw * 512, tag, 1u << t, fold);
            glds4(xsrc, ldx + so, vo0, h.vo16, h.vo16, h.vo16);
            glds4(xsrc + 4096, ldx + so + 4096, h.vo16, h.vo16, h.vo16, h.vo16);
            if (LOCAL) glds1(xsrc + 8192, ldx + so + 8192, h.vo16);
            xsrc += 2 * SEQ * 64;
        };
        issue(0, 0);
        STAMP(1);
        issue(1, XS);
        WAITB(NX);
        issue(2, 2 * XS);
        WAITB(NX);
        int so = 0;
#pragma unroll 1
        for (int t = 3; t < 16; ++t) {
            wq_issue(h, W, t, lw & 1);
            issue(t, so);
            so = so == 2 * XS ? 0 : so + XS;
            WAITB(NX + 4);
        }
        WAITB(0);
        BAR(); BAR();
    } else if (lw == 2) {
        WAITB(0);
        BAR();
#pragma unroll 1
        for (int t = 3; t < 15; ++t) { wq_issue(h, W, t, 2); WAITB(16); }
        wq_issue(h, W, 15, 2);
        if (LOCAL) {
            wo_issue(h, Wo, 0, 2);
            WAITB(32);
            wo_issue(h, Wo, 2, 5);
            WAITB(40);
            WAITV(24);
            wo_issue(h, Wo, 5, 8);
            BAR(); BAR();
            WAITV(24);
            wo_issue(h, Wo, 8, 10);
        } else {
            WAITB(16);
            WAITB(0);
            BAR(); BAR();
        }
    } else {
        if (layer < 2) {
            asm volatile("s_mov_b32 m0, %2\n\ts_nop 4\n\tglobal_load_lds_dword %0, %1\n\tglobal_load_lds_dword %3, %1" ::"v"(lane * 64),
                         "s"(Wo + (size_t)blockIdx.x * 4096), "s"(h.lds + DUMP), "v"(lane * 64 + 4096) : "memory");
        }
        if (LOCAL && layer == 0) {
            const int kg = blockIdx.x >> 4, j = blockIdx.x & 15;
            const unsigned vo = ((kg * 64 + (lane >> 3)) * 3072 + j * 64 + (lane & 7) * 8) * 2;
            char* dst = (char*)csum + (j * 16 + kg) * 768 + (lane & 7) * 32 + (lane >> 4) * 8;
#pragma unroll 1
            for (int w = 0; w < 2; ++w) {
                u32x4 sv[24];
                const u16* src = w ? W2 : W;
#pragma unroll
                for (int i = 0; i < 24; ++i)
                    asm volatile("s_nop 4\n\tglobal_load_dwordx4 %0, %1, %2" : "=&v"(sv[i]) : "v"(vo + (i & 7) * (8 * 6144) + (i >> 3) * 2048), "s"(src) : "memory");
                bars(3);
                WAITV(0);
#pragma unroll
                for (int i = 0; i < 24; ++i) asm volatile("" : "+v"(sv[i]));
#pragma unroll
                for (int part = 0; part < 3; ++part) {
                    f32x2 a[4];
#pragma unroll
                    for (int e = 0; e < 4; ++e) {
                        a[e] = (f32x2){0.f, 0.f};
#pragma unroll
                        for (int i = 0; i < 8; ++i) a[e] += (f32x2){lo16(sv[part * 8 + i][e]), hi16(sv[part * 8 + i][e])};
                        a[e][0] = dppadd<0x128>(a[e][0]); a[e][1] = dppadd<0x128>(a[e][1]);
                    }
                    float r[2];
#pragma unroll
                    for (int o = 0; o < 2; ++o) {
                        const f32x2 x = sw16f(a[0][o], a[1][o]), y = sw16f(a[2][o], a[3][o]);
                        const f32x2 c = sw32f(x[0] + x[1], y[0] + y[1]);
                        r[o] = c[0] + c[1];
                    }
                    if (!(lane & 8)) st8wt(dst + w * (256 * 768) + part * 256, (u32x2){__float_as_uint(r[0]), __float_as_uint(r[1])});
                    SB();
                    BARL();
                }
            }
            bars(3);
            publish(fl + FL_C + j * 16 + kg, lane, tag);
            bars(3);
        } else {
            auto fin = [&](float s, float q, int o) {
                const float mean = s * (1.0f / DM);
                const float var = fmx(q * (1.0f / DM) - mean * mean, 0.f);
                *(LAS float*)(smem + SNM + o) = -bfr(mean);
                *(LAS float*)(smem + SRS + o) = 1.0f / bfr(sqrtf(bfr(bfr(var) + 1e-5f)));
            };
            bars(3);
            poll(fx, tag, 0xffffu, fold);
            poll(fx + 512, tag, 0xffffu, fold);
            f32x4 sp[32], sh[4];
            const unsigned so = (layer - 1) * (unsigned)(MB / 2);
#pragma unroll
            for (int i = 0; i < 32; ++i)
                asm volatile("s_nop 4\n\tglobal_load_dwordx4 %0, %1, %2 sc1" : "=&v"(sp[i]) : "v"(so + m0 * 8 + lane * 16 + i * (SEQ * 8)), "s"(stats) : "memory");
            if (LOCAL) {
#pragma unroll
                for (int i = 0; i < 4; ++i)
                    asm volatile("s_nop 4\n\tglobal_load_dwordx4 %0, %1, %2 sc1" : "=&v"(sh[i])
                                 : "v"(so + (halo ? m0 - 16 : 0) * 8 + (lane & 7) * 16 + ((lane >> 3) * 4 + i) * (SEQ * 8)), "s"(stats) : "memory");
            }
            bars(3);
            WAITV(0);
#pragma unroll
            for (int i = 0; i < 32; ++i) asm volatile("" : "+v"(sp[i]));
            if (LOCAL) {
#pragma unroll
                for (int i = 0; i < 4; ++i) asm volatile("" : "+v"(sh[i]));
            }
            {
                f32x4 a = sp[0];
#pragma unroll
                for (int i = 1; i < 32; ++i) a += sp[i];
                fin(a[0], a[1], 64 + lane * 8);
                fin(a[2], a[3], 68 + lane * 8);
            }
            SB();
            BARL();
            if (LOCAL) {
                f32x4 a = (sh[0] + sh[1]) + (sh[2] + sh[3]);
#pragma unroll
                for (int e = 0; e < 4; ++e) {
                    float v = dppadd<0x128>(a[e]);
                    const f32x2 x = sw16f(v, v);
                    v = x[0] + x[1];
                    const f32x2 y = sw32f(v, v);
                    a[e] = y[0] + y[1];
                }
                fin(a[0], a[1], (lane & 7) * 8);
                fin(a[2], a[3], (lane & 7) * 8 + 4);
                bars(11);
            } else if (layer == 1) {
                poll(fl + FL_C + hp * 16 + (lane & 15), tag, 0xffffu, [](u64 m) { return (unsigned)m; });
                const unsigned vo = hp * (16 * 768) + (lane < 48 ? lane * 16 : 0);
#pragma unroll
                for (int i = 0; i < 32; ++i)
                    asm volatile("s_nop 4\n\tglobal_load_dwordx4 %0, %1, %2 sc1" : "=&v"(sp[i]) : "v"(vo + (i & 15) * 768 + (i >> 4) * (256 * 768)), "s"(csum) : "memory");
                bars(3);
                WAITV(0);
#pragma unroll
                for (int i = 0; i < 32; ++i) asm volatile("" : "+v"(sp[i]));
                f32x4 a = sp[0], b = sp[16];
#pragma unroll
                for (int i = 1; i < 16; ++i) { a += sp[i]; b += sp[16 + i]; }
                if (lane < 48) { *(LAS f32x4*)(smem + SCOL + lane * 16) = a; *(LAS f32x4*)(smem + SCOL + 768 + lane * 16) = b; }
                bars(8);
            } else {
                bars(11);
            }
        }
    }
}

DEV void attn_helper(const HW& h, const u16* kv, const u16* Wo, const u64* fkv, u64 tag, int g, int hp, int lane, int lw STPARG) {
    OPAQUE(lane, lw, g, hp);
    const int part = lw & 1, hd = lw >> 1;
    const char* src = (const char*)kv + (size_t)(part * NH + hp * 2 + hd) * SEQ * 64;
    const unsigned ld = h.lds + hd * 16384 + part * 4096;
    const u64* f = fkv + hd * 32 + (lane & 31);
    unsigned rdy = 0u;
    int reg = AR1;
    auto issue = [&](int c) {
        const int blk = c - 1 + (c - 1 >= g ? 1 : 0);
        if (((rdy >> (2 * blk)) & 3) != 3) rdy = poll(f, tag, 3u << (2 * blk), [](u64 m) { return (unsigned)m; });
        glds4(src + blk * 8192, ld + reg, h.vo16, h.vo16, h.vo16, h.vo16);
        glds4(src + blk * 8192 + 4096, ld + reg + 8192, h.vo16, h.vo16, h.vo16, h.vo16);
        reg = reg == AR1 ? AR2 : reg == AR2 ? 0 : AR1;
    };
    issue(1);
    STAMP(7);
#pragma unroll 1
    for (int c = 0; c < 16; ++c) {
        WAITB(0);
        if (c + 2 < 16) issue(c + 2);
        if (lw == 2) {
            if (c >= 4 && c < 11) wo_issue(h, Wo, c - 4, c - 3);
            if (c == 14) wo_issue(h, Wo, 7, 10);
        }
    }
    BAR();
}

DEV void wo_helper(const HW& h, const u16* O, const u16* Wo, const u16* Wn, const u64* fo, u64 tag, int g, int lane, int lw STPARG) {
    int dummy = 0;
    OPAQUE(lane, lw, g, dummy);
    const int p = lw & 1, rh = lw >> 1;
    const char* xsrc = (const char*)O + ((size_t)p * SEQ + g * 128 + rh * 64) * 64;
    const unsigned ldx = h.lds + p * 8192 + rh * 4096;
    const u64* f = fo + (p * 32 + g * 2 + rh) * 16 + (lane & 15);
    const char* wsrc = (const char*)Wo + (size_t)(640 + lw * 16) * 2048;
    unsigned rdy = 0u;
    auto issue = [&](int t) {
        if (!((rdy >> t) & 1)) rdy = poll(f, tag, 1u << t, [](u64 m) { return (unsigned)m; });
        glds4(xsrc, ldx + (t & 3) * 16384, h.vo16, h.vo16, h.vo16, h.vo16);
        xsrc += 2 * SEQ * 64;
    };
    auto tail = [&](int s) { glds2(wsrc + (size_t)s * (64 * 2048), h.lds + WO0 + ((10 + s) % 11) * 8192 + lw * 2048, h.wo[0], h.wo[1]); };
    STAMP(0);
    issue(0);
    STAMP(1);
    issue(1); issue(2); issue(3);
    WAITB(12); tail(0);
    WAITB(10); issue(4); tail(1);
    WAITB(12); issue(5); tail(2);
    WAITB(14); issue(6); tail(3);
    WAITB(14); issue(7); tail(4);
    WAITB(14); issue(8); tail(5);
    WAITB(14); issue(9);
    WAITB(12); issue(10);
    WAITB(10); issue(11);
#pragma unroll 1
    for (int t = 12; t < 16; ++t) { WAITB(8); issue(t); }
    const int pf = Wn ? lw : 3;
    wq_issue(h, Wn, 0, pf);
    wq_issue(h, Wn, 2, pf);
    if (pf < 2) { WAITB(16); WAITB(12); WAITB(8); }
    else if (pf == 2) { WAITB(40); WAITB(36); WAITB(32); }
    else { WAITB(8); WAITB(4); WAITB(0); }
    BAR();
    wq_issue(h, Wn, 1, pf);
}

__global__ __launch_bounds__(512) void mega(const u16* __restrict__ xin, const u16* __restrict__ wqa, const u16* __restrict__ woa,
                                            const u16* __restrict__ wqb, const u16* __restrict__ wob, u16* __restrict__ out,
                                            char* __restrict__ ws) {
    __shared__ __attribute__((aligned(16))) char smem_[LDS_BYTES];
    lchar* smem = (lchar*)smem_;
    const int bid = blockIdx.x, xcd = bid & 7, idx = bid >> 3;
    const int lane = threadIdx.x & 63, wid = __builtin_amdgcn_readfirstlane(threadIdx.x >> 6);
    const int g = (xcd >> 2) * 8 + (idx >> 2), hp = (xcd & 3) * 4 + (idx & 3);
    const int g2 = (xcd >> 1) * 4 + (idx >> 3), cb = (xcd & 1) * 8 + (idx & 7);
    u64* fl = (u64*)(ws + WS_FL);
    u16* ob = (u16*)(ws + WS_O);
    auto mktag = [](u64 e) {
        e += 1;
        return ((u64)(unsigned)__builtin_amdgcn_readfirstlane((int)(e >> 32)) << 32) | (unsigned)__builtin_amdgcn_readfirstlane((int)e);
    };
    if (wid < 4) {
        __builtin_amdgcn_s_setprio(1);
        u64 tag;
        {
            u32x4 x[4];
            u64 e;
            const int row = g * 128 + hp * 8 + wid * 2 + ((lane >> 2) & 1);
            const u16* p = xin + (size_t)row * DM + (lane >> 3) * 32 + (lane & 3) * 8;
            const char* d = ws + WS_XN + ((size_t)(lane >> 3) * SEQ + row) * 64 + (((lane & 3) ^ fz(row)) << 4);
            asm volatile("global_load_dwordx4 %0, %5, off\n\tglobal_load_dwordx4 %1, %5, off offset:512\n\tglobal_load_dwordx4 %2, %5, off offset:1024\n\t"
                         "global_load_dwordx4 %3, %5, off offset:1536\n\tglobal_load_dwordx2 %4, %6, off sc1\n\ts_waitcnt vmcnt(0)"
                         : "=&v"(x[0]), "=&v"(x[1]), "=&v"(x[2]), "=&v"(x[3]), "=&v"(e) : "v"(p), "v"(fl + FL_EP) : "memory");
            STAMP(14);
            ln0_rows(x);
#pragma unroll
            for (int i = 0; i < 4; ++i) st16wt<0>(d + i * MB, x[i]);
            tag = mktag(e);
            STAMP(15);
            publish(fl + FL_L + g * 64 + hp * 4 + wid, lane, tag);
            STAMP(9);
        }
#pragma unroll 1
        for (int layer = 0; layer < 4; ++layer) {
            if (layer & 1) qb_compute(smem, (u16*)(ws + WS_KV + (layer >> 1) * 8 * MB), ob, fl + FL_KV + (layer >> 1) * 1024 + hp * 64,
                                      fl + FL_O + layer * 1024, tag, g, hp, lane, wid STPPASS(layer * 32));
            else qa_compute(smem, layer > 0, ob, fl + FL_O + layer * 1024, tag, g, hp, lane, wid STPPASS(layer * 32));
            const bool last = layer == 3;
            wo_compute(smem, layer == 0 ? xin : (const u16*)(ws + (last ? WS_XN : WS_X)), layer > 0,
                       last ? out : (u16*)(ws + (layer == 2 ? WS_XN : WS_X)), last ? nullptr : ws + WS_ST + layer * (MB / 2),
                       fl + FL_X + layer * 1024, tag, g2, cb, lane, wid STPPASS(layer * 32 + 16));
        }
        if (bid == 0 && threadIdx.x == 0) stflag(fl + FL_EP, tag);
    } else {
        const int lw = wid - 4;
        HW h;
        h.lds = (unsigned)(uintptr_t)smem;
        h.vo16 = lane * 16;
#pragma unroll
        for (int i = 0; i < 16; ++i) {
            const int q = ((lw < 2 ? lw * 4 : 8) + i) * 64 + lane, k = q / 24, pc = q % 24;
            h.wq[i] = (k * 3072 + (pc >> 3) * 1024 + hp * 64 + (((((pc >> 1) & 3) ^ fswz(k)) << 1) + (pc & 1)) * 8) * 2 - (i & 3) * 1024;
        }
        wq_issue(h, wqa, 0, lw); wq_issue(h, wqa, 1, lw); wq_issue(h, wqa, 2, lw);
#pragma unroll
        for (int i = 0; i < 4; ++i) {
            const int k = 8 * i + (lane >> 3), pc = lane & 7;
            h.wo[i] = (k * 1024 + cb * 64 + ((((pc >> 1) ^ fswz(k)) << 1) + (pc & 1)) * 8) * 2 - i * 1024;
        }
        const u64 tag = mktag(ldflag(fl + FL_EP));
#pragma unroll 1
        for (int layer = 0; layer < 4; ++layer) {
            const u16* X = (const u16*)(ws + ((layer == 0 || layer == 3) ? WS_XN : WS_X));
            const char* st = ws + WS_ST;
            const u16* wo = (layer & 1) ? wob : woa;
            if (layer & 1) {
                qkv_helper<false>(smem, h, layer, X, wqb, wqb, wo, st, (float*)(ws + WS_CS), fl, tag, g, hp, lane, lw STPPASS(layer * 32));
                attn_helper(h, (const u16*)(ws + WS_KV + (layer >> 1) * 8 * MB), wo, fl + FL_KV + (layer >> 1) * 1024 + hp * 64, tag, g, hp, lane,
                            lw STPPASS(layer * 32));
            } else {
                qkv_helper<true>(smem, h, layer, X, wqa, wqb, wo, st, (float*)(ws + WS_CS), fl, tag, g, hp, lane, lw STPPASS(layer * 32));
            }
            wo_helper(h, ob, wo, layer == 3 ? nullptr : (layer & 1) ? wqa : wqb, fl + FL_O + layer * 1024, tag, g2, lane, lw STPPASS(layer * 32 + 16));
        }
    }
}

extern "C" void kernel_launch(void* const* d_in, const int* in_sizes, int n_in, void* d_out, int out_size, void* d_ws,
                              size_t ws_size, hipStream_t stream) {
    mega<<<256, 512, 0, stream>>>((const u16*)d_in[0], (const u16*)d_in[1], (const u16*)d_in[2], (const u16*)d_in[4], (const u16*)d_in[5],
                                  (u16*)d_out, (char*)d_ws);
}
```

```cpp
#include <hip/hip_runtime.h>
#include <stdint.h>

typedef unsigned short u16;
typedef unsigned long long u64;
typedef __attribute__((ext_vector_type(8))) short bf16x8;
typedef __attribute__((ext_vector_type(4))) short s16x4;
typedef __attribute__((ext_vector_type(4))) float f32x4;
typedef __attribute__((ext_vector_type(16))) float f32x16;
typedef __attribute__((ext_vector_type(4))) unsigned u32x4;
typedef __attribute__((ext_vector_type(2))) unsigned u32x2;
typedef __attribute__((ext_vector_type(2))) __bf16 bf16x2_t;
typedef __attribute__((ext_vector_type(2))) float f32x2;

#define LAS __attribute__((address_space(3)))
#define GAS __attribute__((address_space(1)))
#define DEV __device__ __forceinline__
typedef LAS char lchar;

constexpr int SEQ = 2048, DM = 1024, NH = 32;
constexpr float QSCALE = 0.17677669529663687f, LOG2E = 1.4426950408889634f;
#define POLLMAX (1 << 16)
#define EMU 1

DEV unsigned cvtpk(float lo, float hi) {
    f32x2 v = {lo, hi};
    bf16x2_t b = __builtin_convertvector(v, bf16x2_t);
    return __builtin_bit_cast(unsigned, b);
}
DEV float lo16(unsigned v) { return __uint_as_float(v << 16); }
DEV float hi16(unsigned v) { return __uint_as_float(v & 0xffff0000u); }
DEV float bfr(float f) { return lo16(cvtpk(f, f)); }
DEV u32x2 pk4(f32x4 a) { return (u32x2){cvtpk(a[0], a[1]), cvtpk(a[2], a[3])}; }
DEV bf16x8 cat(u32x2 a, u32x2 b) { return __builtin_bit_cast(bf16x8, (u32x4){a[0], a[1], b[0], b[1]}); }
DEV u32x2 sw16(unsigned a, unsigned b) { auto r = __builtin_amdgcn_permlane16_swap(a, b, false, false); return (u32x2){r[0], r[1]}; }
DEV u32x2 sw32(unsigned a, unsigned b) { auto r = __builtin_amdgcn_permlane32_swap(a, b, false, false); return (u32x2){r[0], r[1]}; }
DEV f32x2 sw16f(float a, float b) { return __builtin_bit_cast(f32x2, sw16(__float_as_uint(a), __float_as_uint(b))); }
DEV f32x2 sw32f(float a, float b) { return __builtin_bit_cast(f32x2, sw32(__float_as_uint(a), __float_as_uint(b))); }
DEV u32x4 unit16(u32x2 a, u32x2 b) {
    const u32x2 x = sw16(a[0], b[0]), y = sw16(a[1], b[1]);
    return (u32x4){x[0], y[0], x[1], y[1]};
}
template <int OFF> DEV void st16wt(const void* p, u32x4 v) {
    asm volatile("global_store_dwordx4 %0, %1, off offset:%2 sc1\n\ts_nop 1" ::"v"(p), "v"(v), "n"(OFF) : "memory");
}
DEV void st8wt(const void* p, u32x2 v) { asm volatile("global_store_dwordx2 %0, %1, off sc1\n\ts_nop 1" ::"v"(p), "v"(v) : "memory"); }
template <int CTRL> DEV float dppadd(float v) {
    return v + __builtin_bit_cast(float, __builtin_amdgcn_update_dpp(0, __builtin_bit_cast(int, v), CTRL, 0xf, 0xf, true));
}
DEV int fswz(int k) { return ((k >> 1) & 1) | (((k >> 3) & 1) << 1); }
DEV int fz(int tok) { return (-(tok >> 2)) & 3; }
DEV s16x4 ldtr4(const lchar* p) { return __builtin_bit_cast(s16x4, __builtin_amdgcn_ds_read_tr16_b64_v4i16((LAS s16x4*)p)); }
DEV bf16x8 cat8(s16x4 a, s16x4 b) { return (bf16x8){a[0], a[1], a[2], a[3], b[0], b[1], b[2], b[3]}; }
DEV bf16x8 ldk(const lchar* p) { return *(const LAS bf16x8*)p; }
DEV bf16x8 ldv(const lchar* p) { return cat8(ldtr4(p), ldtr4(p + 512)); }

#define STAMP(i)
#define STPARG
#define STPPASS(o)
#define STAMPB(i)
#define BAR() asm volatile("s_barrier" ::: "memory")
#define BARL() asm volatile("s_waitcnt lgkmcnt(0)\n\ts_barrier" ::: "memory")
#define WAITB(n) asm volatile("s_waitcnt vmcnt(%0)\n\ts_barrier" ::"n"(n) : "memory")
#define WAITV(n) asm volatile("s_waitcnt vmcnt(%0)" ::"n"(n) : "memory")
#define SB() __builtin_amdgcn_sched_barrier(0)
#define LGKM0() __builtin_amdgcn_s_waitcnt(0xc07f)
#define MFMA32(a, b, c) __builtin_amdgcn_mfma_f32_32x32x16_bf16(a, b, c, 0, 0, 0)
#define MFMA16(a, b, c) __builtin_amdgcn_mfma_f32_16x16x32_bf16(a, b, c, 0, 0, 0)

DEV void mfma16t(f32x4& c, const bf16x8& a, const bf16x8& b) { asm volatile("v_mfma_f32_16x16x32_bf16 %0, %1, %2, %0" : "+v"(c) : "v"(a), "v"(b)); }
#define MFMA_DONE() do { SB(); asm volatile("s_nop 7\n\ts_nop 3"); SB(); } while (0)
DEV float fmx(float a, float b) { return __builtin_elementwise_maximum(a, b); }
DEV float rsd(float var) { return __builtin_amdgcn_rcpf(bfr(__builtin_amdgcn_sqrtf(bfr(bfr(var) + 1e-5f)))); }

constexpr int XS = 18432;
constexpr int WQ0 = 81920, WQS = 24576;
constexpr int WO0 = 65536;
constexpr int EXO = 147456;
constexpr int AR1 = 122880, AR2 = 32768;
constexpr int SNM = 155648, SRS = SNM + 576, SCOL = SNM + 1280, DUMP = SNM + 3072;
constexpr int LDS_BYTES = 163840;
constexpr size_t MB = 1u << 20;
constexpr size_t WS_XN = 1 * MB, WS_O = 6 * MB, WS_X = 22 * MB, WS_KV = 34 * MB, WS_ST = 50 * MB, WS_CS = 52 * MB, WS_FL = 53 * MB;
constexpr int FL_O = 0;
constexpr int FL_X = 4096;
constexpr int FL_KV = 8192;
constexpr int FL_L = 10240;
constexpr int FL_C = 11264;
constexpr int FL_EP = 12288;

#define GL_HEAD "s_mov_b32 m0, %1\n\ts_nop 4\n\tglobal_load_lds_dwordx4 %2, %0 sc1"
DEV void glds1(const void* sb, unsigned lds, unsigned v0) { asm volatile(GL_HEAD ::"s"(sb), "s"(lds), "v"(v0) : "memory"); }
DEV void glds3(const void* sb, unsigned lds, unsigned v0, unsigned v1, unsigned v2) {
    asm volatile(GL_HEAD "\n\tglobal_load_lds_dwordx4 %3, %0 offset:1024 sc1\n\tglobal_load_lds_dwordx4 %4, %0 offset:2048 sc1"
                 ::"s"(sb), "s"(lds), "v"(v0), "v"(v1), "v"(v2) : "memory");
}
DEV void glds4(const void* sb, unsigned lds, unsigned v0, unsigned v1, unsigned v2, unsigned v3) {
    asm volatile(GL_HEAD "\n\tglobal_load_lds_dwordx4 %3, %0 offset:1024 sc1\n\tglobal_load_lds_dwordx4 %4, %0 offset:2048 sc1\n\t"
                 "global_load_lds_dwordx4 %5, %0 offset:3072 sc1" ::"s"(sb), "s"(lds), "v"(v0), "v"(v1), "v"(v2), "v"(v3) : "memory");
}
DEV void touch(const void* sb, unsigned lds, unsigned v0) {
    asm volatile("s_mov_b32 m0, %1\n\ts_nop 4\n\tglobal_load_lds_dword %2, %0 sc1" ::"s"(sb), "s"(lds), "v"(v0) : "memory");
}
DEV u64 ldflag(const u64* p) { return __hip_atomic_load((const GAS u64*)p, __ATOMIC_RELAXED, __HIP_MEMORY_SCOPE_AGENT); }
DEV void stflag(u64* p, u64 v) { __hip_atomic_store((GAS u64*)p, v, __ATOMIC_RELAXED, __HIP_MEMORY_SCOPE_AGENT); }
template <class F> DEV unsigned poll(const u64* p, u64 tag, unsigned need, F fold) {
    unsigned m = ~0u;
    for (int it = 0; it < POLLMAX; ++it) {
        const unsigned r = fold(__ballot(ldflag(p) == tag));
        if ((r & need) == need) { m = r; break; }
        __builtin_amdgcn_s_sleep(1);
    }
    asm volatile("" ::: "memory");
    return m;
}
DEV void publish(u64* f, int lane, u64 tag) {
    WAITV(0);
    if (lane == 0) stflag(f, tag);
}
#define OPAQUE(l, a, b, c) do { asm volatile("" : "+v"(l)); asm volatile("" : "+s"(a), "+s"(b), "+s"(c)); } while (0)

template <bool LOCAL, class F>
DEV void qkv_main(lchar* smem, f32x4 (&acc)[6][4], f32x4 (&acch)[2], int lane, int wm, int wn, F tailread STPARG) {
    constexpr int XC_B = LOCAL ? 9216 : 8192, HO = LOCAL ? 1024 : 0, NR = LOCAL ? 17 : 16;
    const int l15 = lane & 15, lg = lane >> 4, hs = LOCAL ? wm : 0;
    const lchar* xhb = smem + l15 * 64 + ((lg ^ fz(l15)) << 4);
    const lchar* xb = xhb + HO + wm * 4096;
    const int k0 = 8 * lg + (l15 >> 2);
    const lchar* wb0 = smem + WQ0 + lg * 3072 + (l15 >> 2) * 128 + (((wn * 2 + hs) ^ fswz(k0)) << 5) + (l15 & 3) * 8;
    const lchar* wb1 = smem + WQ0 + lg * 3072 + (l15 >> 2) * 128 + (((wn * 2 + (hs ^ 1)) ^ fswz(k0)) << 5) + (l15 & 3) * 8;
#pragma unroll
    for (int a = 0; a < 6; ++a)
#pragma unroll
        for (int b = 0; b < 4; ++b) { acc[a][b] = (f32x4){0.f, 0.f, 0.f, 0.f}; asm volatile("" : "+v"(acc[a][b])); }
    acch[0] = acch[1] = (f32x4){0.f, 0.f, 0.f, 0.f};
    if (LOCAL) asm volatile("" : "+v"(acch[0]), "+v"(acch[1]), "+v"(xhb));
    asm volatile("" : "+v"(xb), "+v"(wb0), "+v"(wb1));
    bf16x8 xf[2][5];
    s16x4 wh[2][6][2];
    auto rd = [&](int b, int j, int xo, int wo) {
        if (j < 4) xf[b][j] = ldk(xb + xo + j * 1024);
        else if (j < 16) wh[b][(j - 4) >> 1][j & 1] = ldtr4(((j & 2) ? wb1 : wb0) + wo + ((j - 4) >> 2) * 1024 + (j & 1) * 512);
        else xf[b][4] = ldk(xhb + xo);
    };
    auto kstep = [&](int b, int xo, int wo, bool last) {
        int j = 0;
#pragma unroll
        for (int ni = 0; ni < 6; ++ni) {
            const bf16x8 w = cat8(wh[b][ni][0], wh[b][ni][1]);
#pragma unroll
            for (int ti = 0; ti < 5; ++ti) {
                if (ti == 4) {
                    if (!LOCAL || (ni != 2 && ni != 4)) continue;
                    if (ni == 2) mfma16t(acch[0], w, xf[b][4]);
                    else mfma16t(acch[1], xf[b][4], w);
                } else {
                    if (LOCAL && ni >= 4) mfma16t(acc[ni][ti], xf[b][ti], w);
                    else mfma16t(acc[ni][ti], w, xf[b][ti]);
                }
                SB();
                if (last) tailread(j);
                else if (j < NR) rd(b ^ 1, j, xo, wo);
                SB();
                ++j;
            }
        }
        if (!last) { LGKM0(); SB(); }
    };
    BAR(); SB();
    STAMP(4);
#pragma unroll
    for (int j = 0; j < NR; ++j) rd(0, j, 0, 2 * WQS);
    LGKM0(); SB();
#pragma unroll 1
    for (int it = 0; it < 5; ++it) {
#pragma unroll
        for (int s = 0; s < 3; ++s) {
            kstep(0, s * XS + XC_B, ((s + 2) % 3) * WQS + WQS / 2, false);
            BAR(); SB();
            STAMPB(it * 3 + s);
            kstep(1, ((s + 1) % 3) * XS, s * WQS, false);
        }
    }
    kstep(0, XC_B, 2 * WQS + WQS / 2, false);
    BAR(); SB();
    STAMPB(15);
    kstep(1, 0, 0, true);
    MFMA_DONE();
}

DEV void qa_compute(lchar* smem, bool fold, u16* O, int g, int hp, int lane, int wid STPARG) {
    OPAQUE(lane, wid, g, hp);
    const int wm = wid >> 1, wn = wid & 1, l15 = lane & 15, lg = lane >> 4;
    f32x4 acc[6][4], acch[2];
    float nm[5], rs[5], csv[2];
    f32x4 nmv[5], rsv[5], cs[4];
    const lchar* sa = smem + SNM + l15 * 4;
    const lchar* sb = smem + SNM + lg * 16;
    const lchar* sc = smem + SCOL + wn * 128;
    const int ro = 64 + wm * 256;
    f32x4 cprev, ccur;
#pragma unroll
    for (int j = 0; j < 4; ++j) {
        const int d = l15 - 4 * lg - j;
        ccur[j] = (d >= 0 && !(d & 1)) ? 0.f : -3.0e38f;
        cprev[j] = (d <= -2 && !(d & 1)) ? 0.f : -3.0e38f;
    }
    const char* optr = (const char*)O + ((size_t)(hp * 2 + wn) * SEQ + g * 128 + wm * 64 + l15) * 64 +
                       (((((lg & 1) << 1) | (lg >> 1)) ^ (wm << 1) ^ fz(l15)) << 4);
    asm volatile("" : "+v"(cprev), "+v"(ccur), "+v"(optr));
    qkv_main<true>(smem, acc, acch, lane, wm, wn, [&](int j) {
        if (j < 4) cs[j] = *(const LAS f32x4*)(sc + (j >> 1) * 256 + (((j & 1) ^ wm) << 6) + lg * 16);
        else if (j < 9) { const int o = j < 8 ? ro + (j - 4) * 64 : 0; nm[j - 4] = *(const LAS float*)(sa + o); rs[j - 4] = *(const LAS float*)(sa + o + 576); }
        else if (j < 11) csv[j - 9] = *(const LAS float*)(sc + 512 + (((j - 9) ^ wm) << 6) + l15 * 4);
        else if (j < 21) {
            const int t = (j - 11) >> 1, o = t < 4 ? ro + t * 64 : 0;
            if ((j - 11) & 1) rsv[t] = *(const LAS f32x4*)(sb + o + 576);
            else nmv[t] = *(const LAS f32x4*)(sb + o);
        }
    } STPPASS(0));
    STAMP(2);
    if (fold) {
#pragma unroll
        for (int ti = 0; ti < 4; ++ti) {
#pragma unroll
            for (int ni = 0; ni < 4; ++ni) acc[ni][ti] = (acc[ni][ti] + nm[ti] * cs[ni]) * rs[ti];
#pragma unroll
            for (int h = 0; h < 2; ++h) acc[4 + h][ti] = (acc[4 + h][ti] + nmv[ti] * csv[h]) * rsv[ti];
        }
        acch[0] = (acch[0] + nm[4] * cs[2]) * rs[4];
        acch[1] = (acch[1] + nmv[4] * csv[0]) * rsv[4];
    }
    u32x2 kp[5][2], vp[5][2];
    lchar* ex = smem + EXO + wn * 4096 + lane * 16;
    {
        const u32x2 a = pk4(acch[0]), b = pk4(acch[1]);
        *(LAS u32x4*)(ex + wm * 1024) = (u32x4){a[0], a[1], b[0], b[1]};
    }
#pragma unroll
    for (int h = 0; h < 2; ++h) { kp[4][h] = pk4(acc[2 + h][3]); vp[4][h] = pk4(acc[4 + h][3]); }
    if (wm == 0) {
#pragma unroll
        for (int h = 0; h < 2; ++h) *(LAS u32x4*)(ex + 2048 + h * 1024) = (u32x4){kp[4][h][0], kp[4][h][1], vp[4][h][0], vp[4][h][1]};
    }
    bf16x8 qf[4];
#pragma unroll
    for (int ti = 0; ti < 4; ++ti) {
        qf[ti] = cat(pk4(acc[0][ti]), pk4(acc[1][ti]));
        if (ti < 3) {
#pragma unroll
            for (int h = 0; h < 2; ++h) { kp[ti + 1][h] = pk4(acc[2 + h][ti]); vp[ti + 1][h] = pk4(acc[4 + h][ti]); }
        }
    }
    SB();
    BARL();
#pragma unroll
    for (int h = 0; h < 2; ++h) {
        const u32x4 e = *(const LAS u32x4*)(ex + wm * 2048 + ((h ^ wm) << 10));
        kp[0][h] = (u32x2){e[0], e[1]}; vp[0][h] = (u32x2){e[2], e[3]};
    }
    f32x4 cp0 = cprev;
    if (g == 0 && wm == 0) cp0 = (f32x4){-3.0e38f, -3.0e38f, -3.0e38f, -3.0e38f};
    f32x4 sv[4];
    float mx[4];
#pragma unroll
    for (int t = 0; t < 4; ++t) {
        const int ti = 3 - t;
        const f32x4 s1 = MFMA16(cat(kp[ti + 1][0], kp[ti + 1][1]), qf[ti], ccur);
        const f32x4 s0 = MFMA16(cat(kp[ti][0], kp[ti][1]), qf[ti], ti ? cprev : cp0);
        f32x4 v = {fmx(s0[0], s0[1]), fmx(s0[2], s0[3]), fmx(s1[0], s1[1]), fmx(s1[2], s1[3])};
        if (EMU) {
            const u32x2 r1 = pk4(v);
            const u32x2 r2 = pk4((f32x4){lo16(r1[0]), hi16(r1[0]), lo16(r1[1]), hi16(r1[1])} * QSCALE);
            v = (f32x4){lo16(r2[0]), hi16(r2[0]), lo16(r2[1]), hi16(r2[1])};
        }
        sv[ti] = v;
        mx[ti] = fmx(fmx(fmx(v[0], v[1]), v[2]), v[3]);
    }
    {
        const f32x2 a = sw16f(mx[0], mx[1]), b = sw16f(mx[2], mx[3]);
        const f32x2 c = sw32f(fmx(a[0], a[1]), fmx(b[0], b[1]));
        const float r = fmx(c[0], c[1]) * -(EMU ? LOG2E : QSCALE * LOG2E);
        const f32x2 d = sw32f(r, r), e = sw16f(d[0], d[0]), f = sw16f(d[1], d[1]);
        mx[0] = e[0]; mx[1] = e[1]; mx[2] = f[0]; mx[3] = f[1];
    }
    const unsigned sel0 = (lane & 1) ? 0x01000c0cu : 0x0c0c0100u, sel1 = (lane & 1) ? 0x03020c0cu : 0x0c0c0302u;
    const bf16x8 ones = {0x3f80, 0x3f80, 0x3f80, 0x3f80, 0x3f80, 0x3f80, 0x3f80, 0x3f80};
    const f32x4 z = {0.f, 0.f, 0.f, 0.f};
    u32x4 ov[4];
#pragma unroll
    for (int ti = 0; ti < 4; ++ti) {
        const f32x4 x = sv[ti] * (EMU ? LOG2E : QSCALE * LOG2E) + mx[ti];
        const unsigned c0 = cvtpk(__builtin_amdgcn_exp2f(x[0]), __builtin_amdgcn_exp2f(x[1]));
        const unsigned c1 = cvtpk(__builtin_amdgcn_exp2f(x[2]), __builtin_amdgcn_exp2f(x[3]));
        const bf16x8 p = __builtin_bit_cast(bf16x8, (u32x4){__builtin_amdgcn_perm(c0, c0, sel0), __builtin_amdgcn_perm(c0, c0, sel1),
                                                            __builtin_amdgcn_perm(c1, c1, sel0), __builtin_amdgcn_perm(c1, c1, sel1)});
        const f32x4 l = MFMA16(ones, p, z);
        const f32x4 o0 = MFMA16(cat(vp[ti][0], vp[ti + 1][0]), p, z);
        const f32x4 o1 = MFMA16(cat(vp[ti][1], vp[ti + 1][1]), p, z);
        const float inv = __builtin_amdgcn_rcpf(l[0]);
        ov[ti] = unit16(pk4(o0 * inv), pk4(o1 * inv));
    }
    st16wt<0>(optr, ov[0]); st16wt<1024>(optr, ov[1]); st16wt<2048>(optr, ov[2]); st16wt<3072>(optr, ov[3]);
    STAMP(10);
    BAR();
}

#define MX3(a, b, c) fmx(fmx((a), (b)), (c))
DEV float rowmax32(const f32x16& s0) {
    float ma = MX3(s0[0], s0[1], s0[2]), mb = MX3(s0[3], s0[4], s0[5]);
    ma = MX3(ma, s0[6], s0[7]); mb = MX3(mb, s0[8], s0[9]);
    ma = MX3(ma, s0[10], s0[11]); mb = MX3(mb, s0[12], s0[13]);
    ma = fmx(MX3(ma, s0[14], s0[15]), mb);
    const f32x2 b = sw32f(ma, ma);
    return fmx(b[0], b[1]);
}
#define EX(i) do { S[i] = __builtin_amdgcn_exp2f(S[i]); SB(); } while (0)
#define CV(d, i) do { d = cvtpk(S[i], S[i + 1]); SB(); } while (0)
template <class F0, class F1, class F2>
DEV void block(f32x16& S, f32x16& N, const bf16x8& kf0, const bf16x8& kf1, const bf16x8& q0, const bf16x8& q1, const f32x16& c,
               f32x16& oP, f32x4& lP, const bf16x8& vf1, u32x4& p1, f32x16& oC, f32x4& lC, const bf16x8& vf0,
               const bf16x8& sel, F0 ldK, F1 ldV1, F2 ldV0) {
    u32x4 p0;
    N = MFMA32(kf0, q0, c); SB();
    EX(0); EX(1); EX(2);
    N = MFMA32(kf1, q1, N); SB();
    ldK(); SB();
    EX(3); CV(p0[0], 0); CV(p0[1], 2); EX(4);
    oP = MFMA32(vf1, __builtin_bit_cast(bf16x8, p1), oP); SB();
    ldV1(); SB();
    EX(5); EX(6); EX(7);
    lP = MFMA16(sel, __builtin_bit_cast(bf16x8, p1), lP); SB();
    CV(p0[2], 4); CV(p0[3], 6); EX(8);
    oC = MFMA32(vf0, __builtin_bit_cast(bf16x8, p0), oC); SB();
    ldV0(); SB();
    EX(9); EX(10); EX(11);
    lC = MFMA16(sel, __builtin_bit_cast(bf16x8, p0), lC); SB();
    EX(12); CV(p1[0], 8); CV(p1[1], 10); EX(13); EX(14); EX(15); CV(p1[2], 12); CV(p1[3], 14);
    asm volatile("" : "+v"(p1)); SB();
}
DEV void attn_slow(const u16* kh, const u16* vh, const bf16x8& q0, const bf16x8& q1, f32x16& o, float& l, int lane) {
    const int r = lane & 31, hh = lane >> 5;
    f32x16 c;
#pragma unroll
    for (int i = 0; i < 16; ++i) { o[i] = 0.f; c[i] = 0.f; }
    float m = 0.f;
    l = 0.f;
#pragma unroll 1
    for (int t = 0; t < SEQ / 32; ++t) {
        u32x4 k0, k1;
        const u16* kp = kh + (t * 32 + r) * 32;
        asm volatile("global_load_dwordx4 %0, %2, off sc1\n\tglobal_load_dwordx4 %1, %3, off sc1\n\ts_waitcnt vmcnt(0)"
                     : "=&v"(k0), "=&v"(k1) : "v"(kp + ((hh ^ ((r >> 2) & 3)) << 3)), "v"(kp + (((2 + hh) ^ ((r >> 2) & 3)) << 3)) : "memory");
        bf16x8 v0, v1;
#pragma unroll
        for (int e = 0; e < 8; ++e) {
            const int key = (e & 3) + 8 * (e >> 2) + 4 * hh, pos = (((r >> 3) ^ ((key >> 1) & 3)) << 3) + (r & 7);
            v0[e] = (short)__hip_atomic_load((const GAS u16*)vh + (t * 32 + key) * 32 + pos, __ATOMIC_RELAXED, __HIP_MEMORY_SCOPE_AGENT);
            v1[e] = (short)__hip_atomic_load((const GAS u16*)vh + (t * 32 + 16 + key) * 32 + pos, __ATOMIC_RELAXED, __HIP_MEMORY_SCOPE_AGENT);
        }
        f32x16 s = MFMA32(__builtin_bit_cast(bf16x8, k0), q0, c);
        s = MFMA32(__builtin_bit_cast(bf16x8, k1), q1, s);
        const float mx = rowmax32(s);
        const float dl = t == 0 ? mx : fmx(mx, 0.f);
        const float alpha = t == 0 ? 0.f : __builtin_amdgcn_exp2f(-dl);
        m += dl;
        l *= alpha;
        unsigned p[8];
#pragma unroll
        for (int i = 0; i < 16; ++i) { o[i] *= alpha; c[i] = -m; }
#pragma unroll
        for (int i = 0; i < 8; ++i) {
            p[i] = cvtpk(__builtin_amdgcn_exp2f(s[2 * i] - dl), __builtin_amdgcn_exp2f(s[2 * i + 1] - dl));
            l += lo16(p[i]) + hi16(p[i]);
        }
        o = MFMA32(v0, __builtin_bit_cast(bf16x8, (u32x4){p[0], p[1], p[2], p[3]}), o);
        o = MFMA32(v1, __builtin_bit_cast(bf16x8, (u32x4){p[4], p[5], p[6], p[7]}), o);
    }
    const f32x2 b = sw32f(l, l);
    l = b[0] + b[1];
}

DEV void qb_compute(lchar* smem, u16* kv, u16* O, u64* fkv, u64 tag, int g, int hp, int lane, int wid STPARG) {
    OPAQUE(lane, wid, g, hp);
    const int wm = wid >> 1, wn = wid & 1, l15 = lane & 15, lg = lane >> 4, h = hp * 2 + wn;
    bf16x8 qa0, qa1, qb0, qb1;
    lchar* gs = smem + wn * 16384;
    {
        f32x4 acc[6][4], acch[2];
        float nm[4], rs[4];
        f32x4 cs[6];
        const lchar* sa = smem + SNM + 64 + wm * 256 + l15 * 4;
        const lchar* sc = smem + SCOL + wn * 128 + lg * 16;
        const int un = ((lg & 1) << 1) | (lg >> 1);
        const int ko = l15 * 64 + ((un ^ (l15 >> 2)) << 4), vo = l15 * 64 + ((un ^ ((l15 >> 1) & 3)) << 4);
        const char* kptr = (const char*)kv + ((size_t)h * SEQ + g * 128 + wm * 64) * 64 + ko;
        const char* vptr = (const char*)kv + ((size_t)(NH + h) * SEQ + g * 128 + wm * 64) * 64 + vo;
        lchar* kl = gs + wm * 8192 + ko;
        lchar* vl = gs + wm * 8192 + 4096 + vo;
        asm volatile("" : "+v"(kptr), "+v"(vptr));
        qkv_main<false>(smem, acc, acch, lane, wm, wn, [&](int j) {
            if (j < 6) cs[j] = *(const LAS f32x4*)(sc + (j >> 1) * 256 + (j & 1) * 64);
            else if (j < 10) { nm[j - 6] = *(const LAS float*)(sa + (j - 6) * 64); rs[j - 6] = *(const LAS float*)(sa + (j - 6) * 64 + 576); }
        } STPPASS(0));
        STAMP(2);
        auto tile = [&](int ni, int ti, float sc_) { return pk4((acc[ni][ti] + nm[ti] * cs[ni]) * sc_); };
#define KVOUT(ni, ti, gp, lp) do { const u32x4 v = unit16(tile(ni, ti, rs[ti]), tile(ni + 1, ti, rs[ti])); st16wt<ti * 1024>(gp, v); *(LAS u32x4*)(lp + ti * 1024) = v; } while (0)
        KVOUT(2, 0, kptr, kl); KVOUT(2, 1, kptr, kl); KVOUT(2, 2, kptr, kl); KVOUT(2, 3, kptr, kl);
        KVOUT(4, 0, vptr, vl); KVOUT(4, 1, vptr, vl); KVOUT(4, 2, vptr, vl); KVOUT(4, 3, vptr, vl);
#pragma unroll
        for (int ti = 0; ti < 4; ++ti) rs[ti] *= QSCALE * LOG2E;
        qa0 = __builtin_bit_cast(bf16x8, unit16(tile(0, 0, rs[0]), tile(0, 1, rs[1])));
        qa1 = __builtin_bit_cast(bf16x8, unit16(tile(1, 0, rs[0]), tile(1, 1, rs[1])));
        qb0 = __builtin_bit_cast(bf16x8, unit16(tile(0, 2, rs[2]), tile(0, 3, rs[3])));
        qb1 = __builtin_bit_cast(bf16x8, unit16(tile(1, 2, rs[2]), tile(1, 3, rs[3])));
    }
    STAMP(3);
    const int r = lane & 31, hh = lane >> 5, sw = (r >> 2) & 3;
    const int vrow = 4 * (lg >> 1) + (l15 >> 2);
    const lchar* kb0 = gs + r * 64 + ((hh ^ sw) << 4);
    const lchar* kb1 = gs + r * 64 + (((2 + hh) ^ sw) << 4);
    const lchar* vb = gs + 4096 + vrow * 64 + (((((lg & 1) << 1) | ((l15 >> 1) & 1)) ^ ((vrow >> 1) & 3)) << 4) + (l15 & 1) * 8;
    const short one = ((l15 & 1) == (lg & 1)) ? (short)0x3f80 : (short)0;
    const bf16x8 sel = {one, one, one, one, one, one, one, one};
    f32x16 oA, oB, cA, cB, sA, sB;
    f32x4 lA = {0.f, 0.f, 0.f, 0.f}, lB = {0.f, 0.f, 0.f, 0.f};
#pragma unroll
    for (int i = 0; i < 16; ++i) { oA[i] = 0.f; oB[i] = 0.f; }
    bf16x8 kf0, kf1, vf0, vf1;
    u32x4 p1 = {0u, 0u, 0u, 0u};
    auto nop = [&]() {};
    auto tilestep = [&](int cu, int nx) {
        block(sA, sB, kf0, kf1, qb0, qb1, cB, oB, lB, vf1, p1, oA, lA, vf0, sel,
              [&]() { kf0 = ldk(kb0 + nx); kf1 = ldk(kb1 + nx); }, [&]() { vf1 = ldv(vb + cu + 1024); }, nop);
        block(sB, sA, kf0, kf1, qa0, qa1, cA, oA, lA, vf1, p1, oB, lB, vf0, sel, nop, nop, [&]() { vf0 = ldv(vb + nx); });
    };
    {
        const int own = wm * 8192, oth = own ^ 8192;
        kf0 = ldk(kb0 + own); kf1 = ldk(kb1 + own);
        vf0 = ldv(vb + own); vf1 = vf0;
        sA = MFMA32(kf0, qa0, oA); sB = MFMA32(kf0, qb0, oA);
        sA = MFMA32(kf1, qa1, sA); sB = MFMA32(kf1, qb1, sB);
        const float mA = rowmax32(sA), mB = rowmax32(sB);
#pragma unroll
        for (int i = 0; i < 16; ++i) { cA[i] = -mA; cB[i] = -mB; sA[i] -= mA; }
        asm volatile("" : "+v"(cA), "+v"(cB));
        SB();
        publish(fkv + wn * 32 + g * 2 + wm, lane, tag);
        SB();
        STAMP(5);
        tilestep(own, own + 2048);
        BARL(); SB();
        tilestep(own + 2048, oth);
        tilestep(oth, oth + 2048);
        BARL(); SB();
        STAMP(6);
        tilestep(oth + 2048, AR1);
    }
#pragma unroll 1
    for (int it = 0; it < 5; ++it) {
#pragma unroll
        for (int s = 0; s < 3; ++s) {
            const int cur = s == 0 ? AR1 : s == 1 ? AR2 : 0, nxt = s == 0 ? AR2 : s == 1 ? 0 : AR1;
            tilestep(cur, cur + 2048);
            tilestep(cur + 2048, cur + 8192);
            tilestep(cur + 8192, cur + 10240);
            BARL(); SB();
            tilestep(cur + 10240, nxt);
        }
    }
    oB = MFMA32(vf1, __builtin_bit_cast(bf16x8, p1), oB);
    lB = MFMA16(sel, __builtin_bit_cast(bf16x8, p1), lB);
    STAMP(9);
    float la = (lane & 16) ? lA[1] : lA[0], lb = (lane & 16) ? lB[1] : lB[0];
    if (__any(!(la <= 1073741824.0f) || !(lb <= 1073741824.0f))) {
        const u16* kh = kv + (size_t)h * SEQ * 32;
#pragma unroll 1
        for (int ps = 0; ps < 2; ++ps) {
            oA = oB; la = lb;
            attn_slow(kh, kh + (size_t)NH * SEQ * 32, ps ? qb0 : qa0, ps ? qb1 : qa1, oB, lb, lane);
        }
    }
    const char* op = (const char*)O + ((size_t)h * SEQ + g * 128 + wm * 64 + l15) * 64 + (((((lg & 1) << 1) | (lg >> 1)) ^ fz(l15)) << 4);
    auto fin = [&](const f32x16& o, float l, u32x4& u0, u32x4& u1) {
        const f32x16 y = o * __builtin_amdgcn_rcpf(l);
        const u32x2 a = sw32(cvtpk(y[0], y[1]), cvtpk(y[4], y[5])), b = sw32(cvtpk(y[2], y[3]), cvtpk(y[6], y[7]));
        const u32x2 c = sw32(cvtpk(y[8], y[9]), cvtpk(y[12], y[13])), d = sw32(cvtpk(y[10], y[11]), cvtpk(y[14], y[15]));
        const u32x2 e = sw16(a[0], c[0]), f = sw16(b[0], d[0]), i = sw16(a[1], c[1]), j = sw16(b[1], d[1]);
        u0 = (u32x4){e[0], f[0], i[0], j[0]};
        u1 = (u32x4){e[1], f[1], i[1], j[1]};
    };
    u32x4 u0, u1;
    fin(oA, la, u0, u1);
    st16wt<0>(op, u0); st16wt<1024>(op, u1);
    fin(oB, lb, u0, u1);
    st16wt<2048>(op, u0); st16wt<3072>(op, u1);
    STAMP(10);
}

constexpr int NRW = 7;
DEV unsigned wo_voff(int lane, int cb, int i) {
    const int k = 8 * i + (lane >> 3), pc = lane & 7;
    return (k * 1024 + cb * 64 + ((((pc >> 1) ^ fswz(k)) << 1) + (pc & 1)) * 8) * 2 - i * 1024;
}
DEV void wo_compute(lchar* smem, u64* fo, const u16* Wo, const u16* R, bool rcm, u16* C, char* stats, u64* fx, u64 tag, int g, int cb, int lane,
                    int wid STPARG) {
    OPAQUE(lane, wid, g, cb);
    const int wm = wid >> 1, wn = wid & 1, l15 = lane & 15, lg = lane >> 4;
    const int k0 = 8 * lg + (l15 >> 2);
    const lchar* wb0 = smem + WO0 + k0 * 128 + (((wn * 2) ^ fswz(k0)) << 5) + (l15 & 3) * 8;
    const lchar* wb1 = smem + WO0 + k0 * 128 + (((wn * 2 + 1) ^ fswz(k0)) << 5) + (l15 & 3) * 8;
    asm volatile("" : "+v"(wb0), "+v"(wb1));
    bf16x8 xf[2][8];
    s16x4 wh[16][8];
    auto rdw = [&](int t, int j) { wh[t][j] = ldtr4(((j & 2) ? wb1 : wb0) + (t < 11 ? t : t - 9) * 8192 + (j >> 2) * 4096 + (j & 1) * 512); };
    SB();
#pragma unroll
    for (int t = 0; t < NRW; ++t)
#pragma unroll
        for (int j = 0; j < 8; ++j) rdw(t, j);
    SB();
    LGKM0(); SB();
    BAR();
    publish(fo, lane, tag);
    STAMP(5);
    asm volatile("" : "+v"(lane));
    const int un = ((lg & 1) << 1) | (lg >> 1), row = g * 128 + wm * 64 + l15, ch = cb * 2 + wn;
    const unsigned ocm = (ch * SEQ + row) * 64 + ((un ^ fz(l15)) << 4), orm = row * 2048 + ch * 64 + un * 16;
    u32x4 rr[4];
    {
        const char* p = (const char*)R + (rcm ? ocm : orm);
        const unsigned st = rcm ? 1024 : 32768;
        asm volatile("global_load_dwordx4 %0, %4, off sc1\n\tglobal_load_dwordx4 %1, %5, off sc1\n\tglobal_load_dwordx4 %2, %6, off sc1\n\t"
                     "global_load_dwordx4 %3, %7, off sc1"
                     : "=&v"(rr[0]), "=&v"(rr[1]), "=&v"(rr[2]), "=&v"(rr[3]) : "v"(p), "v"(p + st), "v"(p + 2 * st), "v"(p + 3 * st) : "memory");
    }
#pragma unroll
    for (int i = 0; i < 3; ++i) {
        const int k = wid * 3 + i;
        glds4((const char*)Wo + (unsigned)(20 + k) * (32 * 2048), (unsigned)(uintptr_t)smem + WO0 + (k < 2 ? 20 + k : 2 + k) * 4096,
              wo_voff(lane, cb, 0), wo_voff(lane, cb, 1), wo_voff(lane, cb, 2), wo_voff(lane, cb, 3));
    }
    const char* cp = (const char*)C + (stats ? ocm : orm);
    const char* sp = stats + ((g * 2 + wm) * 32 + ch) * 512 + lane * 8;
    const lchar* xb = smem + (wm * 64 + l15) * 64 + ((lg ^ fz(l15)) << 4);
    asm volatile("" : "+v"(cp), "+v"(sp), "+v"(xb));
    WAITV(12);
    asm volatile("" : "+v"(rr[0]), "+v"(rr[1]), "+v"(rr[2]), "+v"(rr[3]));
    f32x4 acc[2][4];
#pragma unroll
    for (int ti = 0; ti < 4; ++ti) {
        const u32x2 x = sw16(rr[ti][0], rr[ti][2]), y = sw16(rr[ti][1], rr[ti][3]);
#pragma unroll
        for (int ni = 0; ni < 2; ++ni) acc[ni][ti] = (f32x4){lo16(x[ni]), hi16(x[ni]), lo16(y[ni]), hi16(y[ni])};
    }
    auto rdx = [&](int t, int j) { xf[t & 1][j] = ldk(xb + (t % 5) * 16384 + (j >> 2) * 8192 + (j & 3) * 1024); };
    auto mm = [&](int t, int kk, int j) {
        const int ti = j >> 1, ni = j & 1;
        mfma16t(acc[ni][ti], cat8(wh[t][kk * 4 + ni * 2], wh[t][kk * 4 + ni * 2 + 1]), xf[t & 1][kk * 4 + ti]);
    };
    SB();
#pragma unroll
    for (int t = 0; t < 16; ++t) {
        if (t == 9) WAITV(0);
        BAR(); SB();
        STAMPB(t);
        if (t == 0) {
            STAMP(4);
#pragma unroll
            for (int j = 0; j < 8; ++j) rdx(0, j);
            SB();
        } else {
#pragma unroll
            for (int j = 0; j < 8; ++j) { rdx(t, j); SB(); mm(t - 1, 1, j); SB(); }
        }
        const bool more = t + 1 >= NRW && t < 15;
#pragma unroll
        for (int j = 0; j < 8; ++j) { mm(t, 0, j); SB(); if (more) { rdw(t + 1, j); SB(); } }
        if (more) __builtin_amdgcn_s_waitcnt(0xc87f); else LGKM0();
        SB();
    }
#pragma unroll
    for (int j = 0; j < 8; ++j) mm(15, 1, j);
    MFMA_DONE();
    STAMP(2);
    u32x4 ov[4];
#pragma unroll
    for (int ti = 0; ti < 4; ++ti) ov[ti] = unit16(pk4(acc[0][ti]), pk4(acc[1][ti]));
    if (stats) {
        st16wt<0>(cp, ov[0]); st16wt<1024>(cp, ov[1]); st16wt<2048>(cp, ov[2]); st16wt<3072>(cp, ov[3]);
        float s[4], q[4];
        const bf16x2_t one2 = __builtin_bit_cast(bf16x2_t, 0x3f803f80u);
#pragma unroll
        for (int ti = 0; ti < 4; ++ti) {
            s[ti] = 0.f; q[ti] = 0.f;
#pragma unroll
            for (int e = 0; e < 4; ++e) {
                const unsigned u = ov[ti][e];
                const bf16x2_t v = __builtin_bit_cast(bf16x2_t, u);
                s[ti] = __builtin_amdgcn_fdot2_f32_bf16(v, one2, s[ti], false);
                q[ti] = __builtin_amdgcn_fdot2_f32_bf16(v, v, q[ti], false);
            }
        }
        auto red = [&](const float (&v)[4]) {
            const f32x2 a = sw16f(v[0], v[1]), b = sw16f(v[2], v[3]);
            const f32x2 c = sw32f(a[0] + a[1], b[0] + b[1]);
            return c[0] + c[1];
        };
        st8wt(sp, (u32x2){__float_as_uint(red(s)), __float_as_uint(red(q))});
        STAMP(10);
        BAR();
        publish(fx + (wn * 32 + g * 2 + wm) * 16 + cb, lane, tag);
        STAMP(11);
    } else {
#pragma unroll
        for (int ti = 0; ti < 4; ++ti) *(GAS u32x4*)(cp + ti * 32768) = ov[ti];
        STAMP(10);
    }
}

DEV void ln0_rows(u32x4 (&x)[4]) {
    auto sum32 = [&](float v) {
        v = dppadd<0x128>(dppadd<0x4E>(dppadd<0xB1>(v)));
        const f32x2 a = sw16f(v, v);
        v = a[0] + a[1];
        const f32x2 b = sw32f(v, v);
        return b[0] + b[1];
    };
    f32x2 v[16];
#pragma unroll
    for (int i = 0; i < 16; ++i) v[i] = (f32x2){lo16(x[i >> 2][i & 3]), hi16(x[i >> 2][i & 3])};
    f32x2 s = v[0];
#pragma unroll
    for (int i = 1; i < 16; ++i) s += v[i];
    const float mean = sum32(s[0] + s[1]) * (1.0f / DM);
    f32x2 q = {0.f, 0.f};
#pragma unroll
    for (int i = 0; i < 16; ++i) { const f32x2 d = v[i] - mean; q += d * d; }
    const float var = sum32(q[0] + q[1]) * (1.0f / DM);
    const float mb = bfr(mean);
    const float inv = rsd(var);
#pragma unroll
    for (int i = 0; i < 16; ++i) {
        const f32x2 e = v[i] - mb;
        const unsigned d = cvtpk(e[0], e[1]);
        const f32x2 y = (f32x2){lo16(d), hi16(d)} * inv;
        x[i >> 2][i & 3] = cvtpk(y[0], y[1]);
    }
}

struct HW { unsigned wq[2][3], wo[4], vo16, lds, tq, to; };
DEV void touch_wq(const HW& h, const u16* W, int i) { touch((const char*)W + (i / 3) * (256 * 6144) + (i % 3) * 2048, h.lds + DUMP, h.tq); }
DEV void touch_wo(const HW& h, const u16* W, int i) { touch((const char*)W + i * (128 * 2048), h.lds + DUMP, h.to); }
template <int N> DEV void wq_issue(const HW& h, const u16* W, int t, int r0) {
    const char* src = (const char*)W + (unsigned)(t * 8 + r0) * (8 * 6144);
    const unsigned l = h.lds + WQ0 + ((t + 2) % 3) * WQS + r0 * 3072;
#pragma unroll
    for (int i = 0; i < N; ++i) glds3(src + i * (8 * 6144), l + i * 3072, h.wq[i & 1][0], h.wq[i & 1][1], h.wq[i & 1][2]);
}
DEV void wo_issue(const HW& h, const u16* W, int t0, int t1) {
#pragma unroll 1
    for (int i = 2 * t0; i < 2 * t1; ++i) glds4((const char*)W + (size_t)i * (32 * 2048), h.lds + WO0 + i * 4096, h.wo[0], h.wo[1], h.wo[2], h.wo[3]);
}
template <int ST> DEV void ld8(f32x4 (&d)[8], const char* sb, unsigned vo) {
    asm volatile("s_nop 4\n\tglobal_load_dwordx4 %0, %8, %9 offset:%10 sc1\n\tglobal_load_dwordx4 %1, %8, %9 offset:%11 sc1\n\t"
                 "global_load_dwordx4 %2, %8, %9 offset:%12 sc1\n\tglobal_load_dwordx4 %3, %8, %9 offset:%13 sc1\n\t"
                 "global_load_dwordx4 %4, %8, %9 sc1\n\tglobal_load_dwordx4 %5, %8, %9 offset:%14 sc1\n\t"
                 "global_load_dwordx4 %6, %8, %9 offset:%15 sc1\n\tglobal_load_dwordx4 %7, %8, %9 offset:%16 sc1"
                 : "=&v"(d[0]), "=&v"(d[1]), "=&v"(d[2]), "=&v"(d[3]), "=&v"(d[4]), "=&v"(d[5]), "=&v"(d[6]), "=&v"(d[7])
                 : "v"(vo), "s"(sb + 4 * ST), "n"(-4 * ST), "n"(-3 * ST), "n"(-2 * ST), "n"(-ST), "n"(ST), "n"(2 * ST), "n"(3 * ST) : "memory");
}

template <bool LOCAL>
DEV void qkv_helper(lchar* smem, const HW& h, int layer, const u16* X, const u16* W, const u16* W2, const u16* Wo, const char* stats, float* csum,
                    u64* fl, u64 tag, int g, int hp, int lane, int lw STPARG) {
    OPAQUE(lane, lw, g, hp);
    constexpr int NX = LOCAL ? 9 : 8, HO = LOCAL ? 16 : 0;
    const int m0 = g * 128;
    const bool halo = LOCAL && g > 0;
    const u64* fx = fl + FL_X + (layer - 1) * 1024 + (g * 2 - (halo ? 1 : 0)) * 16 + (lane < (halo ? 48 : 32) ? lane : 0);
    auto fold = [&](u64 m) { return (unsigned)(m & (m >> 16) & (halo ? m >> 32 : m)); };
    if (lw < 2) {
        const unsigned vo0 = (LOCAL && g == 0) ? h.vo16 + 1024 : h.vo16;
        const char* xsrc = (const char*)X + ((size_t)lw * SEQ + m0 - HO) * 64;
        const unsigned ldx = h.lds + lw * (NX * 1024);
        unsigned rdy = 0u;
        STAMP(0);
        if (LOCAL && layer == 0) {
            const u64* p = fl + FL_L + g * 64 - (halo ? 8 : 0) + (lane < (halo ? 36 : 32) ? lane * 2 : 0);
            for (int it = 0; it < POLLMAX; ++it) {
                u32x4 f;
                asm volatile("global_load_dwordx4 %0, %1, off sc1\n\ts_waitcnt vmcnt(0)" : "=&v"(f) : "v"(p) : "memory");
                const unsigned tl = (unsigned)tag, th = (unsigned)(tag >> 32);
                if (__all(f[0] == tl && f[1] == th && f[2] == tl && f[3] == th)) break;
                __builtin_amdgcn_s_sleep(1);
            }
            rdy = ~0u;
        }
        auto issue = [&](int t, int so) {
            if (!((rdy >> t) & 1)) rdy = poll(fx + lw * 512, tag, 1u << t, fold);
            glds4(xsrc, ldx + so, vo0, h.vo16, h.vo16, h.vo16);
            glds4(xsrc + 4096, ldx + so + 4096, h.vo16, h.vo16, h.vo16, h.vo16);
            if (LOCAL) glds1(xsrc + 8192, ldx + so + 8192, h.vo16);
            xsrc += 2 * SEQ * 64;
        };
        issue(0, 0);
        STAMP(1);
        issue(1, XS);
        if (rdy & 4) {
            issue(2, 2 * XS);
            if (LOCAL && layer == 0) BAR();
            WAITB(2 * NX);
        } else {
            WAITB(NX);
            issue(2, 2 * XS);
        }
        WAITB(NX);
        int so = 0;
#pragma unroll 1
        for (int t = 3; t < 16; ++t) {
            wq_issue<1>(h, W, t, lw);
            issue(t, so);
            so = so == 2 * XS ? 0 : so + XS;
            WAITB(NX + 3);
        }
        WAITB(0);
        BAR(); BAR();
    } else if (lw == 2) {
        if (LOCAL && layer == 0) BAR();
        WAITB(0);
        BAR();
#pragma unroll 1
        for (int t = 3; t < 15; ++t) { wq_issue<6>(h, W, t, 2); WAITB(18); }
        wq_issue<6>(h, W, 15, 2);
        if (LOCAL) {
            wo_issue(h, Wo, 0, 2);
            WAITB(34);
            wo_issue(h, Wo, 2, 5);
            WAITB(40);
            WAITV(24);
            wo_issue(h, Wo, 5, 8);
            BAR(); BAR();
            WAITV(24);
            wo_issue(h, Wo, 8, 10);
        } else {
            WAITB(18);
            WAITB(0);
            BAR(); BAR();
        }
    } else {
        auto warm = [&]() {
            asm volatile("s_mov_b32 m0, %2\n\ts_nop 4\n\tglobal_load_lds_dword %0, %1\n\tglobal_load_lds_dword %3, %1" ::"v"(lane * 64),
                         "s"(Wo + (size_t)blockIdx.x * 4096), "s"(h.lds + DUMP), "v"(lane * 64 + 4096) : "memory");
        };
        if (LOCAL && layer == 0) {
            const int kg = g, j = hp;
            const unsigned vo = ((kg * 64 + (lane >> 3)) * 3072 + j * 64 + (lane & 7) * 8) * 2 + 2048;
            const char* dst = (const char*)csum + (j * 16 + kg) * 768 + (lane & 7) * 32 + (lane >> 4) * 16;
            u32x4 sv[2][3][8];
            auto ld = [&](int w, int i) {
                asm volatile("s_nop 4\n\tglobal_load_dwordx4 %0, %3, %4 offset:-2048\n\tglobal_load_dwordx4 %1, %3, %4\n\t"
                             "global_load_dwordx4 %2, %3, %4 offset:2048" : "=&v"(sv[w][0][i]), "=&v"(sv[w][1][i]), "=&v"(sv[w][2][i])
                             : "v"(vo), "s"((const char*)(w ? W2 : W) + i * (8 * 6144)) : "memory");
            };
            const short one = 0x3f80;
            bf16x8 sel;
#pragma unroll
            for (int e = 0; e < 8; ++e) sel[e] = (lane & 7) == e ? one : (short)0;
            f32x4 c;
            auto half = [&](int w, int k) {
                if (!(k & 1)) c = (f32x4){0.f, 0.f, 0.f, 0.f};
#pragma unroll
                for (int i = 0; i < 4; ++i) c = MFMA16(sel, __builtin_bit_cast(bf16x8, sv[w][k >> 1][(k & 1) * 4 + i]), c);
                if (k & 1) {
#pragma unroll
                    for (int e = 0; e < 4; ++e) c[e] = dppadd<0x128>(c[e]);
                    if (lane < 32 && !(lane & 8)) st16wt<0>(dst + w * (256 * 768) + (k >> 1) * 256, __builtin_bit_cast(u32x4, c));
                }
                SB();
            };
            BAR();
#pragma unroll
            for (int i = 0; i < 8; ++i) ld(0, i);
            warm();
            WAITB(26);
            BAR(); BAR(); BAR(); BAR();
            WAITV(0);
#pragma unroll
            for (int i = 0; i < 24; ++i) asm volatile("" : "+v"(sv[0][i >> 3][i & 7]));
#pragma unroll
            for (int k = 0; k < 6; ++k) { half(0, k); BAR(); }
            BAR(); BAR(); BAR(); BAR(); BAR(); BAR();
#pragma unroll
            for (int i = 0; i < 8; ++i) { ld(1, i); if (i == 3) BAR(); }
            BAR(); BAR();
            WAITV(0);
#pragma unroll
            for (int i = 0; i < 24; ++i) asm volatile("" : "+v"(sv[1][i >> 3][i & 7]));
#pragma unroll
            for (int k = 0; k < 6; ++k) half(1, k);
            return;
        } else {
            auto fin = [&](float s, float q, int o) {
                const float mean = s * (1.0f / DM);
                *(LAS float*)(smem + SNM + o) = -bfr(mean);
                *(LAS float*)(smem + SRS + o) = rsd(fmx(q * (1.0f / DM) - mean * mean, 0.f));
            };
            f32x4 sp[3][8], a = {0.f, 0.f, 0.f, 0.f};
            auto add8 = [&](f32x4 (&d)[8]) {
#pragma unroll
                for (int i = 0; i < 8; ++i) {
                    asm volatile("" : "+v"(d[i]));
#pragma unroll
                    for (int e = 0; e < 4; ++e) asm volatile("v_add_f32 %0, %0, %1" : "+v"(a[e]) : "v"(d[i][e]));
                }
            };
            const u64* fc = fl + FL_C + hp * 16 + (lane & 15);
            const char* sb = stats;
            const char* cb = (const char*)csum + (LOCAL ? 0 : 256 * 768);
            const unsigned so = (layer - 1) * (unsigned)(MB / 2) + g * 32768;
            const unsigned vo = so + (lane >> 5) * 16384 + (lane & 31) * 16, vc = hp * (16 * 768) + (lane < 48 ? lane * 16 : 0);
            WAITB(0);
            BAR(); BAR();
            u64 f0, f1;
            asm volatile("global_load_dwordx2 %0, %2, off sc1\n\tglobal_load_dwordx2 %1, %3, off sc1" : "=&v"(f0), "=&v"(f1) : "v"(fx), "v"(fx + 512) : "memory");
            BAR();
            WAITV(0);
            asm volatile("" : "+v"(f0), "+v"(f1));
            if (!__all(f0 == tag && f1 == tag)) {
                poll(fx, tag, 0xffffu, fold);
                poll(fx + 512, tag, 0xffffu, fold);
            }
            ld8<512>(sp[0], sb, vo);
            BAR();
            ld8<512>(sp[1], sb + 4096, vo);
            BAR();
            ld8<512>(sp[2], sb + 8192, vo);
            asm volatile("global_load_dwordx2 %0, %1, off sc1" : "=&v"(f0) : "v"(fc) : "memory");
            WAITV(17);
            add8(sp[0]);
            BAR();
            ld8<512>(sp[0], sb + 12288, vo);
            WAITV(17);
            add8(sp[1]);
            BAR();
            WAITV(8);
            asm volatile("" : "+v"(f0));
            if (!__all(f0 == tag)) poll(fc, tag, 0xffffu, [](u64 m) { return (unsigned)m; });
            ld8<768>(sp[1], cb, vc);
            add8(sp[2]);
            BAR();
            ld8<768>(sp[2], cb + 6144, vc);
            WAITV(16);
            add8(sp[0]);
            BAR();
            if (LOCAL) ld8<512>(sp[0], sb, so - (halo ? 16384 : 0) + (lane >> 4) * 4096 + 384 + (lane & 7) * 16);
            fin(a[0], a[1], 64 + lane * 8);
            fin(a[2], a[3], 68 + lane * 8);
            SB();
            BAR();
            WAITV(LOCAL ? 8 : 0);
            a = (f32x4){0.f, 0.f, 0.f, 0.f};
            add8(sp[1]);
            BAR();
            add8(sp[2]);
            if (lane < 48) *(LAS f32x4*)(smem + SCOL + lane * 16) = a;
            SB();
            BAR();
            if (LOCAL) {
                WAITV(0);
                a = (f32x4){0.f, 0.f, 0.f, 0.f};
                add8(sp[0]);
            }
            BAR();
            if (LOCAL) {
#pragma unroll
                for (int e = 0; e < 4; ++e) {
                    const f32x2 x = sw16f(a[e], a[e]);
                    const float v = x[0] + x[1];
                    const f32x2 y = sw32f(v, v);
                    a[e] = y[0] + y[1];
                }
                fin(a[0], a[1], (lane & 7) * 8);
                fin(a[2], a[3], (lane & 7) * 8 + 4);
            }
            BARL();
            BAR(); BAR(); BAR();
        }
    }
    if (LOCAL) { WAITB(16); BAR(); }
}

DEV void attn_helper(const HW& h, const u16* kv, const u16* Wo, const u64* fkv, u64 tag, int g, int hp, int lane, int lw STPARG) {
    OPAQUE(lane, lw, g, hp);
    const int part = lw & 1, hd = lw >> 1;
    const char* src = (const char*)kv + (size_t)(part * NH + hp * 2 + hd) * SEQ * 64;
    const unsigned ld = h.lds + hd * 16384 + part * 4096;
    const u64* f = fkv + hd * 32 + (lane & 31);
    unsigned rdy = 0u;
    int reg = AR1;
    auto issue = [&](int c) {
        const int blk = g ^ c;
        if (((rdy >> (2 * blk)) & 3) != 3) rdy = poll(f, tag, 3u << (2 * blk), [](u64 m) { return (unsigned)m; });
        glds4(src + blk * 8192, ld + reg, h.vo16, h.vo16, h.vo16, h.vo16);
        glds4(src + blk * 8192 + 4096, ld + reg + 8192, h.vo16, h.vo16, h.vo16, h.vo16);
        reg = reg == AR1 ? AR2 : reg == AR2 ? 0 : AR1;
    };
    issue(1);
    STAMP(7);
#pragma unroll 1
    for (int c = 0; c < 16; ++c) {
        WAITB(0);
        if (c + 2 < 16) issue(c + 2);
        if (lw == 2) {
            if (c >= 4 && c < 11) wo_issue(h, Wo, c - 4, c - 3);
            if (c == 14) wo_issue(h, Wo, 7, 10);
        }
    }
    BAR();
}

DEV void wo_helper(const HW& h, const u16* O, const u16* Wn, u64* fc, const u64* fo, u64 tag, int g, int lane, int lw STPARG) {
    int dummy = 0;
    OPAQUE(lane, lw, g, dummy);
    const int p = lw & 1, rh = lw >> 1;
    const char* xsrc = (const char*)O + ((size_t)p * SEQ + g * 128 + rh * 64) * 64;
    const unsigned ldx = h.lds + p * 8192 + rh * 4096;
    const u64* f = fo + (p * 32 + g * 2 + rh) * 16 + (lane & 15);
    unsigned so = 0, rdy = 0u;
    auto issue = [&](int t) {
        if (!((rdy >> t) & 1)) rdy = poll(f, tag, 1u << t, [](u64 m) { return (unsigned)m; });
        glds4(xsrc, ldx + so, h.vo16, h.vo16, h.vo16, h.vo16);
        xsrc += 2 * SEQ * 64;
        so = so == 65536 ? 0 : so + 16384;
    };
    STAMP(0);
    issue(0);
    STAMP(1);
    issue(1); issue(2); issue(3); issue(4);
    WAITB(16);
#pragma unroll 1
    for (int t = 5; t < 16; ++t) { WAITB(12); issue(t); }
    WAITB(12); WAITB(8); WAITB(4); WAITB(0);
    if (fc && lane == 0) stflag(fc, tag);
    if (Wn) {
        if (lw >= 2) { wq_issue<4>(h, Wn, 0, lw * 4 - 8); wq_issue<4>(h, Wn, 1, lw * 4 - 8); }
        BAR();
        if (lw >= 2) wq_issue<4>(h, Wn, 2, lw * 4 - 8);
    }
}

__global__ __launch_bounds__(512) void mega(const u16* __restrict__ xin, const u16* __restrict__ wqa, const u16* __restrict__ woa,
                                            const u16* __restrict__ wqb, const u16* __restrict__ wob, u16* __restrict__ out,
                                            char* __restrict__ ws) {
    __shared__ __attribute__((aligned(16))) char smem_[LDS_BYTES];
    lchar* smem = (lchar*)smem_;
    const int bid = blockIdx.x, xcd = bid & 7, idx = bid >> 3;
    const int lane = threadIdx.x & 63, wid = __builtin_amdgcn_readfirstlane(threadIdx.x >> 6);
    const int g = (xcd >> 2) * 8 + (idx >> 2), hp = (xcd & 3) * 4 + (idx & 3);
    const int g2 = (xcd >> 1) * 4 + (idx >> 3), cb = (xcd & 1) * 8 + (idx & 7);
    u64* fl = (u64*)(ws + WS_FL);
    u16* ob = (u16*)(ws + WS_O);
    auto mktag = [](u64 e) {
        e += 1;
        return ((u64)(unsigned)__builtin_amdgcn_readfirstlane((int)(e >> 32)) << 32) | (unsigned)__builtin_amdgcn_readfirstlane((int)e);
    };
    if (wid < 4) {
        __builtin_amdgcn_s_setprio(1);
        u64 tag;
        {
            u32x4 x[4];
            u64 e;
            const int row = g * 128 + hp * 8 + wid * 2 + ((lane >> 2) & 1);
            const u16* p = xin + (size_t)row * DM + (lane >> 3) * 32 + (lane & 3) * 8;
            const char* d = ws + WS_XN + ((size_t)(lane >> 3) * SEQ + row) * 64 + (((lane & 3) ^ fz(row)) << 4);
            asm volatile("global_load_dwordx4 %0, %5, off\n\tglobal_load_dwordx4 %1, %5, off offset:512\n\tglobal_load_dwordx4 %2, %5, off offset:1024\n\t"
                         "global_load_dwordx4 %3, %5, off offset:1536\n\tglobal_load_dwordx2 %4, %6, off sc1\n\ts_waitcnt vmcnt(0)"
                         : "=&v"(x[0]), "=&v"(x[1]), "=&v"(x[2]), "=&v"(x[3]), "=&v"(e) : "v"(p), "v"(fl + FL_EP) : "memory");
            STAMP(14);
            ln0_rows(x);
#pragma unroll
            for (int i = 0; i < 4; ++i) st16wt<0>(d + i * MB, x[i]);
            BAR();
            tag = mktag(e);
            STAMP(15);
            publish(fl + FL_L + g * 64 + hp * 4 + wid, lane, tag);
            STAMP(9);
            BAR();
        }
#pragma unroll 1
        for (int layer = 0; layer < 4; ++layer) {
            if (layer & 1) qb_compute(smem, (u16*)(ws + WS_KV + (layer >> 1) * 8 * MB), ob, fl + FL_KV + (layer >> 1) * 1024 + hp * 64,
                                      tag, g, hp, lane, wid STPPASS(layer * 32));
            else qa_compute(smem, layer > 0, ob, g, hp, lane, wid STPPASS(layer * 32));
            const bool last = layer == 3;
            wo_compute(smem, fl + FL_O + layer * 1024 + ((wid & 1) * 32 + g * 2 + (wid >> 1)) * 16 + hp, (layer & 1) ? wob : woa,
                       layer == 0 ? xin : (const u16*)(ws + (last ? WS_XN : WS_X)), layer > 0,
                       last ? out : (u16*)(ws + (layer == 2 ? WS_XN : WS_X)), last ? nullptr : ws + WS_ST + layer * (MB / 2),
                       fl + FL_X + layer * 1024, tag, g2, cb, lane, wid STPPASS(layer * 32 + 16));
        }
        if (bid == 0 && threadIdx.x == 0) stflag(fl + FL_EP, tag);
    } else {
        const int lw = wid - 4;
        HW h;
        h.lds = (unsigned)(uintptr_t)smem;
        h.vo16 = lane * 16;
#pragma unroll
        for (int i = 0; i < 6; ++i) {
            const int k = (lane >> 3) + (((i / 3) ^ (lw == 1)) << 3), pc = lane & 7;
            h.wq[i / 3][i % 3] = (lane >> 3) * 6144 + (i % 3) * 1024 + hp * 128 + ((((pc >> 1) ^ fswz(k)) << 1) + (pc & 1)) * 16;
        }
#pragma unroll
        for (int i = 0; i < 4; ++i) h.wo[i] = wo_voff(lane, cb, i);
        h.tq = (idx + (lane >> 3) * 32) * 6144 + (xcd & 3) * 512 + (lane & 7) * 64;
        h.to = (idx + (lane >> 4) * 32) * 2048 + (xcd & 1) * 1024 + (lane & 15) * 64;
        const u64 tag = mktag(ldflag(fl + FL_EP));
        BAR();
        if (lw >= 2) { wq_issue<4>(h, wqa, 0, lw * 4 - 8); wq_issue<4>(h, wqa, 1, lw * 4 - 8); wq_issue<4>(h, wqa, 2, lw * 4 - 8); }
#pragma unroll 1
        for (int layer = 0; layer < 4; ++layer) {
            const u16* X = (const u16*)(ws + ((layer == 0 || layer == 3) ? WS_XN : WS_X));
            const char* st = ws + WS_ST;
            const u16* wo = (layer & 1) ? wob : woa;
            if (layer & 1) {
                qkv_helper<false>(smem, h, layer, X, wqb, wqb, wo, st, (float*)(ws + WS_CS), fl, tag, g, hp, lane, lw STPPASS(layer * 32));
                attn_helper(h, (const u16*)(ws + WS_KV + (layer >> 1) * 8 * MB), wo, fl + FL_KV + (layer >> 1) * 1024 + hp * 64, tag, g, hp, lane,
                            lw STPPASS(layer * 32));
            } else {
                qkv_helper<true>(smem, h, layer, X, wqa, wqb, wo, st, (float*)(ws + WS_CS), fl, tag, g, hp, lane, lw STPPASS(layer * 32));
            }
            wo_helper(h, ob, layer == 3 ? nullptr : (layer & 1) ? wqa : wqb, (layer == 0 && lw == 3) ? fl + FL_C + hp * 16 + g : nullptr,
                      fl + FL_O + layer * 1024, tag, g2, lane, lw STPPASS(layer * 32 + 16));
        }
    }
}

extern "C" void kernel_launch(void* const* d_in, const int* in_sizes, int n_in, void* d_out, int out_size, void* d_ws,
                              size_t ws_size, hipStream_t stream) {
    mega<<<256, 512, 0, stream>>>((const u16*)d_in[0], (const u16*)d_in[1], (const u16*)d_in[2], (const u16*)d_in[4], (const u16*)d_in[5],
                                  (u16*)d_out, (char*)d_ws);
}
```

```cpp
#include <hip/hip_runtime.h>
#include <stdint.h>

typedef unsigned short u16;
typedef unsigned long long u64;
typedef __attribute__((ext_vector_type(8))) short bf16x8;
typedef __attribute__((ext_vector_type(4))) short s16x4;
typedef __attribute__((ext_vector_type(4))) float f32x4;
typedef __attribute__((ext_vector_type(16))) float f32x16;
typedef __attribute__((ext_vector_type(4))) unsigned u32x4;
typedef __attribute__((ext_vector_type(2))) unsigned u32x2;
typedef __attribute__((ext_vector_type(2))) __bf16 bf16x2_t;
typedef __attribute__((ext_vector_type(2))) float f32x2;

#define LAS __attribute__((address_space(3)))
#define GAS __attribute__((address_space(1)))
#define DEV __device__ __forceinline__
typedef LAS char lchar;

constexpr int SEQ = 2048, DM = 1024, NH = 32;
constexpr float QSCALE = 0.17677669529663687f, LOG2E = 1.4426950408889634f;
#define POLLMAX (1 << 16)
#define EMU 1
#define WT 0
#define WQE 1
#define W2L 1

DEV unsigned cvtpk(float lo, float hi) {
    f32x2 v = {lo, hi};
    bf16x2_t b = __builtin_convertvector(v, bf16x2_t);
    return __builtin_bit_cast(unsigned, b);
}
DEV float lo16(unsigned v) { return __uint_as_float(v << 16); }
DEV float hi16(unsigned v) { return __uint_as_float(v & 0xffff0000u); }
DEV float bfr(float f) { return lo16(cvtpk(f, f)); }
DEV u32x2 pk4(f32x4 a) { return (u32x2){cvtpk(a[0], a[1]), cvtpk(a[2], a[3])}; }
DEV bf16x8 cat(u32x2 a, u32x2 b) { return __builtin_bit_cast(bf16x8, (u32x4){a[0], a[1], b[0], b[1]}); }
DEV u32x2 sw16(unsigned a, unsigned b) { auto r = __builtin_amdgcn_permlane16_swap(a, b, false, false); return (u32x2){r[0], r[1]}; }
DEV u32x2 sw32(unsigned a, unsigned b) { auto r = __builtin_amdgcn_permlane32_swap(a, b, false, false); return (u32x2){r[0], r[1]}; }
DEV f32x2 sw16f(float a, float b) { return __builtin_bit_cast(f32x2, sw16(__float_as_uint(a), __float_as_uint(b))); }
DEV f32x2 sw32f(float a, float b) { return __builtin_bit_cast(f32x2, sw32(__float_as_uint(a), __float_as_uint(b))); }
DEV u32x4 unit16(u32x2 a, u32x2 b) {
    const u32x2 x = sw16(a[0], b[0]), y = sw16(a[1], b[1]);
    return (u32x4){x[0], y[0], x[1], y[1]};
}
template <int OFF> DEV void st16wt(const void* p, u32x4 v) {
    asm volatile("global_store_dwordx4 %0, %1, off offset:%2 sc1\n\ts_nop 1" ::"v"(p), "v"(v), "n"(OFF) : "memory");
}
DEV void st8wt(const void* p, u32x2 v) { asm volatile("global_store_dwordx2 %0, %1, off sc1\n\ts_nop 1" ::"v"(p), "v"(v) : "memory"); }
template <int CTRL> DEV float dppadd(float v) {
    return v + __builtin_bit_cast(float, __builtin_amdgcn_update_dpp(0, __builtin_bit_cast(int, v), CTRL, 0xf, 0xf, true));
}
DEV int fswz(int k) { return ((k >> 1) & 1) | (((k >> 3) & 1) << 1); }
DEV int fz(int tok) { return (-(tok >> 2)) & 3; }
DEV s16x4 ldtr4(const lchar* p) { return __builtin_bit_cast(s16x4, __builtin_amdgcn_ds_read_tr16_b64_v4i16((LAS s16x4*)p)); }
DEV bf16x8 cat8(s16x4 a, s16x4 b) { return (bf16x8){a[0], a[1], a[2], a[3], b[0], b[1], b[2], b[3]}; }
DEV bf16x8 ldk(const lchar* p) { return *(const LAS bf16x8*)p; }
DEV bf16x8 ldv(const lchar* p) { return cat8(ldtr4(p), ldtr4(p + 512)); }

#define STAMP(i)
#define STAMPW(i)
#define STPARG
#define STPPASS(o)
#define STAMPB(i)
#define BAR() asm volatile("s_barrier" ::: "memory")
#define BARL() asm volatile("s_waitcnt lgkmcnt(0)\n\ts_barrier" ::: "memory")
#define WAITB(n) asm volatile("s_waitcnt vmcnt(%0)\n\ts_barrier" ::"n"(n) : "memory")
#define WAITV(n) asm volatile("s_waitcnt vmcnt(%0)" ::"n"(n) : "memory")
#define SB() __builtin_amdgcn_sched_barrier(0)
#define LGKM0() __builtin_amdgcn_s_waitcnt(0xc07f)
#define MFMA32(a, b, c) __builtin_amdgcn_mfma_f32_32x32x16_bf16(a, b, c, 0, 0, 0)
#define MFMA16(a, b, c) __builtin_amdgcn_mfma_f32_16x16x32_bf16(a, b, c, 0, 0, 0)

DEV void mfma16t(f32x4& c, const bf16x8& a, const bf16x8& b) { asm volatile("v_mfma_f32_16x16x32_bf16 %0, %1, %2, %0" : "+v"(c) : "v"(a), "v"(b)); }
#define MFMA_DONE() do { SB(); asm volatile("s_nop 7\n\ts_nop 3"); SB(); } while (0)
DEV float fmx(float a, float b) { return __builtin_elementwise_maximum(a, b); }
DEV float rsd(float var) { return __builtin_amdgcn_rcpf(bfr(__builtin_amdgcn_sqrtf(bfr(bfr(var) + 1e-5f)))); }

constexpr int XS = 18432;
constexpr int WQ0 = 81920, WQS = 24576;
constexpr int WO0 = 65536;
constexpr int EXO = 147456;
constexpr int AR1 = 122880, AR2 = 32768;
constexpr int SNM = 155648, SRS = SNM + 576, SCOL = SNM + 1280, DUMP = SNM + 3072;
constexpr int LDS_BYTES = 163840;
constexpr size_t MB = 1u << 20;
constexpr size_t WS_XN = 1 * MB, WS_O = 6 * MB, WS_X = 22 * MB, WS_KV = 34 * MB, WS_ST = 50 * MB, WS_CS = 52 * MB, WS_FL = 53 * MB;
constexpr int FL_O = 0;
constexpr int FL_X = 4096;
constexpr int FL_KV = 8192;
constexpr int FL_L = 10240;
constexpr int FL_C = 11264;
constexpr int FL_EP = 12288;
#define FLS 16
#define FS 1
DEV int fi(int i) { return (i >> 4) * FLS + (i & 15) * FS; }

#define GL_HEAD "s_mov_b32 m0, %1\n\ts_nop 4\n\tglobal_load_lds_dwordx4 %2, %0 sc1"
DEV void glds1(const void* sb, unsigned lds, unsigned v0) { asm volatile(GL_HEAD ::"s"(sb), "s"(lds), "v"(v0) : "memory"); }
DEV void glds3(const void* sb, unsigned lds, unsigned v0, unsigned v1, unsigned v2) {
    asm volatile(GL_HEAD "\n\tglobal_load_lds_dwordx4 %3, %0 offset:1024 sc1\n\tglobal_load_lds_dwordx4 %4, %0 offset:2048 sc1"
                 ::"s"(sb), "s"(lds), "v"(v0), "v"(v1), "v"(v2) : "memory");
}
DEV void glds4(const void* sb, unsigned lds, unsigned v0, unsigned v1, unsigned v2, unsigned v3) {
    asm volatile(GL_HEAD "\n\tglobal_load_lds_dwordx4 %3, %0 offset:1024 sc1\n\tglobal_load_lds_dwordx4 %4, %0 offset:2048 sc1\n\t"
                 "global_load_lds_dwordx4 %5, %0 offset:3072 sc1" ::"s"(sb), "s"(lds), "v"(v0), "v"(v1), "v"(v2), "v"(v3) : "memory");
}
DEV void touch(const void* sb, unsigned lds, unsigned v0) {
    asm volatile("s_mov_b32 m0, %1\n\ts_nop 4\n\tglobal_load_lds_dword %2, %0 sc1" ::"s"(sb), "s"(lds), "v"(v0) : "memory");
}
DEV u64 ldep(const u64* p) { return __hip_atomic_load((const GAS u64*)p, __ATOMIC_RELAXED, __HIP_MEMORY_SCOPE_AGENT); }
#define FLD(d, a, z) "global_atomic_add_x2 " d ", " a ", " z ", off sc0 sc1"
DEV u64 ldflag(const u64* p) {
    u64 v;
    asm volatile(FLD("%0", "%1", "%2") "\n\ts_waitcnt vmcnt(0)" : "=&v"(v) : "v"(p), "v"(0ull) : "memory");
    return v;
}
DEV void stflag(u64* p, u64 v) { asm volatile("global_atomic_swap_x2 %0, %1, off sc1\n\ts_nop 1" ::"v"(p), "v"(v) : "memory"); }
template <class F> DEV unsigned poll(const u64* p, bool on, u64 tag, unsigned need, F fold) {
    unsigned m = ~0u;
    for (int it = 0; it < POLLMAX; ++it) {
        u64 v = tag;
        if (on) v = ldflag(p);
        const unsigned r = fold(__ballot(v == tag));
        if ((r & need) == need) { m = r; break; }
        __builtin_amdgcn_s_sleep(1);
    }
    asm volatile("" ::: "memory");
    return m;
}
DEV void publish(u64* f, int lane, u64 tag) {
    WAITV(0);
    if (lane == 0) stflag(f, tag);
}
#define OPAQUE(l, a, b, c) do { asm volatile("" : "+v"(l)); asm volatile("" : "+s"(a), "+s"(b), "+s"(c)); } while (0)

template <bool LOCAL, class F>
DEV void qkv_main(lchar* smem, f32x4 (&acc)[6][4], f32x4 (&acch)[2], int lane, int wm, int wn, F tailread STPARG) {
    constexpr int XC_B = LOCAL ? 9216 : 8192, HO = LOCAL ? 1024 : 0, NR = LOCAL ? 17 : 16;
    const int l15 = lane & 15, lg = lane >> 4, hs = LOCAL ? wm : 0;
    const lchar* xhb = smem + l15 * 64 + ((lg ^ fz(l15)) << 4);
    const lchar* xb = xhb + HO + wm * 4096;
    const int k0 = 8 * lg + (l15 >> 2);
    const lchar* wb0 = smem + WQ0 + lg * 3072 + (l15 >> 2) * 128 + (((wn * 2 + hs) ^ fswz(k0)) << 5) + (l15 & 3) * 8;
    const lchar* wb1 = smem + WQ0 + lg * 3072 + (l15 >> 2) * 128 + (((wn * 2 + (hs ^ 1)) ^ fswz(k0)) << 5) + (l15 & 3) * 8;
#pragma unroll
    for (int a = 0; a < 6; ++a)
#pragma unroll
        for (int b = 0; b < 4; ++b) { acc[a][b] = (f32x4){0.f, 0.f, 0.f, 0.f}; asm volatile("" : "+v"(acc[a][b])); }
    acch[0] = acch[1] = (f32x4){0.f, 0.f, 0.f, 0.f};
    if (LOCAL) asm volatile("" : "+v"(acch[0]), "+v"(acch[1]), "+v"(xhb));
    asm volatile("" : "+v"(xb), "+v"(wb0), "+v"(wb1));
    bf16x8 xf[2][5];
    s16x4 wh[2][6][2];
    auto rd = [&](int b, int j, int xo, int wo) {
        if (j < 4) xf[b][j] = ldk(xb + xo + j * 1024);
        else if (j < 16) wh[b][(j - 4) >> 1][j & 1] = ldtr4(((j & 2) ? wb1 : wb0) + wo + ((j - 4) >> 2) * 1024 + (j & 1) * 512);
        else xf[b][4] = ldk(xhb + xo);
    };
    auto kstep = [&](int b, int xo, int wo, bool last) {
        int j = 0;
#pragma unroll
        for (int ni = 0; ni < 6; ++ni) {
            const bf16x8 w = cat8(wh[b][ni][0], wh[b][ni][1]);
#pragma unroll
            for (int ti = 0; ti < 5; ++ti) {
                if (ti == 4) {
                    if (!LOCAL || (ni != 2 && ni != 4)) continue;
                    if (ni == 2) mfma16t(acch[0], w, xf[b][4]);
                    else mfma16t(acch[1], xf[b][4], w);
                } else {
                    if (LOCAL && ni >= 4) mfma16t(acc[ni][ti], xf[b][ti], w);
                    else mfma16t(acc[ni][ti], w, xf[b][ti]);
                }
                SB();
                if (last) tailread(j);
                else if (j < NR) rd(b ^ 1, j, xo, wo);
                SB();
                ++j;
            }
        }
        if (!last) { LGKM0(); SB(); }
    };
    BAR(); SB();
    STAMP(4);
#pragma unroll
    for (int j = 0; j < NR; ++j) rd(0, j, 0, 2 * WQS);
    LGKM0(); SB();
#pragma unroll 1
    for (int it = 0; it < 5; ++it) {
#pragma unroll
        for (int s = 0; s < 3; ++s) {
            kstep(0, s * XS + XC_B, ((s + 2) % 3) * WQS + WQS / 2, false);
            BAR(); SB();
            STAMPB(it * 3 + s);
            kstep(1, ((s + 1) % 3) * XS, s * WQS, false);
        }
    }
    kstep(0, XC_B, 2 * WQS + WQS / 2, false);
    BAR(); SB();
    STAMPB(15);
    kstep(1, 0, 0, true);
    MFMA_DONE();
}

DEV void qa_compute(lchar* smem, bool fold, u16* O, int g, int hp, int lane, int wid STPARG) {
    OPAQUE(lane, wid, g, hp);
    const int wm = wid >> 1, wn = wid & 1, l15 = lane & 15, lg = lane >> 4;
    f32x4 acc[6][4], acch[2];
    float nm[5], rs[5], csv[2];
    f32x4 nmv[5], rsv[5], cs[4];
    const lchar* sa = smem + SNM + l15 * 4;
    const lchar* sb = smem + SNM + lg * 16;
    const lchar* sc = smem + SCOL + wn * 128;
    const int ro = 64 + wm * 256;
    f32x4 cprev, ccur;
#pragma unroll
    for (int j = 0; j < 4; ++j) {
        const int d = l15 - 4 * lg - j;
        ccur[j] = (d >= 0 && !(d & 1)) ? 0.f : -3.0e38f;
        cprev[j] = (d <= -2 && !(d & 1)) ? 0.f : -3.0e38f;
    }
    const char* optr = (const char*)O + ((size_t)(hp * 2 + wn) * SEQ + g * 128 + wm * 64 + l15) * 64 +
                       (((((lg & 1) << 1) | (lg >> 1)) ^ (wm << 1) ^ fz(l15)) << 4);
    asm volatile("" : "+v"(cprev), "+v"(ccur), "+v"(optr));
    qkv_main<true>(smem, acc, acch, lane, wm, wn, [&](int j) {
        if (j < 4) cs[j] = *(const LAS f32x4*)(sc + (j >> 1) * 256 + (((j & 1) ^ wm) << 6) + lg * 16);
        else if (j < 9) { const int o = j < 8 ? ro + (j - 4) * 64 : 0; nm[j - 4] = *(const LAS float*)(sa + o); rs[j - 4] = *(const LAS float*)(sa + o + 576); }
        else if (j < 11) csv[j - 9] = *(const LAS float*)(sc + 512 + (((j - 9) ^ wm) << 6) + l15 * 4);
        else if (j < 21) {
            const int t = (j - 11) >> 1, o = t < 4 ? ro + t * 64 : 0;
            if ((j - 11) & 1) rsv[t] = *(const LAS f32x4*)(sb + o + 576);
            else nmv[t] = *(const LAS f32x4*)(sb + o);
        }
    } STPPASS(0));
    STAMP(2);
    if (fold) {
#pragma unroll
        for (int ti = 0; ti < 4; ++ti) {
#pragma unroll
            for (int ni = 0; ni < 4; ++ni) acc[ni][ti] = (acc[ni][ti] + nm[ti] * cs[ni]) * rs[ti];
#pragma unroll
            for (int h = 0; h < 2; ++h) acc[4 + h][ti] = (acc[4 + h][ti] + nmv[ti] * csv[h]) * rsv[ti];
        }
        acch[0] = (acch[0] + nm[4] * cs[2]) * rs[4];
        acch[1] = (acch[1] + nmv[4] * csv[0]) * rsv[4];
    }
    u32x2 kp[5][2], vp[5][2];
    lchar* ex = smem + EXO + wn * 4096 + lane * 16;
    {
        const u32x2 a = pk4(acch[0]), b = pk4(acch[1]);
        *(LAS u32x4*)(ex + wm * 1024) = (u32x4){a[0], a[1], b[0], b[1]};
    }
#pragma unroll
    for (int h = 0; h < 2; ++h) { kp[4][h] = pk4(acc[2 + h][3]); vp[4][h] = pk4(acc[4 + h][3]); }
    if (wm == 0) {
#pragma unroll
        for (int h = 0; h < 2; ++h) *(LAS u32x4*)(ex + 2048 + h * 1024) = (u32x4){kp[4][h][0], kp[4][h][1], vp[4][h][0], vp[4][h][1]};
    }
    bf16x8 qf[4];
#pragma unroll
    for (int ti = 0; ti < 4; ++ti) {
        qf[ti] = cat(pk4(acc[0][ti]), pk4(acc[1][ti]));
        if (ti < 3) {
#pragma unroll
            for (int h = 0; h < 2; ++h) { kp[ti + 1][h] = pk4(acc[2 + h][ti]); vp[ti + 1][h] = pk4(acc[4 + h][ti]); }
        }
    }
    SB();
    BARL();
#pragma unroll
    for (int h = 0; h < 2; ++h) {
        const u32x4 e = *(const LAS u32x4*)(ex + wm * 2048 + ((h ^ wm) << 10));
        kp[0][h] = (u32x2){e[0], e[1]}; vp[0][h] = (u32x2){e[2], e[3]};
    }
    f32x4 cp0 = cprev;
    if (g == 0 && wm == 0) cp0 = (f32x4){-3.0e38f, -3.0e38f, -3.0e38f, -3.0e38f};
    f32x4 sv[4];
    float mx[4];
#pragma unroll
    for (int t = 0; t < 4; ++t) {
        const int ti = 3 - t;
        const f32x4 s1 = MFMA16(cat(kp[ti + 1][0], kp[ti + 1][1]), qf[ti], ccur);
        const f32x4 s0 = MFMA16(cat(kp[ti][0], kp[ti][1]), qf[ti], ti ? cprev : cp0);
        f32x4 v = {fmx(s0[0], s0[1]), fmx(s0[2], s0[3]), fmx(s1[0], s1[1]), fmx(s1[2], s1[3])};
        if (EMU) {
            const u32x2 r1 = pk4(v);
            const u32x2 r2 = pk4((f32x4){lo16(r1[0]), hi16(r1[0]), lo16(r1[1]), hi16(r1[1])} * QSCALE);
            v = (f32x4){lo16(r2[0]), hi16(r2[0]), lo16(r2[1]), hi16(r2[1])};
        }
        sv[ti] = v;
        mx[ti] = fmx(fmx(fmx(v[0], v[1]), v[2]), v[3]);
    }
    {
        const f32x2 a = sw16f(mx[0], mx[1]), b = sw16f(mx[2], mx[3]);
        const f32x2 c = sw32f(fmx(a[0], a[1]), fmx(b[0], b[1]));
        const float r = fmx(c[0], c[1]) * -(EMU ? LOG2E : QSCALE * LOG2E);
        const f32x2 d = sw32f(r, r), e = sw16f(d[0], d[0]), f = sw16f(d[1], d[1]);
        mx[0] = e[0]; mx[1] = e[1]; mx[2] = f[0]; mx[3] = f[1];
    }
    const unsigned sel0 = (lane & 1) ? 0x01000c0cu : 0x0c0c0100u, sel1 = (lane & 1) ? 0x03020c0cu : 0x0c0c0302u;
    const bf16x8 ones = {0x3f80, 0x3f80, 0x3f80, 0x3f80, 0x3f80, 0x3f80, 0x3f80, 0x3f80};
    const f32x4 z = {0.f, 0.f, 0.f, 0.f};
    u32x4 ov[4];
#pragma unroll
    for (int ti = 0; ti < 4; ++ti) {
        const f32x4 x = sv[ti] * (EMU ? LOG2E : QSCALE * LOG2E) + mx[ti];
        const unsigned c0 = cvtpk(__builtin_amdgcn_exp2f(x[0]), __builtin_amdgcn_exp2f(x[1]));
        const unsigned c1 = cvtpk(__builtin_amdgcn_exp2f(x[2]), __builtin_amdgcn_exp2f(x[3]));
        const bf16x8 p = __builtin_bit_cast(bf16x8, (u32x4){__builtin_amdgcn_perm(c0, c0, sel0), __builtin_amdgcn_perm(c0, c0, sel1),
                                                            __builtin_amdgcn_perm(c1, c1, sel0), __builtin_amdgcn_perm(c1, c1, sel1)});
        const f32x4 l = MFMA16(ones, p, z);
        const f32x4 o0 = MFMA16(cat(vp[ti][0], vp[ti + 1][0]), p, z);
        const f32x4 o1 = MFMA16(cat(vp[ti][1], vp[ti + 1][1]), p, z);
        const float inv = __builtin_amdgcn_rcpf(l[0]);
        ov[ti] = unit16(pk4(o0 * inv), pk4(o1 * inv));
    }
    st16wt<0>(optr, ov[0]); st16wt<1024>(optr, ov[1]); st16wt<2048>(optr, ov[2]); st16wt<3072>(optr, ov[3]);
    STAMP(10);
    BAR();
}

#define MX3(a, b, c) fmx(fmx((a), (b)), (c))
DEV float rowmax32(const f32x16& s0) {
    float ma = MX3(s0[0], s0[1], s0[2]), mb = MX3(s0[3], s0[4], s0[5]);
    ma = MX3(ma, s0[6], s0[7]); mb = MX3(mb, s0[8], s0[9]);
    ma = MX3(ma, s0[10], s0[11]); mb = MX3(mb, s0[12], s0[13]);
    ma = fmx(MX3(ma, s0[14], s0[15]), mb);
    const f32x2 b = sw32f(ma, ma);
    return fmx(b[0], b[1]);
}
#define EX(i) do { S[i] = __builtin_amdgcn_exp2f(S[i]); SB(); } while (0)
#define CV(d, i) do { d = cvtpk(S[i], S[i + 1]); SB(); } while (0)
template <class F0, class F1, class F2>
DEV void block(f32x16& S, f32x16& N, const bf16x8& kf0, const bf16x8& kf1, const bf16x8& q0, const bf16x8& q1, const f32x16& c,
               f32x16& oP, f32x4& lP, const bf16x8& vf1, u32x4& p1, f32x16& oC, f32x4& lC, const bf16x8& vf0,
               const bf16x8& sel, F0 ldK, F1 ldV1, F2 ldV0) {
    u32x4 p0;
    N = MFMA32(kf0, q0, c); SB();
    EX(0); EX(1); EX(2);
    N = MFMA32(kf1, q1, N); SB();
    ldK(); SB();
    EX(3); CV(p0[0], 0); CV(p0[1], 2); EX(4);
    oP = MFMA32(vf1, __builtin_bit_cast(bf16x8, p1), oP); SB();
    ldV1(); SB();
    EX(5); EX(6); EX(7);
    lP = MFMA16(sel, __builtin_bit_cast(bf16x8, p1), lP); SB();
    CV(p0[2], 4); CV(p0[3], 6); EX(8);
    oC = MFMA32(vf0, __builtin_bit_cast(bf16x8, p0), oC); SB();
    ldV0(); SB();
    EX(9); EX(10); EX(11);
    lC = MFMA16(sel, __builtin_bit_cast(bf16x8, p0), lC); SB();
    EX(12); CV(p1[0], 8); CV(p1[1], 10); EX(13); EX(14); EX(15); CV(p1[2], 12); CV(p1[3], 14);
    asm volatile("" : "+v"(p1)); SB();
}
DEV void attn_slow(const u16* kh, const u16* vh, const bf16x8& q0, const bf16x8& q1, f32x16& o, float& l, int lane) {
    const int r = lane & 31, hh = lane >> 5;
    f32x16 c;
#pragma unroll
    for (int i = 0; i < 16; ++i) { o[i] = 0.f; c[i] = 0.f; }
    float m = 0.f;
    l = 0.f;
#pragma unroll 1
    for (int t = 0; t < SEQ / 32; ++t) {
        u32x4 k0, k1;
        const u16* kp = kh + (t * 32 + r) * 32;
        asm volatile("global_load_dwordx4 %0, %2, off sc1\n\tglobal_load_dwordx4 %1, %3, off sc1\n\ts_waitcnt vmcnt(0)"
                     : "=&v"(k0), "=&v"(k1) : "v"(kp + ((hh ^ ((r >> 2) & 3)) << 3)), "v"(kp + (((2 + hh) ^ ((r >> 2) & 3)) << 3)) : "memory");
        bf16x8 v0, v1;
#pragma unroll
        for (int e = 0; e < 8; ++e) {
            const int key = (e & 3) + 8 * (e >> 2) + 4 * hh, pos = (((r >> 3) ^ ((key >> 1) & 3)) << 3) + (r & 7);
            v0[e] = (short)__hip_atomic_load((const GAS u16*)vh + (t * 32 + key) * 32 + pos, __ATOMIC_RELAXED, __HIP_MEMORY_SCOPE_AGENT);
            v1[e] = (short)__hip_atomic_load((const GAS u16*)vh + (t * 32 + 16 + key) * 32 + pos, __ATOMIC_RELAXED, __HIP_MEMORY_SCOPE_AGENT);
        }
        f32x16 s = MFMA32(__builtin_bit_cast(bf16x8, k0), q0, c);
        s = MFMA32(__builtin_bit_cast(bf16x8, k1), q1, s);
        const float mx = rowmax32(s);
        const float dl = t == 0 ? mx : fmx(mx, 0.f);
        const float alpha = t == 0 ? 0.f : __builtin_amdgcn_exp2f(-dl);
        m += dl;
        l *= alpha;
        unsigned p[8];
#pragma unroll
        for (int i = 0; i < 16; ++i) { o[i] *= alpha; c[i] = -m; }
#pragma unroll
        for (int i = 0; i < 8; ++i) {
            p[i] = cvtpk(__builtin_amdgcn_exp2f(s[2 * i] - dl), __builtin_amdgcn_exp2f(s[2 * i + 1] - dl));
            l += lo16(p[i]) + hi16(p[i]);
        }
        o = MFMA32(v0, __builtin_bit_cast(bf16x8, (u32x4){p[0], p[1], p[2], p[3]}), o);
        o = MFMA32(v1, __builtin_bit_cast(bf16x8, (u32x4){p[4], p[5], p[6], p[7]}), o);
    }
    const f32x2 b = sw32f(l, l);
    l = b[0] + b[1];
}

DEV void qb_compute(lchar* smem, u16* kv, u16* O, u64* fkv, u64 tag, int g, int hp, int lane, int wid STPARG) {
    OPAQUE(lane, wid, g, hp);
    const int wm = wid >> 1, wn = wid & 1, l15 = lane & 15, lg = lane >> 4, h = hp * 2 + wn;
    bf16x8 qa0, qa1, qb0, qb1;
    lchar* gs = smem + wn * 16384;
    {
        f32x4 acc[6][4], acch[2];
        float nm[4], rs[4];
        f32x4 cs[6];
        const lchar* sa = smem + SNM + 64 + wm * 256 + l15 * 4;
        const lchar* sc = smem + SCOL + wn * 128 + lg * 16;
        const int un = ((lg & 1) << 1) | (lg >> 1);
        const int ko = l15 * 64 + ((un ^ (l15 >> 2)) << 4), vo = l15 * 64 + ((un ^ ((l15 >> 1) & 3)) << 4);
        const char* kptr = (const char*)kv + ((size_t)h * SEQ + g * 128 + wm * 64) * 64 + ko;
        const char* vptr = (const char*)kv + ((size_t)(NH + h) * SEQ + g * 128 + wm * 64) * 64 + vo;
        lchar* kl = gs + wm * 8192 + ko;
        lchar* vl = gs + wm * 8192 + 4096 + vo;
        asm volatile("" : "+v"(kptr), "+v"(vptr));
        qkv_main<false>(smem, acc, acch, lane, wm, wn, [&](int j) {
            if (j < 6) cs[j] = *(const LAS f32x4*)(sc + (j >> 1) * 256 + (j & 1) * 64);
            else if (j < 10) { nm[j - 6] = *(const LAS float*)(sa + (j - 6) * 64); rs[j - 6] = *(const LAS float*)(sa + (j - 6) * 64 + 576); }
        } STPPASS(0));
        STAMP(2);
        auto tile = [&](int ni, int ti, float sc_) { return pk4((acc[ni][ti] + nm[ti] * cs[ni]) * sc_); };
#define KVOUT(ni, ti, gp, lp) do { const u32x4 v = unit16(tile(ni, ti, rs[ti]), tile(ni + 1, ti, rs[ti])); st16wt<ti * 1024>(gp, v); *(LAS u32x4*)(lp + ti * 1024) = v; } while (0)
        KVOUT(2, 0, kptr, kl); KVOUT(2, 1, kptr, kl); KVOUT(2, 2, kptr, kl); KVOUT(2, 3, kptr, kl);
        KVOUT(4, 0, vptr, vl); KVOUT(4, 1, vptr, vl); KVOUT(4, 2, vptr, vl); KVOUT(4, 3, vptr, vl);
#pragma unroll
        for (int ti = 0; ti < 4; ++ti) rs[ti] *= QSCALE * LOG2E;
        qa0 = __builtin_bit_cast(bf16x8, unit16(tile(0, 0, rs[0]), tile(0, 1, rs[1])));
        qa1 = __builtin_bit_cast(bf16x8, unit16(tile(1, 0, rs[0]), tile(1, 1, rs[1])));
        qb0 = __builtin_bit_cast(bf16x8, unit16(tile(0, 2, rs[2]), tile(0, 3, rs[3])));
        qb1 = __builtin_bit_cast(bf16x8, unit16(tile(1, 2, rs[2]), tile(1, 3, rs[3])));
    }
    STAMP(3);
    const int r = lane & 31, hh = lane >> 5, sw = (r >> 2) & 3;
    const int vrow = 4 * (lg >> 1) + (l15 >> 2);
    const lchar* kb0 = gs + r * 64 + ((hh ^ sw) << 4);
    const lchar* kb1 = gs + r * 64 + (((2 + hh) ^ sw) << 4);
    const lchar* vb = gs + 4096 + vrow * 64 + (((((lg & 1) << 1) | ((l15 >> 1) & 1)) ^ ((vrow >> 1) & 3)) << 4) + (l15 & 1) * 8;
    const short one = ((l15 & 1) == (lg & 1)) ? (short)0x3f80 : (short)0;
    const bf16x8 sel = {one, one, one, one, one, one, one, one};
    f32x16 oA, oB, cA, cB, sA, sB;
    f32x4 lA = {0.f, 0.f, 0.f, 0.f}, lB = {0.f, 0.f, 0.f, 0.f};
#pragma unroll
    for (int i = 0; i < 16; ++i) { oA[i] = 0.f; oB[i] = 0.f; }
    bf16x8 kf0, kf1, vf0, vf1;
    u32x4 p1 = {0u, 0u, 0u, 0u};
    auto nop = [&]() {};
    auto tilestep = [&](int cu, int nx) {
        block(sA, sB, kf0, kf1, qb0, qb1, cB, oB, lB, vf1, p1, oA, lA, vf0, sel,
              [&]() { kf0 = ldk(kb0 + nx); kf1 = ldk(kb1 + nx); }, [&]() { vf1 = ldv(vb + cu + 1024); }, nop);
        block(sB, sA, kf0, kf1, qa0, qa1, cA, oA, lA, vf1, p1, oB, lB, vf0, sel, nop, nop, [&]() { vf0 = ldv(vb + nx); });
    };
    {
        const int own = wm * 8192, oth = own ^ 8192;
        kf0 = ldk(kb0 + own); kf1 = ldk(kb1 + own);
        vf0 = ldv(vb + own); vf1 = vf0;
        sA = MFMA32(kf0, qa0, oA); sB = MFMA32(kf0, qb0, oA);
        sA = MFMA32(kf1, qa1, sA); sB = MFMA32(kf1, qb1, sB);
        const float mA = rowmax32(sA), mB = rowmax32(sB);
#pragma unroll
        for (int i = 0; i < 16; ++i) { cA[i] = -mA; cB[i] = -mB; sA[i] -= mA; }
        asm volatile("" : "+v"(cA), "+v"(cB));
        SB();
        publish(fkv + fi(wn * 32 + wm * 16 + g), lane, tag);
        SB();
        STAMP(5);
        tilestep(own, own + 2048);
        BARL(); SB();
        tilestep(own + 2048, oth);
        tilestep(oth, oth + 2048);
        BARL(); SB();
        STAMP(6);
        tilestep(oth + 2048, AR1);
    }
#pragma unroll 1
    for (int it = 0; it < 5; ++it) {
#pragma unroll
        for (int s = 0; s < 3; ++s) {
            const int cur = s == 0 ? AR1 : s == 1 ? AR2 : 0, nxt = s == 0 ? AR2 : s == 1 ? 0 : AR1;
            tilestep(cur, cur + 2048);
            tilestep(cur + 2048, cur + 8192);
            tilestep(cur + 8192, cur + 10240);
            BARL(); SB();
            tilestep(cur + 10240, nxt);
        }
    }
    oB = MFMA32(vf1, __builtin_bit_cast(bf16x8, p1), oB);
    lB = MFMA16(sel, __builtin_bit_cast(bf16x8, p1), lB);
    STAMP(9);
    float la = (lane & 16) ? lA[1] : lA[0], lb = (lane & 16) ? lB[1] : lB[0];
    if (__any(!(la <= 1073741824.0f) || !(lb <= 1073741824.0f))) {
        const u16* kh = kv + (size_t)h * SEQ * 32;
#pragma unroll 1
        for (int ps = 0; ps < 2; ++ps) {
            oA = oB; la = lb;
            attn_slow(kh, kh + (size_t)NH * SEQ * 32, ps ? qb0 : qa0, ps ? qb1 : qa1, oB, lb, lane);
        }
    }
    const char* op = (const char*)O + ((size_t)h * SEQ + g * 128 + wm * 64 + l15) * 64 + (((((lg & 1) << 1) | (lg >> 1)) ^ fz(l15)) << 4);
    auto fin = [&](const f32x16& o, float l, u32x4& u0, u32x4& u1) {
        const f32x16 y = o * __builtin_amdgcn_rcpf(l);
        const u32x2 a = sw32(cvtpk(y[0], y[1]), cvtpk(y[4], y[5])), b = sw32(cvtpk(y[2], y[3]), cvtpk(y[6], y[7]));
        const u32x2 c = sw32(cvtpk(y[8], y[9]), cvtpk(y[12], y[13])), d = sw32(cvtpk(y[10], y[11]), cvtpk(y[14], y[15]));
        const u32x2 e = sw16(a[0], c[0]), f = sw16(b[0], d[0]), i = sw16(a[1], c[1]), j = sw16(b[1], d[1]);
        u0 = (u32x4){e[0], f[0], i[0], j[0]};
        u1 = (u32x4){e[1], f[1], i[1], j[1]};
    };
    u32x4 u0, u1;
    fin(oA, la, u0, u1);
    st16wt<0>(op, u0); st16wt<1024>(op, u1);
    fin(oB, lb, u0, u1);
    st16wt<2048>(op, u0); st16wt<3072>(op, u1);
    STAMP(10);
}

constexpr int NRW = 7;
DEV unsigned wo_voff(int lane, int cb, int i) {
    const int k = 8 * i + (lane >> 3), pc = lane & 7;
    return (k * 1024 + cb * 64 + ((((pc >> 1) ^ fswz(k)) << 1) + (pc & 1)) * 8) * 2 - i * 1024;
}
DEV void wo_compute(lchar* smem, u64* fo, const u16* Wo, u32x4 (&rr)[4], u16* C, char* stats, u64* fx, u64 tag, int g, int cb, int lane,
                    int wid STPARG) {
    OPAQUE(lane, wid, g, cb);
    const int wm = wid >> 1, wn = wid & 1, l15 = lane & 15, lg = lane >> 4;
    const int k0 = 8 * lg + (l15 >> 2);
    const lchar* wb0 = smem + WO0 + k0 * 128 + (((wn * 2) ^ fswz(k0)) << 5) + (l15 & 3) * 8;
    const lchar* wb1 = smem + WO0 + k0 * 128 + (((wn * 2 + 1) ^ fswz(k0)) << 5) + (l15 & 3) * 8;
    asm volatile("" : "+v"(wb0), "+v"(wb1));
    bf16x8 xf[2][8];
    s16x4 wh[16][8];
    auto rdw = [&](int t, int j) { wh[t][j] = ldtr4(((j & 2) ? wb1 : wb0) + (t < 11 ? t : t - 9) * 8192 + (j >> 2) * 4096 + (j & 1) * 512); };
    SB();
#pragma unroll
    for (int t = 0; t < NRW; ++t)
#pragma unroll
        for (int j = 0; j < 8; ++j) rdw(t, j);
    SB();
    LGKM0(); SB();
    BAR();
    publish(fo, lane, tag);
    STAMP(5);
    STAMPW(6 + wid);
    asm volatile("" : "+v"(lane));
    const int un = ((lg & 1) << 1) | (lg >> 1), row = g * 128 + wm * 64 + l15, ch = cb * 2 + wn;
    const unsigned ocm = (ch * SEQ + row) * 64 + ((un ^ fz(l15)) << 4), orm = row * 2048 + ch * 64 + un * 16;
#pragma unroll
    for (int i = 0; i < 3 && WT == 0; ++i) {
        const int k = wid * 3 + i;
        glds4((const char*)Wo + (unsigned)(20 + k) * (32 * 2048), (unsigned)(uintptr_t)smem + WO0 + (k < 2 ? 20 + k : 2 + k) * 4096,
              wo_voff(lane, cb, 0), wo_voff(lane, cb, 1), wo_voff(lane, cb, 2), wo_voff(lane, cb, 3));
    }
    const char* cp = (const char*)C + (stats ? ocm : orm);
    const char* sp = stats + ((g * 2 + wm) * 32 + ch) * 512 + lane * 8;
    const lchar* xb = smem + (wm * 64 + l15) * 64 + ((lg ^ fz(l15)) << 4);
    asm volatile("" : "+v"(cp), "+v"(sp), "+v"(xb));
    f32x4 acc[2][4];
#pragma unroll
    for (int ti = 0; ti < 4; ++ti) {
        const u32x2 x = sw16(rr[ti][0], rr[ti][2]), y = sw16(rr[ti][1], rr[ti][3]);
#pragma unroll
        for (int ni = 0; ni < 2; ++ni) acc[ni][ti] = (f32x4){lo16(x[ni]), hi16(x[ni]), lo16(y[ni]), hi16(y[ni])};
    }
    auto rdx = [&](int t, int j) { xf[t & 1][j] = ldk(xb + (t % 5) * 16384 + (j >> 2) * 8192 + (j & 3) * 1024); };
    auto mm = [&](int t, int kk, int j) {
        const int ti = j >> 1, ni = j & 1;
        mfma16t(acc[ni][ti], cat8(wh[t][kk * 4 + ni * 2], wh[t][kk * 4 + ni * 2 + 1]), xf[t & 1][kk * 4 + ti]);
    };
    SB();
#pragma unroll
    for (int t = 0; t < 16; ++t) {
        if (t == 9 && WT == 0) WAITV(0);
        BAR(); SB();
        STAMPB(t);
        if (t == 0) {
            STAMP(4);
#pragma unroll
            for (int j = 0; j < 8; ++j) rdx(0, j);
            SB();
        } else {
#pragma unroll
            for (int j = 0; j < 8; ++j) { rdx(t, j); SB(); mm(t - 1, 1, j); SB(); }
        }
        const bool more = t + 1 >= NRW && t < 15;
#pragma unroll
        for (int j = 0; j < 8; ++j) { mm(t, 0, j); SB(); if (more) { rdw(t + 1, j); SB(); } }
        if (more) __builtin_amdgcn_s_waitcnt(0xc87f); else LGKM0();
        SB();
    }
#pragma unroll
    for (int j = 0; j < 8; ++j) mm(15, 1, j);
    MFMA_DONE();
    STAMP(2);
    u32x4 (&ov)[4] = rr;
#pragma unroll
    for (int ti = 0; ti < 4; ++ti) ov[ti] = unit16(pk4(acc[0][ti]), pk4(acc[1][ti]));
    if (stats) {
        st16wt<0>(cp, ov[0]); st16wt<1024>(cp, ov[1]); st16wt<2048>(cp, ov[2]); st16wt<3072>(cp, ov[3]);
        float s[4], q[4];
        const bf16x2_t one2 = __builtin_bit_cast(bf16x2_t, 0x3f803f80u);
#pragma unroll
        for (int ti = 0; ti < 4; ++ti) {
            s[ti] = 0.f; q[ti] = 0.f;
#pragma unroll
            for (int e = 0; e < 4; ++e) {
                const unsigned u = ov[ti][e];
                const bf16x2_t v = __builtin_bit_cast(bf16x2_t, u);
                s[ti] = __builtin_amdgcn_fdot2_f32_bf16(v, one2, s[ti], false);
                q[ti] = __builtin_amdgcn_fdot2_f32_bf16(v, v, q[ti], false);
            }
        }
        auto red = [&](const float (&v)[4]) {
            const f32x2 a = sw16f(v[0], v[1]), b = sw16f(v[2], v[3]);
            const f32x2 c = sw32f(a[0] + a[1], b[0] + b[1]);
            return c[0] + c[1];
        };
        st8wt(sp, (u32x2){__float_as_uint(red(s)), __float_as_uint(red(q))});
        STAMP(10);
        BAR();
        publish(fx + fi((wn * 32 + g * 2 + wm) * 16 + cb), lane, tag);
        STAMP(11);
        STAMPW(12 + wid);
    } else {
#pragma unroll
        for (int ti = 0; ti < 4; ++ti) *(GAS u32x4*)(cp + ti * 32768) = ov[ti];
        STAMP(10);
    }
}

DEV void ln0_rows(u32x4 (&x)[4]) {
    auto sum32 = [&](float v) {
        v = dppadd<0x128>(dppadd<0x4E>(dppadd<0xB1>(v)));
        const f32x2 a = sw16f(v, v);
        v = a[0] + a[1];
        const f32x2 b = sw32f(v, v);
        return b[0] + b[1];
    };
    f32x2 v[16];
#pragma unroll
    for (int i = 0; i < 16; ++i) v[i] = (f32x2){lo16(x[i >> 2][i & 3]), hi16(x[i >> 2][i & 3])};
    f32x2 s = v[0];
#pragma unroll
    for (int i = 1; i < 16; ++i) s += v[i];
    const float mean = sum32(s[0] + s[1]) * (1.0f / DM);
    f32x2 q = {0.f, 0.f};
#pragma unroll
    for (int i = 0; i < 16; ++i) { const f32x2 d = v[i] - mean; q += d * d; }
    const float var = sum32(q[0] + q[1]) * (1.0f / DM);
    const float mb = bfr(mean);
    const float inv = rsd(var);
#pragma unroll
    for (int i = 0; i < 16; ++i) {
        const f32x2 e = v[i] - mb;
        const unsigned d = cvtpk(e[0], e[1]);
        const f32x2 y = (f32x2){lo16(d), hi16(d)} * inv;
        x[i >> 2][i & 3] = cvtpk(y[0], y[1]);
    }
}

struct HW { unsigned wq[2][3], wo[4], vo16, lds, tq, to; };
DEV void touch_wq(const HW& h, const u16* W, int i) { touch((const char*)W + (i / 3) * (256 * 6144) + (i % 3) * 2048, h.lds + DUMP, h.tq); }
DEV void touch_wo(const HW& h, const u16* W, int i) { touch((const char*)W + i * (128 * 2048), h.lds + DUMP, h.to); }
template <int N> DEV void wq_issue(const HW& h, const u16* W, int t, int r0) {
    const char* src = (const char*)W + (unsigned)(t * 8 + r0) * (8 * 6144);
    const unsigned l = h.lds + WQ0 + ((t + 2) % 3) * WQS + r0 * 3072;
#pragma unroll
    for (int i = 0; i < N; ++i) glds3(src + i * (8 * 6144), l + i * 3072, h.wq[i & 1][0], h.wq[i & 1][1], h.wq[i & 1][2]);
}
DEV void wo_issue(const HW& h, const u16* W, int t0, int t1) {
#pragma unroll 1
    for (int i = 2 * t0; i < 2 * t1; ++i) glds4((const char*)W + (size_t)i * (32 * 2048), h.lds + WO0 + i * 4096, h.wo[0], h.wo[1], h.wo[2], h.wo[3]);
}
template <int ST> DEV void ld8(f32x4 (&d)[8], const char* sb, unsigned vo) {
    asm volatile("s_nop 4\n\tglobal_load_dwordx4 %0, %8, %9 offset:%10 sc1\n\tglobal_load_dwordx4 %1, %8, %9 offset:%11 sc1\n\t"
                 "global_load_dwordx4 %2, %8, %9 offset:%12 sc1\n\tglobal_load_dwordx4 %3, %8, %9 offset:%13 sc1\n\t"
                 "global_load_dwordx4 %4, %8, %9 sc1\n\tglobal_load_dwordx4 %5, %8, %9 offset:%14 sc1\n\t"
                 "global_load_dwordx4 %6, %8, %9 offset:%15 sc1\n\tglobal_load_dwordx4 %7, %8, %9 offset:%16 sc1"
                 : "=&v"(d[0]), "=&v"(d[1]), "=&v"(d[2]), "=&v"(d[3]), "=&v"(d[4]), "=&v"(d[5]), "=&v"(d[6]), "=&v"(d[7])
                 : "v"(vo), "s"(sb + 4 * ST), "n"(-4 * ST), "n"(-3 * ST), "n"(-2 * ST), "n"(-ST), "n"(ST), "n"(2 * ST), "n"(3 * ST) : "memory");
}

template <bool LOCAL>
DEV void qkv_helper(lchar* smem, const HW& h, int layer, const u16* X, const u16* W, const u16* W2, const u16* Wo, const char* stats, float* csum,
                    u64* fl, u64 tag, int g, int hp, int lane, int lw STPARG) {
    OPAQUE(lane, lw, g, hp);
    constexpr int NX = LOCAL ? 9 : 8, HO = LOCAL ? 16 : 0;
    const int m0 = g * 128;
    const bool halo = LOCAL && g > 0;
    const u64* fx = fl + fi(FL_X + (layer - 1) * 1024 + (g * 2 - (halo ? 1 : 0)) * 16 + (lane < (halo ? 48 : 32) ? lane : 0));
    const bool fon = lane < (halo ? 48 : 32);
    auto fold = [&](u64 m) { return (unsigned)(m & (m >> 16) & (halo ? m >> 32 : m)); };
    if (lw < 2) {
        const unsigned vo0 = (LOCAL && g == 0) ? h.vo16 + 1024 : h.vo16;
        const char* xsrc = (const char*)X + ((size_t)lw * SEQ + m0 - HO) * 64;
        const unsigned ldx = h.lds + lw * (NX * 1024);
        unsigned rdy = 0u;
        STAMP(0);
        if (LOCAL && layer == 0) {
            if (lw == 0) poll(fl + fi(FL_L + g * 16 - (halo ? 2 : 0) + lane), lane < (halo ? 18 : 16), tag, ~0u, [](u64 m) { return (unsigned)m; });
            BAR();
            rdy = ~0u;
        }
        auto issue = [&](int t, int so) {
            if (!((rdy >> t) & 1)) rdy = poll(fx + fi(lw * 512), fon, tag, 1u << t, fold);
            glds4(xsrc, ldx + so, vo0, h.vo16, h.vo16, h.vo16);
            glds4(xsrc + 4096, ldx + so + 4096, h.vo16, h.vo16, h.vo16, h.vo16);
            if (LOCAL) glds1(xsrc + 8192, ldx + so + 8192, h.vo16);
            xsrc += 2 * SEQ * 64;
        };
        issue(0, 0);
        STAMP(1);
        if (lw == 1) STAMPW(11);
        issue(1, XS);
        if (rdy & 4) {
            issue(2, 2 * XS);
            if (LOCAL && layer == 0) BAR();
            WAITB(2 * NX);
        } else {
            WAITB(NX);
            issue(2, 2 * XS);
        }
        WAITB(NX);
        int so = 0;
#pragma unroll 1
        for (int t = 3; t < 16; ++t) {
            wq_issue<1>(h, W, t, lw);
            issue(t, so);
            so = so == 2 * XS ? 0 : so + XS;
            WAITB(NX + 3);
        }
        WAITB(0);
        BAR(); BAR();
    } else if (lw == 2) {
        if (LOCAL && layer == 0) { BAR(); BAR(); }
        WAITB(0);
        if (W2L && layer && (WQE != 2 || layer < 2)) wq_issue<4>(h, W, 2, 0);
        BAR();
#pragma unroll 1
        for (int t = 3; t < 15; ++t) { wq_issue<6>(h, W, t, 2); WAITB(18); }
        wq_issue<6>(h, W, 15, 2);
        if (LOCAL) {
            wo_issue(h, Wo, 0, 2);
            WAITB(34);
            wo_issue(h, Wo, 2, 5);
            WAITB(40);
            WAITV(24);
            wo_issue(h, Wo, 5, 8);
            BAR(); BAR();
            WAITV(24);
            wo_issue(h, Wo, 8, 10);
        } else {
            WAITB(18);
            WAITB(0);
            BAR(); BAR();
        }
    } else {
        auto warm = [&]() {
            asm volatile("s_mov_b32 m0, %2\n\ts_nop 4\n\tglobal_load_lds_dword %0, %1\n\tglobal_load_lds_dword %3, %1" ::"v"(lane * 64),
                         "s"(Wo + (size_t)blockIdx.x * 4096), "s"(h.lds + DUMP), "v"(lane * 64 + 4096) : "memory");
        };
        if (LOCAL && layer == 0) {
            const int kg = g, j = hp;
            const unsigned vo = ((kg * 64 + (lane >> 3)) * 3072 + j * 64 + (lane & 7) * 8) * 2 + 2048;
            const char* dst = (const char*)csum + (j * 16 + kg) * 768 + (lane & 7) * 32 + (lane >> 4) * 16;
            u32x4 sv[2][3][8];
            auto ld = [&](int w, int i) {
                asm volatile("s_nop 4\n\tglobal_load_dwordx4 %0, %3, %4 offset:-2048\n\tglobal_load_dwordx4 %1, %3, %4\n\t"
                             "global_load_dwordx4 %2, %3, %4 offset:2048" : "=&v"(sv[w][0][i]), "=&v"(sv[w][1][i]), "=&v"(sv[w][2][i])
                             : "v"(vo), "s"((const char*)(w ? W2 : W) + i * (8 * 6144)) : "memory");
            };
            const short one = 0x3f80;
            bf16x8 sel;
#pragma unroll
            for (int e = 0; e < 8; ++e) sel[e] = (lane & 7) == e ? one : (short)0;
            f32x4 c;
            auto half = [&](int w, int k) {
                if (!(k & 1)) c = (f32x4){0.f, 0.f, 0.f, 0.f};
#pragma unroll
                for (int i = 0; i < 4; ++i) c = MFMA16(sel, __builtin_bit_cast(bf16x8, sv[w][k >> 1][(k & 1) * 4 + i]), c);
                if (k & 1) {
#pragma unroll
                    for (int e = 0; e < 4; ++e) c[e] = dppadd<0x128>(c[e]);
                    if (lane < 32 && !(lane & 8)) st16wt<0>(dst + w * (256 * 768) + (k >> 1) * 256, __builtin_bit_cast(u32x4, c));
                }
                SB();
            };
            BAR(); BAR();
#pragma unroll
            for (int i = 0; i < 8; ++i) ld(0, i);
            warm();
            WAITB(26);
            BAR(); BAR(); BAR(); BAR();
            WAITV(0);
#pragma unroll
            for (int i = 0; i < 24; ++i) asm volatile("" : "+v"(sv[0][i >> 3][i & 7]));
#pragma unroll
            for (int k = 0; k < 6; ++k) { half(0, k); BAR(); }
            BAR(); BAR(); BAR(); BAR(); BAR(); BAR();
#pragma unroll
            for (int i = 0; i < 8; ++i) { ld(1, i); if (i == 3) BAR(); }
            BAR(); BAR();
            WAITV(0);
#pragma unroll
            for (int i = 0; i < 24; ++i) asm volatile("" : "+v"(sv[1][i >> 3][i & 7]));
#pragma unroll
            for (int k = 0; k < 6; ++k) half(1, k);
            return;
        } else {
            auto fin = [&](float s, float q, int o) {
                const float mean = s * (1.0f / DM);
                *(LAS float*)(smem + SNM + o) = -bfr(mean);
                *(LAS float*)(smem + SRS + o) = rsd(fmx(q * (1.0f / DM) - mean * mean, 0.f));
            };
            f32x4 sp[3][8], a = {0.f, 0.f, 0.f, 0.f};
            auto add8 = [&](f32x4 (&d)[8]) {
#pragma unroll
                for (int i = 0; i < 8; ++i) {
                    asm volatile("" : "+v"(d[i]));
#pragma unroll
                    for (int e = 0; e < 4; ++e) asm volatile("v_add_f32 %0, %0, %1" : "+v"(a[e]) : "v"(d[i][e]));
                }
            };
            const u64* fc = fl + fi(FL_C + hp * 16 + (lane & 15));
            const char* sb = stats;
            const char* cb = (const char*)csum + (LOCAL ? 0 : 256 * 768);
            const unsigned so = (layer - 1) * (unsigned)(MB / 2) + g * 32768;
            const unsigned vo = so + (lane >> 5) * 16384 + (lane & 31) * 16, vc = hp * (16 * 768) + (lane < 48 ? lane * 16 : 0);
            WAITB(0);
            if (W2L && (WQE != 2 || layer < 2)) wq_issue<4>(h, W, 2, 4);
            BAR(); WAITB(0);
            u64 f0, f1;
            const u64 mon = __ballot(fon);
            asm volatile("s_mov_b64 exec, %5\n\t" FLD("%0", "%2", "%4") "\n\t" FLD("%1", "%3", "%4") "\n\ts_mov_b64 exec, -1"
                         : "=&v"(f0), "=&v"(f1) : "v"(fx), "v"(fx + fi(512)), "v"(0ull), "s"(mon) : "memory");
            BAR();
            WAITV(0);
            asm volatile("" : "+v"(f0), "+v"(f1));
            if (!__all((f0 == tag && f1 == tag) || !fon)) {
                poll(fx, fon, tag, 0xffffu, fold);
                poll(fx + fi(512), fon, tag, 0xffffu, fold);
            }
            ld8<512>(sp[0], sb, vo);
            BAR();
            ld8<512>(sp[1], sb + 4096, vo);
            BAR();
            ld8<512>(sp[2], sb + 8192, vo);
            asm volatile("s_mov_b64 exec, 0xffff\n\t" FLD("%0", "%1", "%2") "\n\ts_mov_b64 exec, -1" : "=&v"(f0) : "v"(fc), "v"(0ull) : "memory");
            WAITV(17);
            add8(sp[0]);
            BAR();
            ld8<512>(sp[0], sb + 12288, vo);
            WAITV(17);
            add8(sp[1]);
            BAR();
            WAITV(8);
            asm volatile("" : "+v"(f0));
            if (!__all(f0 == tag || lane >= 16)) poll(fc, lane < 16, tag, 0xffffu, [](u64 m) { return (unsigned)m; });
            ld8<768>(sp[1], cb, vc);
            add8(sp[2]);
            BAR();
            ld8<768>(sp[2], cb + 6144, vc);
            WAITV(16);
            add8(sp[0]);
            BAR();
            if (LOCAL) ld8<512>(sp[0], sb, so - (halo ? 16384 : 0) + (lane >> 4) * 4096 + 384 + (lane & 7) * 16);
            fin(a[0], a[1], 64 + lane * 8);
            fin(a[2], a[3], 68 + lane * 8);
            SB();
            BAR();
            WAITV(LOCAL ? 8 : 0);
            a = (f32x4){0.f, 0.f, 0.f, 0.f};
            add8(sp[1]);
            BAR();
            add8(sp[2]);
            if (lane < 48) *(LAS f32x4*)(smem + SCOL + lane * 16) = a;
            SB();
            BAR();
            if (LOCAL) {
                WAITV(0);
                a = (f32x4){0.f, 0.f, 0.f, 0.f};
                add8(sp[0]);
            }
            BAR();
            if (LOCAL) {
#pragma unroll
                for (int e = 0; e < 4; ++e) {
                    const f32x2 x = sw16f(a[e], a[e]);
                    const float v = x[0] + x[1];
                    const f32x2 y = sw32f(v, v);
                    a[e] = y[0] + y[1];
                }
                fin(a[0], a[1], (lane & 7) * 8);
                fin(a[2], a[3], (lane & 7) * 8 + 4);
            }
            BARL();
            BAR(); BAR(); BAR();
        }
    }
    if (LOCAL) { WAITB(16); BAR(); }
}

DEV void attn_helper(const HW& h, const u16* kv, const u16* Wo, const u64* fkv, u64 tag, int g, int hp, int lane, int lw STPARG) {
    OPAQUE(lane, lw, g, hp);
    const int rh = lw & 1, hd = lw >> 1;
    const char* src = (const char*)kv + (unsigned)((hp * 2 + hd) * SEQ * 64 + rh * 4096);
    const unsigned ld = h.lds + hd * 16384 + rh * 8192;
    const u64* f = fkv + fi(lw * 16 + (lane & 15));
    unsigned rdy = 0u;
    int reg = AR1;
    auto issue = [&](int c) {
        const int blk = g ^ c;
        if (!((rdy >> blk) & 1)) rdy = poll(f, lane < 16, tag, 1u << blk, [](u64 m) { return (unsigned)m; });
        glds4(src + blk * 8192, ld + reg, h.vo16, h.vo16, h.vo16, h.vo16);
        glds4(src + blk * 8192 + NH * SEQ * 64, ld + reg + 4096, h.vo16, h.vo16, h.vo16, h.vo16);
        reg = reg == AR1 ? AR2 : reg == AR2 ? 0 : AR1;
    };
    issue(1);
    STAMP(7);
#pragma unroll 1
    for (int c = 0; c < 16; ++c) {
        WAITB(0);
        if (c + 2 < 16) issue(c + 2);
        if (lw == 2) {
            if (c >= 4 && c < 11) wo_issue(h, Wo, c - 4, c - 3);
            if (c == 14) wo_issue(h, Wo, 7, 10);
        }
    }
    BAR();
}

DEV void wo_helper(bool skip, const HW& h, const u16* O, const u16* Wo, const u16* Wn, u64* fc, const u64* fo, u64 tag, int g, int lane, int lw STPARG) {
    int dummy = 0;
    OPAQUE(lane, lw, g, dummy);
    const int p = lw & 1, rh = lw >> 1;
    const char* xsrc = (const char*)O + ((size_t)p * SEQ + g * 128 + rh * 64) * 64;
    const unsigned ldx = h.lds + p * 8192 + rh * 4096;
    const u64* f = fo + fi((p * 32 + g * 2 + rh) * 16 + (lane & 15));
    unsigned so = 0, rdy = 0u;
    auto issue = [&](int t) {
        if (!((rdy >> t) & 1)) rdy = poll(f, lane < 16, tag, 1u << t, [](u64 m) { return (unsigned)m; });
        glds4(xsrc, ldx + so, h.vo16, h.vo16, h.vo16, h.vo16);
        xsrc += 2 * SEQ * 64;
        so = so == 65536 ? 0 : so + 16384;
    };
    STAMP(0);
    issue(0);
    STAMP(1);
    issue(1); issue(2); issue(3); issue(4);
    auto tail = [&](int i) {
        const int k = i * 4 + lw;
        glds4((const char*)Wo + (unsigned)(20 + k) * (32 * 2048), h.lds + WO0 + (k < 2 ? 20 + k : 2 + k) * 4096, h.wo[0], h.wo[1], h.wo[2], h.wo[3]);
    };
    int t0 = 5;
    if (WT == 1) {
        WAITB(16); tail(0);
        WAITB(16); issue(5);
        WAITB(16); tail(1); issue(6);
        WAITB(20); issue(7);
        WAITB(20); tail(2); issue(8);
        WAITB(20); issue(9);
        WAITB(16); issue(10);
        WAITB(16); issue(11);
        t0 = 12;
    } else if (WT == 2) {
        tail(0); tail(1); tail(2);
        WAITB(28);
        WAITB(24); issue(5);
        WAITB(24); issue(6);
        WAITB(24); issue(7);
        WAITB(24); issue(8);
        t0 = 9;
    } else WAITB(16);
#pragma unroll 1
    for (int t = t0; t < 16; ++t) { WAITB(12); issue(t); }
    if (WQE == 2 && Wn && skip) {
        WAITB(12); WAITB(8); WAITB(4); WAITB(0); BAR();
        return;
    }
    if (WQE && Wn && lw >= 2) {
        wq_issue<4>(h, Wn, 0, lw * 4 - 8);
        WAITB(24); WAITB(20); WAITB(16);
        wq_issue<4>(h, Wn, 1, lw * 4 - 8);
        WAITB(24);
        if (fc && lane == 0) stflag(fc, tag);
        BAR();
        if (!W2L) wq_issue<4>(h, Wn, 2, lw * 4 - 8);
        return;
    }
    WAITB(12); WAITB(8); WAITB(4); WAITB(0);
    if (fc && lane == 0) stflag(fc, tag);
    if (Wn) {
        if (lw >= 2) { wq_issue<4>(h, Wn, 0, lw * 4 - 8); wq_issue<4>(h, Wn, 1, lw * 4 - 8); }
        BAR();
        if (lw >= 2 && !W2L) wq_issue<4>(h, Wn, 2, lw * 4 - 8);
    }
}

__global__ __launch_bounds__(512) void mega(const u16* __restrict__ xin, const u16* __restrict__ wqa, const u16* __restrict__ woa,
                                            const u16* __restrict__ wqb, const u16* __restrict__ wob, u16* __restrict__ out,
                                            char* __restrict__ ws) {
    __shared__ __attribute__((aligned(16))) char smem_[LDS_BYTES];
    lchar* smem = (lchar*)smem_;
    const int bid = blockIdx.x, xcd = bid & 7, idx = bid >> 3;
    const int lane = threadIdx.x & 63, wid = __builtin_amdgcn_readfirstlane(threadIdx.x >> 6);
    const int g = (xcd >> 2) * 8 + (idx >> 2), hp = (xcd & 3) * 4 + (idx & 3);
    const int g2 = (xcd >> 1) * 4 + (idx >> 3), cb = (xcd & 1) * 8 + (idx & 7);
    u64* fl = (u64*)(ws + WS_FL);
    u16* ob = (u16*)(ws + WS_O);
    auto mktag = [](u64 e) {
        e += 1;
        return ((u64)(unsigned)__builtin_amdgcn_readfirstlane((int)(e >> 32)) << 32) | (unsigned)__builtin_amdgcn_readfirstlane((int)e);
    };
    if (wid < 4) {
        __builtin_amdgcn_s_setprio(1);
        u64 tag;
        u32x4 res[4];
        {
            u32x4 x[4];
            u64 e;
            const int row = g * 128 + hp * 8 + wid * 2 + ((lane >> 2) & 1);
            const u16* p = xin + (size_t)row * DM + (lane >> 3) * 32 + (lane & 3) * 8;
            const char* d = ws + WS_XN + ((size_t)(lane >> 3) * SEQ + row) * 64 + (((lane & 3) ^ fz(row)) << 4);
            const unsigned ro = (g2 * 128 + (wid >> 1) * 64 + (lane & 15)) * 2048 + (cb * 2 + (wid & 1)) * 64 + ((lane & 16) << 1) + ((lane & 32) >> 1);
            asm volatile("global_load_dwordx4 %0, %5, off\n\tglobal_load_dwordx4 %1, %5, off offset:512\n\tglobal_load_dwordx4 %2, %5, off offset:1024\n\t"
                         "global_load_dwordx4 %3, %5, off offset:1536\n\tglobal_load_dwordx2 %4, %6, off sc1\n\ts_waitcnt vmcnt(0)"
                         : "=&v"(x[0]), "=&v"(x[1]), "=&v"(x[2]), "=&v"(x[3]), "=&v"(e) : "v"(p), "v"(fl + fi(FL_EP)) : "memory");
            STAMP(14);
            if (wid == 0) *(LAS unsigned*)(smem + DUMP + 1024) = 0u;
            ln0_rows(x);
#pragma unroll
            for (int i = 0; i < 4; ++i) st16wt<0>(d + i * MB, x[i]);
            asm volatile("global_load_dwordx4 %0, %4, %8\n\tglobal_load_dwordx4 %1, %5, %8\n\tglobal_load_dwordx4 %2, %6, %8\n\t"
                         "global_load_dwordx4 %3, %7, %8" : "=&v"(res[0]), "=&v"(res[1]), "=&v"(res[2]), "=&v"(res[3])
                         : "v"(ro), "v"(ro + 32768), "v"(ro + 65536), "v"(ro + 98304), "s"(xin) : "memory");
            BARL();
            tag = mktag(e);
            STAMP(15);
            WAITV(4);
            if (lane == 0 && __hip_atomic_fetch_add((LAS unsigned*)(smem + DUMP + 1024), 1u, __ATOMIC_RELAXED, __HIP_MEMORY_SCOPE_WORKGROUP) == 3)
                stflag(fl + fi(FL_L + g * 16 + hp), tag);
            STAMP(9);
            WAITV(0);
            asm volatile("" : "+v"(res[0]), "+v"(res[1]), "+v"(res[2]), "+v"(res[3]));
            BAR(); BAR();
        }
#pragma unroll 1
        for (int layer = 0; layer < 4; ++layer) {
            if (layer & 1) qb_compute(smem, (u16*)(ws + WS_KV + (layer >> 1) * 8 * MB), ob, fl + fi(FL_KV + (layer >> 1) * 1024 + hp * 64),
                                      tag, g, hp, lane, wid STPPASS(layer * 32));
            else qa_compute(smem, layer > 0, ob, g, hp, lane, wid STPPASS(layer * 32));
            const bool last = layer == 3;
            wo_compute(smem, fl + fi(FL_O + layer * 1024 + ((wid & 1) * 32 + g * 2 + (wid >> 1)) * 16 + hp), (layer & 1) ? wob : woa, res,
                       last ? out : (u16*)(ws + (layer == 2 ? WS_XN : WS_X)), last ? nullptr : ws + WS_ST + layer * (MB / 2),
                       fl + fi(FL_X + layer * 1024), tag, g2, cb, lane, wid STPPASS(layer * 32 + 16));
        }
        if (bid == 0 && threadIdx.x == 0) stflag(fl + fi(FL_EP), tag);
    } else {
        const int lw = wid - 4;
        HW h;
        h.lds = (unsigned)(uintptr_t)smem;
        h.vo16 = lane * 16;
#pragma unroll
        for (int i = 0; i < 6; ++i) {
            const int k = (lane >> 3) + (((i / 3) ^ (lw == 1)) << 3), pc = lane & 7;
            h.wq[i / 3][i % 3] = (lane >> 3) * 6144 + (i % 3) * 1024 + hp * 128 + ((((pc >> 1) ^ fswz(k)) << 1) + (pc & 1)) * 16;
        }
#pragma unroll
        for (int i = 0; i < 4; ++i) h.wo[i] = wo_voff(lane, cb, i);
        h.tq = (idx + (lane >> 3) * 32) * 6144 + (xcd & 3) * 512 + (lane & 7) * 64;
        h.to = (idx + (lane >> 4) * 32) * 2048 + (xcd & 1) * 1024 + (lane & 15) * 64;
        if (lw == 3 && lane < 3) asm volatile("global_atomic_add_x2 %0, %1, %2 sc1" ::"v"(fi(bid * 48 + lane * 16) * 8), "v"(0ull), "s"(fl) : "memory");
        const u64 tag = mktag(ldep(fl + fi(FL_EP)));
        BAR();
        if (lw >= 2) { wq_issue<4>(h, wqa, 0, lw * 4 - 8); wq_issue<4>(h, wqa, 1, lw * 4 - 8); wq_issue<4>(h, wqa, 2, lw * 4 - 8); }
#pragma unroll 1
        for (int layer = 0; layer < 4; ++layer) {
            const u16* X = (const u16*)(ws + ((layer == 0 || layer == 3) ? WS_XN : WS_X));
            const char* st = ws + WS_ST;
            const u16* wo = (layer & 1) ? wob : woa;
            if (layer & 1) {
                qkv_helper<false>(smem, h, layer, X, wqb, wqb, wo, st, (float*)(ws + WS_CS), fl, tag, g, hp, lane, lw STPPASS(layer * 32));
                attn_helper(h, (const u16*)(ws + WS_KV + (layer >> 1) * 8 * MB), wo, fl + fi(FL_KV + (layer >> 1) * 1024 + hp * 64), tag, g, hp, lane,
                            lw STPPASS(layer * 32));
            } else {
                qkv_helper<true>(smem, h, layer, X, wqa, wqb, wo, st, (float*)(ws + WS_CS), fl, tag, g, hp, lane, lw STPPASS(layer * 32));
            }
            wo_helper(layer > 0, h, ob, wo, layer == 3 ? nullptr : (layer & 1) ? wqa : wqb, (layer == 0 && lw == 3) ? fl + fi(FL_C + hp * 16 + g) : nullptr,
                      fl + fi(FL_O + layer * 1024), tag, g2, lane, lw STPPASS(layer * 32 + 16));
        }
    }
}

extern "C" void kernel_launch(void* const* d_in, const int* in_sizes, int n_in, void* d_out, int out_size, void* d_ws,
                              size_t ws_size, hipStream_t stream) {
    mega<<<256, 512, 0, stream>>>((const u16*)d_in[0], (const u16*)d_in[1], (const u16*)d_in[2], (const u16*)d_in[4], (const u16*)d_in[5],
                                  (u16*)d_out, (char*)d_ws);
}
```

```cpp
#include <hip/hip_runtime.h>
#include <stdint.h>

typedef unsigned short u16;
typedef unsigned long long u64;
typedef __attribute__((ext_vector_type(8))) short bf16x8;
typedef __attribute__((ext_vector_type(4))) short s16x4;
typedef __attribute__((ext_vector_type(4))) float f32x4;
typedef __attribute__((ext_vector_type(16))) float f32x16;
typedef __attribute__((ext_vector_type(4))) unsigned u32x4;
typedef __attribute__((ext_vector_type(2))) unsigned u32x2;
typedef __attribute__((ext_vector_type(2))) __bf16 bf16x2_t;
typedef __attribute__((ext_vector_type(2))) float f32x2;

#define LAS __attribute__((address_space(3)))
#define GAS __attribute__((address_space(1)))
#define DEV __device__ __forceinline__
typedef LAS char lchar;

constexpr int SEQ = 2048, DM = 1024, NH = 32;
constexpr float QSCALE = 0.17677669529663687f, LOG2E = 1.4426950408889634f;
#define POLLMAX (1 << 16)
#define HPR 2
#define A0 1
#define PP 1
#define EMU 1

DEV unsigned cvtpk(float lo, float hi) {
    f32x2 v = {lo, hi};
    bf16x2_t b = __builtin_convertvector(v, bf16x2_t);
    return __builtin_bit_cast(unsigned, b);
}
DEV float lo16(unsigned v) { return __uint_as_float(v << 16); }
DEV float hi16(unsigned v) { return __uint_as_float(v & 0xffff0000u); }
DEV float bfr(float f) { return lo16(cvtpk(f, f)); }
DEV u32x2 pk4(f32x4 a) { return (u32x2){cvtpk(a[0], a[1]), cvtpk(a[2], a[3])}; }
DEV bf16x8 cat(u32x2 a, u32x2 b) { return __builtin_bit_cast(bf16x8, (u32x4){a[0], a[1], b[0], b[1]}); }
DEV u32x2 sw16(unsigned a, unsigned b) { auto r = __builtin_amdgcn_permlane16_swap(a, b, false, false); return (u32x2){r[0], r[1]}; }
DEV u32x2 sw32(unsigned a, unsigned b) { auto r = __builtin_amdgcn_permlane32_swap(a, b, false, false); return (u32x2){r[0], r[1]}; }
DEV f32x2 sw16f(float a, float b) { return __builtin_bit_cast(f32x2, sw16(__float_as_uint(a), __float_as_uint(b))); }
DEV f32x2 sw32f(float a, float b) { return __builtin_bit_cast(f32x2, sw32(__float_as_uint(a), __float_as_uint(b))); }
DEV u32x4 unit16(u32x2 a, u32x2 b) {
    const u32x2 x = sw16(a[0], b[0]), y = sw16(a[1], b[1]);
    return (u32x4){x[0], y[0], x[1], y[1]};
}
template <int OFF> DEV void st16wt(const void* p, u32x4 v) {
    asm volatile("global_store_dwordx4 %0, %1, off offset:%2 sc1\n\ts_nop 1" ::"v"(p), "v"(v), "n"(OFF) : "memory");
}
DEV void st8wt(const void* p, u32x2 v) { asm volatile("global_store_dwordx2 %0, %1, off sc1\n\ts_nop 1" ::"v"(p), "v"(v) : "memory"); }
template <int CTRL> DEV float dppadd(float v) {
    return v + __builtin_bit_cast(float, __builtin_amdgcn_update_dpp(0, __builtin_bit_cast(int, v), CTRL, 0xf, 0xf, true));
}
DEV int fswz(int k) { return ((k >> 1) & 1) | (((k >> 3) & 1) << 1); }
DEV int fz(int tok) { return (-(tok >> 2)) & 3; }
DEV s16x4 ldtr4(const lchar* p) { return __builtin_bit_cast(s16x4, __builtin_amdgcn_ds_read_tr16_b64_v4i16((LAS s16x4*)p)); }
DEV bf16x8 cat8(s16x4 a, s16x4 b) { return (bf16x8){a[0], a[1], a[2], a[3], b[0], b[1], b[2], b[3]}; }
DEV bf16x8 ldk(const lchar* p) { return *(const LAS bf16x8*)p; }

#define STAMP(i)
#define STAMPW(i)
#define STPARG
#define STPPASS(o)
#define STAMPB(i)
#define BAR() asm volatile("s_barrier" ::: "memory")
#define BARL() asm volatile("s_waitcnt lgkmcnt(0)\n\ts_barrier" ::: "memory")
#define WAITB(n) asm volatile("s_waitcnt vmcnt(%0)\n\ts_barrier" ::"n"(n) : "memory")
#define WAITV(n) asm volatile("s_waitcnt vmcnt(%0)" ::"n"(n) : "memory")
#define SB() __builtin_amdgcn_sched_barrier(0)
#define LGKM0() __builtin_amdgcn_s_waitcnt(0xc07f)
#define MFMA32(a, b, c) __builtin_amdgcn_mfma_f32_32x32x16_bf16(a, b, c, 0, 0, 0)
#define MFMA16(a, b, c) __builtin_amdgcn_mfma_f32_16x16x32_bf16(a, b, c, 0, 0, 0)

DEV void mfma16t(f32x4& c, const bf16x8& a, const bf16x8& b) { asm volatile("v_mfma_f32_16x16x32_bf16 %0, %1, %2, %0" : "+v"(c) : "v"(a), "v"(b)); }
#define MFMA_DONE() do { SB(); asm volatile("s_nop 7\n\ts_nop 3"); SB(); } while (0)
DEV float fmx(float a, float b) { return __builtin_elementwise_maximum(a, b); }
DEV float rsd(float var) { return __builtin_amdgcn_rcpf(bfr(__builtin_amdgcn_sqrtf(bfr(bfr(var) + 1e-5f)))); }

constexpr int XS = 18432;
constexpr int WQ0 = 81920, WQS = 24576;
constexpr int WO0 = 65536;
constexpr int EXO = 147456;
constexpr int AR1 = 122880, AR2 = 32768;
constexpr int SNM = 155648, SRS = SNM + 576, SCOL = SNM + 1280, DUMP = SNM + 3072;
constexpr int LDS_BYTES = 163840;
constexpr size_t MB = 1u << 20;
constexpr size_t WS_ST = 0, WS_CS = 3 * MB / 2, WS_FL = 15 * MB / 8, WS_XN = 2 * MB, WS_O = 6 * MB, WS_X = 10 * MB, WS_KV = 14 * MB;
constexpr int FL_O = 0;
constexpr int FL_X = 4096;
constexpr int FL_KV = 8192;
constexpr int FL_L = 10240;
constexpr int FL_C = 11264;
constexpr int FL_EP = 12288;
#define FLS 16
#define FS 1
DEV int fi(int i) { return (i >> 4) * FLS + (i & 15) * FS; }

#define GL_HEAD "s_mov_b32 m0, %1\n\ts_nop 4\n\tglobal_load_lds_dwordx4 %2, %0 sc1"
DEV void glds1(const void* sb, unsigned lds, unsigned v0) { asm volatile(GL_HEAD ::"s"(sb), "s"(lds), "v"(v0) : "memory"); }
DEV void glds3(const void* sb, unsigned lds, unsigned v0, unsigned v1, unsigned v2) {
    asm volatile(GL_HEAD "\n\tglobal_load_lds_dwordx4 %3, %0 offset:1024 sc1\n\tglobal_load_lds_dwordx4 %4, %0 offset:2048 sc1"
                 ::"s"(sb), "s"(lds), "v"(v0), "v"(v1), "v"(v2) : "memory");
}
DEV void glds4(const void* sb, unsigned lds, unsigned v0, unsigned v1, unsigned v2, unsigned v3) {
    asm volatile(GL_HEAD "\n\tglobal_load_lds_dwordx4 %3, %0 offset:1024 sc1\n\tglobal_load_lds_dwordx4 %4, %0 offset:2048 sc1\n\t"
                 "global_load_lds_dwordx4 %5, %0 offset:3072 sc1" ::"s"(sb), "s"(lds), "v"(v0), "v"(v1), "v"(v2), "v"(v3) : "memory");
}
DEV void touch(const void* sb, unsigned lds, unsigned v0) {
    asm volatile("s_mov_b32 m0, %1\n\ts_nop 4\n\tglobal_load_lds_dword %2, %0 sc1" ::"s"(sb), "s"(lds), "v"(v0) : "memory");
}
DEV u64 ldep(const u64* p) { return __hip_atomic_load((const GAS u64*)p, __ATOMIC_RELAXED, __HIP_MEMORY_SCOPE_AGENT); }
#define FLD(d, a, z) "global_atomic_add_x2 " d ", " a ", " z ", off sc0 sc1"
DEV u64 ldflag(const u64* p) {
    u64 v;
    asm volatile(FLD("%0", "%1", "%2") "\n\ts_waitcnt vmcnt(0)" : "=&v"(v) : "v"(p), "v"(0ull) : "memory");
    return v;
}
DEV void stflag(u64* p, u64 v) { asm volatile("global_atomic_swap_x2 %0, %1, off sc1\n\ts_nop 1" ::"v"(p), "v"(v) : "memory"); }
template <class F> DEV unsigned poll(const u64* p, bool on, u64 tag, unsigned need, F fold) {
    unsigned m = ~0u;
    for (int it = 0; it < POLLMAX; ++it) {
        u64 v = tag;
        if (on) v = ldflag(p);
        const unsigned r = fold(__ballot(v == tag));
        if ((r & need) == need) { m = r; break; }
        __builtin_amdgcn_s_sleep(1);
    }
    asm volatile("" ::: "memory");
    return m;
}
DEV void publish(u64* f, int lane, u64 tag) {
    WAITV(0);
    if (lane == 0) stflag(f, tag);
}
#define OPAQUE(l, a, b, c) do { asm volatile("" : "+v"(l)); asm volatile("" : "+s"(a), "+s"(b), "+s"(c)); } while (0)

template <bool LOCAL, class F>
DEV void qkv_main(lchar* smem, f32x4 (&acc)[6][4], f32x4 (&acch)[2], int lane, int wm, int wn, F tailread STPARG) {
    constexpr int XC_B = LOCAL ? 9216 : 8192, HO = LOCAL ? 1024 : 0, NR = LOCAL ? 17 : 16;
    const int l15 = lane & 15, lg = lane >> 4, hs = LOCAL ? wm : 0;
    const lchar* xhb = smem + l15 * 64 + ((lg ^ fz(l15)) << 4);
    const lchar* xb = xhb + HO + wm * 4096;
    const int k0 = 8 * lg + (l15 >> 2);
    const lchar* wb0 = smem + WQ0 + lg * 3072 + (l15 >> 2) * 128 + (((wn * 2 + hs) ^ fswz(k0)) << 5) + (l15 & 3) * 8;
    const lchar* wb1 = smem + WQ0 + lg * 3072 + (l15 >> 2) * 128 + (((wn * 2 + (hs ^ 1)) ^ fswz(k0)) << 5) + (l15 & 3) * 8;
#pragma unroll
    for (int a = 0; a < 6; ++a)
#pragma unroll
        for (int b = 0; b < 4; ++b) { acc[a][b] = (f32x4){0.f, 0.f, 0.f, 0.f}; asm volatile("" : "+v"(acc[a][b])); }
    acch[0] = acch[1] = (f32x4){0.f, 0.f, 0.f, 0.f};
    if (LOCAL) asm volatile("" : "+v"(acch[0]), "+v"(acch[1]), "+v"(xhb));
    asm volatile("" : "+v"(xb), "+v"(wb0), "+v"(wb1));
    bf16x8 xf[2][5];
    s16x4 wh[2][6][2];
    auto rd = [&](int b, int j, int xo, int wo) {
        if (j < 4) xf[b][j] = ldk(xb + xo + j * 1024);
        else if (j < 16) wh[b][(j - 4) >> 1][j & 1] = ldtr4(((j & 2) ? wb1 : wb0) + wo + ((j - 4) >> 2) * 1024 + (j & 1) * 512);
        else xf[b][4] = ldk(xhb + xo);
    };
    auto kstep = [&](int b, int xo, int wo, bool last) {
        int j = 0;
#pragma unroll
        for (int ni = 0; ni < 6; ++ni) {
            const bf16x8 w = cat8(wh[b][ni][0], wh[b][ni][1]);
#pragma unroll
            for (int ti = 0; ti < 5; ++ti) {
                if (ti == 4) {
                    if (!LOCAL || (ni != 2 && ni != 4)) continue;
                    if (ni == 2) mfma16t(acch[0], w, xf[b][4]);
                    else mfma16t(acch[1], xf[b][4], w);
                } else {
                    if (ni >= 4) mfma16t(acc[ni][ti], xf[b][ti], w);
                    else mfma16t(acc[ni][ti], w, xf[b][ti]);
                }
                SB();
                if (last) tailread(j);
                else if (j < NR) rd(b ^ 1, j, xo, wo);
                SB();
                ++j;
            }
        }
        if (!last) { LGKM0(); SB(); }
    };
    BAR(); SB();
    STAMP(4);
#pragma unroll
    for (int j = 0; j < NR; ++j) rd(0, j, 0, 2 * WQS);
    LGKM0(); SB();
#pragma unroll 1
    for (int it = 0; it < 5; ++it) {
#pragma unroll
        for (int s = 0; s < 3; ++s) {
            kstep(0, s * XS + XC_B, ((s + 2) % 3) * WQS + WQS / 2, false);
            BAR(); SB();
            STAMPB(it * 3 + s);
            kstep(1, ((s + 1) % 3) * XS, s * WQS, false);
        }
    }
    kstep(0, XC_B, 2 * WQS + WQS / 2, false);
    BAR(); SB();
    STAMPB(15);
    kstep(1, 0, 0, true);
    MFMA_DONE();
}

DEV void qa_compute(lchar* smem, bool fold, u16* O, int g, int hp, int lane, int wid STPARG) {
    OPAQUE(lane, wid, g, hp);
    const int wm = wid >> 1, wn = wid & 1, l15 = lane & 15, lg = lane >> 4;
    f32x4 acc[6][4], acch[2];
    float nm[5], rs[5], csv[2];
    f32x4 nmv[5], rsv[5], cs[4];
    const lchar* sa = smem + SNM + l15 * 4;
    const lchar* sb = smem + SNM + lg * 16;
    const lchar* sc = smem + SCOL + wn * 128;
    const int ro = 64 + wm * 256;
    f32x4 cprev, ccur;
#pragma unroll
    for (int j = 0; j < 4; ++j) {
        const int d = l15 - 4 * lg - j;
        ccur[j] = (d >= 0 && !(d & 1)) ? 0.f : -3.0e38f;
        cprev[j] = (d <= -2 && !(d & 1)) ? 0.f : -3.0e38f;
    }
    const char* optr = (const char*)O + ((size_t)(hp * 2 + wn) * SEQ + g * 128 + wm * 64 + l15) * 64 +
                       (((((lg & 1) << 1) | (lg >> 1)) ^ (wm << 1) ^ fz(l15)) << 4);
    asm volatile("" : "+v"(cprev), "+v"(ccur), "+v"(optr));
    qkv_main<true>(smem, acc, acch, lane, wm, wn, [&](int j) {
        if (j < 4) cs[j] = *(const LAS f32x4*)(sc + (j >> 1) * 256 + (((j & 1) ^ wm) << 6) + lg * 16);
        else if (j < 9) { const int o = j < 8 ? ro + (j - 4) * 64 : 0; nm[j - 4] = *(const LAS float*)(sa + o); rs[j - 4] = *(const LAS float*)(sa + o + 576); }
        else if (j < 11) csv[j - 9] = *(const LAS float*)(sc + 512 + (((j - 9) ^ wm) << 6) + l15 * 4);
        else if (j < 21) {
            const int t = (j - 11) >> 1, o = t < 4 ? ro + t * 64 : 0;
            if ((j - 11) & 1) rsv[t] = *(const LAS f32x4*)(sb + o + 576);
            else nmv[t] = *(const LAS f32x4*)(sb + o);
        }
    } STPPASS(0));
    STAMP(2);
    if (fold) {
#pragma unroll
        for (int ti = 0; ti < 4; ++ti) {
#pragma unroll
            for (int ni = 0; ni < 4; ++ni) acc[ni][ti] = (acc[ni][ti] + nm[ti] * cs[ni]) * rs[ti];
#pragma unroll
            for (int h = 0; h < 2; ++h) acc[4 + h][ti] = (acc[4 + h][ti] + nmv[ti] * csv[h]) * rsv[ti];
        }
        acch[0] = (acch[0] + nm[4] * cs[2]) * rs[4];
        acch[1] = (acch[1] + nmv[4] * csv[0]) * rsv[4];
    }
    u32x2 kp[5][2], vp[5][2];
    lchar* ex = smem + EXO + wn * 4096 + lane * 16;
    {
        const u32x2 a = pk4(acch[0]), b = pk4(acch[1]);
        *(LAS u32x4*)(ex + wm * 1024) = (u32x4){a[0], a[1], b[0], b[1]};
    }
#pragma unroll
    for (int h = 0; h < 2; ++h) { kp[4][h] = pk4(acc[2 + h][3]); vp[4][h] = pk4(acc[4 + h][3]); }
    if (wm == 0) {
#pragma unroll
        for (int h = 0; h < 2; ++h) *(LAS u32x4*)(ex + 2048 + h * 1024) = (u32x4){kp[4][h][0], kp[4][h][1], vp[4][h][0], vp[4][h][1]};
    }
    bf16x8 qf[4];
#pragma unroll
    for (int ti = 0; ti < 4; ++ti) {
        qf[ti] = cat(pk4(acc[0][ti]), pk4(acc[1][ti]));
        if (ti < 3) {
#pragma unroll
            for (int h = 0; h < 2; ++h) { kp[ti + 1][h] = pk4(acc[2 + h][ti]); vp[ti + 1][h] = pk4(acc[4 + h][ti]); }
        }
    }
    SB();
    BARL();
#pragma unroll
    for (int h = 0; h < 2; ++h) {
        const u32x4 e = *(const LAS u32x4*)(ex + wm * 2048 + ((h ^ wm) << 10));
        kp[0][h] = (u32x2){e[0], e[1]}; vp[0][h] = (u32x2){e[2], e[3]};
    }
    f32x4 cp0 = cprev;
    if (g == 0 && wm == 0) cp0 = (f32x4){-3.0e38f, -3.0e38f, -3.0e38f, -3.0e38f};
    f32x4 sv[4];
    float mx[4];
#pragma unroll
    for (int t = 0; t < 4; ++t) {
        const int ti = 3 - t;
        const f32x4 s1 = MFMA16(cat(kp[ti + 1][0], kp[ti + 1][1]), qf[ti], ccur);
        const f32x4 s0 = MFMA16(cat(kp[ti][0], kp[ti][1]), qf[ti], ti ? cprev : cp0);
        f32x4 v = {fmx(s0[0], s0[1]), fmx(s0[2], s0[3]), fmx(s1[0], s1[1]), fmx(s1[2], s1[3])};
        if (EMU) {
            const u32x2 r1 = pk4(v);
            const u32x2 r2 = pk4((f32x4){lo16(r1[0]), hi16(r1[0]), lo16(r1[1]), hi16(r1[1])} * QSCALE);
            v = (f32x4){lo16(r2[0]), hi16(r2[0]), lo16(r2[1]), hi16(r2[1])};
        }
        sv[ti] = v;
        mx[ti] = fmx(fmx(fmx(v[0], v[1]), v[2]), v[3]);
    }
    {
        const f32x2 a = sw16f(mx[0], mx[1]), b = sw16f(mx[2], mx[3]);
        const f32x2 c = sw32f(fmx(a[0], a[1]), fmx(b[0], b[1]));
        const float r = fmx(c[0], c[1]) * -(EMU ? LOG2E : QSCALE * LOG2E);
        const f32x2 d = sw32f(r, r), e = sw16f(d[0], d[0]), f = sw16f(d[1], d[1]);
        mx[0] = e[0]; mx[1] = e[1]; mx[2] = f[0]; mx[3] = f[1];
    }
    const unsigned sel0 = (lane & 1) ? 0x01000c0cu : 0x0c0c0100u, sel1 = (lane & 1) ? 0x03020c0cu : 0x0c0c0302u;
    const bf16x8 ones = {0x3f80, 0x3f80, 0x3f80, 0x3f80, 0x3f80, 0x3f80, 0x3f80, 0x3f80};
    const f32x4 z = {0.f, 0.f, 0.f, 0.f};
    u32x4 ov[4];
#pragma unroll
    for (int ti = 0; ti < 4; ++ti) {
        const f32x4 x = sv[ti] * (EMU ? LOG2E : QSCALE * LOG2E) + mx[ti];
        const unsigned c0 = cvtpk(__builtin_amdgcn_exp2f(x[0]), __builtin_amdgcn_exp2f(x[1]));
        const unsigned c1 = cvtpk(__builtin_amdgcn_exp2f(x[2]), __builtin_amdgcn_exp2f(x[3]));
        const bf16x8 p = __builtin_bit_cast(bf16x8, (u32x4){__builtin_amdgcn_perm(c0, c0, sel0), __builtin_amdgcn_perm(c0, c0, sel1),
                                                            __builtin_amdgcn_perm(c1, c1, sel0), __builtin_amdgcn_perm(c1, c1, sel1)});
        const f32x4 l = MFMA16(ones, p, z);
        const f32x4 o0 = MFMA16(cat(vp[ti][0], vp[ti + 1][0]), p, z);
        const f32x4 o1 = MFMA16(cat(vp[ti][1], vp[ti + 1][1]), p, z);
        const float inv = __builtin_amdgcn_rcpf(l[0]);
        ov[ti] = unit16(pk4(o0 * inv), pk4(o1 * inv));
    }
    st16wt<0>(optr, ov[0]); st16wt<1024>(optr, ov[1]); st16wt<2048>(optr, ov[2]); st16wt<3072>(optr, ov[3]);
    STAMP(10);
    BAR();
}

#define MX3(a, b, c) fmx(fmx((a), (b)), (c))
DEV float rowmax32(const f32x16& s0) {
    float ma = MX3(s0[0], s0[1], s0[2]), mb = MX3(s0[3], s0[4], s0[5]);
    ma = MX3(ma, s0[6], s0[7]); mb = MX3(mb, s0[8], s0[9]);
    ma = MX3(ma, s0[10], s0[11]); mb = MX3(mb, s0[12], s0[13]);
    ma = fmx(MX3(ma, s0[14], s0[15]), mb);
    const f32x2 b = sw32f(ma, ma);
    return fmx(b[0], b[1]);
}
#define EX(i) do { S[i] = __builtin_amdgcn_exp2f(S[i]); SB(); } while (0)
#define EP(i) do { p1[i] = __builtin_amdgcn_exp2f(S[8 + i]); SB(); } while (0)
typedef __attribute__((ext_vector_type(16))) __bf16 bf16x16_t;
typedef __attribute__((ext_vector_type(8))) __bf16 bf16x8_t;
typedef __attribute__((ext_vector_type(8))) float f32x8;
template <int H> DEV f32x8 half8(const f32x16& s) { return __builtin_shufflevector(s, s, H, H + 1, H + 2, H + 3, H + 4, H + 5, H + 6, H + 7); }
#define SMF32(a, b, c) __builtin_amdgcn_smfmac_f32_32x32x32_bf16(__builtin_bit_cast(bf16x8_t, a), __builtin_bit_cast(bf16x16_t, b), c, hi, 0, 0)
#define SMF16(a, b, c) __builtin_amdgcn_smfmac_f32_16x16x64_bf16(__builtin_bit_cast(bf16x8_t, a), __builtin_bit_cast(bf16x16_t, b), c, hi, 0, 0)
template <class F0, class F1, class F2, class F3>
DEV void block(f32x16& S, f32x16& N, const bf16x8& kf0, const bf16x8& kf1, const bf16x8& q0, const bf16x8& q1, const f32x16& c,
               f32x16& oP, f32x4& lP, const bf16x8& vf1, f32x8& p1, f32x16& oC, f32x4& lC, const bf16x8& vf0,
               const bf16x8& sel, int hi, F0 fb, F1 fc, F2 fd, F3 ff) {
    N = MFMA32(kf0, q0, c); SB();
    EX(0); EX(1); EX(2);
    oP = SMF32(vf1, p1, oP); SB();
    fb(); SB();
    EX(3); EX(4); EX(5);
    lP = SMF16(sel, p1, lP); SB();
    fc(); SB();
    EX(6);
    N = MFMA32(kf1, q1, N); SB();
    fd(); SB();
    EX(7); EP(0); EP(1);
    oC = SMF32(vf0, half8<0>(S), oC); SB();
    EP(2); EP(3); EP(4);
    lC = SMF16(sel, half8<0>(S), lC); SB();
    ff(); SB();
    EP(5); EP(6); EP(7);
}
DEV void attn_slow(const char* kh, const char* vh, const bf16x8& q0, const bf16x8& q1, f32x16& o, float& l, int hi) {
    f32x16 c;
#pragma unroll
    for (int i = 0; i < 16; ++i) { o[i] = 0.f; c[i] = 0.f; }
    float m = 0.f;
    l = 0.f;
#pragma unroll 1
    for (int t = 0; t < SEQ / 32; ++t) {
        bf16x8 k0, k1, v0, v1;
        const char* kp = kh + t * 2048;
        const char* vp = vh + t * 2048;
        asm volatile("global_load_dwordx4 %0, %4, off sc1\n\tglobal_load_dwordx4 %1, %5, off sc1\n\tglobal_load_dwordx4 %2, %6, off sc1\n\t"
                     "global_load_dwordx4 %3, %7, off sc1\n\ts_waitcnt vmcnt(0)" : "=&v"(k0), "=&v"(k1), "=&v"(v0), "=&v"(v1)
                     : "v"(kp), "v"((const char*)((uintptr_t)kp ^ 32)), "v"(vp), "v"((const char*)((uintptr_t)vp ^ 32)) : "memory");
        f32x16 s = MFMA32(k0, q0, c);
        s = MFMA32(k1, q1, s);
        const float mx = rowmax32(s);
        const float dl = t == 0 ? mx : fmx(mx, 0.f);
        const float alpha = t == 0 ? 0.f : __builtin_amdgcn_exp2f(-dl);
        m += dl;
        l *= alpha;
#pragma unroll
        for (int i = 0; i < 16; ++i) {
            o[i] *= alpha; c[i] = -m;
            s[i] = __builtin_amdgcn_exp2f(s[i] - dl);
            l += hi16(__float_as_uint(s[i]));
        }
        o = SMF32(v0, half8<0>(s), o);
        o = SMF32(v1, half8<8>(s), o);
    }
    const f32x2 b = sw32f(l, l);
    l = b[0] + b[1];
}

DEV void qb_compute(lchar* smem, u16* kv, u16* O, u64* fkv, u64 tag, int g, int hp, int lane, int wid STPARG) {
    OPAQUE(lane, wid, g, hp);
    const int wm = wid >> 1, wn = wid & 1, l15 = lane & 15, lg = lane >> 4, h = hp * 2 + wn;
    bf16x8 qa0, qa1, qb0, qb1;
    lchar* gs = smem + wn * 16384;
    {
        f32x4 acc[6][4], acch[2];
        float nm[4], rs[4], csv[2];
        f32x4 cs[4], nmv[4], rsv[4];
        const lchar* sa = smem + SNM + 64 + wm * 256 + l15 * 4;
        const lchar* sb = smem + SNM + 64 + wm * 256 + lg * 16;
        const lchar* sc = smem + SCOL + wn * 128;
        const int un = ((lg & 1) << 1) | (lg >> 1);
        const int ko = l15 * 64 + ((un ^ (l15 >> 2)) << 4), vo = l15 * 64 + ((lg ^ (l15 >> 2)) << 4);
        const char* kptr = (const char*)kv + ((size_t)h * SEQ + g * 128 + wm * 64) * 64 + ko;
        const char* vptr = (const char*)kv + ((size_t)(NH + h) * SEQ + g * 128 + wm * 64) * 64 + vo;
        lchar* kl = gs + wm * 8192 + ko;
        lchar* vl = gs + wm * 8192 + 4096 + vo;
        asm volatile("" : "+v"(kptr), "+v"(vptr));
        qkv_main<false>(smem, acc, acch, lane, wm, wn, [&](int j) {
            if (j < 4) cs[j] = *(const LAS f32x4*)(sc + (j >> 1) * 256 + (j & 1) * 64 + lg * 16);
            else if (j < 8) { nm[j - 4] = *(const LAS float*)(sa + (j - 4) * 64); rs[j - 4] = *(const LAS float*)(sa + (j - 4) * 64 + 576); }
            else if (j < 10) csv[j - 8] = *(const LAS float*)(sc + 512 + (j - 8) * 64 + l15 * 4);
            else if (j < 18) {
                if (j & 1) rsv[(j - 10) >> 1] = *(const LAS f32x4*)(sb + ((j - 10) >> 1) * 64 + 576);
                else nmv[(j - 10) >> 1] = *(const LAS f32x4*)(sb + ((j - 10) >> 1) * 64);
            }
        } STPPASS(0));
        STAMP(2);
        auto tile = [&](int ni, int ti, float sc_) { return pk4((acc[ni][ti] + nm[ti] * cs[ni]) * sc_); };
        auto tilev = [&](int h_, int ti) { return pk4((acc[4 + h_][ti] + nmv[ti] * csv[h_]) * rsv[ti]); };
#define KOUT(ti) do { const u32x4 v = unit16(tile(2, ti, rs[ti]), tile(3, ti, rs[ti])); st16wt<ti * 1024>(kptr, v); *(LAS u32x4*)(kl + ti * 1024) = v; } while (0)
#define VOUT(j) do { const u32x4 v = __builtin_bit_cast(u32x4, cat(tilev(j & 1, j & 2), tilev(j & 1, (j & 2) + 1))); st16wt<j * 1024>(vptr, v); *(LAS u32x4*)(vl + j * 1024) = v; } while (0)
        KOUT(0); KOUT(1); KOUT(2); KOUT(3);
        VOUT(0); VOUT(1); VOUT(2); VOUT(3);
        if (A0 == 1) { SB(); BARL(); SB(); }
        if (PP == 3) { publish(fkv + fi(wn * 32 + wm * 16 + g), lane, tag); SB(); }
#pragma unroll
        for (int ti = 0; ti < 4; ++ti) rs[ti] *= QSCALE * LOG2E;
        qa0 = __builtin_bit_cast(bf16x8, unit16(tile(0, 0, rs[0]), tile(0, 1, rs[1])));
        qa1 = __builtin_bit_cast(bf16x8, unit16(tile(1, 0, rs[0]), tile(1, 1, rs[1])));
        qb0 = __builtin_bit_cast(bf16x8, unit16(tile(0, 2, rs[2]), tile(0, 3, rs[3])));
        qb1 = __builtin_bit_cast(bf16x8, unit16(tile(1, 2, rs[2]), tile(1, 3, rs[3])));
    }
    STAMP(3);
    const int r = lane & 31, hh = lane >> 5;
    const int kro = ((r & 4) * 4 + (r >> 3) * 4 + (r & 3)) * 64 + ((hh ^ (r >> 3)) << 4), vro = r * 64 + ((hh ^ ((r >> 2) & 3)) << 4);
    const lchar* kb0 = gs + kro;
    const lchar* kb1 = gs + (kro ^ 32);
    const lchar* vb0 = gs + 4096 + vro;
    const lchar* vb1 = gs + 4096 + (vro ^ 32);
    const short se = (l15 & 1) ? (short)0 : (short)0x3f80, so = se ^ 0x3f80;
    const bf16x8 sel = {se, se, se, se, so, so, so, so};
    int hi = 0xdddd;
    asm volatile("" : "+v"(hi));
    f32x16 oA, oB, cA, cB, sA, sB;
    f32x4 lA = {0.f, 0.f, 0.f, 0.f}, lB = {0.f, 0.f, 0.f, 0.f};
#pragma unroll
    for (int i = 0; i < 16; ++i) { oA[i] = 0.f; oB[i] = 0.f; }
    bf16x8 kf0, kf1, vf0, vf1[2];
    f32x8 p1 = {0.f, 0.f, 0.f, 0.f, 0.f, 0.f, 0.f, 0.f};
    auto nop = [&]() {};
    auto tilestep = [&](int par, int nx, bool bar) {
        block(sA, sB, kf0, kf1, qb0, qb1, cB, oB, lB, vf1[par ^ 1], p1, oA, lA, vf0, sel, hi, [&]() { if (bar) BAR(); },
              [&]() { kf0 = ldk(kb0 + nx); }, [&]() { __builtin_amdgcn_s_waitcnt(0xc17f); }, [&]() { LGKM0(); SB(); kf1 = ldk(kb1 + nx); });
        block(sB, sA, kf0, kf1, qa0, qa1, cA, oA, lA, vf1[par], p1, oB, lB, vf0, sel, hi, nop,
              [&]() { LGKM0(); SB(); vf1[par ^ 1] = ldk(vb1 + nx); }, nop, [&]() { vf0 = ldk(vb0 + nx); });
    };
    {
        const int own = wm * 8192, oth = own ^ 8192;
        kf0 = ldk(kb0 + own); kf1 = ldk(kb1 + own);
        vf0 = ldk(vb0 + own); vf1[0] = ldk(vb1 + own); vf1[1] = sel;
        if (PP == 1) { asm volatile("" : "+v"(qa0), "+v"(qa1), "+v"(qb0), "+v"(qb1)); SB(); publish(fkv + fi(wn * 32 + wm * 16 + g), lane, tag); SB(); }
        sA = MFMA32(kf0, qa0, oA); sB = MFMA32(kf0, qb0, oA);
        sA = MFMA32(kf1, qa1, sA); sB = MFMA32(kf1, qb1, sB);
        if (PP == 2) { SB(); publish(fkv + fi(wn * 32 + wm * 16 + g), lane, tag); SB(); }
        const float mA = rowmax32(sA), mB = rowmax32(sB);
#pragma unroll
        for (int i = 0; i < 16; ++i) { cA[i] = -mA; cB[i] = -mB; sA[i] -= mA; }
        asm volatile("" : "+v"(cA), "+v"(cB));
        SB();
        if (PP == 0) publish(fkv + fi(wn * 32 + wm * 16 + g), lane, tag);
        SB();
        STAMP(5);
        tilestep(0, own + 2048, false);
        tilestep(1, oth, !A0);
        tilestep(0, oth + 2048, false);
        tilestep(1, AR1, true);
        STAMP(6);
    }
#pragma unroll 1
    for (int it = 0; it < 5; ++it) {
#pragma unroll
        for (int s = 0; s < 3; ++s) {
            const int cur = s == 0 ? AR1 : s == 1 ? AR2 : 0, nxt = s == 0 ? AR2 : s == 1 ? 0 : AR1;
            tilestep(0, cur + 2048, false);
            tilestep(1, cur + 8192, false);
            tilestep(0, cur + 10240, false);
            tilestep(1, nxt, true);
        }
    }
    oB = SMF32(vf1[1], p1, oB);
    lB = SMF16(sel, p1, lB);
    STAMP(9);
    float la = (lane & 16) ? lA[1] : lA[0], lb = (lane & 16) ? lB[1] : lB[0];
    if (__any(!(la <= 1073741824.0f) || !(lb <= 1073741824.0f))) {
        const char* kh = (const char*)kv + (size_t)h * SEQ * 64;
#pragma unroll 1
        for (int ps = 0; ps < 2; ++ps) {
            oA = oB; la = lb;
            attn_slow(kh + kro, kh + (size_t)NH * SEQ * 64 + vro, ps ? qb0 : qa0, ps ? qb1 : qa1, oB, lb, hi);
        }
    }
    const char* op = (const char*)O + ((size_t)h * SEQ + g * 128 + wm * 64 + l15) * 64 + (((((lg & 1) << 1) | (lg >> 1)) ^ fz(l15)) << 4);
    auto fin = [&](const f32x16& o, float l, u32x4& u0, u32x4& u1) {
        const f32x16 y = o * __builtin_amdgcn_rcpf(l);
        const u32x2 a = sw32(cvtpk(y[0], y[1]), cvtpk(y[4], y[5])), b = sw32(cvtpk(y[2], y[3]), cvtpk(y[6], y[7]));
        const u32x2 c = sw32(cvtpk(y[8], y[9]), cvtpk(y[12], y[13])), d = sw32(cvtpk(y[10], y[11]), cvtpk(y[14], y[15]));
        const u32x2 e = sw16(a[0], c[0]), f = sw16(b[0], d[0]), i = sw16(a[1], c[1]), j = sw16(b[1], d[1]);
        u0 = (u32x4){e[0], f[0], i[0], j[0]};
        u1 = (u32x4){e[1], f[1], i[1], j[1]};
    };
    u32x4 u0, u1;
    fin(oA, la, u0, u1);
    st16wt<0>(op, u0); st16wt<1024>(op, u1);
    fin(oB, lb, u0, u1);
    st16wt<2048>(op, u0); st16wt<3072>(op, u1);
    STAMP(10);
}

constexpr int NRW = 7;
DEV constexpr int wo_slot(int t) { return t < 7 ? t : t < 10 ? t + 1 : t - 8; }
DEV unsigned wo_voff(int lane, int cb, int i) {
    const int k = 8 * i + (lane >> 3), pc = lane & 7;
    return (k * 1024 + cb * 64 + ((((pc >> 1) ^ fswz(k)) << 1) + (pc & 1)) * 8) * 2 - i * 1024;
}
DEV void wo_compute(lchar* smem, u64* fo, u32x4 (&rr)[4], u16* C, char* stats, u64* fx, u64 tag, int g, int cb, int lane,
                    int wid STPARG) {
    OPAQUE(lane, wid, g, cb);
    const int wm = wid >> 1, wn = wid & 1, l15 = lane & 15, lg = lane >> 4;
    const int k0 = 8 * lg + (l15 >> 2);
    const lchar* wb0 = smem + WO0 + k0 * 128 + (((wn * 2) ^ fswz(k0)) << 5) + (l15 & 3) * 8;
    const lchar* wb1 = smem + WO0 + k0 * 128 + (((wn * 2 + 1) ^ fswz(k0)) << 5) + (l15 & 3) * 8;
    asm volatile("" : "+v"(wb0), "+v"(wb1));
    bf16x8 xf[2][8];
    s16x4 wh[16][8];
    auto rdw = [&](int t, int j) { wh[t][j] = ldtr4(((j & 2) ? wb1 : wb0) + wo_slot(t) * 8192 + (j >> 2) * 4096 + (j & 1) * 512); };
    SB();
#pragma unroll
    for (int t = 0; t < NRW; ++t)
#pragma unroll
        for (int j = 0; j < 8; ++j) rdw(t, j);
    SB();
    LGKM0(); SB();
    BAR();
    publish(fo, lane, tag);
    STAMP(5);
    STAMPW(6 + wid);
    asm volatile("" : "+v"(lane));
    const int un = ((lg & 1) << 1) | (lg >> 1), row = g * 128 + wm * 64 + l15, ch = cb * 2 + wn;
    const unsigned ocm = (ch * SEQ + row) * 64 + ((un ^ fz(l15)) << 4), orm = row * 2048 + ch * 64 + un * 16;
    const char* cp = (const char*)C + (stats ? ocm : orm);
    const char* sp = stats + ((g * 2 + wm) * 32 + ch) * 512 + lane * 8;
    const lchar* xb = smem + (wm * 64 + l15) * 64 + ((lg ^ fz(l15)) << 4);
    asm volatile("" : "+v"(cp), "+v"(sp), "+v"(xb));
    f32x4 acc[2][4];
#pragma unroll
    for (int ti = 0; ti < 4; ++ti) {
        const u32x2 x = sw16(rr[ti][0], rr[ti][2]), y = sw16(rr[ti][1], rr[ti][3]);
#pragma unroll
        for (int ni = 0; ni < 2; ++ni) acc[ni][ti] = (f32x4){lo16(x[ni]), hi16(x[ni]), lo16(y[ni]), hi16(y[ni])};
    }
    auto rdx = [&](int t, int j) { xf[t & 1][j] = ldk(xb + (t % 5) * 16384 + (j >> 2) * 8192 + (j & 3) * 1024); };
    auto mm = [&](int t, int kk, int j) {
        const int ti = j >> 1, ni = j & 1;
        mfma16t(acc[ni][ti], cat8(wh[t][kk * 4 + ni * 2], wh[t][kk * 4 + ni * 2 + 1]), xf[t & 1][kk * 4 + ti]);
    };
    SB();
#pragma unroll
    for (int t = 0; t < 16; ++t) {
        BAR(); SB();
        STAMPB(t);
        if (t == 0) {
            STAMP(4);
#pragma unroll
            for (int j = 0; j < 8; ++j) rdx(0, j);
            SB();
        } else {
#pragma unroll
            for (int j = 0; j < 8; ++j) { rdx(t, j); SB(); mm(t - 1, 1, j); SB(); }
        }
        const bool more = t + 1 >= NRW && t < 15;
#pragma unroll
        for (int j = 0; j < 8; ++j) { mm(t, 0, j); SB(); if (more) { rdw(t + 1, j); SB(); } }
        if (more) __builtin_amdgcn_s_waitcnt(0xc87f); else LGKM0();
        SB();
    }
#pragma unroll
    for (int j = 0; j < 8; ++j) mm(15, 1, j);
    MFMA_DONE();
    STAMP(2);
    u32x4 (&ov)[4] = rr;
#pragma unroll
    for (int ti = 0; ti < 4; ++ti) ov[ti] = unit16(pk4(acc[0][ti]), pk4(acc[1][ti]));
    if (stats) {
        st16wt<0>(cp, ov[0]); st16wt<1024>(cp, ov[1]); st16wt<2048>(cp, ov[2]); st16wt<3072>(cp, ov[3]);
        float s[4], q[4];
        const bf16x2_t one2 = __builtin_bit_cast(bf16x2_t, 0x3f803f80u);
#pragma unroll
        for (int ti = 0; ti < 4; ++ti) {
            s[ti] = 0.f; q[ti] = 0.f;
#pragma unroll
            for (int e = 0; e < 4; ++e) {
                const unsigned u = ov[ti][e];
                const bf16x2_t v = __builtin_bit_cast(bf16x2_t, u);
                s[ti] = __builtin_amdgcn_fdot2_f32_bf16(v, one2, s[ti], false);
                q[ti] = __builtin_amdgcn_fdot2_f32_bf16(v, v, q[ti], false);
            }
        }
        auto red = [&](const float (&v)[4]) {
            const f32x2 a = sw16f(v[0], v[1]), b = sw16f(v[2], v[3]);
            const f32x2 c = sw32f(a[0] + a[1], b[0] + b[1]);
            return c[0] + c[1];
        };
        st8wt(sp, (u32x2){__float_as_uint(red(s)), __float_as_uint(red(q))});
        STAMP(10);
        BAR();
        publish(fx + fi((wn * 32 + g * 2 + wm) * 16 + cb), lane, tag);
        STAMP(11);
        STAMPW(12 + wid);
    } else {
#pragma unroll
        for (int ti = 0; ti < 4; ++ti) *(GAS u32x4*)(cp + ti * 32768) = ov[ti];
        STAMP(10);
    }
}

DEV void ln0_rows(u32x4 (&x)[4]) {
    auto sum32 = [&](float v) {
        v = dppadd<0x128>(dppadd<0x4E>(dppadd<0xB1>(v)));
        const f32x2 a = sw16f(v, v);
        v = a[0] + a[1];
        const f32x2 b = sw32f(v, v);
        return b[0] + b[1];
    };
    f32x2 v[16];
#pragma unroll
    for (int i = 0; i < 16; ++i) v[i] = (f32x2){lo16(x[i >> 2][i & 3]), hi16(x[i >> 2][i & 3])};
    f32x2 s = v[0];
#pragma unroll
    for (int i = 1; i < 16; ++i) s += v[i];
    const float mean = sum32(s[0] + s[1]) * (1.0f / DM);
    f32x2 q = {0.f, 0.f};
#pragma unroll
    for (int i = 0; i < 16; ++i) { const f32x2 d = v[i] - mean; q += d * d; }
    const float var = sum32(q[0] + q[1]) * (1.0f / DM);
    const float mb = bfr(mean);
    const float inv = rsd(var);
#pragma unroll
    for (int i = 0; i < 16; ++i) {
        const f32x2 e = v[i] - mb;
        const unsigned d = cvtpk(e[0], e[1]);
        const f32x2 y = (f32x2){lo16(d), hi16(d)} * inv;
        x[i >> 2][i & 3] = cvtpk(y[0], y[1]);
    }
}

struct HW { unsigned wq[2][3], wo[4], wt, vo16, lds, tq; };
typedef u32x4 Tail[18];
DEV void touch_wq(const HW& h, const u16* W, int i) { touch((const char*)W + (i / 3) * (256 * 6144) + (i % 3) * 2048, h.lds + DUMP, h.tq); }
template <int N> DEV void wq_issue(const HW& h, const u16* W, int t, int r0) {
    const char* src = (const char*)W + (unsigned)(t * 8 + r0) * (8 * 6144);
    const unsigned l = h.lds + WQ0 + ((t + 2) % 3) * WQS + r0 * 3072;
#pragma unroll
    for (int i = 0; i < N; ++i) glds3(src + i * (8 * 6144), l + i * 3072, h.wq[i & 1][0], h.wq[i & 1][1], h.wq[i & 1][2]);
}
template <int K0, int K1> DEV void tail_ld(const HW& h, Tail& tw, const u16* W) {
#pragma unroll
    for (int k = K0; k < K1; ++k)
        asm volatile("s_nop 4\n\tglobal_load_dwordx4 %0, %1, %2" : "=&v"(tw[k]) : "v"(h.wt + k * (32 * 2048)), "s"(W) : "memory");
}
DEV void tail_pin(Tail& tw) {
#pragma unroll
    for (int k = 0; k < 18; ++k) asm volatile("" : "+v"(tw[k]));
}
DEV void wo_issue(const HW& h, const u16* W, int t0, int t1) {
#pragma unroll 1
    for (int i = 2 * t0; i < 2 * t1; ++i) glds4((const char*)W + (size_t)i * (32 * 2048), h.lds + WO0 + i * 4096, h.wo[0], h.wo[1], h.wo[2], h.wo[3]);
}
template <int ST> DEV void ld8(f32x4 (&d)[8], const char* sb, unsigned vo) {
    asm volatile("s_nop 4\n\tglobal_load_dwordx4 %0, %8, %9 offset:%10 sc1\n\tglobal_load_dwordx4 %1, %8, %9 offset:%11 sc1\n\t"
                 "global_load_dwordx4 %2, %8, %9 offset:%12 sc1\n\tglobal_load_dwordx4 %3, %8, %9 offset:%13 sc1\n\t"
                 "global_load_dwordx4 %4, %8, %9 sc1\n\tglobal_load_dwordx4 %5, %8, %9 offset:%14 sc1\n\t"
                 "global_load_dwordx4 %6, %8, %9 offset:%15 sc1\n\tglobal_load_dwordx4 %7, %8, %9 offset:%16 sc1"
                 : "=&v"(d[0]), "=&v"(d[1]), "=&v"(d[2]), "=&v"(d[3]), "=&v"(d[4]), "=&v"(d[5]), "=&v"(d[6]), "=&v"(d[7])
                 : "v"(vo), "s"(sb + 4 * ST), "n"(-4 * ST), "n"(-3 * ST), "n"(-2 * ST), "n"(-ST), "n"(ST), "n"(2 * ST), "n"(3 * ST) : "memory");
}

template <bool LOCAL>
DEV void qkv_helper(lchar* smem, const HW& h, Tail& tw, int layer, const u16* X, const u16* W, const u16* W2, const u16* Wo, const char* stats, float* csum,
                    u64* fl, u64 tag, int g, int hp, int lane, int lw STPARG) {
    OPAQUE(lane, lw, g, hp);
    constexpr int NX = LOCAL ? 9 : 8, HO = LOCAL ? 16 : 0;
    const int m0 = g * 128;
    const bool halo = LOCAL && g > 0;
    const u64* fx = fl + fi(FL_X + (layer - 1) * 1024 + (g * 2 - (halo ? 1 : 0)) * 16 + (lane < (halo ? 48 : 32) ? lane : 0));
    const bool fon = lane < (halo ? 48 : 32);
    auto fold = [&](u64 m) { return (unsigned)(m & (m >> 16) & (halo ? m >> 32 : m)); };
    if (lw < 2) {
        const unsigned vo0 = (LOCAL && g == 0) ? h.vo16 + 1024 : h.vo16;
        const char* xsrc = (const char*)X + ((size_t)lw * SEQ + m0 - HO) * 64;
        const unsigned ldx = h.lds + lw * (NX * 1024);
        unsigned rdy = 0u;
        STAMP(0);
        if (LOCAL && layer == 0) {
            if (lw == 0) poll(fl + fi(FL_L + g * 16 - (halo ? 2 : 0) + lane), lane < (halo ? 18 : 16), tag, ~0u, [](u64 m) { return (unsigned)m; });
            BAR();
            rdy = ~0u;
        }
        auto issue = [&](int t, int so) {
            if (!((rdy >> t) & 1)) rdy = poll(fx + fi(lw * 512), fon, tag, 1u << t, fold);
            glds4(xsrc, ldx + so, vo0, h.vo16, h.vo16, h.vo16);
            glds4(xsrc + 4096, ldx + so + 4096, h.vo16, h.vo16, h.vo16, h.vo16);
            if (LOCAL) glds1(xsrc + 8192, ldx + so + 8192, h.vo16);
            xsrc += 2 * SEQ * 64;
        };
        issue(0, 0);
        STAMP(1);
        if (lw == 1) STAMPW(11);
        issue(1, XS);
        if (rdy & 4) {
            issue(2, 2 * XS);
            if (LOCAL && layer == 0) BAR();
            WAITB(2 * NX);
        } else {
            WAITB(NX);
            issue(2, 2 * XS);
        }
        WAITB(NX);
        int so = 0;
#pragma unroll 1
        for (int t = 3; t < 16; ++t) {
            wq_issue<1>(h, W, t, lw);
            issue(t, so);
            so = so == 2 * XS ? 0 : so + XS;
            WAITB(NX + 3);
        }
        if (LOCAL) {
            tail_ld<0, 6>(h, tw, Wo);
            WAITB(6);
            tail_ld<6, 12>(h, tw, Wo);
            BAR();
            tail_ld<12, 18>(h, tw, Wo);
            BAR();
            WAITV(0);
            tail_pin(tw);
        } else {
            WAITB(0);
            BAR(); BAR();
        }
    } else if (lw == 2) {
        if (LOCAL && layer == 0) { BAR(); BAR(); }
        WAITB(0);
        if (layer) wq_issue<4>(h, W, 2, 0);
        BAR();
#pragma unroll 1
        for (int t = 3; t < 15; ++t) {
            wq_issue<6>(h, W, t, 2);
            if (LOCAL && t >= 10 && t < 14) {
                glds4((const char*)Wo + (unsigned)(t - 10) * (32 * 2048), h.lds + WO0 + (t - 10) * 4096, h.wo[0], h.wo[1], h.wo[2], h.wo[3]);
                WAITB(22);
            } else WAITB(18);
        }
        wq_issue<6>(h, W, 15, 2);
        if (LOCAL) {
            WAITB(18);
            wo_issue(h, Wo, 2, 5);
            WAITB(24);
            wo_issue(h, Wo, 5, 7);
            tail_ld<0, 6>(h, tw, Wo);
            BAR();
            tail_ld<6, 18>(h, tw, Wo);
            BAR();
            WAITV(0);
            tail_pin(tw);
        } else {
            WAITB(18);
            WAITB(0);
            BAR(); BAR();
        }
    } else {
        auto warm = [&]() {
            asm volatile("s_mov_b32 m0, %2\n\ts_nop 4\n\tglobal_load_lds_dword %0, %1\n\tglobal_load_lds_dword %3, %1" ::"v"(lane * 64),
                         "s"(Wo + (size_t)blockIdx.x * 4096), "s"(h.lds + DUMP), "v"(lane * 64 + 4096) : "memory");
        };
        if (LOCAL && layer == 0) {
            const int kg = g, j = hp;
            const unsigned vo = ((kg * 64 + (lane >> 3)) * 3072 + j * 64 + (lane & 7) * 8) * 2 + 2048;
            const char* dst = (const char*)csum + (j * 16 + kg) * 768 + (lane & 7) * 32 + (lane >> 4) * 16;
            u32x4 sv[2][3][8];
            auto ld = [&](int w, int i) {
                asm volatile("s_nop 4\n\tglobal_load_dwordx4 %0, %3, %4 offset:-2048\n\tglobal_load_dwordx4 %1, %3, %4\n\t"
                             "global_load_dwordx4 %2, %3, %4 offset:2048" : "=&v"(sv[w][0][i]), "=&v"(sv[w][1][i]), "=&v"(sv[w][2][i])
                             : "v"(vo), "s"((const char*)(w ? W2 : W) + i * (8 * 6144)) : "memory");
            };
            const short one = 0x3f80;
            bf16x8 sel;
#pragma unroll
            for (int e = 0; e < 8; ++e) sel[e] = (lane & 7) == e ? one : (short)0;
            f32x4 c;
            auto half = [&](int w, int k) {
                if (!(k & 1)) c = (f32x4){0.f, 0.f, 0.f, 0.f};
#pragma unroll
                for (int i = 0; i < 4; ++i) c = MFMA16(sel, __builtin_bit_cast(bf16x8, sv[w][k >> 1][(k & 1) * 4 + i]), c);
                if (k & 1) {
#pragma unroll
                    for (int e = 0; e < 4; ++e) c[e] = dppadd<0x128>(c[e]);
                    if (lane < 32 && !(lane & 8)) st16wt<0>(dst + w * (256 * 768) + (k >> 1) * 256, __builtin_bit_cast(u32x4, c));
                }
                SB();
            };
            BAR(); BAR();
#pragma unroll
            for (int i = 0; i < 8; ++i) ld(0, i);
            warm();
            WAITB(26);
            BAR(); BAR(); BAR(); BAR();
            WAITV(0);
#pragma unroll
            for (int i = 0; i < 24; ++i) asm volatile("" : "+v"(sv[0][i >> 3][i & 7]));
#pragma unroll
            for (int k = 0; k < 6; ++k) { half(0, k); BAR(); }
            tail_ld<0, 5>(h, tw, Wo); BAR();
            tail_ld<5, 10>(h, tw, Wo); BAR();
            tail_ld<10, 14>(h, tw, Wo); BAR();
            tail_ld<14, 18>(h, tw, Wo); BAR();
            BAR(); BAR();
#pragma unroll
            for (int i = 0; i < 8; ++i) { ld(1, i); if (i == 3) BAR(); }
            BAR(); BAR();
            WAITV(0);
            tail_pin(tw);
#pragma unroll
            for (int i = 0; i < 24; ++i) asm volatile("" : "+v"(sv[1][i >> 3][i & 7]));
#pragma unroll
            for (int k = 0; k < 6; ++k) half(1, k);
            return;
        } else {
            auto fin = [&](float s, float q, int o) {
                const float mean = s * (1.0f / DM);
                *(LAS float*)(smem + SNM + o) = -bfr(mean);
                *(LAS float*)(smem + SRS + o) = rsd(fmx(q * (1.0f / DM) - mean * mean, 0.f));
            };
            f32x4 sp[3][8], a = {0.f, 0.f, 0.f, 0.f};
            auto add8 = [&](f32x4 (&d)[8]) {
#pragma unroll
                for (int i = 0; i < 8; ++i) {
                    asm volatile("" : "+v"(d[i]));
#pragma unroll
                    for (int e = 0; e < 4; ++e) asm volatile("v_add_f32 %0, %0, %1" : "+v"(a[e]) : "v"(d[i][e]));
                }
            };
            const u64* fc = fl + fi(FL_C + hp * 16 + (lane & 15));
            const char* sb = stats;
            const char* cb = (const char*)csum + (LOCAL ? 0 : 256 * 768);
            const unsigned so = (layer - 1) * (unsigned)(MB / 2) + g * 32768;
            const unsigned vo = so + (lane >> 5) * 16384 + (lane & 31) * 16, vc = hp * (16 * 768) + (lane < 48 ? lane * 16 : 0);
            WAITB(0);
            wq_issue<4>(h, W, 2, 4);
            BAR(); WAITB(0);
            u64 f0, f1;
            asm volatile("global_load_dwordx2 %0, %2, off sc1\n\tglobal_load_dwordx2 %1, %3, off sc1" : "=&v"(f0), "=&v"(f1) : "v"(fx), "v"(fx + fi(512)) : "memory");
            BAR();
            WAITV(0);
            asm volatile("" : "+v"(f0), "+v"(f1));
            if (!__all(f0 == tag && f1 == tag)) {
                poll(fx, fon, tag, 0xffffu, fold);
                poll(fx + fi(512), fon, tag, 0xffffu, fold);
            }
            ld8<512>(sp[0], sb, vo);
            BAR();
            ld8<512>(sp[1], sb + 4096, vo);
            BAR();
            ld8<512>(sp[2], sb + 8192, vo);
            asm volatile("global_load_dwordx2 %0, %1, off sc1" : "=&v"(f0) : "v"(fc) : "memory");
            WAITV(17);
            add8(sp[0]);
            BAR();
            ld8<512>(sp[0], sb + 12288, vo);
            WAITV(17);
            add8(sp[1]);
            BAR();
            WAITV(8);
            asm volatile("" : "+v"(f0));
            if (!__all(f0 == tag)) poll(fc, lane < 16, tag, 0xffffu, [](u64 m) { return (unsigned)m; });
            ld8<768>(sp[1], cb, vc);
            add8(sp[2]);
            BAR();
            ld8<768>(sp[2], cb + 6144, vc);
            WAITV(16);
            add8(sp[0]);
            BAR();
            if (LOCAL) ld8<512>(sp[0], sb, so - (halo ? 16384 : 0) + (lane >> 4) * 4096 + 384 + (lane & 7) * 16);
            fin(a[0], a[1], 64 + lane * 8);
            fin(a[2], a[3], 68 + lane * 8);
            SB();
            BAR();
            if (LOCAL) tail_ld<0, 5>(h, tw, Wo);
            WAITV(LOCAL ? 13 : 0);
            a = (f32x4){0.f, 0.f, 0.f, 0.f};
            add8(sp[1]);
            BAR();
            if (LOCAL) tail_ld<5, 10>(h, tw, Wo);
            add8(sp[2]);
            if (lane < 48) *(LAS f32x4*)(smem + SCOL + lane * 16) = a;
            SB();
            BAR();
            if (LOCAL) {
                tail_ld<10, 14>(h, tw, Wo);
                WAITV(14);
                a = (f32x4){0.f, 0.f, 0.f, 0.f};
                add8(sp[0]);
            }
            BAR();
            if (LOCAL) {
                tail_ld<14, 18>(h, tw, Wo);
#pragma unroll
                for (int e = 0; e < 4; ++e) {
                    const f32x2 x = sw16f(a[e], a[e]);
                    const float v = x[0] + x[1];
                    const f32x2 y = sw32f(v, v);
                    a[e] = y[0] + y[1];
                }
                fin(a[0], a[1], (lane & 7) * 8);
                fin(a[2], a[3], (lane & 7) * 8 + 4);
            }
            BARL();
            BAR(); BAR(); BAR();
            if (LOCAL) { WAITV(0); tail_pin(tw); }
        }
    }
    if (LOCAL) { BAR(); BAR(); }
}

DEV void attn_helper(const HW& h, Tail& tw, const u16* kv, const u16* Wo, const u64* fkv, u64 tag, int g, int hp, int lane, int lw STPARG) {
    if (HPR == 1 || HPR == 2 || HPR == 5) __builtin_amdgcn_s_setprio(2);
    OPAQUE(lane, lw, g, hp);
    const int rh = lw & 1, hd = lw >> 1;
    const char* src = (const char*)kv + (unsigned)((hp * 2 + hd) * SEQ * 64 + rh * 4096);
    const unsigned ld = h.lds + hd * 16384 + rh * 8192;
    const u64* f = fkv + fi(lw * 16 + (lane & 15));
    unsigned rdy = 0u;
    int reg = AR1;
    auto issue = [&](int c) {
        const int blk = g ^ c;
        if (!((rdy >> blk) & 1)) rdy = poll(f, lane < 16, tag, 1u << blk, [](u64 m) { return (unsigned)m; });
        glds4(src + blk * 8192, ld + reg, h.vo16, h.vo16, h.vo16, h.vo16);
        glds4(src + blk * 8192 + NH * SEQ * 64, ld + reg + 4096, h.vo16, h.vo16, h.vo16, h.vo16);
        reg = reg == AR1 ? AR2 : reg == AR2 ? 0 : AR1;
    };
    issue(1);
    if (HPR == 1) __builtin_amdgcn_s_setprio(0);
    STAMP(7);
#pragma unroll 1
    for (int c = 0; c < 11; ++c) {
        WAITB(0);
        issue(c + 2);
        if (lw == 2 && c >= 4) wo_issue(h, Wo, c - 4, c - 3);
    }
    WAITB(0); issue(13); tail_ld<0, 6>(h, tw, Wo);
    WAITB(6); issue(14); tail_ld<6, 12>(h, tw, Wo);
    WAITB(6); issue(15); tail_ld<12, 18>(h, tw, Wo);
    WAITB(6);
    WAITB(0);
    tail_pin(tw);
    if (HPR == 2 || (HPR == 5 && lw == 3)) __builtin_amdgcn_s_setprio(0);
    BAR();
}

DEV void wo_helper(lchar* smem, const HW& h, Tail& tw, const u16* O, const u16* Wn, u64* fc, const u64* fo, u64 tag, int g, int lane, int lw STPARG) {
    int dummy = 0;
    OPAQUE(lane, lw, g, dummy);
    const int p = lw & 1, rh = lw >> 1;
    const char* xsrc = (const char*)O + ((size_t)p * SEQ + g * 128 + rh * 64) * 64;
    const unsigned ldx = h.lds + p * 8192 + rh * 4096;
    const u64* f = fo + fi((p * 32 + g * 2 + rh) * 16 + (lane & 15));
    unsigned so = 0, rdy;
    auto issue = [&](int t) {
        if (!((rdy >> t) & 1)) rdy = poll(f, lane < 16, tag, 1u << t, [](u64 m) { return (unsigned)m; });
        glds4(xsrc, ldx + so, h.vo16, h.vo16, h.vo16, h.vo16);
        xsrc += 2 * SEQ * 64;
        so = so == 65536 ? 0 : so + 16384;
    };
    STAMP(0);
    {
        u64 f0;
        asm volatile("s_mov_b64 exec, 0xffff\n\t" FLD("%0", "%1", "%2") "\n\ts_mov_b64 exec, -1" : "=&v"(f0) : "v"(f), "v"(0ull) : "memory");
        lchar* d = smem + WO0 + 16384 + lw * 1024 + lane * 16;
#pragma unroll
        for (int k = 0; k < 18; ++k) *(LAS u32x4*)(d + (wo_slot(NRW + (k >> 1)) - 2) * 8192 + (k & 1) * 4096) = tw[k];
        WAITV(0);
        asm volatile("" : "+v"(f0));
        rdy = (unsigned)__ballot(f0 == tag);
    }
    issue(0);
    STAMP(1);
    issue(1); issue(2); issue(3); issue(4);
    LGKM0();
    WAITB(16);
#pragma unroll 1
    for (int t = 5; t < 16; ++t) { WAITB(12); issue(t); }
    if (Wn && lw >= 2) {
        wq_issue<4>(h, Wn, 0, lw * 4 - 8);
        WAITB(24); WAITB(20);
        wq_issue<4>(h, Wn, 1, lw * 4 - 8);
        WAITB(28); WAITB(24);
        if (fc && lane == 0) stflag(fc, tag);
    } else {
        WAITB(12); WAITB(8); WAITB(4); WAITB(0);
    }
    if (Wn) BAR();
}

__global__ __launch_bounds__(512) void mega(const u16* __restrict__ xin, const u16* __restrict__ wqa, const u16* __restrict__ woa,
                                            const u16* __restrict__ wqb, const u16* __restrict__ wob, u16* __restrict__ out,
                                            char* __restrict__ ws) {
    __shared__ __attribute__((aligned(16))) char smem_[LDS_BYTES];
    lchar* smem = (lchar*)smem_;
    const int bid = blockIdx.x, xcd = bid & 7, idx = bid >> 3;
    const int lane = threadIdx.x & 63, wid = __builtin_amdgcn_readfirstlane(threadIdx.x >> 6);
    const int g = (xcd >> 2) * 8 + (idx >> 2), hp = (xcd & 3) * 4 + (idx & 3);
    const int g2 = (xcd >> 1) * 4 + (idx >> 3), cb = (xcd & 1) * 8 + (idx & 7);
    u64* fl = (u64*)(ws + WS_FL);
    u16* ob = (u16*)(ws + WS_O);
    auto mktag = [](u64 e) {
        e += 1;
        return ((u64)(unsigned)__builtin_amdgcn_readfirstlane((int)(e >> 32)) << 32) | (unsigned)__builtin_amdgcn_readfirstlane((int)e);
    };
    if (wid < 4) {
        __builtin_amdgcn_s_setprio(1);
        u64 tag;
        u32x4 res[4];
        {
            u32x4 x[4];
            u64 e;
            const int row = g * 128 + hp * 8 + wid * 2 + ((lane >> 2) & 1);
            const u16* p = xin + (size_t)row * DM + (lane >> 3) * 32 + (lane & 3) * 8;
            const char* d = ws + WS_XN + ((size_t)(lane >> 3) * SEQ + row) * 64 + (((lane & 3) ^ fz(row)) << 4);
            const unsigned ro = (g2 * 128 + (wid >> 1) * 64 + (lane & 15)) * 2048 + (cb * 2 + (wid & 1)) * 64 + ((lane & 16) << 1) + ((lane & 32) >> 1);
            if (wid == 0) *(LAS unsigned*)(smem + DUMP + 1024) = 0u;
            asm volatile("global_load_dwordx4 %0, %5, off\n\tglobal_load_dwordx4 %1, %5, off offset:512\n\tglobal_load_dwordx4 %2, %5, off offset:1024\n\t"
                         "global_load_dwordx4 %3, %5, off offset:1536\n\tglobal_load_dwordx2 %4, %6, off sc1\n\t"
                         "s_waitcnt lgkmcnt(0)\n\ts_barrier\n\ts_waitcnt vmcnt(0)"
                         : "=&v"(x[0]), "=&v"(x[1]), "=&v"(x[2]), "=&v"(x[3]), "=&v"(e) : "v"(p), "v"(fl + fi(FL_EP)) : "memory");
            STAMP(14);
            ln0_rows(x);
#pragma unroll
            for (int i = 0; i < 4; ++i) st16wt<0>(d + i * MB, x[i]);
            asm volatile("global_load_dwordx4 %0, %4, %8\n\tglobal_load_dwordx4 %1, %5, %8\n\tglobal_load_dwordx4 %2, %6, %8\n\t"
                         "global_load_dwordx4 %3, %7, %8" : "=&v"(res[0]), "=&v"(res[1]), "=&v"(res[2]), "=&v"(res[3])
                         : "v"(ro), "v"(ro + 32768), "v"(ro + 65536), "v"(ro + 98304), "s"(xin) : "memory");
            BAR();
            tag = mktag(e);
            STAMP(15);
            WAITV(4);
            if (lane == 0 && __hip_atomic_fetch_add((LAS unsigned*)(smem + DUMP + 1024), 1u, __ATOMIC_RELAXED, __HIP_MEMORY_SCOPE_WORKGROUP) == 3)
                stflag(fl + fi(FL_L + g * 16 + hp), tag);
            STAMP(9);
            WAITV(0);
            asm volatile("" : "+v"(res[0]), "+v"(res[1]), "+v"(res[2]), "+v"(res[3]));
            BAR(); BAR();
        }
#pragma unroll 1
        for (int layer = 0; layer < 4; ++layer) {
            if (layer & 1) qb_compute(smem, (u16*)(ws + WS_KV + (layer >> 1) * 8 * MB), ob, fl + fi(FL_KV + (layer >> 1) * 1024 + hp * 64),
                                      tag, g, hp, lane, wid STPPASS(layer * 32));
            else qa_compute(smem, layer > 0, ob, g, hp, lane, wid STPPASS(layer * 32));
            const bool last = layer == 3;
            wo_compute(smem, fl + fi(FL_O + layer * 1024 + ((wid & 1) * 32 + g * 2 + (wid >> 1)) * 16 + hp), res,
                       last ? out : (u16*)(ws + (layer == 2 ? WS_XN : WS_X)), last ? nullptr : ws + WS_ST + layer * (MB / 2),
                       fl + fi(FL_X + layer * 1024), tag, g2, cb, lane, wid STPPASS(layer * 32 + 16));
        }
        if (bid == 0 && threadIdx.x == 0) stflag(fl + fi(FL_EP), tag);
    } else {
        const int lw = wid - 4;
        if (HPR == 3 || (HPR == 5 && lw < 3)) __builtin_amdgcn_s_setprio(2);
        HW h;
        h.lds = (unsigned)(uintptr_t)smem;
        h.vo16 = lane * 16;
#pragma unroll
        for (int i = 0; i < 6; ++i) {
            const int k = (lane >> 3) + (((i / 3) ^ (lw == 1)) << 3), pc = lane & 7;
            h.wq[i / 3][i % 3] = (lane >> 3) * 6144 + (i % 3) * 1024 + hp * 128 + ((((pc >> 1) ^ fswz(k)) << 1) + (pc & 1)) * 16;
        }
#pragma unroll
        for (int i = 0; i < 4; ++i) h.wo[i] = wo_voff(lane, cb, i);
        h.tq = (idx + (lane >> 3) * 32) * 6144 + (xcd & 3) * 512 + (lane & 7) * 64;
        h.wt = wo_voff(lane, cb, lw) + lw * 1024 + 2 * NRW * (32 * 2048);
        if (lw == 3 && lane < 3) asm volatile("global_atomic_add_x2 %0, %1, %2 sc1" ::"v"(fi(bid * 48 + lane * 16) * 8), "v"(0ull), "s"(fl) : "memory");
        BAR();
        if (lw) touch_wq(h, wqa, lw - 1);
        const u64 tag = mktag(ldep(fl + fi(FL_EP)));
        WAITV(0);
        BAR();
        if (lw >= 2) { wq_issue<4>(h, wqa, 0, lw * 4 - 8); wq_issue<4>(h, wqa, 1, lw * 4 - 8); wq_issue<4>(h, wqa, 2, lw * 4 - 8); }
#pragma unroll 1
        for (int layer = 0; layer < 4; ++layer) {
            const u16* X = (const u16*)(ws + ((layer == 0 || layer == 3) ? WS_XN : WS_X));
            const char* st = ws + WS_ST;
            const u16* wo = (layer & 1) ? wob : woa;
            Tail tw;
            if (layer & 1) {
                qkv_helper<false>(smem, h, tw, layer, X, wqb, wqb, wo, st, (float*)(ws + WS_CS), fl, tag, g, hp, lane, lw STPPASS(layer * 32));
                attn_helper(h, tw, (const u16*)(ws + WS_KV + (layer >> 1) * 8 * MB), wo, fl + fi(FL_KV + (layer >> 1) * 1024 + hp * 64), tag, g, hp, lane,
                            lw STPPASS(layer * 32));
            } else {
                qkv_helper<true>(smem, h, tw, layer, X, wqa, wqb, wo, st, (float*)(ws + WS_CS), fl, tag, g, hp, lane, lw STPPASS(layer * 32));
            }
            wo_helper(smem, h, tw, ob, layer == 3 ? nullptr : (layer & 1) ? wqa : wqb, (layer == 0 && lw == 3) ? fl + fi(FL_C + hp * 16 + g) : nullptr,
                      fl + fi(FL_O + layer * 1024), tag, g2, lane, lw STPPASS(layer * 32 + 16));
        }
    }
}

extern "C" void kernel_launch(void* const* d_in, const int* in_sizes, int n_in, void* d_out, int out_size, void* d_ws,
                              size_t ws_size, hipStream_t stream) {
    mega<<<256, 512, 0, stream>>>((const u16*)d_in[0], (const u16*)d_in[1], (const u16*)d_in[2], (const u16*)d_in[4], (const u16*)d_in[5],
                                  (u16*)d_out, (char*)d_ws);
}
```

```cpp
#include <hip/hip_runtime.h>
#include <stdint.h>

typedef unsigned short u16;
typedef unsigned long long u64;
typedef __attribute__((ext_vector_type(8))) short bf16x8;
typedef __attribute__((ext_vector_type(4))) short s16x4;
typedef __attribute__((ext_vector_type(4))) float f32x4;
typedef __attribute__((ext_vector_type(16))) float f32x16;
typedef __attribute__((ext_vector_type(4))) unsigned u32x4;
typedef __attribute__((ext_vector_type(2))) unsigned u32x2;
typedef __attribute__((ext_vector_type(2))) __bf16 bf16x2_t;
typedef __attribute__((ext_vector_type(2))) float f32x2;

#define LAS __attribute__((address_space(3)))
#define GAS __attribute__((address_space(1)))
#define DEV __device__ __forceinline__
typedef LAS char lchar;

constexpr int SEQ = 2048, DM = 1024, NH = 32;
constexpr float QSCALE = 0.17677669529663687f, LOG2E = 1.4426950408889634f;
#define POLLMAX (1 << 16)
#define T0 3
#define NE 4
#define NTX " nt"
#define RD 1
#define WC 0
#define OA 0
#define CJ 2
#define CX 1
#define EMU 1

DEV unsigned cvtpk(float lo, float hi) {
    f32x2 v = {lo, hi};
    bf16x2_t b = __builtin_convertvector(v, bf16x2_t);
    return __builtin_bit_cast(unsigned, b);
}
DEV float lo16(unsigned v) { return __uint_as_float(v << 16); }
DEV float hi16(unsigned v) { return __uint_as_float(v & 0xffff0000u); }
DEV float bfr(float f) { return lo16(cvtpk(f, f)); }
DEV u32x2 pk4(f32x4 a) { return (u32x2){cvtpk(a[0], a[1]), cvtpk(a[2], a[3])}; }
DEV bf16x8 cat(u32x2 a, u32x2 b) { return __builtin_bit_cast(bf16x8, (u32x4){a[0], a[1], b[0], b[1]}); }
DEV u32x2 sw16(unsigned a, unsigned b) { auto r = __builtin_amdgcn_permlane16_swap(a, b, false, false); return (u32x2){r[0], r[1]}; }
DEV u32x2 sw32(unsigned a, unsigned b) { auto r = __builtin_amdgcn_permlane32_swap(a, b, false, false); return (u32x2){r[0], r[1]}; }
DEV f32x2 sw16f(float a, float b) { return __builtin_bit_cast(f32x2, sw16(__float_as_uint(a), __float_as_uint(b))); }
DEV f32x2 sw32f(float a, float b) { return __builtin_bit_cast(f32x2, sw32(__float_as_uint(a), __float_as_uint(b))); }
DEV u32x4 unit16(u32x2 a, u32x2 b) {
    const u32x2 x = sw16(a[0], b[0]), y = sw16(a[1], b[1]);
    return (u32x4){x[0], y[0], x[1], y[1]};
}
template <int OFF> DEV void st16wt(const void* p, u32x4 v) {
    asm volatile("global_store_dwordx4 %0, %1, off offset:%2 sc1\n\ts_nop 1" ::"v"(p), "v"(v), "n"(OFF) : "memory");
}
DEV void st8wt(const void* p, u32x2 v) { asm volatile("global_store_dwordx2 %0, %1, off sc1\n\ts_nop 1" ::"v"(p), "v"(v) : "memory"); }
template <int CTRL> DEV float dppadd(float v) {
    return v + __builtin_bit_cast(float, __builtin_amdgcn_update_dpp(0, __builtin_bit_cast(int, v), CTRL, 0xf, 0xf, true));
}
DEV int fswz(int k) { return ((k >> 1) & 1) | (((k >> 3) & 1) << 1); }
DEV int fz(int tok) { return (-(tok >> 2)) & 3; }
DEV s16x4 ldtr4(const lchar* p) { return __builtin_bit_cast(s16x4, __builtin_amdgcn_ds_read_tr16_b64_v4i16((LAS s16x4*)p)); }
DEV bf16x8 cat8(s16x4 a, s16x4 b) { return (bf16x8){a[0], a[1], a[2], a[3], b[0], b[1], b[2], b[3]}; }
DEV bf16x8 ldk(const lchar* p) { return *(const LAS bf16x8*)p; }

#define STAMP(i)
#define STAMPW(i)
#define STPARG
#define STPPASS(o)
#define STAMPB(i)
#define BAR() asm volatile("s_barrier" ::: "memory")
#define BARL() asm volatile("s_waitcnt lgkmcnt(0)\n\ts_barrier" ::: "memory")
#define WAITB(n) asm volatile("s_waitcnt vmcnt(%0)\n\ts_barrier" ::"n"(n) : "memory")
#define WAITV(n) asm volatile("s_waitcnt vmcnt(%0)" ::"n"(n) : "memory")
#define SB() __builtin_amdgcn_sched_barrier(0)
#define LGKM0() __builtin_amdgcn_s_waitcnt(0xc07f)
#define MFMA32(a, b, c) __builtin_amdgcn_mfma_f32_32x32x16_bf16(a, b, c, 0, 0, 0)
#define MFMA16(a, b, c) __builtin_amdgcn_mfma_f32_16x16x32_bf16(a, b, c, 0, 0, 0)

DEV void mfma16t(f32x4& c, const bf16x8& a, const bf16x8& b) { asm volatile("v_mfma_f32_16x16x32_bf16 %0, %1, %2, %0" : "+v"(c) : "v"(a), "v"(b)); }
#define MFMA_DONE() do { SB(); asm volatile("s_nop 7\n\ts_nop 3"); SB(); } while (0)
DEV float fmx(float a, float b) { return __builtin_elementwise_maximum(a, b); }
DEV float rsd(float var) { return __builtin_amdgcn_rcpf(bfr(__builtin_amdgcn_sqrtf(bfr(bfr(var) + 1e-5f)))); }

constexpr int XS = 18432;
constexpr int WQ0 = 81920, WQS = 24576;
constexpr int WO0 = 65536;
constexpr int EXO = 147456;
constexpr int AR1 = 122880, AR2 = 32768;
constexpr int SNM = 155648, SRS = SNM + 576, SCOL = SNM + 1280, DUMP = SNM + 3072;
constexpr int LDS_BYTES = 163840;
constexpr size_t MB = 1u << 20;
constexpr size_t WS_ST = 0, WS_CS = 3 * MB / 2, WS_FL = 15 * MB / 8, WS_XN = 2 * MB, WS_O = 6 * MB, WS_X = 10 * MB, WS_KV = 14 * MB, WS_WC = 30 * MB;
constexpr int FL_O = 0;
constexpr int FL_X = 4096;
constexpr int FL_KV = 8192;
constexpr int FL_L = 10240;
constexpr int FL_W = 10752;
constexpr int FL_C = 11264;
constexpr int FL_S = 12288;
constexpr int FL_EP = 16368;
#define FLS 16
#define FS 1
DEV int fi(int i) { return (i >> 4) * FLS + (i & 15) * FS; }

#define GL_HEAD "s_mov_b32 m0, %1\n\ts_nop 4\n\tglobal_load_lds_dwordx4 %2, %0 sc1"
DEV void glds1(const void* sb, unsigned lds, unsigned v0) { asm volatile(GL_HEAD ::"s"(sb), "s"(lds), "v"(v0) : "memory"); }
DEV void glds3(const void* sb, unsigned lds, unsigned v0, unsigned v1, unsigned v2) {
    asm volatile(GL_HEAD "\n\tglobal_load_lds_dwordx4 %3, %0 offset:1024 sc1\n\tglobal_load_lds_dwordx4 %4, %0 offset:2048 sc1"
                 ::"s"(sb), "s"(lds), "v"(v0), "v"(v1), "v"(v2) : "memory");
}
DEV void glds4(const void* sb, unsigned lds, unsigned v0, unsigned v1, unsigned v2, unsigned v3) {
    asm volatile(GL_HEAD "\n\tglobal_load_lds_dwordx4 %3, %0 offset:1024 sc1\n\tglobal_load_lds_dwordx4 %4, %0 offset:2048 sc1\n\t"
                 "global_load_lds_dwordx4 %5, %0 offset:3072 sc1" ::"s"(sb), "s"(lds), "v"(v0), "v"(v1), "v"(v2), "v"(v3) : "memory");
}
DEV void touch(const void* sb, unsigned lds, unsigned v0) {
    asm volatile("s_mov_b32 m0, %1\n\ts_nop 4\n\tglobal_load_lds_dword %2, %0 sc1" ::"s"(sb), "s"(lds), "v"(v0) : "memory");
}
DEV u64 ldep(const u64* p) { return __hip_atomic_load((const GAS u64*)p, __ATOMIC_RELAXED, __HIP_MEMORY_SCOPE_AGENT); }
#define FLD(d, a, z) "global_atomic_add_x2 " d ", " a ", " z ", off sc0 sc1"
DEV u64 ldflag(const u64* p) {
    u64 v;
    asm volatile(FLD("%0", "%1", "%2") "\n\ts_waitcnt vmcnt(0)" : "=&v"(v) : "v"(p), "v"(0ull) : "memory");
    return v;
}
DEV void stflag(u64* p, u64 v) { asm volatile("global_atomic_swap_x2 %0, %1, off sc1\n\ts_nop 1" ::"v"(p), "v"(v) : "memory"); }
template <class F> DEV unsigned poll(const u64* p, bool on, u64 tag, unsigned need, F fold) {
    unsigned m = ~0u;
    for (int it = 0; it < POLLMAX; ++it) {
        u64 v = tag;
        if (on) v = ldflag(p);
        const unsigned r = fold(__ballot(v == tag));
        if ((r & need) == need) { m = r; break; }
        __builtin_amdgcn_s_sleep(1);
    }
    asm volatile("" ::: "memory");
    return m;
}
template <int N = 0> DEV void publish(u64* f, int lane, u64 tag) {
    asm volatile("s_mov_b64 exec, 1\n\ts_waitcnt vmcnt(%2)\n\tglobal_atomic_swap_x2 %0, %1, off sc1\n\ts_mov_b64 exec, -1\n\ts_nop 0" ::"v"(f), "v"(tag), "n"(N) : "memory");
}
#define OPAQUE(l, a, b, c) do { asm volatile("" : "+v"(l)); asm volatile("" : "+s"(a), "+s"(b), "+s"(c)); } while (0)

template <bool LOCAL, class F, class G>
DEV void qkv_main(lchar* smem, f32x4 (&acc)[6][4], f32x4 (&acch)[2], int lane, int wm, int wn, G mid, F tailread STPARG) {
    constexpr int XC_B = LOCAL ? 9216 : 8192, HO = LOCAL ? 1024 : 0, NR = LOCAL ? 17 : 16;
    const int l15 = lane & 15, lg = lane >> 4, hs = LOCAL ? wm : 0;
    const lchar* xhb = smem + l15 * 64 + ((lg ^ fz(l15)) << 4);
    const lchar* xb = xhb + HO + wm * 4096;
    const int k0 = 8 * lg + (l15 >> 2);
    const lchar* wb0 = smem + WQ0 + lg * 3072 + (l15 >> 2) * 128 + (((wn * 2 + hs) ^ fswz(k0)) << 5) + (l15 & 3) * 8;
    const lchar* wb1 = smem + WQ0 + lg * 3072 + (l15 >> 2) * 128 + (((wn * 2 + (hs ^ 1)) ^ fswz(k0)) << 5) + (l15 & 3) * 8;
#pragma unroll
    for (int a = 0; a < 6; ++a)
#pragma unroll
        for (int b = 0; b < 4; ++b) { acc[a][b] = (f32x4){0.f, 0.f, 0.f, 0.f}; asm volatile("" : "+v"(acc[a][b])); }
    acch[0] = acch[1] = (f32x4){0.f, 0.f, 0.f, 0.f};
    if (LOCAL) asm volatile("" : "+v"(acch[0]), "+v"(acch[1]), "+v"(xhb));
    asm volatile("" : "+v"(xb), "+v"(wb0), "+v"(wb1));
    bf16x8 xf[2][5];
    s16x4 wh[2][6][2];
    auto rd = [&](int b, int j, int xo, int wo) {
        if (j < 4) xf[b][j] = ldk(xb + xo + j * 1024);
        else if (j < 16) wh[b][(j - 4) >> 1][j & 1] = ldtr4(((j & 2) ? wb1 : wb0) + wo + ((j - 4) >> 2) * 1024 + (j & 1) * 512);
        else xf[b][4] = ldk(xhb + xo);
    };
    auto kstep = [&](int b, int xo, int wo, bool last) {
        int j = 0;
#pragma unroll
        for (int ni = 0; ni < 6; ++ni) {
            const bf16x8 w = cat8(wh[b][ni][0], wh[b][ni][1]);
#pragma unroll
            for (int ti = 0; ti < 5; ++ti) {
                if (ti == 4) {
                    if (!LOCAL || (ni != 2 && ni != 4)) continue;
                    if (ni == 2) mfma16t(acch[0], w, xf[b][4]);
                    else mfma16t(acch[1], xf[b][4], w);
                } else {
                    if (ni >= 4) mfma16t(acc[ni][ti], xf[b][ti], w);
                    else mfma16t(acc[ni][ti], w, xf[b][ti]);
                }
                SB();
                if (last) tailread(j);
                else if (j < NR) rd(b ^ 1, j, xo, wo);
                SB();
                ++j;
            }
        }
        if (!last) { LGKM0(); SB(); }
    };
    BAR(); SB();
    STAMP(4);
#pragma unroll
    for (int j = 0; j < NR; ++j) rd(0, j, 0, 2 * WQS);
    LGKM0(); SB();
#pragma unroll 1
    for (int it = 0; it < 5; ++it) {
        mid(it);
#pragma unroll
        for (int s = 0; s < 3; ++s) {
            kstep(0, s * XS + XC_B, ((s + 2) % 3) * WQS + WQS / 2, false);
            BAR(); SB();
            STAMPB(it * 3 + s);
            kstep(1, ((s + 1) % 3) * XS, s * WQS, false);
        }
    }
    kstep(0, XC_B, 2 * WQS + WQS / 2, false);
    BAR(); SB();
    STAMPB(15);
    kstep(1, 0, 0, true);
    MFMA_DONE();
}

DEV void qa_compute(lchar* smem, bool fold, const u16* xin, u32x4 (&rr)[4], u16* O, int g, int hp, int lane, int wid STPARG) {
    OPAQUE(lane, wid, g, hp);
    const int wm = wid >> 1, wn = wid & 1, l15 = lane & 15, lg = lane >> 4;
    f32x4 acc[6][4], acch[2];
    float nm[5], rs[5], csv[2];
    f32x4 nmv[5], rsv[5], cs[4];
    const lchar* sa = smem + SNM + l15 * 4;
    const lchar* sb = smem + SNM + lg * 16;
    const lchar* sc = smem + SCOL + wn * 128;
    const int ro = 64 + wm * 256;
    f32x4 cprev, ccur;
#pragma unroll
    for (int j = 0; j < 4; ++j) {
        const int d = l15 - 4 * lg - j;
        ccur[j] = (d >= 0 && !(d & 1)) ? 0.f : -3.0e38f;
        cprev[j] = (d <= -2 && !(d & 1)) ? 0.f : -3.0e38f;
    }
    const char* optr = (const char*)O + ((size_t)(hp * 2 + wn) * SEQ + g * 128 + wm * 64 + l15) * 64 +
                       (((((lg & 1) << 1) | (lg >> 1)) ^ (wm << 1) ^ fz(l15)) << 4);
    unsigned xo = rr[0][0];
    asm volatile("" : "+v"(cprev), "+v"(ccur), "+v"(optr), "+v"(xo));
    qkv_main<true>(smem, acc, acch, lane, wm, wn, [&](int it) {
        if (RD) asm volatile("s_cmp_lg_u32 %6, %7\n\ts_cbranch_scc1 1f\n\tglobal_load_dwordx4 %0, %4, %5\n\tv_add_u32 %4, 0x8000, %4\n\t"
                     "global_load_dwordx4 %1, %4, %5\n\tv_add_u32 %4, 0x8000, %4\n\tglobal_load_dwordx4 %2, %4, %5\n\tv_add_u32 %4, 0x8000, %4\n\t"
                     "global_load_dwordx4 %3, %4, %5\n1:" : "+v"(rr[0]), "+v"(rr[1]), "+v"(rr[2]), "+v"(rr[3]), "+v"(xo)
                     : "s"(xin), "s"(it), "s"(fold ? -1 : 2) : "memory", "scc");
    }, [&](int j) {
        if (j < 4) cs[j] = *(const LAS f32x4*)(sc + (j >> 1) * 256 + (((j & 1) ^ wm) << 6) + lg * 16);
        else if (j < 9) { const int o = j < 8 ? ro + (j - 4) * 64 : 0; nm[j - 4] = *(const LAS float*)(sa + o); rs[j - 4] = *(const LAS float*)(sa + o + 576); }
        else if (j < 11) csv[j - 9] = *(const LAS float*)(sc + 512 + (((j - 9) ^ wm) << 6) + l15 * 4);
        else if (j < 21) {
            const int t = (j - 11) >> 1, o = t < 4 ? ro + t * 64 : 0;
            if ((j - 11) & 1) rsv[t] = *(const LAS f32x4*)(sb + o + 576);
            else nmv[t] = *(const LAS f32x4*)(sb + o);
        }
    } STPPASS(0));
    STAMP(2);
    if (fold) {
#pragma unroll
        for (int ti = 0; ti < 4; ++ti) {
#pragma unroll
            for (int ni = 0; ni < 4; ++ni) acc[ni][ti] = (acc[ni][ti] + nm[ti] * cs[ni]) * rs[ti];
#pragma unroll
            for (int h = 0; h < 2; ++h) acc[4 + h][ti] = (acc[4 + h][ti] + nmv[ti] * csv[h]) * rsv[ti];
        }
        acch[0] = (acch[0] + nm[4] * cs[2]) * rs[4];
        acch[1] = (acch[1] + nmv[4] * csv[0]) * rsv[4];
    }
    u32x2 kp[5][2], vp[5][2];
    lchar* ex = smem + EXO + wn * 4096 + lane * 16;
    {
        const u32x2 a = pk4(acch[0]), b = pk4(acch[1]);
        *(LAS u32x4*)(ex + wm * 1024) = (u32x4){a[0], a[1], b[0], b[1]};
    }
#pragma unroll
    for (int h = 0; h < 2; ++h) { kp[4][h] = pk4(acc[2 + h][3]); vp[4][h] = pk4(acc[4 + h][3]); }
    if (wm == 0) {
#pragma unroll
        for (int h = 0; h < 2; ++h) *(LAS u32x4*)(ex + 2048 + h * 1024) = (u32x4){kp[4][h][0], kp[4][h][1], vp[4][h][0], vp[4][h][1]};
    }
    bf16x8 qf[4];
#pragma unroll
    for (int ti = 0; ti < 4; ++ti) {
        qf[ti] = cat(pk4(acc[0][ti]), pk4(acc[1][ti]));
        if (ti < 3) {
#pragma unroll
            for (int h = 0; h < 2; ++h) { kp[ti + 1][h] = pk4(acc[2 + h][ti]); vp[ti + 1][h] = pk4(acc[4 + h][ti]); }
        }
    }
    SB();
    BARL();
#pragma unroll
    for (int h = 0; h < 2; ++h) {
        const u32x4 e = *(const LAS u32x4*)(ex + wm * 2048 + ((h ^ wm) << 10));
        kp[0][h] = (u32x2){e[0], e[1]}; vp[0][h] = (u32x2){e[2], e[3]};
    }
    f32x4 cp0 = cprev;
    if (g == 0 && wm == 0) cp0 = (f32x4){-3.0e38f, -3.0e38f, -3.0e38f, -3.0e38f};
    f32x4 sv[4];
    float mx[4];
#pragma unroll
    for (int t = 0; t < 4; ++t) {
        const int ti = 3 - t;
        const f32x4 s1 = MFMA16(cat(kp[ti + 1][0], kp[ti + 1][1]), qf[ti], ccur);
        const f32x4 s0 = MFMA16(cat(kp[ti][0], kp[ti][1]), qf[ti], ti ? cprev : cp0);
        f32x4 v = {fmx(s0[0], s0[1]), fmx(s0[2], s0[3]), fmx(s1[0], s1[1]), fmx(s1[2], s1[3])};
        if (EMU) {
            const u32x2 r1 = pk4(v);
            const u32x2 r2 = pk4((f32x4){lo16(r1[0]), hi16(r1[0]), lo16(r1[1]), hi16(r1[1])} * QSCALE);
            v = (f32x4){lo16(r2[0]), hi16(r2[0]), lo16(r2[1]), hi16(r2[1])};
        }
        sv[ti] = v;
        mx[ti] = fmx(fmx(fmx(v[0], v[1]), v[2]), v[3]);
    }
    {
        const f32x2 a = sw16f(mx[0], mx[1]), b = sw16f(mx[2], mx[3]);
        const f32x2 c = sw32f(fmx(a[0], a[1]), fmx(b[0], b[1]));
        const float r = fmx(c[0], c[1]) * -(EMU ? LOG2E : QSCALE * LOG2E);
        const f32x2 d = sw32f(r, r), e = sw16f(d[0], d[0]), f = sw16f(d[1], d[1]);
        mx[0] = e[0]; mx[1] = e[1]; mx[2] = f[0]; mx[3] = f[1];
    }
    const unsigned sel0 = (lane & 1) ? 0x01000c0cu : 0x0c0c0100u, sel1 = (lane & 1) ? 0x03020c0cu : 0x0c0c0302u;
    const bf16x8 ones = {0x3f80, 0x3f80, 0x3f80, 0x3f80, 0x3f80, 0x3f80, 0x3f80, 0x3f80};
    const f32x4 z = {0.f, 0.f, 0.f, 0.f};
    u32x4 ov[4];
#pragma unroll
    for (int ti = 0; ti < 4; ++ti) {
        const f32x4 x = sv[ti] * (EMU ? LOG2E : QSCALE * LOG2E) + mx[ti];
        const unsigned c0 = cvtpk(__builtin_amdgcn_exp2f(x[0]), __builtin_amdgcn_exp2f(x[1]));
        const unsigned c1 = cvtpk(__builtin_amdgcn_exp2f(x[2]), __builtin_amdgcn_exp2f(x[3]));
        const bf16x8 p = __builtin_bit_cast(bf16x8, (u32x4){__builtin_amdgcn_perm(c0, c0, sel0), __builtin_amdgcn_perm(c0, c0, sel1),
                                                            __builtin_amdgcn_perm(c1, c1, sel0), __builtin_amdgcn_perm(c1, c1, sel1)});
        const f32x4 l = MFMA16(ones, p, z);
        const f32x4 o0 = MFMA16(cat(vp[ti][0], vp[ti + 1][0]), p, z);
        const f32x4 o1 = MFMA16(cat(vp[ti][1], vp[ti + 1][1]), p, z);
        const float inv = __builtin_amdgcn_rcpf(l[0]);
        ov[ti] = unit16(pk4(o0 * inv), pk4(o1 * inv));
    }
    st16wt<0>(optr, ov[0]); st16wt<1024>(optr, ov[1]); st16wt<2048>(optr, ov[2]); st16wt<3072>(optr, ov[3]);
    STAMP(10);
    BAR();
}

#define MX3(a, b, c) fmx(fmx((a), (b)), (c))
DEV float rowmax32(const f32x16& s0) {
    float ma = MX3(s0[0], s0[1], s0[2]), mb = MX3(s0[3], s0[4], s0[5]);
    ma = MX3(ma, s0[6], s0[7]); mb = MX3(mb, s0[8], s0[9]);
    ma = MX3(ma, s0[10], s0[11]); mb = MX3(mb, s0[12], s0[13]);
    ma = fmx(MX3(ma, s0[14], s0[15]), mb);
    const f32x2 b = sw32f(ma, ma);
    return fmx(b[0], b[1]);
}
#define EX(i) do { S[i] = __builtin_amdgcn_exp2f(S[i]); SB(); } while (0)
#define EP(i) do { p1[i] = __builtin_amdgcn_exp2f(S[8 + i]); SB(); } while (0)
typedef __attribute__((ext_vector_type(16))) __bf16 bf16x16_t;
typedef __attribute__((ext_vector_type(8))) __bf16 bf16x8_t;
typedef __attribute__((ext_vector_type(8))) float f32x8;
template <int H> DEV f32x8 half8(const f32x16& s) { return __builtin_shufflevector(s, s, H, H + 1, H + 2, H + 3, H + 4, H + 5, H + 6, H + 7); }
#define SMF32(a, b, c) __builtin_amdgcn_smfmac_f32_32x32x32_bf16(__builtin_bit_cast(bf16x8_t, a), __builtin_bit_cast(bf16x16_t, b), c, hi, 0, 0)
#define SMF16(a, b, c) __builtin_amdgcn_smfmac_f32_16x16x64_bf16(__builtin_bit_cast(bf16x8_t, a), __builtin_bit_cast(bf16x16_t, b), c, hi, 0, 0)
template <class F0, class F1, class F2, class F3>
DEV void block(f32x16& S, f32x16& N, const bf16x8& kf0, const bf16x8& kf1, const bf16x8& q0, const bf16x8& q1, const f32x16& c,
               f32x16& oP, f32x4& lP, const bf16x8& vf1, f32x8& p1, f32x16& oC, f32x4& lC, const bf16x8& vf0,
               const bf16x8& sel, int hi, F0 fb, F1 fc, F2 fd, F3 ff) {
    N = MFMA32(kf0, q0, c); SB();
    EX(0); EX(1); EX(2);
    oP = SMF32(vf1, p1, oP); SB();
    fb(); SB();
    EX(3); EX(4); EX(5);
    lP = SMF16(sel, p1, lP); SB();
    fc(); SB();
    EX(6);
    N = MFMA32(kf1, q1, N); SB();
    fd(); SB();
    EX(7); EP(0); EP(1);
    oC = SMF32(vf0, half8<0>(S), oC); SB();
    EP(2); EP(3); EP(4);
    lC = SMF16(sel, half8<0>(S), lC); SB();
    ff(); SB();
    EP(5); EP(6); EP(7);
}
DEV void attn_slow(const char* kh, const char* vh, const bf16x8& q0, const bf16x8& q1, f32x16& o, float& l, int hi) {
    f32x16 c;
#pragma unroll
    for (int i = 0; i < 16; ++i) { o[i] = 0.f; c[i] = 0.f; }
    float m = 0.f;
    l = 0.f;
#pragma unroll 1
    for (int t = 0; t < SEQ / 32; ++t) {
        bf16x8 k0, k1, v0, v1;
        const char* kp = kh + t * 2048;
        const char* vp = vh + t * 2048;
        asm volatile("global_load_dwordx4 %0, %4, off sc1\n\tglobal_load_dwordx4 %1, %5, off sc1\n\tglobal_load_dwordx4 %2, %6, off sc1\n\t"
                     "global_load_dwordx4 %3, %7, off sc1\n\ts_waitcnt vmcnt(0)" : "=&v"(k0), "=&v"(k1), "=&v"(v0), "=&v"(v1)
                     : "v"(kp), "v"((const char*)((uintptr_t)kp ^ 32)), "v"(vp), "v"((const char*)((uintptr_t)vp ^ 32)) : "memory");
        f32x16 s = MFMA32(k0, q0, c);
        s = MFMA32(k1, q1, s);
        const float mx = rowmax32(s);
        const float dl = t == 0 ? mx : fmx(mx, 0.f);
        const float alpha = t == 0 ? 0.f : __builtin_amdgcn_exp2f(-dl);
        m += dl;
        l *= alpha;
#pragma unroll
        for (int i = 0; i < 16; ++i) {
            o[i] *= alpha; c[i] = -m;
            s[i] = __builtin_amdgcn_exp2f(s[i] - dl);
            l += hi16(__float_as_uint(s[i]));
        }
        o = SMF32(v0, half8<0>(s), o);
        o = SMF32(v1, half8<8>(s), o);
    }
    const f32x2 b = sw32f(l, l);
    l = b[0] + b[1];
}

DEV void qb_compute(lchar* smem, u16* kv, u16* O, u64* fkv, u64 tag, int g, int hp, int lane, int wid STPARG) {
    OPAQUE(lane, wid, g, hp);
    const int wm = wid >> 1, wn = wid & 1, l15 = lane & 15, lg = lane >> 4, h = hp * 2 + wn;
    bf16x8 qa0, qa1, qb0, qb1;
    lchar* gs = smem + wn * 16384;
    {
        f32x4 acc[6][4], acch[2];
        float nm[4], rs[4], csv[2];
        f32x4 cs[4], nmv[4], rsv[4];
        const lchar* sa = smem + SNM + 64 + wm * 256 + l15 * 4;
        const lchar* sb = smem + SNM + 64 + wm * 256 + lg * 16;
        const lchar* sc = smem + SCOL + wn * 128;
        const int un = ((lg & 1) << 1) | (lg >> 1);
        const int ko = l15 * 64 + ((un ^ (l15 >> 2)) << 4), vo = l15 * 64 + ((lg ^ (l15 >> 2)) << 4);
        const char* kptr = (const char*)kv + ((size_t)h * SEQ + g * 128 + wm * 64) * 64 + ko;
        const char* vptr = (const char*)kv + ((size_t)(NH + h) * SEQ + g * 128 + wm * 64) * 64 + vo;
        lchar* kl = gs + wm * 8192 + ko;
        lchar* vl = gs + wm * 8192 + 4096 + vo;
        asm volatile("" : "+v"(kptr), "+v"(vptr));
        qkv_main<false>(smem, acc, acch, lane, wm, wn, [](int) {}, [&](int j) {
            if (j < 4) cs[j] = *(const LAS f32x4*)(sc + (j >> 1) * 256 + (j & 1) * 64 + lg * 16);
            else if (j < 8) { nm[j - 4] = *(const LAS float*)(sa + (j - 4) * 64); rs[j - 4] = *(const LAS float*)(sa + (j - 4) * 64 + 576); }
            else if (j < 10) csv[j - 8] = *(const LAS float*)(sc + 512 + (j - 8) * 64 + l15 * 4);
            else if (j < 18) {
                if (j & 1) rsv[(j - 10) >> 1] = *(const LAS f32x4*)(sb + ((j - 10) >> 1) * 64 + 576);
                else nmv[(j - 10) >> 1] = *(const LAS f32x4*)(sb + ((j - 10) >> 1) * 64);
            }
        } STPPASS(0));
        STAMP(2);
        auto tile = [&](int ni, int ti, float sc_) { return pk4((acc[ni][ti] + nm[ti] * cs[ni]) * sc_); };
        auto tilev = [&](int h_, int ti) { return pk4((acc[4 + h_][ti] + nmv[ti] * csv[h_]) * rsv[ti]); };
#define KOUT(ti) do { const u32x4 v = unit16(tile(2, ti, rs[ti]), tile(3, ti, rs[ti])); st16wt<ti * 1024>(kptr, v); *(LAS u32x4*)(kl + ti * 1024) = v; } while (0)
#define VOUT(j) do { const u32x4 v = __builtin_bit_cast(u32x4, cat(tilev(j & 1, j & 2), tilev(j & 1, (j & 2) + 1))); st16wt<j * 1024>(vptr, v); *(LAS u32x4*)(vl + j * 1024) = v; } while (0)
        KOUT(0); KOUT(1); KOUT(2); KOUT(3);
        VOUT(0); VOUT(1); VOUT(2); VOUT(3);
        SB(); BARL(); SB();
#pragma unroll
        for (int ti = 0; ti < 4; ++ti) rs[ti] *= QSCALE * LOG2E;
        qa0 = __builtin_bit_cast(bf16x8, unit16(tile(0, 0, rs[0]), tile(0, 1, rs[1])));
        qa1 = __builtin_bit_cast(bf16x8, unit16(tile(1, 0, rs[0]), tile(1, 1, rs[1])));
        qb0 = __builtin_bit_cast(bf16x8, unit16(tile(0, 2, rs[2]), tile(0, 3, rs[3])));
        qb1 = __builtin_bit_cast(bf16x8, unit16(tile(1, 2, rs[2]), tile(1, 3, rs[3])));
    }
    STAMP(3);
    const int r = lane & 31, hh = lane >> 5;
    const int kro = ((r & 4) * 4 + (r >> 3) * 4 + (r & 3)) * 64 + ((hh ^ (r >> 3)) << 4), vro = r * 64 + ((hh ^ ((r >> 2) & 3)) << 4);
    const lchar* kb0 = gs + kro;
    const lchar* kb1 = gs + (kro ^ 32);
    const lchar* vb0 = gs + 4096 + vro;
    const lchar* vb1 = gs + 4096 + (vro ^ 32);
    const short se = (l15 & 1) ? (short)0 : (short)0x3f80, so = se ^ 0x3f80;
    const bf16x8 sel = {se, se, se, se, so, so, so, so};
    int hi = 0xdddd;
    asm volatile("" : "+v"(hi));
    f32x16 oA, oB, cA, cB, sA, sB;
    f32x4 lA = {0.f, 0.f, 0.f, 0.f}, lB = {0.f, 0.f, 0.f, 0.f};
#pragma unroll
    for (int i = 0; i < 16; ++i) { oA[i] = 0.f; oB[i] = 0.f; }
    bf16x8 kf0, kf1, vf0, vf1[2];
    f32x8 p1 = {0.f, 0.f, 0.f, 0.f, 0.f, 0.f, 0.f, 0.f};
    auto nop = [&]() {};
    auto tilestep = [&](int par, int nx, bool bar) {
        block(sA, sB, kf0, kf1, qb0, qb1, cB, oB, lB, vf1[par ^ 1], p1, oA, lA, vf0, sel, hi, [&]() { if (bar) BAR(); },
              [&]() { kf0 = ldk(kb0 + nx); }, [&]() { __builtin_amdgcn_s_waitcnt(0xc17f); }, [&]() { LGKM0(); SB(); kf1 = ldk(kb1 + nx); });
        block(sB, sA, kf0, kf1, qa0, qa1, cA, oA, lA, vf1[par], p1, oB, lB, vf0, sel, hi, nop,
              [&]() { LGKM0(); SB(); vf1[par ^ 1] = ldk(vb1 + nx); }, nop, [&]() { vf0 = ldk(vb0 + nx); });
    };
    {
        const int own = wm * 8192, oth = own ^ 8192;
        kf0 = ldk(kb0 + own); kf1 = ldk(kb1 + own);
        vf0 = ldk(vb0 + own); vf1[0] = ldk(vb1 + own); vf1[1] = sel;
        asm volatile("" : "+v"(qa0), "+v"(qa1), "+v"(qb0), "+v"(qb1)); SB();
        publish(fkv + fi(wn * 32 + wm * 16 + g), lane, tag); SB();
        sA = MFMA32(kf0, qa0, oA); sB = MFMA32(kf0, qb0, oA);
        sA = MFMA32(kf1, qa1, sA); sB = MFMA32(kf1, qb1, sB);
        const float mA = rowmax32(sA), mB = rowmax32(sB);
#pragma unroll
        for (int i = 0; i < 16; ++i) { cA[i] = -mA; cB[i] = -mB; sA[i] -= mA; }
        asm volatile("" : "+v"(cA), "+v"(cB));
        SB();
        STAMP(5);
        tilestep(0, own + 2048, false);
        tilestep(1, oth, false);
        tilestep(0, oth + 2048, false);
        tilestep(1, AR1, true);
        STAMP(6);
    }
#pragma unroll 1
    for (int it = 0; it < 5; ++it) {
#pragma unroll
        for (int s = 0; s < 3; ++s) {
            const int cur = s == 0 ? AR1 : s == 1 ? AR2 : 0, nxt = s == 0 ? AR2 : s == 1 ? 0 : AR1;
            tilestep(0, cur + 2048, false);
            tilestep(1, cur + 8192, false);
            tilestep(0, cur + 10240, false);
            tilestep(1, nxt, true);
        }
    }
    oB = SMF32(vf1[1], p1, oB);
    lB = SMF16(sel, p1, lB);
    STAMP(9);
    float la = (lane & 16) ? lA[1] : lA[0], lb = (lane & 16) ? lB[1] : lB[0];
    if (__any(!(la <= 1073741824.0f) || !(lb <= 1073741824.0f))) {
        const char* kh = (const char*)kv + (size_t)h * SEQ * 64;
#pragma unroll 1
        for (int ps = 0; ps < 2; ++ps) {
            oA = oB; la = lb;
            attn_slow(kh + kro, kh + (size_t)NH * SEQ * 64 + vro, ps ? qb0 : qa0, ps ? qb1 : qa1, oB, lb, hi);
        }
    }
    const char* op = (const char*)O + ((size_t)h * SEQ + g * 128 + wm * 64 + l15) * 64 + (((((lg & 1) << 1) | (lg >> 1)) ^ fz(l15)) << 4);
    auto fin = [&](const f32x16& o, float l, u32x4& u0, u32x4& u1) {
        const f32x16 y = o * __builtin_amdgcn_rcpf(l);
        const u32x2 a = sw32(cvtpk(y[0], y[1]), cvtpk(y[4], y[5])), b = sw32(cvtpk(y[2], y[3]), cvtpk(y[6], y[7]));
        const u32x2 c = sw32(cvtpk(y[8], y[9]), cvtpk(y[12], y[13])), d = sw32(cvtpk(y[10], y[11]), cvtpk(y[14], y[15]));
        const u32x2 e = sw16(a[0], c[0]), f = sw16(b[0], d[0]), i = sw16(a[1], c[1]), j = sw16(b[1], d[1]);
        u0 = (u32x4){e[0], f[0], i[0], j[0]};
        u1 = (u32x4){e[1], f[1], i[1], j[1]};
    };
    u32x4 u0, u1;
    fin(oA, la, u0, u1);
    st16wt<0>(op, u0); st16wt<1024>(op, u1);
    fin(oB, lb, u0, u1);
    st16wt<2048>(op, u0); st16wt<3072>(op, u1);
    STAMP(10);
}

constexpr int NRW = 7;
DEV constexpr int wo_slot(int t) { return t < 7 ? t : t < 10 ? t + 1 : t - 8; }
DEV unsigned wo_voff(int lane, int cb, int i) {
    const int k = 8 * i + (lane >> 3), pc = lane & 7;
    return (k * 1024 + cb * 64 + ((((pc >> 1) ^ fswz(k)) << 1) + (pc & 1)) * 8) * 2 - i * 1024;
}
DEV void wo_compute(lchar* smem, u64* fo, u32x4 (&rr)[4], u16* C, char* stats, u64* fx, u64 tag, int g, int cb, int lane,
                    int wid STPARG) {
    OPAQUE(lane, wid, g, cb);
    const int wm = wid >> 1, wn = wid & 1, l15 = lane & 15, lg = lane >> 4;
    const int k0 = 8 * lg + (l15 >> 2);
    const lchar* wb0 = smem + WO0 + k0 * 128 + (((wn * 2) ^ fswz(k0)) << 5) + (l15 & 3) * 8;
    const lchar* wb1 = smem + WO0 + k0 * 128 + (((wn * 2 + 1) ^ fswz(k0)) << 5) + (l15 & 3) * 8;
    asm volatile("" : "+v"(wb0), "+v"(wb1));
    bf16x8 xf[2][8];
    s16x4 wh[16][8];
    auto rdw = [&](int t, int j) { wh[t][j] = ldtr4(((j & 2) ? wb1 : wb0) + wo_slot(t) * 8192 + (j >> 2) * 4096 + (j & 1) * 512); };
    SB();
#pragma unroll
    for (int t = 0; t < NRW; ++t)
#pragma unroll
        for (int j = 0; j < 8; ++j) rdw(t, j);
    SB();
    LGKM0(); SB();
    BAR();
    publish(fo, lane, tag);
    STAMP(5);
    STAMPW(6 + wid);
    asm volatile("" : "+v"(lane), "+v"(rr[0]), "+v"(rr[1]), "+v"(rr[2]), "+v"(rr[3]));
    const int un = ((lg & 1) << 1) | (lg >> 1), row = g * 128 + wm * 64 + l15, ch = cb * 2 + wn;
    const unsigned ocm = (ch * SEQ + row) * 64 + ((un ^ fz(l15)) << 4), orm = row * 2048 + ch * 64 + un * 16;
    const char* cp = (const char*)C + (stats ? ocm : orm);
    const unsigned cs = stats ? 1024 : 32768;
    const char* sp = stats + ((g * 2 + wm) * 32 + ch) * 512 + lane * 8;
    const lchar* xb = smem + (wm * 64 + l15) * 64 + ((lg ^ fz(l15)) << 4);
    asm volatile("" : "+v"(cp), "+v"(sp), "+v"(xb));
    f32x4 acc[2][4];
#pragma unroll
    for (int ti = 0; ti < 4; ++ti) {
        const u32x2 x = sw16(rr[ti][0], rr[ti][2]), y = sw16(rr[ti][1], rr[ti][3]);
#pragma unroll
        for (int ni = 0; ni < 2; ++ni) acc[ni][ti] = (f32x4){lo16(x[ni]), hi16(x[ni]), lo16(y[ni]), hi16(y[ni])};
    }
    auto rdx = [&](int t, int j) { xf[t & 1][j] = ldk(xb + (t % 5) * 16384 + (j >> 2) * 8192 + (j & 3) * 1024); };
    auto mm = [&](int t, int kk, int j) {
        const int ti = j >> 1, ni = j & 1;
        mfma16t(acc[ni][ti], cat8(wh[t][kk * 4 + ni * 2], wh[t][kk * 4 + ni * 2 + 1]), xf[t & 1][kk * 4 + ti]);
    };
    SB();
#pragma unroll
    for (int t = 0; t < 16; ++t) {
        BAR(); SB();
        STAMPB(t);
        if (t == 0) {
            STAMP(4);
#pragma unroll
            for (int j = 0; j < 8; ++j) rdx(0, j);
            SB();
        } else {
#pragma unroll
            for (int j = 0; j < 8; ++j) { rdx(t, j); SB(); mm(t - 1, 1, j); SB(); }
        }
        const bool more = t + 1 >= NRW && t < 15;
#pragma unroll
        for (int j = 0; j < 8; ++j) { mm(t, 0, j); SB(); if (more) { rdw(t + 1, j); SB(); } }
        if (more) __builtin_amdgcn_s_waitcnt(0xc87f); else LGKM0();
        SB();
    }
    u32x4 (&ov)[4] = rr;
    auto out = [&](int ti) {
        asm volatile("" : "+v"(acc[0][ti]), "+v"(acc[1][ti]));
        ov[ti] = unit16(pk4(acc[0][ti]), pk4(acc[1][ti]));
        asm volatile("global_store_dwordx4 %0, %1, off sc1\n\ts_nop 1" ::"v"(cp + ti * cs), "v"(ov[ti]) : "memory");
        SB();
    };
#pragma unroll
    for (int j = 0; j < 6; ++j) mm(15, 1, j);
    SB();
    out(0);
    mm(15, 1, 6); mm(15, 1, 7); SB();
    out(1); out(2); out(3);
    STAMP(2);
    if (stats) {
        if (OA) { BAR(); SB(); }
        float s[4], q[4];
        const bf16x2_t one2 = __builtin_bit_cast(bf16x2_t, 0x3f803f80u);
#pragma unroll
        for (int ti = 0; ti < 4; ++ti) {
            s[ti] = 0.f; q[ti] = 0.f;
#pragma unroll
            for (int e = 0; e < 4; ++e) {
                const unsigned u = ov[ti][e];
                const bf16x2_t v = __builtin_bit_cast(bf16x2_t, u);
                s[ti] = __builtin_amdgcn_fdot2_f32_bf16(v, one2, s[ti], false);
                q[ti] = __builtin_amdgcn_fdot2_f32_bf16(v, v, q[ti], false);
            }
        }
        auto red = [&](const float (&v)[4]) {
            const f32x2 a = sw16f(v[0], v[1]), b = sw16f(v[2], v[3]);
            const f32x2 c = sw32f(a[0] + a[1], b[0] + b[1]);
            return c[0] + c[1];
        };
        st8wt(sp, (u32x2){__float_as_uint(red(s)), __float_as_uint(red(q))});
        STAMP(10);
        if (!OA) BAR();
        publish<1>(fx + fi((wn * 32 + g * 2 + wm) * 16 + cb), lane, tag);
        STAMP(11);
        STAMPW(12 + wid);
        publish<1>(fx + fi(FL_S - FL_X + (wn * 32 + g * 2 + wm) * 16 + cb), lane, tag);
    } else {
        STAMP(10);
    }
}

DEV void ln0_rows(u32x4 (&x)[4]) {
    auto sum32 = [&](float v) {
        v = dppadd<0x128>(dppadd<0x4E>(dppadd<0xB1>(v)));
        const f32x2 a = sw16f(v, v);
        v = a[0] + a[1];
        const f32x2 b = sw32f(v, v);
        return b[0] + b[1];
    };
    f32x2 v[16];
#pragma unroll
    for (int i = 0; i < 16; ++i) v[i] = (f32x2){lo16(x[i >> 2][i & 3]), hi16(x[i >> 2][i & 3])};
    f32x2 s = v[0];
#pragma unroll
    for (int i = 1; i < 16; ++i) s += v[i];
    const float mean = sum32(s[0] + s[1]) * (1.0f / DM);
    f32x2 q = {0.f, 0.f};
#pragma unroll
    for (int i = 0; i < 16; ++i) { const f32x2 d = v[i] - mean; q += d * d; }
    const float var = sum32(q[0] + q[1]) * (1.0f / DM);
    const float mb = bfr(mean);
    const float inv = rsd(var);
#pragma unroll
    for (int i = 0; i < 16; ++i) {
        const f32x2 e = v[i] - mb;
        const unsigned d = cvtpk(e[0], e[1]);
        const f32x2 y = (f32x2){lo16(d), hi16(d)} * inv;
        x[i >> 2][i & 3] = cvtpk(y[0], y[1]);
    }
}

struct HW { unsigned wq[2][3], wo[4], wt, vo16, lds, gs; };
typedef u32x4 Tail[18];
template <int N> DEV void wq_issue(const HW& h, const u16* W, int t, int r0) {
    const char* src = (const char*)W + (unsigned)(t * 8 + r0) * h.gs;
    const unsigned l = h.lds + WQ0 + ((t + 2) % 3) * WQS + r0 * 3072;
#pragma unroll
    for (int i = 0; i < N; ++i) { glds3(src, l + i * 3072, h.wq[i & 1][0], h.wq[i & 1][1], h.wq[i & 1][2]); src += h.gs; }
}
DEV void wmode(HW& h, bool copy) {
    h.gs = copy ? 3072 : h.gs;
#pragma unroll
    for (int i = 0; i < 6; ++i) { h.wq[i / 3][i % 3] = copy ? h.vo16 : h.wq[i / 3][i % 3]; asm volatile("" : "+v"(h.wq[i / 3][i % 3])); }
}
template <int K0, int K1> DEV void tail_ld(const HW& h, Tail& tw, const u16* W) {
#pragma unroll
    for (int k = K0; k < K1; ++k)
        asm volatile("s_nop 4\n\tglobal_load_dwordx4 %0, %1, %2" : "=&v"(tw[k]) : "v"(h.wt + k * (32 * 2048)), "s"(W) : "memory");
}
DEV void tail_pin(Tail& tw) {
#pragma unroll
    for (int k = 0; k < 18; ++k) asm volatile("" : "+v"(tw[k]));
}
DEV void wo_issue(const HW& h, const u16* W, int t0, int t1) {
#pragma unroll 1
    for (int i = 2 * t0; i < 2 * t1; ++i) glds4((const char*)W + (size_t)i * (32 * 2048), h.lds + WO0 + i * 4096, h.wo[0], h.wo[1], h.wo[2], h.wo[3]);
}
template <int ST> DEV void ld8(f32x4 (&d)[8], const char* sb, unsigned vo) {
    asm volatile("s_nop 4\n\tglobal_load_dwordx4 %0, %8, %9 offset:%10 sc1\n\tglobal_load_dwordx4 %1, %8, %9 offset:%11 sc1\n\t"
                 "global_load_dwordx4 %2, %8, %9 offset:%12 sc1\n\tglobal_load_dwordx4 %3, %8, %9 offset:%13 sc1\n\t"
                 "global_load_dwordx4 %4, %8, %9 sc1\n\tglobal_load_dwordx4 %5, %8, %9 offset:%14 sc1\n\t"
                 "global_load_dwordx4 %6, %8, %9 offset:%15 sc1\n\tglobal_load_dwordx4 %7, %8, %9 offset:%16 sc1"
                 : "=&v"(d[0]), "=&v"(d[1]), "=&v"(d[2]), "=&v"(d[3]), "=&v"(d[4]), "=&v"(d[5]), "=&v"(d[6]), "=&v"(d[7])
                 : "v"(vo), "s"(sb + 4 * ST), "n"(-4 * ST), "n"(-3 * ST), "n"(-2 * ST), "n"(-ST), "n"(ST), "n"(2 * ST), "n"(3 * ST) : "memory");
}

template <bool LOCAL>
DEV void qkv_helper(lchar* smem, HW& h, Tail& tw, int layer, const u16* X, const u16* W, const u16* Wc, const u16* W2, const u16* Wo, const char* stats, float* csum,
                    u64* fl, u64 tag, int g, int hp, int lane, int lw STPARG) {
    OPAQUE(lane, lw, g, hp);
    constexpr int NX = LOCAL ? 9 : 8, HO = LOCAL ? 16 : 0;
    constexpr int TS = LOCAL ? T0 : 16;
    const int m0 = g * 128;
    const bool halo = LOCAL && g > 0, slow = WC && (hp & 7) == 3;
    const u64* fx = fl + fi(FL_X + (layer - 1) * 1024 + (g * 2 - (halo ? 1 : 0)) * 16 + (lane < (halo ? 48 : 32) ? lane : 0));
    const bool fon = lane < (halo ? 48 : 32);
    auto fold = [&](u64 m) { return (unsigned)(m & (m >> 16) & (halo ? m >> 32 : m)); };
    if (lw < 2) {
        const unsigned vo0 = (LOCAL && g == 0) ? h.vo16 + 1024 : h.vo16;
        const char* xsrc = (const char*)X + ((size_t)lw * SEQ + m0 - HO) * 64;
        const unsigned ldx = h.lds + lw * (NX * 1024);
        unsigned rdy = 0u;
        STAMP(0);
        if (LOCAL && layer == 0) {
            const int fi0 = g * 16 - (halo ? 2 : 0) + (lane & 31);
            if (lw == 0) poll(fl + fi(FL_L + fi0 + (lane >> 5) * 256), (lane & 31) < (halo ? 18 : 16) && (lane < 32 ? (NE > 4 || fi0 < 128) : (NE < 8 && fi0 >= 128)),
                              tag, ~0u, [](u64 m) { return (unsigned)(m & (m >> 32)); });
            BAR();
            rdy = ~0u;
        }
        auto issue = [&](int t, int so) {
            if (!((rdy >> t) & 1)) rdy = poll(fx + fi(lw * 512), fon, tag, 1u << t, fold);
            glds4(xsrc, ldx + so, vo0, h.vo16, h.vo16, h.vo16);
            glds4(xsrc + 4096, ldx + so + 4096, h.vo16, h.vo16, h.vo16, h.vo16);
            if (LOCAL) glds1(xsrc + 8192, ldx + so + 8192, h.vo16);
            xsrc += 2 * SEQ * 64;
        };
        issue(0, 0);
        STAMP(1);
        if (lw == 1) STAMPW(11);
        issue(1, XS);
        if (rdy & 4) {
            issue(2, 2 * XS);
            if (LOCAL && layer == 0) BAR();
            WAITB(2 * NX);
        } else {
            WAITB(NX);
            issue(2, 2 * XS);
        }
        WAITB(NX);
        int so = 0;
        auto run = [&](int t0, int t1) {
#pragma unroll 1
            for (int t = t0; t < t1; ++t) {
                wq_issue<1>(h, W, t, lw);
                issue(t, so);
                so = so == 2 * XS ? 0 : so + XS;
                WAITB(NX + 3);
            }
        };
        if (TS < 16) { run(3, TS); wmode(h, slow); W = Wc; run(TS, 16); }
        else run(3, 16);
        if (LOCAL) {
            tail_ld<0, 6>(h, tw, Wo);
            WAITB(6);
            tail_ld<6, 12>(h, tw, Wo);
            BAR();
            tail_ld<12, 18>(h, tw, Wo);
            BAR();
            WAITV(0);
            tail_pin(tw);
        } else {
            WAITB(0);
            BAR(); BAR();
        }
    } else if (lw == 2) {
        if (LOCAL && layer == 0) { BAR(); BAR(); }
        WAITB(0);
        if (layer) wq_issue<4>(h, W, 2, 0);
        BAR();
#pragma unroll 1
        for (int t = 3; t < (TS < 16 ? TS : 3); ++t) { wq_issue<6>(h, W, t, 2); WAITB(18); }
        if (TS < 16) { wmode(h, slow); W = Wc; }
#pragma unroll 1
        for (int t = (TS < 16 ? TS : 3); t < 15; ++t) {
            wq_issue<6>(h, W, t, 2);
            if (LOCAL && t >= 10 && t < 14) {
                glds4((const char*)Wo + (unsigned)(t - 10) * (32 * 2048), h.lds + WO0 + (t - 10) * 4096, h.wo[0], h.wo[1], h.wo[2], h.wo[3]);
                WAITB(22);
            } else WAITB(18);
        }
        wq_issue<6>(h, W, 15, 2);
        if (LOCAL) {
            WAITB(18);
            wo_issue(h, Wo, 2, 5);
            WAITB(24);
            wo_issue(h, Wo, 5, 7);
            tail_ld<0, 6>(h, tw, Wo);
            BAR();
            tail_ld<6, 18>(h, tw, Wo);
            BAR();
            WAITV(0);
            tail_pin(tw);
        } else {
            WAITB(18);
            WAITB(0);
            BAR(); BAR();
        }
    } else {
        auto warm = [&]() {
            asm volatile("s_mov_b32 m0, %2\n\ts_nop 4\n\tglobal_load_lds_dword %0, %1\n\tglobal_load_lds_dword %3, %1" ::"v"(lane * 64),
                         "s"(Wo + (size_t)blockIdx.x * 4096), "s"(h.lds + DUMP), "v"(lane * 64 + 4096) : "memory");
        };
        if (LOCAL && layer == 0) {
            const int kg = g, j = hp ^ CJ;
            const unsigned vo = ((kg * 64 + (lane >> 3)) * 3072 + j * 64 + (lane & 7) * 8) * 2 + 2048;
            const char* dst = (const char*)csum + (j * 16 + kg) * 768 + (lane & 7) * 32 + (lane >> 4) * 16;
            u32x4 sv[2][3][8];
            auto ld = [&](int w, int i) {
                asm volatile("s_nop 4\n\tglobal_load_dwordx4 %0, %3, %4 offset:-2048\n\tglobal_load_dwordx4 %1, %3, %4\n\t"
                             "global_load_dwordx4 %2, %3, %4 offset:2048" : "=&v"(sv[w][0][i]), "=&v"(sv[w][1][i]), "=&v"(sv[w][2][i])
                             : "v"(vo), "s"((const char*)(w ? W2 : W) + i * (8 * 6144)) : "memory");
            };
            const short one = 0x3f80;
            bf16x8 sel;
#pragma unroll
            for (int e = 0; e < 8; ++e) sel[e] = (lane & 7) == e ? one : (short)0;
            f32x4 c;
            auto half = [&](int w, int k) {
                if (!(k & 1)) c = (f32x4){0.f, 0.f, 0.f, 0.f};
#pragma unroll
                for (int i = 0; i < 4; ++i) c = MFMA16(sel, __builtin_bit_cast(bf16x8, sv[w][k >> 1][(k & 1) * 4 + i]), c);
                if (k & 1) {
#pragma unroll
                    for (int e = 0; e < 4; ++e) c[e] = dppadd<0x128>(c[e]);
                    if (lane < 32 && !(lane & 8)) st16wt<0>(dst + w * (256 * 768) + (k >> 1) * 256, __builtin_bit_cast(u32x4, c));
                }
                SB();
            };
            const u64* fw = fl + fi(FL_W + (hp & 8) * 16 + (slow ? lane : 0));
            u64 f0, f1, f2, f3;
            BAR(); BAR();
            asm volatile("global_load_dwordx2 %0, %4, off sc1\n\tglobal_load_dwordx2 %1, %4, off offset:512 sc1\n\tglobal_load_dwordx2 %2, %4, off offset:2048 sc1\n\t"
                         "global_load_dwordx2 %3, %4, off offset:2560 sc1" : "=&v"(f0), "=&v"(f1), "=&v"(f2), "=&v"(f3) : "v"(fw) : "memory");
#pragma unroll
            for (int i = 0; i < 8; ++i) ld(0, i);
            warm();
            WAITB(30);
            WAITV(26);
            asm volatile("" : "+v"(f0), "+v"(f1), "+v"(f2), "+v"(f3));
            if (slow && !__all(f0 == tag && f1 == tag && f2 == tag && f3 == tag)) {
#pragma unroll 1
                for (int i = 0; i < 4; ++i) poll(fw + (i >> 1) * 256 + (i & 1) * 64, true, tag, ~0u, [](u64 m) { return (unsigned)(m & (m >> 32)); });
            }
            wmode(h, slow);
            BAR(); BAR(); BAR(); BAR();
            WAITV(0);
#pragma unroll
            for (int i = 0; i < 24; ++i) asm volatile("" : "+v"(sv[0][i >> 3][i & 7]));
#pragma unroll
            for (int k = 0; k < 6; ++k) { half(0, k); BAR(); }
            tail_ld<0, 5>(h, tw, Wo); BAR();
            tail_ld<5, 10>(h, tw, Wo); BAR();
            tail_ld<10, 14>(h, tw, Wo); BAR();
            tail_ld<14, 18>(h, tw, Wo); BAR();
            BAR(); BAR();
#pragma unroll
            for (int i = 0; i < 8; ++i) { ld(1, i); if (i == 3) BAR(); }
            BAR(); BAR();
            WAITV(0);
            tail_pin(tw);
#pragma unroll
            for (int i = 0; i < 24; ++i) asm volatile("" : "+v"(sv[1][i >> 3][i & 7]));
#pragma unroll
            for (int k = 0; k < 6; ++k) half(1, k);
            return;
        } else {
            auto fin = [&](float s, float q, int o) {
                const float mean = s * (1.0f / DM);
                *(LAS float*)(smem + SNM + o) = -bfr(mean);
                *(LAS float*)(smem + SRS + o) = rsd(fmx(q * (1.0f / DM) - mean * mean, 0.f));
            };
            f32x4 sp[3][8], a = {0.f, 0.f, 0.f, 0.f};
            auto add8 = [&](f32x4 (&d)[8]) {
#pragma unroll
                for (int i = 0; i < 8; ++i) {
                    asm volatile("" : "+v"(d[i]));
#pragma unroll
                    for (int e = 0; e < 4; ++e) asm volatile("v_add_f32 %0, %0, %1" : "+v"(a[e]) : "v"(d[i][e]));
                }
            };
            const u64* fc = fl + fi(FL_C + hp * 16 + (lane & 15));
            const u64* fs = fx + fi(FL_S - FL_X);
            const char* sb = stats;
            const char* cb = (const char*)csum + (LOCAL ? 0 : 256 * 768);
            const unsigned so = (layer - 1) * (unsigned)(MB / 2) + g * 32768;
            const unsigned vo = so + (lane >> 5) * 16384 + (lane & 31) * 16, vc = hp * (16 * 768) + (lane < 48 ? lane * 16 : 0);
            WAITB(0);
            wq_issue<4>(h, W, 2, 4);
            BAR(); WAITB(0);
            u64 f0, f1, f2;
            asm volatile("global_load_dwordx2 %0, %3, off sc1\n\tglobal_load_dwordx2 %1, %4, off sc1\n\tglobal_load_dwordx2 %2, %5, off sc1"
                         : "=&v"(f0), "=&v"(f1), "=&v"(f2) : "v"(fs), "v"(fs + fi(512)), "v"(fc) : "memory");
            BAR();
            WAITV(0);
            asm volatile("" : "+v"(f0), "+v"(f1), "+v"(f2));
            if (!__all(f0 == tag && f1 == tag && f2 == tag)) {
                poll(fs, fon, tag, 0xffffu, fold);
                poll(fs + fi(512), fon, tag, 0xffffu, fold);
                poll(fc, lane < 16, tag, 0xffffu, [](u64 m) { return (unsigned)m; });
            }
            ld8<512>(sp[0], sb, vo);
            BAR();
            ld8<512>(sp[1], sb + 4096, vo);
            BAR();
            ld8<512>(sp[2], sb + 8192, vo);
            WAITV(16);
            add8(sp[0]);
            BAR();
            ld8<512>(sp[0], sb + 12288, vo);
            WAITV(16);
            add8(sp[1]);
            BAR();
            WAITV(8);
            ld8<768>(sp[1], cb, vc);
            add8(sp[2]);
            BAR();
            ld8<768>(sp[2], cb + 6144, vc);
            WAITV(16);
            add8(sp[0]);
            BAR();
            if (LOCAL) ld8<512>(sp[0], sb, so - (halo ? 16384 : 0) + (lane >> 4) * 4096 + 384 + (lane & 7) * 16);
            fin(a[0], a[1], 64 + lane * 8);
            fin(a[2], a[3], 68 + lane * 8);
            SB();
            BAR();
            if (LOCAL) tail_ld<0, 5>(h, tw, Wo);
            WAITV(LOCAL ? 13 : 0);
            a = (f32x4){0.f, 0.f, 0.f, 0.f};
            add8(sp[1]);
            BAR();
            if (LOCAL) tail_ld<5, 10>(h, tw, Wo);
            add8(sp[2]);
            if (lane < 48) *(LAS f32x4*)(smem + SCOL + lane * 16) = a;
            SB();
            BAR();
            if (LOCAL) {
                tail_ld<10, 14>(h, tw, Wo);
                WAITV(14);
                a = (f32x4){0.f, 0.f, 0.f, 0.f};
                add8(sp[0]);
            }
            BAR();
            if (LOCAL) {
                tail_ld<14, 18>(h, tw, Wo);
#pragma unroll
                for (int e = 0; e < 4; ++e) {
                    const f32x2 x = sw16f(a[e], a[e]);
                    const float v = x[0] + x[1];
                    const f32x2 y = sw32f(v, v);
                    a[e] = y[0] + y[1];
                }
                fin(a[0], a[1], (lane & 7) * 8);
                fin(a[2], a[3], (lane & 7) * 8 + 4);
            }
            BARL();
            BAR(); BAR(); BAR();
            if (LOCAL) { WAITV(0); tail_pin(tw); }
        }
    }
    if (LOCAL) { BAR(); BAR(); }
}

DEV void attn_helper(const HW& h, Tail& tw, const u16* kv, const u16* Wo, const u64* fkv, u64 tag, int g, int hp, int lane, int lw STPARG) {
    __builtin_amdgcn_s_setprio(2);
    OPAQUE(lane, lw, g, hp);
    const int rh = lw & 1, hd = lw >> 1;
    const char* src = (const char*)kv + (unsigned)((hp * 2 + hd) * SEQ * 64 + rh * 4096);
    const unsigned ld = h.lds + hd * 16384 + rh * 8192;
    const u64* f = fkv + fi(lw * 16 + (lane & 15));
    unsigned rdy = 0u;
    int reg = AR1;
    auto issue = [&](int c) {
        const int blk = g ^ c;
        if (!((rdy >> blk) & 1)) rdy = poll(f, lane < 16, tag, 1u << blk, [](u64 m) { return (unsigned)m; });
        glds4(src + blk * 8192, ld + reg, h.vo16, h.vo16, h.vo16, h.vo16);
        glds4(src + blk * 8192 + NH * SEQ * 64, ld + reg + 4096, h.vo16, h.vo16, h.vo16, h.vo16);
        reg = reg == AR1 ? AR2 : reg == AR2 ? 0 : AR1;
    };
    issue(1);
    STAMP(7);
#pragma unroll 1
    for (int c = 0; c < 11; ++c) {
        WAITB(0);
        issue(c + 2);
        if (lw == 2 && c >= 4) wo_issue(h, Wo, c - 4, c - 3);
    }
    WAITB(0); issue(13); tail_ld<0, 6>(h, tw, Wo);
    WAITB(6); issue(14); tail_ld<6, 12>(h, tw, Wo);
    WAITB(6); issue(15); tail_ld<12, 18>(h, tw, Wo);
    WAITB(6);
    WAITB(0);
    tail_pin(tw);
    __builtin_amdgcn_s_setprio(0);
    BAR();
}

DEV void wo_helper(lchar* smem, const HW& h, Tail& tw, const u16* O, const u16* Wn, u64* fc, const u64* fo, u64 tag, int g, int lane, int lw STPARG) {
    int dummy = 0;
    OPAQUE(lane, lw, g, dummy);
    const int p = lw & 1, rh = lw >> 1;
    const char* xsrc = (const char*)O + ((size_t)p * SEQ + g * 128 + rh * 64) * 64;
    const unsigned ldx = h.lds + p * 8192 + rh * 4096;
    const u64* f = fo + fi((p * 32 + g * 2 + rh) * 16 + (lane & 15));
    unsigned so = 0, rdy;
    auto issue = [&](int t) {
        if (!((rdy >> t) & 1)) rdy = poll(f, lane < 16, tag, 1u << t, [](u64 m) { return (unsigned)m; });
        glds4(xsrc, ldx + so, h.vo16, h.vo16, h.vo16, h.vo16);
        xsrc += 2 * SEQ * 64;
        so = so == 65536 ? 0 : so + 16384;
    };
    STAMP(0);
    {
        u64 f0;
        asm volatile("s_mov_b64 exec, 0xffff\n\t" FLD("%0", "%1", "%2") "\n\ts_mov_b64 exec, -1" : "=&v"(f0) : "v"(f), "v"(0ull) : "memory");
        lchar* d = smem + WO0 + 16384 + lw * 1024 + lane * 16;
#pragma unroll
        for (int k = 0; k < 18; ++k) *(LAS u32x4*)(d + (wo_slot(NRW + (k >> 1)) - 2) * 8192 + (k & 1) * 4096) = tw[k];
        WAITV(0);
        asm volatile("" : "+v"(f0));
        rdy = (unsigned)__ballot(f0 == tag);
        if (fc && lane == 0) stflag(fc, tag);
    }
    issue(0);
    STAMP(1);
    issue(1); issue(2); issue(3); issue(4);
    LGKM0();
    WAITB(16);
#pragma unroll 1
    for (int t = 5; t < 16; ++t) { WAITB(12); issue(t); }
    if (Wn && lw >= 2) {
        wq_issue<4>(h, Wn, 0, lw * 4 - 8);
        WAITB(24); WAITB(20);
        wq_issue<4>(h, Wn, 1, lw * 4 - 8);
        WAITB(28); WAITB(24);
    } else {
        WAITB(12); WAITB(8); WAITB(4); WAITB(0);
    }
    if (Wn) BAR();
}

__global__ __launch_bounds__(512) void mega(const u16* __restrict__ xin, const u16* __restrict__ wqa, const u16* __restrict__ woa,
                                            const u16* __restrict__ wqb, const u16* __restrict__ wob, u16* __restrict__ out,
                                            char* __restrict__ ws) {
    __shared__ __attribute__((aligned(16))) char smem_[LDS_BYTES];
    lchar* smem = (lchar*)smem_;
    const int bid = blockIdx.x, xcd = bid & 7, idx = bid >> 3;
    const int lane = threadIdx.x & 63, wid = __builtin_amdgcn_readfirstlane(threadIdx.x >> 6);
    const int g = (xcd >> 2) * 8 + (idx >> 2), hp = (xcd & 3) * 4 + (idx & 3);
    const int g2 = (xcd >> 1) * 4 + (idx >> 3), cb = (xcd & 1) * 8 + ((idx & 7) ^ CX);
    const int gr = g | (wid >> 2) * 8, row = gr * 128 + hp * 8 + ((wid < 4 ? wid : 7 - wid) & 3) * 2 + ((lane >> 2) & 1);
    const bool early = xcd < 4;
    unsigned xo = row * 2048 + (lane >> 3) * 64 + (lane & 3) * 16, eo = fi(FL_EP) * 8;
    asm volatile("" : "+v"(xo), "+v"(eo));
    SB();
    u64* fl = (u64*)(ws + WS_FL);
    u16* ob = (u16*)(ws + WS_O);
    auto mktag = [](u64 e) {
        e += 1;
        return ((u64)(unsigned)__builtin_amdgcn_readfirstlane((int)(e >> 32)) << 32) | (unsigned)__builtin_amdgcn_readfirstlane((int)e);
    };
    u64 tag;
    const unsigned lds0 = (unsigned)(uintptr_t)smem;
    auto warmup = [&]() {
        if (wid > 4) touch((const char*)wqa + (wid - 5) * 2048, lds0 + DUMP, (idx + (lane >> 3) * 32) * 6144 + (xcd & 3) * 512 + (lane & 7) * 64);
        else if (wid == 4) {
            const int f = lane < 4 ? (bid < 240 ? bid : 0) * 64 + lane * 16 : FL_L + (g & 7) * 16 + (lane & 1) * 128 + (early ? 0 : 256);
            asm volatile("s_mov_b64 exec, 63\n\tglobal_atomic_add_x2 %0, %1, %2 sc1\n\ts_mov_b64 exec, -1" ::"v"(fi(f) * 8), "v"(0ull), "s"(fl) : "memory");
        }
    };
    LAS unsigned* cnt = (LAS unsigned*)(smem + DUMP + 1024 + (wid >> 2) * 4);
    if (wid >= (early ? NE : 8 - NE)) {
        *cnt = 0u;
        BARL();
        warmup();
        tag = mktag(ldep(fl + fi(FL_EP)));
        WAITV(0);
    } else {
        u32x4 x[4];
        u64 e;
        asm volatile("global_load_dwordx4 %0, %5, %6" NTX "\n\tglobal_load_dwordx4 %1, %5, %6 offset:512" NTX "\n\tglobal_load_dwordx4 %2, %5, %6 offset:1024" NTX "\n\t"
                     "global_load_dwordx4 %3, %5, %6 offset:1536" NTX "\n\tglobal_load_dwordx2 %4, %7, %8 sc1"
                     : "=&v"(x[0]), "=&v"(x[1]), "=&v"(x[2]), "=&v"(x[3]), "=&v"(e)
                     : "v"(xo), "s"(xin), "v"(eo), "s"(fl) : "memory");
        SB();
        *cnt = 0u;
        BARL();
        warmup();
        unsigned d = ((lane >> 3) * SEQ + row) * 64 + (((lane & 3) ^ fz(row)) << 4);
        asm volatile("" : "+v"(d));
        if (wid >= 4) WAITV(1); else WAITV(0);
        asm volatile("" : "+v"(x[0]), "+v"(x[1]), "+v"(x[2]), "+v"(x[3]), "+v"(e));
        if (wid == 0) STAMP(14);
        ln0_rows(x);
#pragma unroll
        for (int i = 0; i < 4; ++i)
            asm volatile("global_store_dwordx4 %0, %1, %2 sc1\n\ts_nop 1" ::"v"(d), "v"(x[i]), "s"(ws + WS_XN + i * MB) : "memory");
        tag = mktag(e);
        if (wid == 0) STAMP(15);
    }
    BAR();
    if (wid < (early ? NE : 8 - NE)) {
        WAITV(0);
        if (lane == 0 && __hip_atomic_fetch_add(cnt, 1u, __ATOMIC_RELAXED, __HIP_MEMORY_SCOPE_WORKGROUP) == (!early ? 7 - NE : wid < 4 ? 3 : NE - 5))
            stflag(fl + fi(FL_L + (early ? 0 : 256) + gr * 16 + hp), tag);
        if (wid == 0) STAMP(9);
    }
    if (WC && !early && wid < 4) {
        u32x4 x[3];
        const int rg = (xcd - 4) * 32 + idx;
        const unsigned so = (rg * 8 + (lane >> 3)) * 6144 + 2048 + ((wid & 1) * 8 + 3) * 128 + (((lane & 7) ^ (fswz((rg & 1) * 8 + (lane >> 3)) << 1)) << 4);
        const char* cd = ws + WS_WC + (wid * 128 + rg) * 3072 + lane * 16;
        asm volatile("s_nop 4\n\tglobal_load_dwordx4 %0, %3, %4 offset:-2048\n\tglobal_load_dwordx4 %1, %3, %4\n\tglobal_load_dwordx4 %2, %3, %4 offset:2048\n\t"
                     "s_waitcnt vmcnt(0)" : "=&v"(x[0]), "=&v"(x[1]), "=&v"(x[2]) : "v"(so), "s"(wid < 2 ? wqa : wqb) : "memory");
        st16wt<0>(cd, x[0]); st16wt<1024>(cd, x[1]); st16wt<2048>(cd, x[2]);
        publish(fl + fi(FL_W + wid * 128 + rg), lane, tag);
    }
    if (wid < 4) {
        __builtin_amdgcn_s_setprio(1);
        u32x4 res[4];
        {
            const unsigned ro = (g2 * 128 + (wid >> 1) * 64 + (lane & 15)) * 2048 + (cb * 2 + (wid & 1)) * 64 + ((lane & 16) << 1) + ((lane & 32) >> 1);
            if (RD) {
#pragma unroll
                for (int i = 0; i < 4; ++i) res[i] = (u32x4){ro, ro, ro, ro};
            } else
            asm volatile("global_load_dwordx4 %0, %4, %8\n\tglobal_load_dwordx4 %1, %5, %8\n\tglobal_load_dwordx4 %2, %6, %8\n\t"
                         "global_load_dwordx4 %3, %7, %8\n\ts_waitcnt vmcnt(0)" : "=&v"(res[0]), "=&v"(res[1]), "=&v"(res[2]), "=&v"(res[3])
                         : "v"(ro), "v"(ro + 32768), "v"(ro + 65536), "v"(ro + 98304), "s"(xin) : "memory");
            BAR(); BAR();
        }
#pragma unroll 1
        for (int layer = 0; layer < 4; ++layer) {
            if (layer & 1) qb_compute(smem, (u16*)(ws + WS_KV + (layer >> 1) * 8 * MB), ob, fl + fi(FL_KV + (layer >> 1) * 1024 + hp * 64),
                                      tag, g, hp, lane, wid STPPASS(layer * 32));
            else qa_compute(smem, layer > 0, xin, res, ob, g, hp, lane, wid STPPASS(layer * 32));
            const bool last = layer == 3;
            wo_compute(smem, fl + fi(FL_O + layer * 1024 + ((wid & 1) * 32 + g * 2 + (wid >> 1)) * 16 + hp), res,
                       last ? out : (u16*)(ws + (layer == 2 ? WS_XN : WS_X)), last ? nullptr : ws + WS_ST + layer * (MB / 2),
                       fl + fi(FL_X + layer * 1024), tag, g2, cb, lane, wid STPPASS(layer * 32 + 16));
        }
        if (bid == 0 && threadIdx.x == 0) stflag(fl + fi(FL_EP), tag);
    } else {
        const int lw = wid - 4;
        HW h;
        h.lds = lds0;
        h.vo16 = lane * 16;
#pragma unroll
        for (int i = 0; i < 6; ++i) {
            const int k = (lane >> 3) + (((i / 3) ^ (lw == 1)) << 3), pc = lane & 7;
            h.wq[i / 3][i % 3] = (lane >> 3) * 6144 + (i % 3) * 1024 + hp * 128 + ((((pc >> 1) ^ fswz(k)) << 1) + (pc & 1)) * 16;
        }
#pragma unroll
        for (int i = 0; i < 4; ++i) h.wo[i] = wo_voff(lane, cb, i);
        h.wt = wo_voff(lane, cb, lw) + lw * 1024 + 2 * NRW * (32 * 2048);
        h.gs = 8 * 6144;
        const bool slow = WC && (hp & 7) == 3;
        const u16* wa = slow ? (const u16*)(ws + WS_WC + (hp >> 3) * (128 * 3072)) : wqa;
        const u16* wb = slow ? (const u16*)(ws + WS_WC + (2 + (hp >> 3)) * (128 * 3072)) : wqb;
        if (lw >= 2) { wq_issue<4>(h, wqa, 0, lw * 4 - 8); wq_issue<4>(h, wqa, 1, lw * 4 - 8); wq_issue<4>(h, wqa, 2, lw * 4 - 8); }
#pragma unroll 1
        for (int layer = 0; layer < 4; ++layer) {
            const u16* X = (const u16*)(ws + ((layer == 0 || layer == 3) ? WS_XN : WS_X));
            const char* st = ws + WS_ST;
            const u16* wo = (layer & 1) ? wob : woa;
            Tail tw;
            if (layer & 1) {
                qkv_helper<false>(smem, h, tw, layer, X, wb, wb, wqb, wo, st, (float*)(ws + WS_CS), fl, tag, g, hp, lane, lw STPPASS(layer * 32));
                attn_helper(h, tw, (const u16*)(ws + WS_KV + (layer >> 1) * 8 * MB), wo, fl + fi(FL_KV + (layer >> 1) * 1024 + hp * 64), tag, g, hp, lane,
                            lw STPPASS(layer * 32));
            } else {
                qkv_helper<true>(smem, h, tw, layer, X, layer ? wa : wqa, wa, wqb, wo, st, (float*)(ws + WS_CS), fl, tag, g, hp, lane, lw STPPASS(layer * 32));
            }
            wo_helper(smem, h, tw, ob, layer == 3 ? nullptr : (layer & 1) ? wa : wb, (layer == 0 && lw == 3) ? fl + fi(FL_C + (hp ^ CJ) * 16 + g) : nullptr,
                      fl + fi(FL_O + layer * 1024), tag, g2, lane, lw STPPASS(layer * 32 + 16));
        }
    }
}

extern "C" void kernel_launch(void* const* d_in, const int* in_sizes, int n_in, void* d_out, int out_size, void* d_ws,
                              size_t ws_size, hipStream_t stream) {
    mega<<<256, 512, 0, stream>>>((const u16*)d_in[0], (const u16*)d_in[1], (const u16*)d_in[2], (const u16*)d_in[4], (const u16*)d_in[5],
                                  (u16*)d_out, (char*)d_ws);
}
```

```cpp
#include <hip/hip_runtime.h>
#include <stdint.h>

typedef unsigned short u16;
typedef unsigned long long u64;
typedef __attribute__((ext_vector_type(8))) short bf16x8;
typedef __attribute__((ext_vector_type(4))) short s16x4;
typedef __attribute__((ext_vector_type(4))) float f32x4;
typedef __attribute__((ext_vector_type(16))) float f32x16;
typedef __attribute__((ext_vector_type(4))) unsigned u32x4;
typedef __attribute__((ext_vector_type(2))) unsigned u32x2;
typedef __attribute__((ext_vector_type(2))) __bf16 bf16x2_t;
typedef __attribute__((ext_vector_type(2))) float f32x2;

#define LAS __attribute__((address_space(3)))
#define GAS __attribute__((address_space(1)))
#define DEV __device__ __forceinline__
typedef LAS char lchar;

constexpr int SEQ = 2048, DM = 1024, NH = 32;
constexpr float QSCALE = 0.17677669529663687f, LOG2E = 1.4426950408889634f;
#define POLLMAX (1 << 16)
#define NTX " nt"
#define CT 0
#define CT0 0
#define OQ 1
#define PW 1
#define VF 1
#define RW 0
#define EF 3
#define EO 1
#define EXP 0
#define WS 2
#define HPW 2
#define HPQ 0
#define NP 1
#define PRE 0
#define F0 0
#define FE 5
#define FN 2
#define FD 2
#define FA 4
#define E1 1
#define AW 0
#define PQ 1
#define Q2 1
#define NCK 6
#define RI 4
#define KORD 0xb3fedc7654a98210ull
#define CJ 2
#define CX 1
#define EMU 1

DEV unsigned cvtpk(float lo, float hi) {
    f32x2 v = {lo, hi};
    bf16x2_t b = __builtin_convertvector(v, bf16x2_t);
    return __builtin_bit_cast(unsigned, b);
}
DEV float lo16(unsigned v) { return __uint_as_float(v << 16); }
DEV float hi16(unsigned v) { return __uint_as_float(v & 0xffff0000u); }
DEV float bfr(float f) { return lo16(cvtpk(f, f)); }
DEV u32x2 pk4(f32x4 a) { return (u32x2){cvtpk(a[0], a[1]), cvtpk(a[2], a[3])}; }
DEV bf16x8 cat(u32x2 a, u32x2 b) { return __builtin_bit_cast(bf16x8, (u32x4){a[0], a[1], b[0], b[1]}); }
DEV u32x2 sw16(unsigned a, unsigned b) { auto r = __builtin_amdgcn_permlane16_swap(a, b, false, false); return (u32x2){r[0], r[1]}; }
DEV u32x2 sw32(unsigned a, unsigned b) { auto r = __builtin_amdgcn_permlane32_swap(a, b, false, false); return (u32x2){r[0], r[1]}; }
DEV f32x2 sw16f(float a, float b) { return __builtin_bit_cast(f32x2, sw16(__float_as_uint(a), __float_as_uint(b))); }
DEV f32x2 sw32f(float a, float b) { return __builtin_bit_cast(f32x2, sw32(__float_as_uint(a), __float_as_uint(b))); }
DEV u32x4 unit16(u32x2 a, u32x2 b) {
    const u32x2 x = sw16(a[0], b[0]), y = sw16(a[1], b[1]);
    return (u32x4){x[0], y[0], x[1], y[1]};
}
template <int OFF> DEV void st16wt(const void* p, u32x4 v) {
    asm volatile("global_store_dwordx4 %0, %1, off offset:%2 sc1\n\ts_nop 1" ::"v"(p), "v"(v), "n"(OFF) : "memory");
}
DEV void st8wt(const void* p, u32x2 v) { asm volatile("global_store_dwordx2 %0, %1, off sc1\n\ts_nop 1" ::"v"(p), "v"(v) : "memory"); }
template <int CTRL> DEV float dppadd(float v) {
    return v + __builtin_bit_cast(float, __builtin_amdgcn_update_dpp(0, __builtin_bit_cast(int, v), CTRL, 0xf, 0xf, true));
}
DEV int fswz(int k) { return ((k >> 1) & 1) | (((k >> 3) & 1) << 1); }
DEV int fz(int tok) { return (-(tok >> 2)) & 3; }
DEV s16x4 ldtr4(const lchar* p) { return __builtin_bit_cast(s16x4, __builtin_amdgcn_ds_read_tr16_b64_v4i16((LAS s16x4*)p)); }
DEV bf16x8 cat8(s16x4 a, s16x4 b) { return (bf16x8){a[0], a[1], a[2], a[3], b[0], b[1], b[2], b[3]}; }
DEV bf16x8 ldk(const lchar* p) { return *(const LAS bf16x8*)p; }

#define STAMP(i)
#define STAMPW(i)
#define STPARG
#define STPPASS(o)
#define STAMPB(i)
#define HT0()
#define HT1(i)
#define BAR() asm volatile("s_barrier" ::: "memory")
#define BARL() asm volatile("s_waitcnt lgkmcnt(0)\n\ts_barrier" ::: "memory")
#define WAITB(n) asm volatile("s_waitcnt vmcnt(%0)\n\ts_barrier" ::"n"(n) : "memory")
#define WAITV(n) asm volatile("s_waitcnt vmcnt(%0)" ::"n"(n) : "memory")
#define SB() __builtin_amdgcn_sched_barrier(0)
#define LGKM0() __builtin_amdgcn_s_waitcnt(0xc07f)
#define MFMA32(a, b, c) __builtin_amdgcn_mfma_f32_32x32x16_bf16(a, b, c, 0, 0, 0)
#define MFMA16(a, b, c) __builtin_amdgcn_mfma_f32_16x16x32_bf16(a, b, c, 0, 0, 0)

DEV void mfma16t(f32x4& c, const bf16x8& a, const bf16x8& b) { asm volatile("v_mfma_f32_16x16x32_bf16 %0, %1, %2, %0" : "+v"(c) : "v"(a), "v"(b)); }
#define MFMA_DONE() do { SB(); asm volatile("s_nop 7\n\ts_nop 3"); SB(); } while (0)
DEV float fmx(float a, float b) { return __builtin_elementwise_maximum(a, b); }
DEV float rsd(float var) { return __builtin_amdgcn_rcpf(bfr(__builtin_amdgcn_sqrtf(bfr(bfr(var) + 1e-5f)))); }

constexpr int XS = 18432;
constexpr int WQ0 = 81920, WQS = 24576;
constexpr int EXO = 57344;
constexpr int AR0 = 57344, AR1 = 65536, AR2 = 32768;
constexpr int SNM = 155648, SRS = SNM + 576, SCOL = SNM + 1280, DUMP = SNM + 3072;
constexpr int LDS_BYTES = 163840;
constexpr size_t MB = 1u << 20;
constexpr size_t WS_ST = 0, WS_CS = 3 * MB / 2, WS_FL = 15 * MB / 8, WS_X = 2 * MB, WS_O = 18 * MB, WS_KV = 26 * MB;
constexpr int FL_O = 0;
constexpr int FL_X = 4096;
constexpr int FL_KV = 8192;
constexpr int FL_L = 10240;
constexpr int FL_C = 7168;
constexpr int FL_E = 10496;
constexpr int FL_S = 12288;
constexpr int FL_EP = 16368;
#define FLS 16
#define FS 1
DEV int fi(int i) { return (i >> 4) * FLS + (i & 15) * FS; }

#define GL_HEAD "s_mov_b32 m0, %1\n\ts_nop 4\n\tglobal_load_lds_dwordx4 %2, %0 sc1"
DEV void glds1(const void* sb, unsigned lds, unsigned v0) { asm volatile(GL_HEAD ::"s"(sb), "s"(lds), "v"(v0) : "memory"); }
DEV void glds3(const void* sb, unsigned lds, unsigned v0, unsigned v1, unsigned v2) {
    asm volatile(GL_HEAD "\n\tglobal_load_lds_dwordx4 %3, %0 offset:1024 sc1\n\tglobal_load_lds_dwordx4 %4, %0 offset:2048 sc1"
                 ::"s"(sb), "s"(lds), "v"(v0), "v"(v1), "v"(v2) : "memory");
}
DEV void glds4(const void* sb, unsigned lds, unsigned v0, unsigned v1, unsigned v2, unsigned v3) {
    asm volatile(GL_HEAD "\n\tglobal_load_lds_dwordx4 %3, %0 offset:1024 sc1\n\tglobal_load_lds_dwordx4 %4, %0 offset:2048 sc1\n\t"
                 "global_load_lds_dwordx4 %5, %0 offset:3072 sc1" ::"s"(sb), "s"(lds), "v"(v0), "v"(v1), "v"(v2), "v"(v3) : "memory");
}
DEV void touch(const void* sb, unsigned lds, unsigned v0) {
    asm volatile("s_mov_b32 m0, %1\n\ts_nop 4\n\tglobal_load_lds_dword %2, %0 sc1" ::"s"(sb), "s"(lds), "v"(v0) : "memory");
}
DEV u64 ldep(const u64* p) { return __hip_atomic_load((const GAS u64*)p, __ATOMIC_RELAXED, __HIP_MEMORY_SCOPE_AGENT); }
#define FLD(d, a, z) "global_atomic_add_x2 " d ", " a ", " z ", off sc0 sc1"
DEV u64 ldflag(const u64* p) {
    u64 v;
    asm volatile(FLD("%0", "%1", "%2") "\n\ts_waitcnt vmcnt(0)" : "=&v"(v) : "v"(p), "v"(0ull) : "memory");
    return v;
}
DEV void stflag(u64* p, u64 v) { asm volatile("global_atomic_swap_x2 %0, %1, off sc1\n\ts_nop 1" ::"v"(p), "v"(v) : "memory"); }
template <class F> DEV unsigned poll(const u64* p, bool on, u64 tag, unsigned need, F fold) {
    unsigned m = ~0u;
    for (int it = 0; it < POLLMAX; ++it) {
        u64 v = tag;
        if (on) v = ldflag(p);
        const unsigned r = fold(__ballot(v == tag));
        if ((r & need) == need) { m = r; break; }
        __builtin_amdgcn_s_sleep(1);
    }
    asm volatile("" ::: "memory");
    return m;
}
template <int N = 0> DEV void publish(u64* f, int lane, u64 tag) {
    asm volatile("s_mov_b64 exec, 1\n\ts_waitcnt vmcnt(%2)\n\tglobal_atomic_swap_x2 %0, %1, off sc1\n\ts_mov_b64 exec, -1\n\ts_nop 0" ::"v"(f), "v"(tag), "n"(N) : "memory");
}
DEV void raise(u64* f, u64 tag) {
    asm volatile("s_mov_b64 exec, 1\n\tglobal_atomic_swap_x2 %0, %1, off sc1\n\ts_mov_b64 exec, -1\n\ts_nop 0" ::"v"(f), "v"(tag) : "memory");
}
#define OPAQUE(l, a, b, c) do { asm volatile("" : "+v"(l)); asm volatile("" : "+s"(a), "+s"(b), "+s"(c)); } while (0)

template <bool LOCAL, class F, class G>
DEV void qkv_main(lchar* smem, f32x4 (&acc)[6][4], f32x4 (&acch)[2], int lane, int wm, int wn, G mid, F tailread STPARG) {
    constexpr int XC_B = LOCAL ? 9216 : 8192, HO = LOCAL ? 1024 : 0, NR = LOCAL ? 17 : 16;
    const int l15 = lane & 15, lg = lane >> 4, hs = LOCAL ? wm : 0;
    const lchar* xhb = smem + l15 * 64 + ((lg ^ fz(l15)) << 4);
    const lchar* xb = xhb + HO + wm * 4096;
    const int k0 = 8 * lg + (l15 >> 2);
    const lchar* wb0 = smem + WQ0 + lg * 3072 + (l15 >> 2) * 128 + (((wn * 2 + hs) ^ fswz(k0)) << 5) + (l15 & 3) * 8;
    const lchar* wb1 = smem + WQ0 + lg * 3072 + (l15 >> 2) * 128 + (((wn * 2 + (hs ^ 1)) ^ fswz(k0)) << 5) + (l15 & 3) * 8;
#pragma unroll
    for (int a = 0; a < 6; ++a)
#pragma unroll
        for (int b = 0; b < 4; ++b) { acc[a][b] = (f32x4){0.f, 0.f, 0.f, 0.f}; asm volatile("" : "+v"(acc[a][b])); }
    acch[0] = acch[1] = (f32x4){0.f, 0.f, 0.f, 0.f};
    if (LOCAL) asm volatile("" : "+v"(acch[0]), "+v"(acch[1]), "+v"(xhb));
    asm volatile("" : "+v"(xb), "+v"(wb0), "+v"(wb1));
    bf16x8 xf[2][5];
    s16x4 wh[2][6][2];
    auto rd = [&](int b, int j, int xo, int wo) {
        if (j < 4) xf[b][j] = ldk(xb + xo + j * 1024);
        else if (j < 16) wh[b][(j - 4) >> 1][j & 1] = ldtr4(((j & 2) ? wb1 : wb0) + wo + ((j - 4) >> 2) * 1024 + (j & 1) * 512);
        else xf[b][4] = ldk(xhb + xo);
    };
    auto kstep = [&](int b, int xo, int wo, bool last) {
        int j = 0;
#pragma unroll
        for (int ni = 0; ni < 6; ++ni) {
            const bf16x8 w = cat8(wh[b][ni][0], wh[b][ni][1]);
#pragma unroll
            for (int ti = 0; ti < 5; ++ti) {
                if (ti == 4) {
                    if (!LOCAL || (ni != 2 && ni != 4)) continue;
                    if (ni == 2) mfma16t(acch[0], w, xf[b][4]);
                    else mfma16t(acch[1], xf[b][4], w);
                } else {
                    if (ni >= 4) mfma16t(acc[ni][ti], xf[b][ti], w);
                    else mfma16t(acc[ni][ti], w, xf[b][ti]);
                }
                SB();
                if (last) tailread(j);
                else if (j < NR) rd(b ^ 1, j, xo, wo);
                SB();
                ++j;
            }
        }
        if (!last) { LGKM0(); SB(); }
    };
    BAR(); SB();
    STAMP(4);
#pragma unroll
    for (int j = 0; j < NR; ++j) rd(0, j, 0, 2 * WQS);
    LGKM0(); SB();
#pragma unroll 1
    for (int it = 0; it < 5; ++it) {
        if (RI < 5) mid(it);
#pragma unroll
        for (int s = 0; s < 3; ++s) {
            kstep(0, s * XS + XC_B, ((s + 2) % 3) * WQS + WQS / 2, false);
            BAR(); SB();
            STAMPB(it * 3 + s);
            kstep(1, ((s + 1) % 3) * XS, s * WQS, false);
        }
    }
    if (RI == 5) mid(5);
    kstep(0, XC_B, 2 * WQS + WQS / 2, false);
    BAR(); SB();
    if (RI == 6) mid(6);
    STAMPB(15);
    kstep(1, 0, 0, true);
    MFMA_DONE();
}

constexpr int NRW = 7;
typedef s16x4 WReg[NRW * 8];
DEV const lchar* wo_wb(const lchar* smem, int lane, int c) {
    const int k0 = 8 * (lane >> 4) + ((lane & 15) >> 2);
    return smem + k0 * 128 + ((c ^ fswz(k0)) << 5) + (lane & 3) * 8;
}
template <int N0, int N1, int V> DEV void wo_pre(WReg& w, const lchar* b0, const lchar* b1) {
#pragma unroll
    for (int j = N0; j < N1; ++j) w[j] = ldtr4(((j & 2) ? b1 : b0) + (j >> 2) * 4096 + (j & 1) * 512);
    if (V) {
#pragma unroll
        for (int j = N0; j < N1; ++j) { __builtin_amdgcn_sched_group_barrier(0x100, 1, 0); __builtin_amdgcn_sched_group_barrier(0x402, V, 0); }
    }
}

DEV void qa_compute(lchar* smem, bool fold, const u16* xin, u32x4 (&rr)[4], WReg& wr, u16* O, u64* fo, u64 tag, int g, int hp, int lane, int wid STPARG) {
    OPAQUE(lane, wid, g, hp);
    const int wm = wid >> 1, wn = wid & 1, l15 = lane & 15, lg = lane >> 4;
    f32x4 acc[6][4], acch[2];
    float nm[5], rs[5], csv[2];
    f32x4 nmv[5], rsv[5], cs[4];
    const lchar* sa = smem + SNM + l15 * 4;
    const lchar* sb = smem + SNM + lg * 16;
    const lchar* sc = smem + SCOL + wn * 128;
    const int ro = 64 + wm * 256;
    f32x4 cprev, ccur;
#pragma unroll
    for (int j = 0; j < 4; ++j) {
        const int d = l15 - 4 * lg - j;
        ccur[j] = (d >= 0 && !(d & 1)) ? 0.f : -3.0e38f;
        cprev[j] = (d <= -2 && !(d & 1)) ? 0.f : -3.0e38f;
    }
    const char* optr = (const char*)O + ((size_t)wn * SEQ + g * 128 + wm * 64 + l15) * 64 +
                       (((((lg & 1) << 1) | (lg >> 1)) ^ (wm << 1) ^ fz(l15)) << 4);
    unsigned xo = rr[0][0];
    asm volatile("" : "+v"(cprev), "+v"(ccur), "+v"(optr), "+v"(xo));
    qkv_main<true>(smem, acc, acch, lane, wm, wn, [&](int it) {
        asm volatile("s_cmp_lg_u32 %6, %7\n\ts_cbranch_scc1 1f\n\tglobal_load_dwordx4 %0, %4, %5\n\tv_add_u32 %4, 0x8000, %4\n\t"
                     "global_load_dwordx4 %1, %4, %5\n\tv_add_u32 %4, 0x8000, %4\n\tglobal_load_dwordx4 %2, %4, %5\n\tv_add_u32 %4, 0x8000, %4\n\t"
                     "global_load_dwordx4 %3, %4, %5\n1:" : "+v"(rr[0]), "+v"(rr[1]), "+v"(rr[2]), "+v"(rr[3]), "+v"(xo)
                     : "s"(xin), "s"(it), "s"(fold ? -1 : RI) : "memory", "scc");
    }, [&](int j) {
        if (j < 4) cs[j] = *(const LAS f32x4*)(sc + (j >> 1) * 256 + (((j & 1) ^ wm) << 6) + lg * 16);
        else if (j < 9) { const int o = j < 8 ? ro + (j - 4) * 64 : 0; nm[j - 4] = *(const LAS float*)(sa + o); rs[j - 4] = *(const LAS float*)(sa + o + 576); }
        else if (j < 11) csv[j - 9] = *(const LAS float*)(sc + 512 + (((j - 9) ^ wm) << 6) + l15 * 4);
        else if (j < 21) {
            const int t = (j - 11) >> 1, o = t < 4 ? ro + t * 64 : 0;
            if ((j - 11) & 1) rsv[t] = *(const LAS f32x4*)(sb + o + 576);
            else nmv[t] = *(const LAS f32x4*)(sb + o);
        }
    } STPPASS(0));
    STAMP(2);
    if (fold) {
#pragma unroll
        for (int ti = 0; ti < 4; ++ti) {
#pragma unroll
            for (int ni = 0; ni < 4; ++ni) acc[ni][ti] = (acc[ni][ti] + nm[ti] * cs[ni]) * rs[ti];
#pragma unroll
            for (int h = 0; h < 2; ++h) acc[4 + h][ti] = (acc[4 + h][ti] + nmv[ti] * csv[h]) * rsv[ti];
        }
        acch[0] = (acch[0] + nm[4] * cs[2]) * rs[4];
        acch[1] = (acch[1] + nmv[4] * csv[0]) * rsv[4];
    }
    u32x2 kp[5][2], vp[5][2];
    lchar* ex = smem + EXO + wn * 4096 + lane * 16;
    {
        const u32x2 a = pk4(acch[0]), b = pk4(acch[1]);
        *(LAS u32x4*)(ex + wm * 1024) = (u32x4){a[0], a[1], b[0], b[1]};
    }
#pragma unroll
    for (int h = 0; h < 2; ++h) { kp[4][h] = pk4(acc[2 + h][3]); vp[4][h] = pk4(acc[4 + h][3]); }
    if (wm == 0) {
#pragma unroll
        for (int h = 0; h < 2; ++h) *(LAS u32x4*)(ex + 2048 + h * 1024) = (u32x4){kp[4][h][0], kp[4][h][1], vp[4][h][0], vp[4][h][1]};
    }
    bf16x8 qf[4];
#pragma unroll
    for (int ti = 0; ti < 4; ++ti) {
        qf[ti] = cat(pk4(acc[0][ti]), pk4(acc[1][ti]));
        if (ti < 3) {
#pragma unroll
            for (int h = 0; h < 2; ++h) { kp[ti + 1][h] = pk4(acc[2 + h][ti]); vp[ti + 1][h] = pk4(acc[4 + h][ti]); }
        }
    }
    const lchar* wb0 = wo_wb(smem, lane, wn * 2);
    const lchar* wb1 = wo_wb(smem, lane, wn * 2 + 1);
    asm volatile("" : "+v"(wb0), "+v"(wb1));
    SB();
    BARL();
#pragma unroll
    for (int h = 0; h < 2; ++h) {
        const u32x4 e = *(const LAS u32x4*)(ex + wm * 2048 + ((h ^ wm) << 10));
        kp[0][h] = (u32x2){e[0], e[1]}; vp[0][h] = (u32x2){e[2], e[3]};
    }
    SB();
    if (RW) wo_pre<0, 28, 4>(wr, wb0, wb1);
    f32x4 cp0 = cprev;
    if (g == 0 && wm == 0) cp0 = (f32x4){-3.0e38f, -3.0e38f, -3.0e38f, -3.0e38f};
    f32x4 sv[4];
    float mx[4];
#pragma unroll
    for (int t = 0; t < 4; ++t) {
        const int ti = 3 - t;
        const f32x4 s1 = MFMA16(cat(kp[ti + 1][0], kp[ti + 1][1]), qf[ti], ccur);
        const f32x4 s0 = MFMA16(cat(kp[ti][0], kp[ti][1]), qf[ti], ti ? cprev : cp0);
        f32x4 v = {fmx(s0[0], s0[1]), fmx(s0[2], s0[3]), fmx(s1[0], s1[1]), fmx(s1[2], s1[3])};
        if (EMU) {
            const u32x2 r1 = pk4(v);
            const u32x2 r2 = pk4((f32x4){lo16(r1[0]), hi16(r1[0]), lo16(r1[1]), hi16(r1[1])} * QSCALE);
            v = (f32x4){lo16(r2[0]), hi16(r2[0]), lo16(r2[1]), hi16(r2[1])};
        }
        sv[ti] = v;
        mx[ti] = fmx(fmx(fmx(v[0], v[1]), v[2]), v[3]);
    }
    {
        const f32x2 a = sw16f(mx[0], mx[1]), b = sw16f(mx[2], mx[3]);
        const f32x2 c = sw32f(fmx(a[0], a[1]), fmx(b[0], b[1]));
        const float r = fmx(c[0], c[1]) * -(EMU ? LOG2E : QSCALE * LOG2E);
        const f32x2 d = sw32f(r, r), e = sw16f(d[0], d[0]), f = sw16f(d[1], d[1]);
        mx[0] = e[0]; mx[1] = e[1]; mx[2] = f[0]; mx[3] = f[1];
    }
    SB();
    if ((EF & 1) && EO == 1) { raise(fo, tag); SB(); }
    if (RW) wo_pre<28, 56, 4>(wr, wb0, wb1);
    const unsigned sel0 = (lane & 1) ? 0x01000c0cu : 0x0c0c0100u, sel1 = (lane & 1) ? 0x03020c0cu : 0x0c0c0302u;
    const bf16x8 ones = {0x3f80, 0x3f80, 0x3f80, 0x3f80, 0x3f80, 0x3f80, 0x3f80, 0x3f80};
    const f32x4 z = {0.f, 0.f, 0.f, 0.f};
    u32x4 ov[4];
#pragma unroll
    for (int ti = 0; ti < 4; ++ti) {
        const f32x4 x = sv[ti] * (EMU ? LOG2E : QSCALE * LOG2E) + mx[ti];
        const unsigned c0 = cvtpk(__builtin_amdgcn_exp2f(x[0]), __builtin_amdgcn_exp2f(x[1]));
        const unsigned c1 = cvtpk(__builtin_amdgcn_exp2f(x[2]), __builtin_amdgcn_exp2f(x[3]));
        const bf16x8 p = __builtin_bit_cast(bf16x8, (u32x4){__builtin_amdgcn_perm(c0, c0, sel0), __builtin_amdgcn_perm(c0, c0, sel1),
                                                            __builtin_amdgcn_perm(c1, c1, sel0), __builtin_amdgcn_perm(c1, c1, sel1)});
        const f32x4 l = MFMA16(ones, p, z);
        const f32x4 o0 = MFMA16(cat(vp[ti][0], vp[ti + 1][0]), p, z);
        const f32x4 o1 = MFMA16(cat(vp[ti][1], vp[ti + 1][1]), p, z);
        const float inv = __builtin_amdgcn_rcpf(l[0]);
        ov[ti] = unit16(pk4(o0 * inv), pk4(o1 * inv));
    }
    st16wt<0>(optr, ov[0]); st16wt<1024>(optr, ov[1]); st16wt<2048>(optr, ov[2]); st16wt<3072>(optr, ov[3]);
    if ((EF & 1) && EO == 0) raise(fo, tag);
    STAMP(10);
    if (!RW) { SB(); wo_pre<0, 56, 0>(wr, wb0, wb1); }
}

#define MX3(a, b, c) fmx(fmx((a), (b)), (c))
DEV float rowmax32(const f32x16& s0) {
    float ma = MX3(s0[0], s0[1], s0[2]), mb = MX3(s0[3], s0[4], s0[5]);
    ma = MX3(ma, s0[6], s0[7]); mb = MX3(mb, s0[8], s0[9]);
    ma = MX3(ma, s0[10], s0[11]); mb = MX3(mb, s0[12], s0[13]);
    ma = fmx(MX3(ma, s0[14], s0[15]), mb);
    const f32x2 b = sw32f(ma, ma);
    return fmx(b[0], b[1]);
}
#define EX(i) do { S[i] = __builtin_amdgcn_exp2f(S[i]); SB(); } while (0)
#define EQ(i) do { p1[i] = __builtin_amdgcn_exp2f(S[8 + i]); SB(); } while (0)
typedef __attribute__((ext_vector_type(16))) __bf16 bf16x16_t;
typedef __attribute__((ext_vector_type(8))) __bf16 bf16x8_t;
typedef __attribute__((ext_vector_type(8))) float f32x8;
template <int H> DEV f32x8 half8(const f32x16& s) { return __builtin_shufflevector(s, s, H, H + 1, H + 2, H + 3, H + 4, H + 5, H + 6, H + 7); }
#define SMF32(a, b, c) __builtin_amdgcn_smfmac_f32_32x32x32_bf16(__builtin_bit_cast(bf16x8_t, a), __builtin_bit_cast(bf16x16_t, b), c, hi, 0, 0)
#define SMF16(a, b, c) __builtin_amdgcn_smfmac_f32_16x16x64_bf16(__builtin_bit_cast(bf16x8_t, a), __builtin_bit_cast(bf16x16_t, b), c, hi, 0, 0)
template <class G0, class F1, class F2, class F3>
DEV void block(f32x16& S, f32x16& N, const bf16x8& kf0, const bf16x8& kf1, const bf16x8& q0, const bf16x8& q1, const f32x16& c,
               f32x16& oP, f32x4& lP, const bf16x8& vf1, f32x8& p1, f32x16& oC, f32x4& lC, const bf16x8& vf0,
               const bf16x8& sel, int hi, G0 fb, F1 fc, F2 fd, F3 ff) {
    N = MFMA32(kf0, q0, c); SB();
    EX(0); EX(1); EX(2);
    oP = SMF32(vf1, p1, oP); SB();
    fb(); SB();
    EX(3); EX(4); EX(5);
    lP = SMF16(sel, p1, lP); SB();
    fc(); SB();
    EX(6);
    N = MFMA32(kf1, q1, N); SB();
    fd(); SB();
    EX(7); EQ(0); EQ(1);
    oC = SMF32(vf0, half8<0>(S), oC); SB();
    EQ(2); EQ(3); EQ(4);
    lC = SMF16(sel, half8<0>(S), lC); SB();
    ff(); SB();
    EQ(5); EQ(6); EQ(7);
}
DEV void attn_slow(const char* kh, const char* vh, const bf16x8& q0, const bf16x8& q1, f32x16& o, float& l, int hi) {
    f32x16 c;
#pragma unroll
    for (int i = 0; i < 16; ++i) { o[i] = 0.f; c[i] = 0.f; }
    float m = 0.f;
    l = 0.f;
#pragma unroll 1
    for (int t = 0; t < SEQ / 32; ++t) {
        bf16x8 k0, k1, v0, v1;
        const char* kp = kh + t * 2048;
        const char* vp = vh + t * 2048;
        asm volatile("global_load_dwordx4 %0, %4, off sc1\n\tglobal_load_dwordx4 %1, %5, off sc1\n\tglobal_load_dwordx4 %2, %6, off sc1\n\t"
                     "global_load_dwordx4 %3, %7, off sc1\n\ts_waitcnt vmcnt(0)" : "=&v"(k0), "=&v"(k1), "=&v"(v0), "=&v"(v1)
                     : "v"(kp), "v"((const char*)((uintptr_t)kp ^ 32)), "v"(vp), "v"((const char*)((uintptr_t)vp ^ 32)) : "memory");
        f32x16 s = MFMA32(k0, q0, c);
        s = MFMA32(k1, q1, s);
        const float mx = rowmax32(s);
        const float dl = t == 0 ? mx : fmx(mx, 0.f);
        const float alpha = t == 0 ? 0.f : __builtin_amdgcn_exp2f(-dl);
        m += dl;
        l *= alpha;
#pragma unroll
        for (int i = 0; i < 16; ++i) {
            o[i] *= alpha; c[i] = -m;
            s[i] = __builtin_amdgcn_exp2f(s[i] - dl);
            l += hi16(__float_as_uint(s[i]));
        }
        o = SMF32(v0, half8<0>(s), o);
        o = SMF32(v1, half8<8>(s), o);
    }
    const f32x2 b = sw32f(l, l);
    l = b[0] + b[1];
}

DEV void qb_compute(lchar* smem, u16* kv, WReg& wr, u16* O, u64* fo, u64* fkv, u64 tag, int g, int hp, int lane, int wid STPARG) {
    OPAQUE(lane, wid, g, hp);
    const int wm = wid >> 1, wn = wid & 1, l15 = lane & 15, lg = lane >> 4, h = hp * 2 + wn;
    bf16x8 qa0, qa1, qb0, qb1;
    lchar* gs = smem + AR0 + wn * 16384;
    {
        f32x4 acc[6][4], acch[2];
        float nm[4], rs[4], csv[2];
        f32x4 cs[4], nmv[4], rsv[4];
        const lchar* sa = smem + SNM + 64 + wm * 256 + l15 * 4;
        const lchar* sb = smem + SNM + 64 + wm * 256 + lg * 16;
        const lchar* sc = smem + SCOL + wn * 128;
        const int un = ((lg & 1) << 1) | (lg >> 1);
        const int ko = l15 * 64 + ((un ^ (l15 >> 2)) << 4), vo = l15 * 64 + ((lg ^ (l15 >> 2)) << 4);
        const char* kptr = (const char*)kv + ((size_t)h * SEQ + g * 128 + wm * 64) * 64 + ko;
        const char* vptr = (const char*)kv + ((size_t)(NH + h) * SEQ + g * 128 + wm * 64) * 64 + vo;
        lchar* kl = gs + wm * 8192 + ko;
        lchar* vl = gs + wm * 8192 + 4096 + vo;
        asm volatile("" : "+v"(kptr), "+v"(vptr));
        qkv_main<false>(smem, acc, acch, lane, wm, wn, [](int) {}, [&](int j) {
            if (j < 4) cs[j] = *(const LAS f32x4*)(sc + (j >> 1) * 256 + (j & 1) * 64 + lg * 16);
            else if (j < 8) { nm[j - 4] = *(const LAS float*)(sa + (j - 4) * 64); rs[j - 4] = *(const LAS float*)(sa + (j - 4) * 64 + 576); }
            else if (j < 10) csv[j - 8] = *(const LAS float*)(sc + 512 + (j - 8) * 64 + l15 * 4);
            else if (j < 18) {
                if (j & 1) rsv[(j - 10) >> 1] = *(const LAS f32x4*)(sb + ((j - 10) >> 1) * 64 + 576);
                else nmv[(j - 10) >> 1] = *(const LAS f32x4*)(sb + ((j - 10) >> 1) * 64);
            }
        } STPPASS(0));
        STAMP(2);
        auto tile = [&](int ni, int ti, float sc_) { return pk4((acc[ni][ti] + nm[ti] * cs[ni]) * sc_); };
        auto tilev = [&](int h_, int ti) { return pk4((acc[4 + h_][ti] + nmv[ti] * csv[h_]) * rsv[ti]); };
#define KOUT(ti) do { const u32x4 v = unit16(tile(2, ti, rs[ti]), tile(3, ti, rs[ti])); st16wt<ti * 1024>(kptr, v); *(LAS u32x4*)(kl + ti * 1024) = v; } while (0)
#define VOUT(j) do { const u32x4 v = __builtin_bit_cast(u32x4, cat(tilev(j & 1, j & 2), tilev(j & 1, (j & 2) + 1))); st16wt<j * 1024>(vptr, v); *(LAS u32x4*)(vl + j * 1024) = v; } while (0)
        KOUT(0); KOUT(1); KOUT(2); KOUT(3);
        VOUT(0); VOUT(1); VOUT(2); VOUT(3);
        if (EF & 4) raise(fkv + fi(wn * 32 + wm * 16 + g), tag);
        SB(); BARL(); SB();
#pragma unroll
        for (int ti = 0; ti < 4; ++ti) rs[ti] *= QSCALE * LOG2E;
        qa0 = __builtin_bit_cast(bf16x8, unit16(tile(0, 0, rs[0]), tile(0, 1, rs[1])));
        qa1 = __builtin_bit_cast(bf16x8, unit16(tile(1, 0, rs[0]), tile(1, 1, rs[1])));
        qb0 = __builtin_bit_cast(bf16x8, unit16(tile(0, 2, rs[2]), tile(0, 3, rs[3])));
        qb1 = __builtin_bit_cast(bf16x8, unit16(tile(1, 2, rs[2]), tile(1, 3, rs[3])));
    }
    STAMP(3);
    const int r = lane & 31, hh = lane >> 5;
    const int kro = ((r & 4) * 4 + (r >> 3) * 4 + (r & 3)) * 64 + ((hh ^ (r >> 3)) << 4), vro = r * 64 + ((hh ^ ((r >> 2) & 3)) << 4);
    const lchar* kb0 = gs + kro;
    const lchar* kb1 = gs + (kro ^ 32);
    const lchar* vb0 = gs + 4096 + vro;
    const lchar* vb1 = gs + 4096 + (vro ^ 32);
    const short se = (l15 & 1) ? (short)0 : (short)0x3f80, so = se ^ 0x3f80;
    const bf16x8 sel = {se, se, se, se, so, so, so, so};
    int hi = 0xdddd;
    asm volatile("" : "+v"(hi));
    f32x16 oA, oB, cA, cB, sA, sB;
    f32x4 lA = {0.f, 0.f, 0.f, 0.f}, lB = {0.f, 0.f, 0.f, 0.f};
#pragma unroll
    for (int i = 0; i < 16; ++i) { oA[i] = 0.f; oB[i] = 0.f; }
    bf16x8 kf0, kf1, vf0, vf1[2];
    f32x8 p1 = {0.f, 0.f, 0.f, 0.f, 0.f, 0.f, 0.f, 0.f};
    auto nop = [&]() {};
    auto tilestep = [&](int par, int nx, bool bar) {
        block(sA, sB, kf0, kf1, qb0, qb1, cB, oB, lB, vf1[par ^ 1], p1, oA, lA, vf0, sel, hi, [&]() { if (bar) BAR(); },
              [&]() { kf0 = ldk(kb0 + nx); }, [&]() { __builtin_amdgcn_s_waitcnt(0xc17f); }, [&]() { LGKM0(); SB(); kf1 = ldk(kb1 + nx); });
        block(sB, sA, kf0, kf1, qa0, qa1, cA, oA, lA, vf1[par], p1, oB, lB, vf0, sel, hi, nop,
              [&]() { LGKM0(); SB(); vf1[par ^ 1] = ldk(vb1 + nx); }, nop, [&]() { vf0 = ldk(vb0 + nx); });
    };
    {
        const int own = wm * 8192, oth = own ^ 8192;
        kf0 = ldk(kb0 + own); kf1 = ldk(kb1 + own);
        vf0 = ldk(vb0 + own); vf1[0] = ldk(vb1 + own); vf1[1] = sel;
        asm volatile("" : "+v"(qa0), "+v"(qa1), "+v"(qb0), "+v"(qb1)); SB();
        if (!(EF & 4)) publish(fkv + fi(wn * 32 + wm * 16 + g), lane, tag);
        SB();
        sA = MFMA32(kf0, qa0, oA); sB = MFMA32(kf0, qb0, oA);
        sA = MFMA32(kf1, qa1, sA); sB = MFMA32(kf1, qb1, sB);
        const float mA = rowmax32(sA), mB = rowmax32(sB);
#pragma unroll
        for (int i = 0; i < 16; ++i) { cA[i] = -mA; cB[i] = -mB; sA[i] -= mA; }
        asm volatile("" : "+v"(cA), "+v"(cB));
        SB();
        STAMP(5);
        tilestep(0, own + 2048, false);
        tilestep(1, oth, false);
        tilestep(0, oth + 2048, false);
        tilestep(1, AR1 + 8192, true);
        STAMP(6);
    }
#pragma unroll 1
    for (int it = 0; it < 5; ++it) {
#pragma unroll
        for (int s = 0; s < 3; ++s) {
            const int cur = s == 0 ? AR1 : s == 1 ? AR2 : 0, nxt = s == 0 ? AR2 : s == 1 ? 0 : AR1;
            tilestep(0, cur + 10240, false);
            tilestep(1, cur, false);
            tilestep(0, cur + 2048, false);
            tilestep(1, nxt + 8192, true);
        }
    }
    oB = SMF32(vf1[1], p1, oB);
    lB = SMF16(sel, p1, lB);
    STAMP(9);
    float la = (lane & 16) ? lA[1] : lA[0], lb = (lane & 16) ? lB[1] : lB[0];
    if (__any(!(la <= 1073741824.0f) || !(lb <= 1073741824.0f))) {
        const char* kh = (const char*)kv + (size_t)h * SEQ * 64;
#pragma unroll 1
        for (int ps = 0; ps < 2; ++ps) {
            oA = oB; la = lb;
            attn_slow(kh + kro, kh + (size_t)NH * SEQ * 64 + vro, ps ? qb0 : qa0, ps ? qb1 : qa1, oB, lb, hi);
        }
    }
    if ((EF & 1) && EO == 1) { SB(); raise(fo, tag); SB(); }
    const char* op = (const char*)O + ((size_t)wn * SEQ + g * 128 + wm * 64 + l15) * 64 + (((((lg & 1) << 1) | (lg >> 1)) ^ fz(l15)) << 4);
    auto fin = [&](const f32x16& o, float l, u32x4& u0, u32x4& u1) {
        const f32x16 y = o * __builtin_amdgcn_rcpf(l);
        const u32x2 a = sw32(cvtpk(y[0], y[1]), cvtpk(y[4], y[5])), b = sw32(cvtpk(y[2], y[3]), cvtpk(y[6], y[7]));
        const u32x2 c = sw32(cvtpk(y[8], y[9]), cvtpk(y[12], y[13])), d = sw32(cvtpk(y[10], y[11]), cvtpk(y[14], y[15]));
        const u32x2 e = sw16(a[0], c[0]), f = sw16(b[0], d[0]), i = sw16(a[1], c[1]), j = sw16(b[1], d[1]);
        u0 = (u32x4){e[0], f[0], i[0], j[0]};
        u1 = (u32x4){e[1], f[1], i[1], j[1]};
    };
    u32x4 u0, u1;
    const lchar* wb0 = wo_wb(smem, lane, wn * 2);
    const lchar* wb1 = wo_wb(smem, lane, wn * 2 + 1);
    if (RW) wo_pre<0, 28, 2>(wr, wb0, wb1);
    fin(oA, la, u0, u1);
    st16wt<0>(op, u0); st16wt<1024>(op, u1);
    if (RW) wo_pre<28, 56, 1>(wr, wb0, wb1);
    fin(oB, lb, u0, u1);
    st16wt<2048>(op, u0); st16wt<3072>(op, u1);
    if ((EF & 1) && EO == 0) raise(fo, tag);
    STAMP(10);
    if (!RW) { SB(); wo_pre<0, 56, 0>(wr, wb0, wb1); }
}

DEV constexpr int ko(int s) { return (int)(KORD >> (4 * s)) & 15; }
constexpr u64 kinv() { u64 r = 0; for (int s = 0; s < 16; ++s) r |= (u64)s << (4 * ((KORD >> (4 * s)) & 15)); return r; }
DEV constexpr unsigned wo_half(int i) { return (unsigned)(ko(i >> 1) * 2 + (i & 1)) * (32 * 2048); }
DEV constexpr int wo_unit(int t) { return t >= 7 && t < 10 ? t + 9 : t; }
DEV unsigned wo_voff(int lane, int cb, int i) {
    const int k = 8 * i + (lane >> 3), pc = lane & 7;
    return (k * 1024 + cb * 64 + ((((pc >> 1) ^ fswz(k)) << 1) + (pc & 1)) * 8) * 2 - i * 1024;
}
DEV void wo_compute(lchar* smem, u64* fo, u32x4 (&rr)[4], WReg& wr, u16* C, char* stats, u64* fx, u64 tag, int g, int cb, int lane,
                    int wid STPARG) {
    OPAQUE(lane, wid, g, cb);
    const int wm = wid >> 1, wn = wid & 1, l15 = lane & 15, lg = lane >> 4;
    const lchar* wb0 = wo_wb(smem + WQ0, lane, wn * 2);
    const lchar* wb1 = wo_wb(smem + WQ0, lane, wn * 2 + 1);
    asm volatile("" : "+v"(wb0), "+v"(wb1));
    bf16x8 xf[2][8];
    s16x4 wh[16][8];
    auto rdw = [&](int t, int j) { wh[t][j] = ldtr4(((j & 2) ? wb1 : wb0) + wo_unit(t) * 8192 - WQ0 + (j >> 2) * 4096 + (j & 1) * 512); };
#pragma unroll
    for (int j = 0; j < NRW * 8; ++j) wh[j >> 3][j & 7] = wr[j];
    if (!(EF & 1)) publish(fo, lane, tag); else WAITV(5);
    STAMP(5);
    STAMPW(6 + wid);
    asm volatile("" : "+v"(lane), "+v"(rr[0]), "+v"(rr[1]), "+v"(rr[2]), "+v"(rr[3]));
    const int un = ((lg & 1) << 1) | (lg >> 1), row = g * 128 + wm * 64 + l15, ch = cb * 2 + wn;
    const unsigned ocm = (ch * SEQ + row) * 64 + ((un ^ fz(l15)) << 4), orm = row * 2048 + ch * 64 + un * 16;
    const char* cp = (const char*)C + (stats ? ocm : orm);
    const unsigned cs = stats ? 1024 : 32768;
    const char* sp = stats + ((g * 2 + wm) * 32 + ch) * 512 + lane * 8;
    const lchar* xb = smem + (wm * 64 + l15) * 64 + ((lg ^ fz(l15)) << 4);
    asm volatile("" : "+v"(cp), "+v"(sp), "+v"(xb));
    f32x4 acc[2][4];
#pragma unroll
    for (int ti = 0; ti < 4; ++ti) {
        const u32x2 x = sw16(rr[ti][0], rr[ti][2]), y = sw16(rr[ti][1], rr[ti][3]);
#pragma unroll
        for (int ni = 0; ni < 2; ++ni) acc[ni][ti] = (f32x4){lo16(x[ni]), hi16(x[ni]), lo16(y[ni]), hi16(y[ni])};
    }
    auto rdx = [&](int t, int j) { xf[t & 1][j] = ldk(xb + ((t + 4) % 5) * 16384 + (j >> 2) * 8192 + (j & 3) * 1024); };
    auto mm = [&](int t, int kk, int j) {
        const int ti = j >> 1, ni = j & 1;
        mfma16t(acc[ni][ti], cat8(wh[t][kk * 4 + ni * 2], wh[t][kk * 4 + ni * 2 + 1]), xf[t & 1][kk * 4 + ti]);
    };
    SB();
    LGKM0(); SB();
    BAR();
#pragma unroll
    for (int t = 0; t < 16; ++t) {
        BAR(); SB();
        if ((EF & 2) && EXP && t == 16 - EXP && stats) { raise(fx + fi((wn * 32 + g * 2 + wm) * 16 + cb), tag); SB(); }
        STAMPB(t);
        if (t == 0) {
            STAMP(4);
#pragma unroll
            for (int j = 0; j < 8; ++j) rdx(0, j);
            SB();
        } else {
#pragma unroll
            for (int j = 0; j < 8; ++j) { rdx(t, j); SB(); mm(t - 1, 1, j); SB(); }
        }
        const bool more = t + 1 >= NRW && t < 15;
        if (WS) {
            __builtin_amdgcn_s_waitcnt(0xc47f); SB();
#pragma unroll
            for (int j = 0; j < 8; ++j) {
                if (more && (WS == 1 || j)) { rdw(t + 1, WS == 1 ? j : j - 1); SB(); }
                if (more && WS == 2 && j == 7) { rdw(t + 1, 7); SB(); }
                mm(t, 0, j); SB();
            }
        } else
#pragma unroll
        for (int j = 0; j < 8; ++j) { mm(t, 0, j); SB(); if (more) { rdw(t + 1, j); SB(); } }
        if (more) __builtin_amdgcn_s_waitcnt(0xc87f); else LGKM0();
        SB();
    }
    u32x4 (&ov)[4] = rr;
    auto out = [&](int ti) {
        asm volatile("" : "+v"(acc[0][ti]), "+v"(acc[1][ti]));
        ov[ti] = unit16(pk4(acc[0][ti]), pk4(acc[1][ti]));
        asm volatile("global_store_dwordx4 %0, %1, off sc1\n\ts_nop 1" ::"v"(cp + ti * cs), "v"(ov[ti]) : "memory");
        SB();
    };
#pragma unroll
    for (int j = 0; j < 6; ++j) mm(15, 1, j);
    SB();
    out(0);
    mm(15, 1, 6); mm(15, 1, 7); SB();
    out(1); out(2); out(3);
    STAMP(2);
    if (stats) {
        if ((EF & 2) && !EXP) { raise(fx + fi((wn * 32 + g * 2 + wm) * 16 + cb), tag); SB(); }
        float s[4], q[4];
        const bf16x2_t one2 = __builtin_bit_cast(bf16x2_t, 0x3f803f80u);
#pragma unroll
        for (int ti = 0; ti < 4; ++ti) {
            s[ti] = 0.f; q[ti] = 0.f;
#pragma unroll
            for (int e = 0; e < 4; ++e) {
                const unsigned u = ov[ti][e];
                const bf16x2_t v = __builtin_bit_cast(bf16x2_t, u);
                s[ti] = __builtin_amdgcn_fdot2_f32_bf16(v, one2, s[ti], false);
                q[ti] = __builtin_amdgcn_fdot2_f32_bf16(v, v, q[ti], false);
            }
        }
        auto red = [&](const float (&v)[4]) {
            const f32x2 a = sw16f(v[0], v[1]), b = sw16f(v[2], v[3]);
            const f32x2 c = sw32f(a[0] + a[1], b[0] + b[1]);
            return c[0] + c[1];
        };
        st8wt(sp, (u32x2){__float_as_uint(red(s)), __float_as_uint(red(q))});
        STAMP(10);
        if (!(EF & 2)) publish<1>(fx + fi((wn * 32 + g * 2 + wm) * 16 + cb), lane, tag);
        STAMP(11);
        STAMPW(12 + wid);
        publish<(EF & 2) ? 0 : 1>(fx + fi(FL_S - FL_X + (wn * 32 + g * 2 + wm) * 16 + cb), lane, tag);
    } else {
        STAMP(10);
    }
}

DEV void ln0_rows(u32x4 (&x)[4]) {
    auto sum32 = [&](float v) {
        v = dppadd<0x128>(dppadd<0x4E>(dppadd<0xB1>(v)));
        const f32x2 a = sw16f(v, v);
        v = a[0] + a[1];
        const f32x2 b = sw32f(v, v);
        return b[0] + b[1];
    };
    f32x2 v[16];
#pragma unroll
    for (int i = 0; i < 16; ++i) v[i] = (f32x2){lo16(x[i >> 2][i & 3]), hi16(x[i >> 2][i & 3])};
    f32x2 s = v[0];
#pragma unroll
    for (int i = 1; i < 16; ++i) s += v[i];
    const float mean = sum32(s[0] + s[1]) * (1.0f / DM);
    f32x2 q = {0.f, 0.f};
#pragma unroll
    for (int i = 0; i < 16; ++i) { const f32x2 d = v[i] - mean; q += d * d; }
    const float var = sum32(q[0] + q[1]) * (1.0f / DM);
    const float mb = bfr(mean);
    const float inv = rsd(var);
#pragma unroll
    for (int i = 0; i < 16; ++i) {
        const f32x2 e = v[i] - mb;
        const unsigned d = cvtpk(e[0], e[1]);
        const f32x2 y = (f32x2){lo16(d), hi16(d)} * inv;
        x[i >> 2][i & 3] = cvtpk(y[0], y[1]);
    }
}

struct HW { unsigned wq[2][3], wo[4], vo16, lds, po, px, er, ec; int lw; };
typedef u32x4 Cols[24];
template <int N> DEV void wq_issue(const HW& h, const u16* W, int t, int r0) {
    const char* src = (const char*)W + (unsigned)(t * 8 + r0) * (8 * 6144);
    const unsigned l = h.lds + WQ0 + ((t + 2) % 3) * WQS + r0 * 3072;
#pragma unroll
    for (int i = 0; i < N; ++i) { glds3(src, l + i * 3072, h.wq[i & 1][0], h.wq[i & 1][1], h.wq[i & 1][2]); src += 8 * 6144; }
}
DEV void wo_issue(const HW& h, const u16* W, int n, int lim, int a, int b) {
#pragma unroll 1
    for (int j = h.lw; j < n; j += 4) {
        const int i = j + (j < lim ? a : b);
        glds4((const char*)W + wo_half(i), h.lds + wo_unit(i >> 1) * 8192 + (i & 1) * 4096, h.wo[0], h.wo[1], h.wo[2], h.wo[3]);
    }
}
DEV void fill(const void* sb, unsigned vo) {
    asm volatile("s_nop 4\n\tglobal_store_dwordx4 %0, %1, %2 sc1\n\tglobal_store_dwordx4 %0, %1, %2 offset:1024 sc1\n\t"
                 "global_store_dwordx4 %0, %1, %2 offset:2048 sc1\n\tglobal_store_dwordx4 %0, %1, %2 offset:3072 sc1\n\ts_nop 1"
                 ::"v"(vo), "v"((u32x4){~0u, ~0u, ~0u, ~0u}), "s"(sb) : "memory");
}
DEV void filled(const HW& h, const u64* fe, u64 tag) {
    asm volatile("s_mov_b64 exec, 3\n\ts_nop 3\n\tglobal_atomic_swap_x2 %0, %1, %2 sc1\n\ts_mov_b64 exec, -1\n\ts_nop 0" ::"v"(h.er), "v"(tag), "s"(fe) : "memory");
}
#define GL "global_load_lds_dwordx4 "
#define GL4(a, b, s) GL a ", " s " sc1\n\t" GL b ", " s " offset:1024 sc1\n\t" GL b ", " s " offset:2048 sc1\n\t" GL b ", " s " offset:3072 sc1\n\t"
#define XRD(c) "s_setprio 2\n\tds_read2st64_b32 v[240:241], " c " offset1:4\n\tds_read2st64_b32 v[242:243], " c " offset0:8 offset1:12\n\t" \
    "ds_read2st64_b32 v[244:245], " c " offset0:16 offset1:20\n\tds_read2st64_b32 v[246:247], " c " offset0:24 offset1:28\n\t" \
    ".if %[L]\n\tds_read_b32 v248, " c " offset:8192\n\t.endif\n\t"
#define XCMP "s_waitcnt lgkmcnt(0)\n\tv_max3_u32 v240, v240, v241, v242\n\tv_max3_u32 v243, v243, v244, v245\n\tv_max3_u32 v240, v240, v246, v247\n\t" \
    ".if %[L]\n\tv_max3_u32 v240, v240, v243, v248\n\t.else\n\tv_max_u32 v240, v240, v243\n\t.endif\n\tv_cmp_eq_u32 %[bad], -1, v240\n\ts_setprio 0"
#define XCLOB "memory", "v240", "v241", "v242", "v243", "v244", "v245", "v246", "v247", "v248"
template <bool L> DEV bool stale(unsigned c) {
    u64 bad;
    asm volatile(XRD("%[c]") XCMP : [bad] "=s"(bad) : [c] "v"(c), [L] "n"((int)L) : XCLOB);
    return bad != 0;
}
template <bool L, bool CK> DEV bool xstep(const HW& h, const void* sw, unsigned lw, const void* sx, unsigned lx, unsigned vo0, unsigned c) {
    u64 bad;
    asm volatile("s_mov_b32 m0, %[lw]\n\ts_nop 1\n\ts_waitcnt vmcnt(11+%[L])\n\ts_barrier\n\t" GL "%[w0], %[sw] sc1\n\t" GL "%[w1], %[sw] offset:1024 sc1\n\t" GL "%[w2], %[sw] offset:2048 sc1\n\t"
                 "s_mov_b32 m0, %[lx]\n\ts_nop 0\n\t" GL4("%[v0]", "%[va]", "%[sx]")
                 ".if %[CK]\n\ts_waitcnt vmcnt(7)\n\t" XRD("%[c]") ".endif\n\t"
                 "s_add_u32 m0, %[lx], 4096\n\ts_nop 0\n\t" GL4("%[vb]", "%[vb]", "%[sx]")
                 ".if %[L]\n\ts_add_u32 m0, %[lx], 8192\n\ts_nop 0\n\t" GL "%[vc], %[sx] sc1\n\t.endif\n\t"
                 ".if %[CK]\n\t" XCMP "\n\t.endif"
                 : [bad] "=s"(bad)
                 : [lw] "s"(lw), [sw] "s"(sw), [w0] "v"(h.wq[0][0]), [w1] "v"(h.wq[0][1]), [w2] "v"(h.wq[0][2]), [lx] "s"(lx), [sx] "s"(sx), [v0] "v"(vo0),
                   [va] "v"(h.vo16), [vb] "v"(h.vo16 + 4096), [vc] "v"(h.vo16 + 8192), [c] "v"(c), [L] "n"((int)L), [CK] "n"((int)CK) : XCLOB, "scc");
    return CK && bad != 0;
}
DEV void warm(const HW& h, const u16* Wo, int lane) {
    asm volatile("s_mov_b32 m0, %2\n\ts_nop 4\n\tglobal_load_lds_dword %0, %1\n\tglobal_load_lds_dword %3, %1" ::"v"(lane * 64),
                 "s"(Wo + (size_t)blockIdx.x * 4096), "s"(h.lds + DUMP), "v"(lane * 64 + 4096) : "memory");
}
template <int ST> DEV void ld8(f32x4 (&d)[8], const char* sb, unsigned vo) {
    asm volatile("s_nop 4\n\tglobal_load_dwordx4 %0, %8, %9 offset:%10 sc1\n\tglobal_load_dwordx4 %1, %8, %9 offset:%11 sc1\n\t"
                 "global_load_dwordx4 %2, %8, %9 offset:%12 sc1\n\tglobal_load_dwordx4 %3, %8, %9 offset:%13 sc1\n\t"
                 "global_load_dwordx4 %4, %8, %9 sc1\n\tglobal_load_dwordx4 %5, %8, %9 offset:%14 sc1\n\t"
                 "global_load_dwordx4 %6, %8, %9 offset:%15 sc1\n\tglobal_load_dwordx4 %7, %8, %9 offset:%16 sc1"
                 : "=&v"(d[0]), "=&v"(d[1]), "=&v"(d[2]), "=&v"(d[3]), "=&v"(d[4]), "=&v"(d[5]), "=&v"(d[6]), "=&v"(d[7])
                 : "v"(vo), "s"(sb + 4 * ST), "n"(-4 * ST), "n"(-3 * ST), "n"(-2 * ST), "n"(-ST), "n"(ST), "n"(2 * ST), "n"(3 * ST) : "memory");
}

template <class F> DEV void colsum(Cols& d, const void* out, int lane, F next) {
    const char* dst = (const char*)out + (lane & 7) * 32 + (lane >> 4) * 16;
    bf16x8 sel;
#pragma unroll
    for (int e = 0; e < 8; ++e) sel[e] = (lane & 7) == e ? (short)0x3f80 : (short)0;
#pragma unroll
    for (int i = 0; i < 24; ++i) asm volatile("" : "+v"(d[i]));
    f32x4 c;
#pragma unroll
    for (int k = 0; k < 6; ++k) {
        if (!(k & 1)) c = (f32x4){0.f, 0.f, 0.f, 0.f};
#pragma unroll
        for (int i = 0; i < 4; ++i) c = MFMA16(sel, __builtin_bit_cast(bf16x8, d[k * 4 + i]), c);
        if (k & 1) {
#pragma unroll
            for (int e = 0; e < 4; ++e) c[e] = dppadd<0x128>(c[e]);
            if (lane < 32 && !(lane & 8)) st16wt<0>(dst + (k >> 1) * 256, __builtin_bit_cast(u32x4, c));
        }
        SB();
        next(k);
    }
}

template <bool LOCAL>
DEV void qkv_helper(lchar* smem, HW& h, Cols& cw, char* ws, int layer, const u16* X, const u16* W, const u16* W2, const u16* Wo, const char* stats, float* csum,
                    u64* fl, u64 tag, int g, int hp, int lane, int lw STPARG) {
    OPAQUE(lane, lw, g, hp);
    constexpr int NX = LOCAL ? 9 : 8, HO = LOCAL ? 16 : 0;
    const int m0 = g * 128;
    const bool halo = LOCAL && g > 0;
    const u64* fx = fl + fi(FL_X + (layer - 1) * 1024 + (g * 2 - (halo ? 1 : 0)) * 16 + (lane < (halo ? 48 : 32) ? lane : 0));
    const bool fon = lane < (halo ? 48 : 32);
    auto fold = [&](u64 m) { return (unsigned)(m & (m >> 16) & (halo ? m >> 32 : m)); };
    auto first = [&](int r0) {
#pragma unroll 1
        for (int t = 0; t < 3; ++t) wq_issue<4>(h, W, t, r0);
    };
    auto colld = [&](Cols& d, const u16* M) {
        const unsigned vo = ((g * 64 + (lane >> 3)) * 3072 + (hp ^ CJ) * 64 + (lane & 7) * 8) * 2 + 2048;
#pragma unroll
        for (int i = 0; i < 8; ++i)
            asm volatile("s_nop 4\n\tglobal_load_dwordx4 %0, %3, %4 offset:-2048\n\tglobal_load_dwordx4 %1, %3, %4\n\t"
                         "global_load_dwordx4 %2, %3, %4 offset:2048" : "=&v"(d[i]), "=&v"(d[8 + i]), "=&v"(d[16 + i])
                         : "v"(vo), "s"((const char*)M + i * (8 * 6144)) : "memory");
    };
    if (lw < 2) {
        const unsigned vo0 = (LOCAL && g == 0) ? h.vo16 + 1024 : h.vo16;
        const char* xsrc = (const char*)X + ((size_t)lw * SEQ + m0 - HO) * 64;
        const unsigned ldx = h.lds + lw * (NX * 1024), xv = ldx + lane * 16 + (lane >> 4) * 4;
        auto fetch = [&](int t, int so) {
            const char* p = xsrc + t * (2 * SEQ * 64);
            glds4(p, ldx + so, vo0, h.vo16, h.vo16, h.vo16);
            glds4(p + 4096, ldx + so + 4096, h.vo16, h.vo16, h.vo16, h.vo16);
            if (LOCAL) glds1(p + 8192, ldx + so + 8192, h.vo16);
        };
        int bud = POLLMAX;
        auto redo = [&](int t, int so) {
#pragma unroll 1
            do { --bud; fetch(t, so); WAITV(0); } while (stale<LOCAL>(xv + so) && bud > 0);
        };
        STAMP(0);
        if (LOCAL && layer == 0) {
            const u64* f0 = fl + fi(FL_L + g * 16 - (halo ? 2 : 0)) + (lane & 31);
            asm volatile("" : "+v"(f0));
            BAR();
            if (lw == 0) poll(f0, lane < (halo ? 18 : 16), tag, ~0u, [](u64 m) { return (unsigned)m; });
            BAR();
        } else poll(fx + fi(lw * 512), fon, tag, PQ, fold);
        fetch(0, 0);
        STAMP(1);
        if (lw == 1) STAMPW(11);
        fetch(1, XS);
        if (Q2) fetch(2, 2 * XS);
        if (LOCAL && layer == 0) BAR();
        WAITV(Q2 ? 2 * NX : NX);
        if (__builtin_expect(stale<LOCAL>(xv), 0)) redo(0, 0);
        BAR();
        if (!Q2) fetch(2, 2 * XS);
        WAITV(NX);
        if (__builtin_expect(stale<LOCAL>(xv + XS), 0)) redo(1, XS);
        const char* wsrc = (const char*)W + (unsigned)(24 + lw) * (8 * 6144);
        const unsigned ldw = h.lds + WQ0 + lw * 3072;
        int so = 0, sv = 2 * XS, sw = 2 * WQS, t = 3;
        auto next = [&]() {
            wsrc += 64 * 6144;
            sv = so;
            so = so == 2 * XS ? 0 : so + XS;
            sw = sw == 2 * WQS ? 0 : sw + WQS;
        };
#pragma unroll 1
        for (; t <= NCK; ++t) {
            if (__builtin_expect((xstep<LOCAL, true>(h, wsrc, ldw + sw, xsrc + t * (2 * SEQ * 64), ldx + so, vo0, xv + sv)), 0)) redo(t - 1, sv);
            next();
        }
#pragma unroll 1
        for (; t < 16; ++t) {
            xstep<LOCAL, false>(h, wsrc, ldw + sw, xsrc + t * (2 * SEQ * 64), ldx + so, vo0, 0);
            next();
        }
        WAITB(NX + 3);
    } else if (lw == 2) {
        if (LOCAL && layer == 0) {
            BAR();
            first(0);
            STAMPW(6);
            BAR(); BAR();
        }
        WAITB(0);
        if (layer) wq_issue<4>(h, W, 2, 0);
        BAR();
#pragma unroll 1
        for (int t = 3; t < 16; ++t) {
            wq_issue<6>(h, W, t, 2);
            WAITB(18);
        }
    } else {
        if (LOCAL && layer == 0) {
            BAR();
            first(4);
            if (CT) {
                const char* pc;
                asm volatile("s_getpc_b64 %0" : "=s"(pc));
                asm volatile("s_mov_b32 m0, %1\n\ts_nop 4\n\tglobal_load_lds_dword %2, %0" ::"s"(pc - CT0), "s"(h.lds + DUMP),
                             "v"(((blockIdx.x >> 3) * CT + (lane < CT ? lane : 0)) * 64) : "memory");
            }
            BAR(); BAR();
            Cols c0;
            colld(c0, W);
            warm(h, Wo, lane);
            WAITB(26);
            auto fill0 = [&](int k) { fill(ws + (k < 4 ? WS_O : WS_X + 4 * MB) + ((k >> 1) & 1) * (SEQ * 64) + (k & 1) * 4096, k < 4 ? h.po : h.px); };
            auto job = [&](int i) {
#pragma unroll
                for (int k = 0; k < 8; ++k) if (FE + k / FN == i) fill0(k);
                constexpr int L = FE + 7 / FN, R = L + FD;
                if (i == R) { WAITV((L < 6 && R >= 6) + (L < 8 && R >= 8) + (L < 10 && R >= 10)); filled(h, fl + fi(FL_E), tag); }
                BAR();
            };
            job(1); job(2); job(3); job(4);
            WAITV(FE > 4 ? 0 : (5 - FE) * FN >= 8 ? 32 : (5 - FE) * FN * 4);
            colsum(c0, (const char*)csum + ((hp ^ CJ) * 16 + g) * 768, lane, [&](int k) { job(5 + k); });
            job(11); job(12); job(13); job(14);
        } else {
            auto fin = [&](float s, float q, int o) {
                const float mean = s * (1.0f / DM);
                *(LAS float*)(smem + SNM + o) = -bfr(mean);
                *(LAS float*)(smem + SRS + o) = rsd(fmx(q * (1.0f / DM) - mean * mean, 0.f));
            };
            f32x4 sp[3][8], a = {0.f, 0.f, 0.f, 0.f};
            auto add8 = [&](f32x4 (&d)[8]) {
#pragma unroll
                for (int i = 0; i < 8; ++i) {
                    asm volatile("" : "+v"(d[i]));
#pragma unroll
                    for (int e = 0; e < 4; ++e) asm volatile("v_add_f32 %0, %0, %1" : "+v"(a[e]) : "v"(d[i][e]));
                }
            };
            const u64* fc = fl + fi(FL_C + hp * 16 + (lane & 15));
            const u64* fs = fx + fi(FL_S - FL_X);
            const char* sb = stats;
            const char* cb = (const char*)csum + (LOCAL ? 0 : 256 * 768);
            const unsigned so = (layer - 1) * (unsigned)(MB / 2) + g * 32768;
            const unsigned vo = so + (lane >> 5) * 16384 + (lane & 31) * 16, vc = hp * (16 * 768) + (lane < 48 ? lane * 16 : 0);
            WAITB(0);
            wq_issue<4>(h, W, 2, 4);
            BAR(); WAITB(0);
            u64 f0, f1, f2;
            asm volatile("global_load_dwordx2 %0, %3, off sc1\n\tglobal_load_dwordx2 %1, %4, off sc1\n\tglobal_load_dwordx2 %2, %5, off sc1"
                         : "=&v"(f0), "=&v"(f1), "=&v"(f2) : "v"(fs), "v"(fs + fi(512)), "v"(fc) : "memory");
            BAR();
            WAITV(0);
            asm volatile("" : "+v"(f0), "+v"(f1), "+v"(f2));
            if (!__all(f0 == tag && f1 == tag && f2 == tag)) {
                poll(fs, fon, tag, 0xffffu, fold);
                poll(fs + fi(512), fon, tag, 0xffffu, fold);
                poll(fc, lane < 16, tag, 0xffffu, [](u64 m) { return (unsigned)m; });
            }
            ld8<512>(sp[0], sb, vo);
            BAR();
            ld8<512>(sp[1], sb + 4096, vo);
            BAR();
            ld8<512>(sp[2], sb + 8192, vo);
            WAITV(16);
            add8(sp[0]);
            BAR();
            ld8<512>(sp[0], sb + 12288, vo);
            WAITV(16);
            add8(sp[1]);
            BAR();
            WAITV(8);
            ld8<768>(sp[1], cb, vc);
            add8(sp[2]);
            BAR();
            ld8<768>(sp[2], cb + 6144, vc);
            WAITV(16);
            add8(sp[0]);
            BAR();
            if (LOCAL) ld8<512>(sp[0], sb, so - (halo ? 16384 : 0) + (lane >> 4) * 4096 + 384 + (lane & 7) * 16);
            fin(a[0], a[1], 64 + lane * 8);
            fin(a[2], a[3], 68 + lane * 8);
            SB();
            BAR();
            WAITV(LOCAL ? 8 : 0);
            a = (f32x4){0.f, 0.f, 0.f, 0.f};
            add8(sp[1]);
            BAR();
            add8(sp[2]);
            if (lane < 48) *(LAS f32x4*)(smem + SCOL + lane * 16) = a;
            SB();
            BAR();
            if (LOCAL) {
                WAITV(0);
                a = (f32x4){0.f, 0.f, 0.f, 0.f};
                add8(sp[0]);
            }
            BAR();
            if (LOCAL) {
#pragma unroll
                for (int e = 0; e < 4; ++e) {
                    const f32x2 x = sw16f(a[e], a[e]);
                    const float v = x[0] + x[1];
                    const f32x2 y = sw32f(v, v);
                    a[e] = y[0] + y[1];
                }
                fin(a[0], a[1], (lane & 7) * 8);
                fin(a[2], a[3], (lane & 7) * 8 + 4);
            }
            BARL();
        }
    }
    if (LOCAL) wo_issue(h, Wo, 8, 2, 6, 18);
    WAITV(LOCAL ? 8 : 0);
    BAR();
    if (LOCAL) wo_issue(h, Wo, 12, 6, 8, 20);
    BAR();
    if (LOCAL) {
        wo_issue(h, Wo, 12, 6, 0, 8);
        if (layer == 0 && lw == 3) {
            colld(cw, W2);
            WAITB(28);
            WAITV(0);
#pragma unroll
            for (int i = 0; i < 24; ++i) asm volatile("" : "+v"(cw[i]));
        } else WAITB(4);
    } else BAR();
}

DEV void attn_helper(const HW& h, char* ws, int nf, const u16* kv, const u16* Wo, const u64* fe, const u64* fkv, u64 tag, int g, int hp, int lane, int lw STPARG) {
    __builtin_amdgcn_s_setprio(2);
    OPAQUE(lane, lw, g, hp);
    const int rh = lw & 1, hd = lw >> 1;
    const char* src = (const char*)kv + (unsigned)((hp * 2 + hd) * SEQ * 64 + rh * 4096);
    const unsigned ld = h.lds + AR0 + hd * 16384 + rh * 8192;
    const u64* f = fkv + fi(lw * 16 + (lane & 15));
    unsigned rdy = 0u;
    int reg = AR1;
    auto issue = [&](int c) {
        const int blk = g ^ c;
        if (!((rdy >> blk) & 1)) rdy = poll(f, lane < 16, tag, 1u << blk, [](u64 m) { return (unsigned)m; });
        glds4(src + blk * 8192, ld + reg, h.vo16, h.vo16, h.vo16, h.vo16);
        glds4(src + blk * 8192 + NH * SEQ * 64, ld + reg + 4096, h.vo16, h.vo16, h.vo16, h.vo16);
        reg = reg == AR1 ? AR2 : reg == AR2 ? 0 : AR1;
    };
    issue(1);
    STAMP(7);
#pragma unroll 1
    for (int c = 0; c < 14; ++c) {
        WAITB(0);
        issue(c + 2);
        if (c == 1 && lw == 3) warm(h, Wo, lane);
        if ((c >> 2) == (FA == 4 ? 2 : 1)) wo_issue(h, Wo, min(4, 14 - 4 * (c & 3)), 4, 4 * (c & 3), 0);
        if (c >= FA && c < FA + nf)
            fill(ws + (c == FA ? WS_O + 4 * MB : c == FA + 1 ? WS_X + 8 * MB : c == FA + 2 ? WS_O : WS_X + 12 * MB) + hd * (SEQ * 64) + rh * 4096, (c & 1) ? h.px : h.po);
        if (c == FA + 4 && lw == 3) filled(h, fe, tag);
    }
    WAITB(0);
    wo_issue(h, Wo, 8, 6, 14, 24);
    BAR();
    wo_issue(h, Wo, 10, 10, 20, 0);
}

#define TM0
#define TM1(t)
#define CNT(n)
DEV void wo_helper(lchar* smem, HW& h, Cols& cw, const char* csum, const u16* O, const u16* Wn, u64* fc, const u64* fe, const u64* fo, u64 tag, int g, int lane, int lw STPARG) {
    int dummy = 0;
    OPAQUE(lane, lw, g, dummy);
    const int p = lw & 1, rh = lw >> 1;
    const char* xsrc = (const char*)O + ((size_t)p * SEQ + g * 128 + rh * 64) * 64;
    const unsigned ldx = h.lds + p * 8192 + rh * 4096, c0 = ldx + lane * 16 + (lane >> 4) * 4;
    const u64* f = fo + fi((p * 32 + g * 2 + rh) * 16 + (lane & 15));
    unsigned rdy = 0;
    auto need = [&](unsigned m) { if ((rdy & m) != m) rdy = poll(f, lane < 16, tag, m, [](u64 m) { return (unsigned)m; }); };
    STAMP(0);
    u64 e = tag;
    asm volatile("s_cmp_lt_u32 %3, %4\n\ts_nop 3\n\ts_cbranch_scc1 1f\n\tglobal_load_dwordx2 %0, %1, %2 sc1\n1:" : "+v"(e) : "v"(h.ec), "s"(fe), "s"(lw), "n"(E1 ? 3 : 0) : "memory", "scc");
    need(PW);
    WAITV(0);
    asm volatile("" : "+v"(e));
    if (__builtin_expect(!__all(e == tag), 0)) poll((const u64*)((const char*)fe + h.ec), true, tag, ~0u, [](u64 m) { return (unsigned)(m & (m >> 32)); });
    if (NP) rdy = ~0u;
    BAR();
    __builtin_amdgcn_s_setprio(HPW);
    int so = 65536;
    unsigned np = 0;
#pragma unroll 1
    for (int t = 0; t < 4; ++t) {
        if (!((rdy >> t) & 1)) ++np;
        need(1u << t);
        glds4(xsrc + t * (2 * SEQ * 64), ldx + so, h.vo16, h.vo16, h.vo16, h.vo16);
        if (t == 0) STAMP(1);
        so = t * 16384;
    }
    if (fc) colsum(cw, csum, lane, [](int) {});
    unsigned vo = h.vo16 + 4 * (2 * SEQ * 64), sa, sb, sm;
    int bud = POLLMAX;
    u64 ret;
    unsigned tv = 0, cn = 0;
    const char* wn = (const char*)Wn + lw * (8 * 6144);
    unsigned w0 = h.wq[lw == 3][0], w1 = h.wq[lw == 3][1], w2 = h.wq[lw == 3][2];
    asm volatile(
        ".macro WRD t\n.if (\\t %% 5) == 0\n"
        "ds_read2st64_b32 v[240:241], %[c4] offset1:4\nds_read2st64_b32 v[242:243], %[c4] offset0:8 offset1:12\n.else\n"
        "ds_read2st64_b32 v[240:241], %[c0] offset0:(\\t %% 5)*64-64 offset1:(\\t %% 5)*64-60\n"
        "ds_read2st64_b32 v[242:243], %[c0] offset0:(\\t %% 5)*64-56 offset1:(\\t %% 5)*64-52\n.endif\n.endm\n"
        ".macro WCMP\ns_waitcnt lgkmcnt(0)\nv_max3_u32 v240, v240, v241, v242\nv_max_u32 v240, v240, v243\nv_cmp_eq_u32 vcc, -1, v240\n.endm\n"
        ".macro WTS t, x\nWCMP\n" TM1("(\\t+63)&63") "s_cbranch_vccnz .Lwr%=_\\x\\t\n.Lwb%=_\\x\\t:\n.endm\n"
        ".macro WNX t\ns_bitcmp1_b32 %[rdy], \\t+4\ns_barrier\n" TM0 "s_cbranch_scc0 .Lwp%=_\\t\n.Lwq%=_\\t:\n.endm\n"
        ".macro WST t\n" GL "%[vo], %[src] sc1\n" GL "%[vo], %[src] offset:1024 sc1\n"
        "s_waitcnt vmcnt(10)\nWRD \\t\n"
        GL "%[vo], %[src] offset:2048 sc1\n" GL "%[vo], %[src] offset:3072 sc1\n"
        "s_add_u32 m0, %[ldx], ((\\t+3) %% 5)*16384\nv_add_u32 %[vo], 0x40000, %[vo]\n"
        "WTS \\t, a\n"
        ".endm\n"
        ".macro WTL t, n, x\ns_waitcnt vmcnt(\\n)\nWRD \\t\nWTS \\t, \\x\ns_barrier\n" TM0 ".endm\n"
        ".macro WWN o\ns_add_u32 m0, %[wl], \\o\ns_nop 0\n" GL "%[w0], %[wn] sc1\n" GL "%[w1], %[wn] offset:1024 sc1\n" GL "%[w2], %[wn] offset:2048 sc1\n"
        "v_add_u32 %[w0], 0x30000, %[w0]\nv_add_u32 %[w1], 0x30000, %[w1]\nv_add_u32 %[w2], 0x30000, %[w2]\n.endm\n"
        ".macro WCR t, x\n.Lwr%=_\\x\\t:\ns_mov_b32 %[sa], ((\\t+4) %% 5)*16384\ns_mov_b32 %[sb], \\t*0x40000\ns_call_b64 %[ret], .Lwretry%=\ns_branch .Lwb%=_\\x\\t\n.endm\n"
        ".macro WCP t\n.Lwp%=_\\t:\ns_mov_b32 %[sa], \\t+4\ns_call_b64 %[ret], .Lwpoll%=\ns_branch .Lwq%=_\\t\n.endm\n"
        "s_add_u32 m0, %[ldx], 49152\ns_waitcnt vmcnt(12)\nWRD 0\nWTS 0, a\nWNX 0\n"
        ".irp t,1,2,3,4,5,6,7,8,9,10,11\nWST \\t\nWNX \\t\n.endr\n"
        "WST 12\ns_cmp_eq_u64 %[Wn], 0\ns_barrier\n" TM0 "s_cbranch_scc1 .Lwlast%=\n"
        "WWN 2*24576\nWTL 13, 11, a\nWWN 2*24576+12288\nWTL 14, 10, a\nWWN 0\nWTL 15, 9, a\nWWN 12288\n"
        "s_branch .Lwend%=\n"
        ".irp t,0,1,2,3,4,5,6,7,8,9,10,11,12,13,14,15\nWCR \\t, a\n.endr\n"
        ".irp t,13,14,15\nWCR \\t, b\n.endr\n"
        ".irp t,0,1,2,3,4,5,6,7,8,9,10,11\nWCP \\t\n.endr\n"
        ".Lwretry%=:\ns_mov_b32 %[sm], m0\ns_add_u32 m0, %[ldx], %[sa]\nv_add_u32 v244, %[sb], %[v0]\nv_add_u32 v245, %[sa], %[c0]\n"
        "1:\n" GL "v244, %[src] sc1\n" GL "v244, %[src] offset:1024 sc1\n" GL "v244, %[src] offset:2048 sc1\n" GL "v244, %[src] offset:3072 sc1\n"
        "s_waitcnt vmcnt(0)\nds_read2st64_b32 v[240:241], v245 offset1:4\nds_read2st64_b32 v[242:243], v245 offset0:8 offset1:12\n"
        "WCMP\n" CNT("0x10000") "s_sub_i32 %[bud], %[bud], 1\ns_cmp_lt_i32 %[bud], 0\ns_cbranch_scc1 2f\ns_cbranch_vccnz 1b\n"
        "2:\ns_mov_b32 m0, %[sm]\ns_setpc_b64 %[ret]\n"
        ".Lwpoll%=:\ns_mov_b64 exec, 0xffff\n"
        "1:\n" FLD("v[240:241]", "%[f]", "%[z]") "\ns_waitcnt vmcnt(0)\nv_cmp_eq_u64 vcc, %[tag], v[240:241]\n" CNT("1")
        "s_sub_i32 %[bud], %[bud], 1\ns_cmp_lt_i32 %[bud], 0\ns_cselect_b32 %[rdy], -1, vcc_lo\ns_bitcmp1_b32 %[rdy], %[sa]\n"
        "s_cbranch_scc1 2f\ns_sleep 1\ns_branch 1b\n"
        "2:\ns_mov_b64 exec, -1\ns_setpc_b64 %[ret]\n"
        ".Lwlast%=:\nWTL 13, 8, b\nWTL 14, 4, b\nWTL 15, 0, b\n"
        ".Lwend%=:\n"
        ".purgem WRD\n.purgem WCMP\n.purgem WTS\n.purgem WNX\n.purgem WST\n.purgem WTL\n.purgem WWN\n.purgem WCR\n.purgem WCP"
        : [vo] "+v"(vo), [rdy] "+s"(rdy), [bud] "+s"(bud), [sa] "=&s"(sa), [sb] "=&s"(sb), [sm] "=&s"(sm), [ret] "=&s"(ret),
          [w0] "+v"(w0), [w1] "+v"(w1), [w2] "+v"(w2)
        : [src] "s"(xsrc), [ldx] "s"(ldx), [c0] "v"(c0), [c4] "v"(c0 + 65536), [v0] "v"(h.vo16), [f] "v"(f), [z] "v"(0ull), [tag] "s"(tag),
          [wn] "s"(wn), [Wn] "s"(Wn), [wl] "s"(h.lds + WQ0 + lw * 3072)
        : "memory", "scc", "vcc", "v240", "v241", "v242", "v243", "v244", "v245"
        );
    __builtin_amdgcn_s_setprio(HPQ);
    if (fc && lane == 0) stflag(fc, tag);
}

__global__ __launch_bounds__(512) void mega(const u16* xin, const u16* wqa, char* ws, const u16* __restrict__ woa,
                                            const u16* __restrict__ wqb, const u16* __restrict__ wob, u16* __restrict__ out) {
    __shared__ __attribute__((aligned(16))) char smem_[LDS_BYTES];
    asm volatile("s_load_dwordx2 %0, %3, 0\n\ts_load_dwordx2 %1, %3, 8\n\ts_load_dwordx2 %2, %3, 16"
                 : "=&s"(xin), "=&s"(wqa), "=&s"(ws) : "s"(__builtin_amdgcn_kernarg_segment_ptr()));
    SB();
    lchar* smem = (lchar*)smem_;
    const int bid = blockIdx.x, xcd = bid & 7, idx = bid >> 3;
    const int lane = threadIdx.x & 63, wid = __builtin_amdgcn_readfirstlane(threadIdx.x >> 6);
    const int g = (xcd >> 2) * 8 + (idx >> 2), hp = (xcd & 3) * 4 + (idx & 3);
    const int g2 = (xcd >> 1) * 4 + (idx >> 3), cb = (xcd & 1) * 8 + ((idx & 7) ^ CX);
    const int row = g * 128 + hp * 8 + (wid & 3) * 2 + ((lane >> 2) & 1);
    unsigned xo = row * 2048 + (lane >> 3) * 64 + (lane & 3) * 16, eo = WS_FL + fi(FL_EP) * 8;
    asm volatile("" : "+v"(xo), "+v"(eo));
    asm volatile("s_waitcnt lgkmcnt(0)" : "+s"(xin), "+s"(wqa), "+s"(ws));
    SB();
    u64* fl = (u64*)(ws + WS_FL);
    const int pos = (int)(kinv() >> (4 * hp)) & 15;
    auto mktag = [](u64 e) {
        e += 1;
        return ((u64)(unsigned)__builtin_amdgcn_readfirstlane((int)(e >> 32)) << 32) | (unsigned)__builtin_amdgcn_readfirstlane((int)e);
    };
    const unsigned lds0 = (unsigned)(uintptr_t)smem;
    LAS unsigned* cnt = (LAS unsigned*)(smem + DUMP + 1024);
    auto warmup = [&]() {
        *cnt = 0u;
        BARL();
        if (wid > 4) touch((const char*)wqa + (wid - 5) * 2048, lds0 + DUMP, (idx + (lane >> 3) * 32) * 6144 + (xcd & 3) * 512 + (lane & 7) * 64);
        else if (wid == 4) {
            const int f = lane < 4 ? (bid < 240 ? bid : 0) * 64 + lane * 16 : FL_L + (g & 7) * 16 + (lane & 1) * 128;
            asm volatile("s_mov_b64 exec, 63\n\tglobal_atomic_add_x2 %0, %1, %2 sc1\n\ts_mov_b64 exec, -1" ::"v"(fi(f) * 8), "v"(0ull), "s"(fl) : "memory");
        }
    };
    u64 tag;
    if (wid < 4) {
        u32x4 x[4];
        u64 e;
        asm volatile("global_load_dwordx4 %0, %5, %6" NTX "\n\tglobal_load_dwordx4 %1, %5, %6 offset:512" NTX "\n\tglobal_load_dwordx4 %2, %5, %6 offset:1024" NTX "\n\t"
                     "global_load_dwordx4 %3, %5, %6 offset:1536" NTX "\n\tglobal_load_dwordx2 %4, %7, %8 sc1"
                     : "=&v"(x[0]), "=&v"(x[1]), "=&v"(x[2]), "=&v"(x[3]), "=&v"(e)
                     : "v"(xo), "s"(xin), "v"(eo), "s"(ws) : "memory");
        SB();
        warmup();
        unsigned d = ((lane >> 3) * SEQ + row) * 64 + (((lane & 3) ^ fz(row)) << 4);
        asm volatile("" : "+v"(d));
        WAITV(0);
        asm volatile("" : "+v"(x[0]), "+v"(x[1]), "+v"(x[2]), "+v"(x[3]), "+v"(e));
        if (wid == 0) STAMP(14);
        ln0_rows(x);
#pragma unroll
        for (int i = 0; i < 4; ++i)
            asm volatile("global_store_dwordx4 %0, %1, %2 sc1\n\ts_nop 1" ::"v"(d), "v"(x[i]), "s"(ws + WS_X + i * MB) : "memory");
        tag = mktag(e);
        if (wid == 0) STAMP(15);
        WAITV(0);
        if (lane == 0 && __hip_atomic_fetch_add(cnt, 1u, __ATOMIC_RELAXED, __HIP_MEMORY_SCOPE_WORKGROUP) == 3)
            stflag(fl + fi(FL_L + g * 16 + hp), tag);
    } else {
        warmup();
        tag = mktag(ldep(fl + fi(FL_EP)));
    }
    if (wid < 4) {
        BAR();
        if (wid == 0) STAMP(9);
        __builtin_amdgcn_s_setprio(1);
        u32x4 res[4];
        {
            const unsigned ro = (g2 * 128 + (wid >> 1) * 64 + (lane & 15)) * 2048 + (cb * 2 + (wid & 1)) * 64 + ((lane & 16) << 1) + ((lane & 32) >> 1);
#pragma unroll
            for (int i = 0; i < 4; ++i) res[i] = (u32x4){ro, ro, ro, ro};
            BAR();
            BAR();
        }
#pragma unroll 1
        for (int layer = 0; layer < 4; ++layer) {
            u64* fo = fl + fi(FL_O + layer * 1024 + ((wid & 1) * 32 + g * 2 + (wid >> 1)) * 16 + pos);
            u16* ob = (u16*)(ws + WS_O + pos * (2 * SEQ * 64));
            WReg wr;
            if (layer & 1) qb_compute(smem, (u16*)(ws + WS_KV + (layer >> 1) * 8 * MB), wr, ob + 2 * MB, fo, fl + fi(FL_KV + (layer >> 1) * 1024 + hp * 64),
                                      tag, g, hp, lane, wid STPPASS(layer * 32));
            else qa_compute(smem, layer > 0, xin, res, wr, ob, fo, tag, g, hp, lane, wid STPPASS(layer * 32));
            const bool last = layer == 3;
            wo_compute(smem, fo, res, wr,
                       last ? out : (u16*)(ws + WS_X + (layer + 1) * 4 * MB), last ? nullptr : ws + WS_ST + layer * (MB / 2),
                       fl + fi(FL_X + layer * 1024), tag, g2, cb, lane, wid STPPASS(layer * 32 + 16));
        }
        if (bid == 0 && threadIdx.x == 0) stflag(fl + fi(FL_EP), tag);
    } else {
        const int lw = wid - 4;
        HW h;
        h.lds = lds0;
        h.lw = lw;
        h.vo16 = lane * 16;
        h.po = (pos * 2 * SEQ + g * 128) * 64 + lane * 16;
        h.px = (cb * 2 * SEQ + g2 * 128) * 64 + lane * 16;
        h.er = fi(lane ? 256 + g2 * 16 + cb : g * 16 + hp) * 8;
        h.ec = fi((lane < 16 ? g2 : lane < 32 || g == 0 ? 16 + g : 15 + g) * 16 + (lane & 15)) * 8;
#pragma unroll
        for (int i = 0; i < 6; ++i) {
            const int k = (lane >> 3) + (((i / 3) ^ (lw == 1)) << 3), pc = lane & 7;
            h.wq[i / 3][i % 3] = (lane >> 3) * 6144 + (i % 3) * 1024 + hp * 128 + ((((pc >> 1) ^ fswz(k)) << 1) + (pc & 1)) * 16;
        }
#pragma unroll
        for (int i = 0; i < 4; ++i) h.wo[i] = wo_voff(lane, cb, i);
#pragma unroll 1
        for (int layer = 0; layer < 4; ++layer) {
            if (OQ) asm volatile("" : "+s"(wqa), "+s"(wqb), "+s"(ws));
            const u16* X = (const u16*)(ws + WS_X + layer * 4 * MB);
            const char* st = ws + WS_ST;
            const u16* wo = (layer & 1) ? wob : woa;
            Cols cw;
            const u64* fe = fl + fi(FL_E + ((layer + 1) >> 1) * 512);
            if (layer & 1) {
                qkv_helper<false>(smem, h, cw, ws, layer, X, wqb, wqb, wo, st, (float*)(ws + WS_CS), fl, tag, g, hp, lane, lw STPPASS(layer * 32));
                attn_helper(h, ws, layer == 1 ? 4 : 1, (const u16*)(ws + WS_KV + (layer >> 1) * 8 * MB), wo, fe, fl + fi(FL_KV + (layer >> 1) * 1024 + hp * 64), tag, g, hp, lane,
                            lw STPPASS(layer * 32));
            } else {
                qkv_helper<true>(smem, h, cw, ws, layer, X, wqa, wqb, wo, st, (float*)(ws + WS_CS), fl, tag, g, hp, lane, lw STPPASS(layer * 32));
            }
            wo_helper(smem, h, cw, ws + WS_CS + (256 + (hp ^ CJ) * 16 + g) * 768, (const u16*)(ws + WS_O + (layer & 1) * 4 * MB), layer == 3 ? nullptr : (layer & 1) ? wqa : wqb, (layer == 0 && lw == 3) ? fl + fi(FL_C + (hp ^ CJ) * 16 + g) : nullptr,
                      fe, fl + fi(FL_O + layer * 1024), tag, g2, lane, lw STPPASS(layer * 32 + 16));
        }
    }
}

extern "C" void kernel_launch(void* const* d_in, const int* in_sizes, int n_in, void* d_out, int out_size, void* d_ws,
                              size_t ws_size, hipStream_t stream) {
    mega<<<256, 512, 0, stream>>>((const u16*)d_in[0], (const u16*)d_in[1], (char*)d_ws, (const u16*)d_in[2], (const u16*)d_in[4],
                                  (const u16*)d_in[5], (u16*)d_out);
}
```

```cpp
#include <hip/hip_runtime.h>
#include <stdint.h>

typedef unsigned short u16;
typedef unsigned long long u64;
typedef __attribute__((ext_vector_type(8))) short bf16x8;
typedef __attribute__((ext_vector_type(4))) short s16x4;
typedef __attribute__((ext_vector_type(4))) float f32x4;
typedef __attribute__((ext_vector_type(16))) float f32x16;
typedef __attribute__((ext_vector_type(4))) unsigned u32x4;
typedef __attribute__((ext_vector_type(2))) unsigned u32x2;
typedef __attribute__((ext_vector_type(2))) __bf16 bf16x2_t;
typedef __attribute__((ext_vector_type(2))) float f32x2;

#define LAS __attribute__((address_space(3)))
#define GAS __attribute__((address_space(1)))
#define DEV __device__ __forceinline__
typedef LAS char lchar;

constexpr int SEQ = 2048, DM = 1024, NH = 32;
constexpr float QSCALE = 0.17677669529663687f, LOG2E = 1.4426950408889634f;
#define POLLMAX (1 << 16)
#define NTX " nt"
#define CT 1
#define CT0 0
#define OQ 1
#define PW 2
#define VF 1
#define AL 0
#define PB 1
#define FL0 1
#define TL 0
#define LP 1
#define WA 1
#define WB 48
#define EF 3
#define EO 0
#define EXP 0
#define WS 2
#define HPW 2
#define HPQ 0
#define NP 1
#define PRE 0
#define F0 0
#define FE 5
#define FN 2
#define FD 2
#define FA 4
#define E1 1
#define AW 0
#define PQ 1
#define Q2 1
#define NCK 6
#define RI 4
#define KORD 0xb3fedc7654a98210ull
#define CJ 2
#define CX 1
#define EMU 1

DEV unsigned cvtpk(float lo, float hi) {
    f32x2 v = {lo, hi};
    bf16x2_t b = __builtin_convertvector(v, bf16x2_t);
    return __builtin_bit_cast(unsigned, b);
}
DEV float lo16(unsigned v) { return __uint_as_float(v << 16); }
DEV float hi16(unsigned v) { return __uint_as_float(v & 0xffff0000u); }
DEV float bfr(float f) { return lo16(cvtpk(f, f)); }
DEV u32x2 pk4(f32x4 a) { return (u32x2){cvtpk(a[0], a[1]), cvtpk(a[2], a[3])}; }
DEV bf16x8 cat(u32x2 a, u32x2 b) { return __builtin_bit_cast(bf16x8, (u32x4){a[0], a[1], b[0], b[1]}); }
DEV u32x2 sw16(unsigned a, unsigned b) { auto r = __builtin_amdgcn_permlane16_swap(a, b, false, false); return (u32x2){r[0], r[1]}; }
DEV u32x2 sw32(unsigned a, unsigned b) { auto r = __builtin_amdgcn_permlane32_swap(a, b, false, false); return (u32x2){r[0], r[1]}; }
DEV f32x2 sw16f(float a, float b) { return __builtin_bit_cast(f32x2, sw16(__float_as_uint(a), __float_as_uint(b))); }
DEV f32x2 sw32f(float a, float b) { return __builtin_bit_cast(f32x2, sw32(__float_as_uint(a), __float_as_uint(b))); }
DEV u32x4 unit16(u32x2 a, u32x2 b) {
    const u32x2 x = sw16(a[0], b[0]), y = sw16(a[1], b[1]);
    return (u32x4){x[0], y[0], x[1], y[1]};
}
template <int OFF> DEV void st16wt(const void* p, u32x4 v) {
    asm volatile("global_store_dwordx4 %0, %1, off offset:%2 sc1\n\ts_nop 1" ::"v"(p), "v"(v), "n"(OFF) : "memory");
}
DEV void st8wt(const void* p, u32x2 v) { asm volatile("global_store_dwordx2 %0, %1, off sc1\n\ts_nop 1" ::"v"(p), "v"(v) : "memory"); }
template <int CTRL> DEV float dppadd(float v) {
    return v + __builtin_bit_cast(float, __builtin_amdgcn_update_dpp(0, __builtin_bit_cast(int, v), CTRL, 0xf, 0xf, true));
}
DEV int fswz(int k) { return ((k >> 1) & 1) | (((k >> 3) & 1) << 1); }
DEV int fz(int tok) { return (-(tok >> 2)) & 3; }
DEV s16x4 ldtr4(const lchar* p) { return __builtin_bit_cast(s16x4, __builtin_amdgcn_ds_read_tr16_b64_v4i16((LAS s16x4*)p)); }
DEV bf16x8 cat8(s16x4 a, s16x4 b) { return (bf16x8){a[0], a[1], a[2], a[3], b[0], b[1], b[2], b[3]}; }
DEV bf16x8 ldk(const lchar* p) { return *(const LAS bf16x8*)p; }

#define STAMP(i)
#define STAMPW(i)
#define STPARG
#define STPPASS(o)
#define STAMPB(i)
#define HT0()
#define HT1(i)
#define TSD()
#define TS(i)
#define TSW(n)
#define BAR() asm volatile("s_barrier" ::: "memory")
#define BARL() asm volatile("s_waitcnt lgkmcnt(0)\n\ts_barrier" ::: "memory")
#define WAITB(n) asm volatile("s_waitcnt vmcnt(%0)\n\ts_barrier" ::"n"(n) : "memory")
#define WAITV(n) asm volatile("s_waitcnt vmcnt(%0)" ::"n"(n) : "memory")
#define SB() __builtin_amdgcn_sched_barrier(0)
#define LGKM0() __builtin_amdgcn_s_waitcnt(0xc07f)
#define MFMA32(a, b, c) __builtin_amdgcn_mfma_f32_32x32x16_bf16(a, b, c, 0, 0, 0)
#define MFMA16(a, b, c) __builtin_amdgcn_mfma_f32_16x16x32_bf16(a, b, c, 0, 0, 0)

DEV void mfma16t(f32x4& c, const bf16x8& a, const bf16x8& b) { asm volatile("v_mfma_f32_16x16x32_bf16 %0, %1, %2, %0" : "+v"(c) : "v"(a), "v"(b)); }
#define MFMA_DONE() do { SB(); asm volatile("s_nop 7\n\ts_nop 3"); SB(); } while (0)
DEV float fmx(float a, float b) { return __builtin_elementwise_maximum(a, b); }
DEV float rsd(float var) { return __builtin_amdgcn_rcpf(bfr(__builtin_amdgcn_sqrtf(bfr(bfr(var) + 1e-5f)))); }

constexpr int XS = 18432;
constexpr int WQ0 = 81920, WQS = 24576;
constexpr int EXO = 57344;
constexpr int AR0 = 57344, AR1 = 65536, AR2 = 32768;
constexpr int SNM = 155648, SRS = SNM + 576, SCOL = SNM + 1280, DUMP = SNM + 3072;
constexpr int LDS_BYTES = 163840;
constexpr size_t MB = 1u << 20;
constexpr size_t WS_ST = 0, WS_CS = 3 * MB / 2, WS_FL = 15 * MB / 8, WS_X = 2 * MB, WS_O = 18 * MB, WS_KV = 26 * MB;
constexpr int FL_O = 0;
constexpr int FL_X = 4096;
constexpr int FL_KV = 8192;
constexpr int FL_L = 10240;
constexpr int FL_C = 7168;
constexpr int FL_E = 10496;
constexpr int FL_S = 12288;
constexpr int FL_EP = 16368;
#define FLS 16
#define FS 1
DEV int fi(int i) { return (i >> 4) * FLS + (i & 15) * FS; }

#define GL_HEAD "s_mov_b32 m0, %1\n\ts_nop 4\n\tglobal_load_lds_dwordx4 %2, %0 sc1"
DEV void glds1(const void* sb, unsigned lds, unsigned v0) { asm volatile(GL_HEAD ::"s"(sb), "s"(lds), "v"(v0) : "memory"); }
DEV void glds3(const void* sb, unsigned lds, unsigned v0, unsigned v1, unsigned v2) {
    asm volatile(GL_HEAD "\n\tglobal_load_lds_dwordx4 %3, %0 offset:1024 sc1\n\tglobal_load_lds_dwordx4 %4, %0 offset:2048 sc1"
                 ::"s"(sb), "s"(lds), "v"(v0), "v"(v1), "v"(v2) : "memory");
}
DEV void glds4(const void* sb, unsigned lds, unsigned v0, unsigned v1, unsigned v2, unsigned v3) {
    asm volatile(GL_HEAD "\n\tglobal_load_lds_dwordx4 %3, %0 offset:1024 sc1\n\tglobal_load_lds_dwordx4 %4, %0 offset:2048 sc1\n\t"
                 "global_load_lds_dwordx4 %5, %0 offset:3072 sc1" ::"s"(sb), "s"(lds), "v"(v0), "v"(v1), "v"(v2), "v"(v3) : "memory");
}
DEV void touch(const void* sb, unsigned lds, unsigned v0) {
    asm volatile("s_mov_b32 m0, %1\n\ts_nop 4\n\tglobal_load_lds_dword %2, %0 sc1" ::"s"(sb), "s"(lds), "v"(v0) : "memory");
}
DEV u64 ldep(const u64* p) { return __hip_atomic_load((const GAS u64*)p, __ATOMIC_RELAXED, __HIP_MEMORY_SCOPE_AGENT); }
#define FLD(d, a, z) "global_atomic_add_x2 " d ", " a ", " z ", off sc0 sc1"
DEV u64 ldflag(const u64* p) {
    u64 v;
    asm volatile(FLD("%0", "%1", "%2") "\n\ts_waitcnt vmcnt(0)" : "=&v"(v) : "v"(p), "v"(0ull) : "memory");
    return v;
}
DEV void stflag(u64* p, u64 v) { asm volatile("global_atomic_swap_x2 %0, %1, off sc1\n\ts_nop 1" ::"v"(p), "v"(v) : "memory"); }
template <class F> DEV unsigned poll(const u64* p, bool on, u64 tag, unsigned need, F fold) {
    unsigned m = ~0u;
#pragma unroll 1
    for (int it = 0; it < POLLMAX; ++it) {
        u64 v = tag;
        if (on) v = ldflag(p);
        const unsigned r = fold(__ballot(v == tag));
        if ((r & need) == need) { m = r; break; }
        __builtin_amdgcn_s_sleep(1);
    }
    asm volatile("" ::: "memory");
    return m;
}
template <int N = 0> DEV void publish(u64* f, int lane, u64 tag) {
    asm volatile("s_mov_b64 exec, 1\n\ts_waitcnt vmcnt(%2)\n\tglobal_atomic_swap_x2 %0, %1, off sc1\n\ts_mov_b64 exec, -1\n\ts_nop 0" ::"v"(f), "v"(tag), "n"(N) : "memory");
}
DEV void raise(u64* f, u64 tag) {
    asm volatile("s_mov_b64 exec, 1\n\tglobal_atomic_swap_x2 %0, %1, off sc1\n\ts_mov_b64 exec, -1\n\ts_nop 0" ::"v"(f), "v"(tag) : "memory");
}
#define OPAQUE(l, a, b, c) do { asm volatile("" : "+v"(l)); asm volatile("" : "+s"(a), "+s"(b), "+s"(c)); } while (0)

template <bool LOCAL, class F, class G>
DEV void qkv_main(lchar* smem, f32x4 (&acc)[6][4], f32x4 (&acch)[2], int lane, int wm, int wn, bool first, G mid, F tailread STPARG) {
    constexpr int XC_B = LOCAL ? 9216 : 8192, HO = LOCAL ? 1024 : 0, NR = LOCAL ? 17 : 16;
    const int l15 = lane & 15, lg = lane >> 4, hs = LOCAL ? wm : 0;
    const lchar* xhb = smem + l15 * 64 + ((lg ^ fz(l15)) << 4);
    const lchar* xb = xhb + HO + wm * 4096;
    const int k0 = 8 * lg + (l15 >> 2);
    const lchar* wb0 = smem + WQ0 + lg * 3072 + (l15 >> 2) * 128 + (((wn * 2 + hs) ^ fswz(k0)) << 5) + (l15 & 3) * 8;
    const lchar* wb1 = smem + WQ0 + lg * 3072 + (l15 >> 2) * 128 + (((wn * 2 + (hs ^ 1)) ^ fswz(k0)) << 5) + (l15 & 3) * 8;
#pragma unroll
    for (int a = 0; a < 6; ++a)
#pragma unroll
        for (int b = 0; b < 4; ++b) { acc[a][b] = (f32x4){0.f, 0.f, 0.f, 0.f}; asm volatile("" : "+v"(acc[a][b])); }
    acch[0] = acch[1] = (f32x4){0.f, 0.f, 0.f, 0.f};
    if (LOCAL) asm volatile("" : "+v"(acch[0]), "+v"(acch[1]), "+v"(xhb));
    asm volatile("" : "+v"(xb), "+v"(wb0), "+v"(wb1));
    bf16x8 xf[2][5];
    s16x4 wh[2][6][2];
    auto rd = [&](int b, int j, int xo, int wo) {
        if (j < 4) xf[b][j] = ldk(xb + xo + j * 1024);
        else if (j < 16) wh[b][(j - 4) >> 1][j & 1] = ldtr4(((j & 2) ? wb1 : wb0) + wo + ((j - 4) >> 2) * 1024 + (j & 1) * 512);
        else xf[b][4] = ldk(xhb + xo);
    };
    auto kstep = [&](int b, int xo, int wo, bool last) {
        int j = 0;
        if (AL == 1) asm volatile(".p2align 3");
        if (AL == 2) asm volatile(".p2align 3\n\ts_nop 0");
        SB();
#pragma unroll
        for (int ni = 0; ni < 6; ++ni) {
            const bf16x8 w = cat8(wh[b][ni][0], wh[b][ni][1]);
#pragma unroll
            for (int ti = 0; ti < 5; ++ti) {
                if (ti == 4) {
                    if (!LOCAL || (ni != 2 && ni != 4)) continue;
                    if (ni == 2) mfma16t(acch[0], w, xf[b][4]);
                    else mfma16t(acch[1], xf[b][4], w);
                } else {
                    if (ni >= 4) mfma16t(acc[ni][ti], xf[b][ti], w);
                    else mfma16t(acc[ni][ti], w, xf[b][ti]);
                }
                SB();
                if (last) tailread(j);
                else if (j < NR) rd(b ^ 1, j, xo, wo);
                SB();
                ++j;
            }
        }
        if (!last) { LGKM0(); SB(); }
    };
    if (LOCAL && PB) asm volatile("s_cmp_eq_u32 %0, 0\n\ts_cbranch_scc1 1f\n\ts_barrier\n\ts_barrier\n1:" ::"s"((int)first) : "memory", "scc");
    BAR(); SB();
    STAMP(4);
#pragma unroll
    for (int j = 0; j < NR; ++j) rd(0, j, 0, 2 * WQS);
    LGKM0(); SB();
#pragma unroll 1
    for (int it = 0; it < 5; ++it) {
        if (RI < 5) mid(it);
#pragma unroll
        for (int s = 0; s < 3; ++s) {
            kstep(0, s * XS + XC_B, ((s + 2) % 3) * WQS + WQS / 2, false);
            BAR(); SB();
            STAMPB(it * 3 + s);
            kstep(1, ((s + 1) % 3) * XS, s * WQS, false);
        }
    }
    if (RI == 5) mid(5);
    kstep(0, XC_B, 2 * WQS + WQS / 2, false);
    BAR(); SB();
    if (RI == 6) mid(6);
    STAMPB(15);
    kstep(1, 0, 0, true);
    MFMA_DONE();
}

constexpr int NRW = 7;
typedef s16x4 WReg[NRW * 8];
DEV const lchar* wo_wb(const lchar* smem, int lane, int c) {
    const int k0 = 8 * (lane >> 4) + ((lane & 15) >> 2);
    return smem + k0 * 128 + ((c ^ fswz(k0)) << 5) + (lane & 3) * 8;
}
#define PIN2(a, b) asm volatile("" : "+v"(a), "+v"(b))
#define WRD1() do { SB(); wr[wj] = ldtr4(((wj & 2) ? wb1 : wb0) + (wj >> 2) * 4096 + (wj & 1) * 512); ++wj; SB(); } while (0)

DEV void qa_compute(lchar* smem, bool fold, const u16* xin, u32x4 (&rr)[4], WReg& wr, u16* O, u64* fo, u64 tag, int g, int hp, int lane, int wid STPARG) {
    OPAQUE(lane, wid, g, hp);
    const int wm = wid >> 1, wn = wid & 1, l15 = lane & 15, lg = lane >> 4;
    f32x4 acc[6][4], acch[2];
    float nm[5], rs[5], csv[2];
    f32x4 nmv[5], rsv[5], cs[4];
    const lchar* sa = smem + SNM + l15 * 4;
    const lchar* sb = smem + SNM + lg * 16;
    const lchar* sc = smem + SCOL + wn * 128;
    const int ro = 64 + wm * 256;
    f32x4 cprev, ccur;
#pragma unroll
    for (int j = 0; j < 4; ++j) {
        const int d = l15 - 4 * lg - j;
        ccur[j] = (d >= 0 && !(d & 1)) ? 0.f : -3.0e38f;
        cprev[j] = (d <= -2 && !(d & 1)) ? 0.f : -3.0e38f;
    }
    const char* optr = (const char*)O + ((size_t)wn * SEQ + g * 128 + wm * 64 + l15) * 64 +
                       (((((lg & 1) << 1) | (lg >> 1)) ^ (wm << 1) ^ fz(l15)) << 4);
    unsigned xo = rr[0][0];
    asm volatile("" : "+v"(cprev), "+v"(ccur), "+v"(optr), "+v"(xo));
    qkv_main<true>(smem, acc, acch, lane, wm, wn, !fold, [&](int it) {
        asm volatile("s_cmp_lg_u32 %6, %7\n\ts_cbranch_scc1 1f\n\tglobal_load_dwordx4 %0, %4, %5\n\tv_add_u32 %4, 0x8000, %4\n\t"
                     "global_load_dwordx4 %1, %4, %5\n\tv_add_u32 %4, 0x8000, %4\n\tglobal_load_dwordx4 %2, %4, %5\n\tv_add_u32 %4, 0x8000, %4\n\t"
                     "global_load_dwordx4 %3, %4, %5\n1:" : "+v"(rr[0]), "+v"(rr[1]), "+v"(rr[2]), "+v"(rr[3]), "+v"(xo)
                     : "s"(xin), "s"(it), "s"(fold ? -1 : RI) : "memory", "scc");
    }, [&](int j) {
        if (j < 4) cs[j] = *(const LAS f32x4*)(sc + (j >> 1) * 256 + (((j & 1) ^ wm) << 6) + lg * 16);
        else if (j < 9) { const int o = j < 8 ? ro + (j - 4) * 64 : 0; nm[j - 4] = *(const LAS float*)(sa + o); rs[j - 4] = *(const LAS float*)(sa + o + 576); }
        else if (j < 11) csv[j - 9] = *(const LAS float*)(sc + 512 + (((j - 9) ^ wm) << 6) + l15 * 4);
        else if (j < 21) {
            const int t = (j - 11) >> 1, o = t < 4 ? ro + t * 64 : 0;
            if ((j - 11) & 1) rsv[t] = *(const LAS f32x4*)(sb + o + 576);
            else nmv[t] = *(const LAS f32x4*)(sb + o);
        }
    } STPPASS(0));
    STAMP(2);
    TSD(); TS(0);
    if (fold) {
#pragma unroll
        for (int ti = 0; ti < 4; ++ti) {
#pragma unroll
            for (int ni = 0; ni < 4; ++ni) acc[ni][ti] = (acc[ni][ti] + nm[ti] * cs[ni]) * rs[ti];
#pragma unroll
            for (int h = 0; h < 2; ++h) acc[4 + h][ti] = (acc[4 + h][ti] + nmv[ti] * csv[h]) * rsv[ti];
        }
        acch[0] = (acch[0] + nm[4] * cs[2]) * rs[4];
        acch[1] = (acch[1] + nmv[4] * csv[0]) * rsv[4];
    }
    typedef __attribute__((ext_vector_type(10))) unsigned u32x10;
    u32x2 kp[5][2];
    u32x10 vp[2];
    auto setv = [&](int h, int i, u32x2 a) { vp[h][2 * i] = a[0]; vp[h][2 * i + 1] = a[1]; };
    lchar* ex = smem + EXO + wn * 4096 + lane * 16;
    {
        const u32x2 a = pk4(acch[0]), b = pk4(acch[1]);
        *(LAS u32x4*)(ex + wm * 1024) = (u32x4){a[0], a[1], b[0], b[1]};
    }
#pragma unroll
    for (int h = 0; h < 2; ++h) { kp[4][h] = pk4(acc[2 + h][3]); setv(h, 4, pk4(acc[4 + h][3])); }
    if (wm == 0) {
#pragma unroll
        for (int h = 0; h < 2; ++h) *(LAS u32x4*)(ex + 2048 + h * 1024) = (u32x4){kp[4][h][0], kp[4][h][1], vp[h][8], vp[h][9]};
    }
    bf16x8 qf[4];
#pragma unroll
    for (int ti = 0; ti < 4; ++ti) {
        qf[ti] = cat(pk4(acc[0][ti]), pk4(acc[1][ti]));
        if (ti < 3) {
#pragma unroll
            for (int h = 0; h < 2; ++h) { kp[ti + 1][h] = pk4(acc[2 + h][ti]); setv(h, ti + 1, pk4(acc[4 + h][ti])); }
        }
    }
    const lchar* wb0 = wo_wb(smem, lane, wn * 2);
    const lchar* wb1 = wo_wb(smem, lane, wn * 2 + 1);
    const lchar* e0 = ex + wm * 3072;
    const lchar* e1 = ex + wm * 1024 + 1024;
    f32x4 cp0 = cprev;
    if (g == 0 && wm == 0) cp0 = (f32x4){-3.0e38f, -3.0e38f, -3.0e38f, -3.0e38f};
    unsigned sel0 = (lane & 1) ? 0x01000c0cu : 0x0c0c0100u, sel1 = (lane & 1) ? 0x03020c0cu : 0x0c0c0302u;
    bf16x8 ones = {0x3f80, 0x3f80, 0x3f80, 0x3f80, 0x3f80, 0x3f80, 0x3f80, 0x3f80};
    asm volatile("" : "+v"(wb0), "+v"(wb1), "+v"(e0), "+v"(e1), "+v"(cp0), "+v"(sel0), "+v"(sel1), "+v"(ones), "+v"(vp[0]), "+v"(vp[1]));
    SB();
    TS(1);
    BARL();
    TS(2);
    kp[0][0] = *(const LAS u32x2*)e0; SB(); kp[0][1] = *(const LAS u32x2*)e1; SB();
    setv(0, 0, *(const LAS u32x2*)(e0 + 8)); SB(); setv(1, 0, *(const LAS u32x2*)(e1 + 8));
    SB();
    int wj = 0;
    f32x4 s0[4], s1[4];
    f32x2 sv[4][2];
    float mx[4];
#pragma unroll
    for (int ti = 3; ti >= 0; --ti) {
        s1[ti] = MFMA16(cat(kp[ti + 1][0], kp[ti + 1][1]), qf[ti], ccur);
        WRD1();
        s0[ti] = MFMA16(cat(kp[ti][0], kp[ti][1]), qf[ti], ti ? cprev : cp0);
        WRD1();
    }
#pragma unroll
    for (int i = 0; i < 48; ++i) if (wj < 48) WRD1();
#pragma unroll
    for (int i = 7; i >= 0; --i) {
        const f32x4& t = (i & 1) ? s1[i >> 1] : s0[i >> 1];
        sv[i >> 1][i & 1] = (f32x2){fmx(t[0], t[1]), fmx(t[2], t[3])};
        asm volatile("" : "+v"(sv[i >> 1][i & 1]));
    }
    if (WA == 0) { SB(); LGKM0(); BAR(); SB(); }
    if (EMU) {
        unsigned r[8];
#pragma unroll
        for (int ps = 0; ps < 2; ++ps) {
#pragma unroll
            for (int i = 7; i >= 0; --i) r[i] = cvtpk(sv[i >> 1][i & 1][0], sv[i >> 1][i & 1][1]);
#pragma unroll
            for (int i = 7; i >= 0; --i) {
                sv[i >> 1][i & 1] = (f32x2){lo16(r[i]), hi16(r[i])};
                if (ps == 0) sv[i >> 1][i & 1] *= QSCALE;
            }
        }
    }
#pragma unroll
    for (int ti = 3; ti >= 0; --ti) mx[ti] = fmx(fmx(fmx(sv[ti][0][0], sv[ti][0][1]), sv[ti][1][0]), sv[ti][1][1]);
    TS(3);
    {
        const f32x2 a = sw16f(mx[0], mx[1]), b = sw16f(mx[2], mx[3]);
        const f32x2 c = sw32f(fmx(a[0], a[1]), fmx(b[0], b[1]));
        const float r = fmx(c[0], c[1]) * -(EMU ? LOG2E : QSCALE * LOG2E);
        const f32x2 d = sw32f(r, r), e = sw16f(d[0], d[0]), f = sw16f(d[1], d[1]);
        mx[0] = e[0]; mx[1] = e[1]; mx[2] = f[0]; mx[3] = f[1];
    }
    SB();
    TS(4);
    if (WA == 1) { LGKM0(); BAR(); SB(); }
    TS(5);
    if (EO >= 1) { raise(fo, tag); SB(); }
    const f32x4 z = {0.f, 0.f, 0.f, 0.f};
    f32x4 l[4], o0[4], o1[4];
    auto pv = [&](int ti, u32x4 v0, u32x4 v1) {
        const f32x2 x = sv[ti][0] * (EMU ? LOG2E : QSCALE * LOG2E) + mx[ti], y = sv[ti][1] * (EMU ? LOG2E : QSCALE * LOG2E) + mx[ti];
        const unsigned c0 = cvtpk(__builtin_amdgcn_exp2f(x[0]), __builtin_amdgcn_exp2f(x[1]));
        const unsigned c1 = cvtpk(__builtin_amdgcn_exp2f(y[0]), __builtin_amdgcn_exp2f(y[1]));
        const bf16x8 p = __builtin_bit_cast(bf16x8, (u32x4){__builtin_amdgcn_perm(c0, c0, sel0), __builtin_amdgcn_perm(c0, c0, sel1),
                                                            __builtin_amdgcn_perm(c1, c1, sel0), __builtin_amdgcn_perm(c1, c1, sel1)});
        SB();
        l[ti] = MFMA16(ones, p, z);
        WRD1();
        o0[ti] = MFMA16(__builtin_bit_cast(bf16x8, v0), p, z);
        WRD1();
        o1[ti] = MFMA16(__builtin_bit_cast(bf16x8, v1), p, z);
        SB();
    };
    auto fin = [&](int ti) {
        const float inv = __builtin_amdgcn_rcpf(l[ti][0]);
        return unit16(pk4(o0[ti] * inv), pk4(o1[ti] * inv));
    };
#define PV(ti) pv(ti, __builtin_shufflevector(vp[0], vp[0], 2 * ti, 2 * ti + 1, 2 * ti + 2, 2 * ti + 3), \
                      __builtin_shufflevector(vp[1], vp[1], 2 * ti, 2 * ti + 1, 2 * ti + 2, 2 * ti + 3))
    PV(0); PV(1);
    TS(6);
    st16wt<0>(optr, fin(0)); SB();
    if (WA == 2) { __builtin_amdgcn_s_waitcnt(0xc47f); BAR(); SB(); }
    TS(7);
    PV(2);
    st16wt<1024>(optr, fin(1)); SB();
    if (WA == 3) { __builtin_amdgcn_s_waitcnt(0xc67f); BAR(); SB(); }
    TS(8);
    PV(3);
    st16wt<2048>(optr, fin(2)); SB();
    if (WA == 4) { __builtin_amdgcn_s_waitcnt(0xc87f); BAR(); SB(); }
    TS(9);
    st16wt<3072>(optr, fin(3));
    TS(10);
    if (EO == 0) raise(fo, tag);
    TS(11);
    TSW(12);
    STAMP(10);
}

#define MX3(a, b, c) fmx(fmx((a), (b)), (c))
DEV float rowmax32(const f32x16& s0) {
    float ma = MX3(s0[0], s0[1], s0[2]), mb = MX3(s0[3], s0[4], s0[5]);
    ma = MX3(ma, s0[6], s0[7]); mb = MX3(mb, s0[8], s0[9]);
    ma = MX3(ma, s0[10], s0[11]); mb = MX3(mb, s0[12], s0[13]);
    ma = fmx(MX3(ma, s0[14], s0[15]), mb);
    const f32x2 b = sw32f(ma, ma);
    return fmx(b[0], b[1]);
}
#define EX(i) do { S[i] = __builtin_amdgcn_exp2f(S[i]); SB(); } while (0)
#define EQ(i) do { p1[i] = __builtin_amdgcn_exp2f(S[8 + i]); SB(); } while (0)
typedef __attribute__((ext_vector_type(16))) __bf16 bf16x16_t;
typedef __attribute__((ext_vector_type(8))) __bf16 bf16x8_t;
typedef __attribute__((ext_vector_type(8))) float f32x8;
template <int H> DEV f32x8 half8(const f32x16& s) { return __builtin_shufflevector(s, s, H, H + 1, H + 2, H + 3, H + 4, H + 5, H + 6, H + 7); }
#define SMF32(a, b, c) __builtin_amdgcn_smfmac_f32_32x32x32_bf16(__builtin_bit_cast(bf16x8_t, a), __builtin_bit_cast(bf16x16_t, b), c, hi, 0, 0)
#define SMF16(a, b, c) __builtin_amdgcn_smfmac_f32_16x16x64_bf16(__builtin_bit_cast(bf16x8_t, a), __builtin_bit_cast(bf16x16_t, b), c, hi, 0, 0)
template <class G0, class F1, class F2, class F3>
DEV void block(f32x16& S, f32x16& N, const bf16x8& kf0, const bf16x8& kf1, const bf16x8& q0, const bf16x8& q1, const f32x16& c,
               f32x16& oP, f32x4& lP, const bf16x8& vf1, f32x8& p1, f32x16& oC, f32x4& lC, const bf16x8& vf0,
               const bf16x8& sel, int hi, G0 fb, F1 fc, F2 fd, F3 ff) {
    N = MFMA32(kf0, q0, c); SB();
    EX(0); EX(1); EX(2);
    oP = SMF32(vf1, p1, oP); SB();
    fb(); SB();
    EX(3); EX(4); EX(5);
    lP = SMF16(sel, p1, lP); SB();
    fc(); SB();
    EX(6);
    N = MFMA32(kf1, q1, N); SB();
    fd(); SB();
    EX(7); EQ(0); EQ(1);
    oC = SMF32(vf0, half8<0>(S), oC); SB();
    EQ(2); EQ(3); EQ(4);
    lC = SMF16(sel, half8<0>(S), lC); SB();
    ff(); SB();
    EQ(5); EQ(6); EQ(7);
}
DEV void attn_slow(const char* kh, const char* vh, const bf16x8& q0, const bf16x8& q1, f32x16& o, float& l, int hi) {
    f32x16 c;
#pragma unroll
    for (int i = 0; i < 16; ++i) { o[i] = 0.f; c[i] = 0.f; }
    float m = 0.f;
    l = 0.f;
#pragma unroll 1
    for (int t = 0; t < SEQ / 32; ++t) {
        bf16x8 k0, k1, v0, v1;
        const char* kp = kh + t * 2048;
        const char* vp = vh + t * 2048;
        asm volatile("global_load_dwordx4 %0, %4, off sc1\n\tglobal_load_dwordx4 %1, %5, off sc1\n\tglobal_load_dwordx4 %2, %6, off sc1\n\t"
                     "global_load_dwordx4 %3, %7, off sc1\n\ts_waitcnt vmcnt(0)" : "=&v"(k0), "=&v"(k1), "=&v"(v0), "=&v"(v1)
                     : "v"(kp), "v"((const char*)((uintptr_t)kp ^ 32)), "v"(vp), "v"((const char*)((uintptr_t)vp ^ 32)) : "memory");
        f32x16 s = MFMA32(k0, q0, c);
        s = MFMA32(k1, q1, s);
        const float mx = rowmax32(s);
        const float dl = t == 0 ? mx : fmx(mx, 0.f);
        const float alpha = t == 0 ? 0.f : __builtin_amdgcn_exp2f(-dl);
        m += dl;
        l *= alpha;
#pragma unroll
        for (int i = 0; i < 16; ++i) {
            o[i] *= alpha; c[i] = -m;
            s[i] = __builtin_amdgcn_exp2f(s[i] - dl);
            l += hi16(__float_as_uint(s[i]));
        }
        o = SMF32(v0, half8<0>(s), o);
        o = SMF32(v1, half8<8>(s), o);
    }
    const f32x2 b = sw32f(l, l);
    l = b[0] + b[1];
}

DEV void qb_compute(lchar* smem, u16* kv, WReg& wr, u16* O, u64* fo, u64* fkv, u64 tag, int g, int hp, int lane, int wid STPARG) {
    OPAQUE(lane, wid, g, hp);
    const int wm = wid >> 1, wn = wid & 1, l15 = lane & 15, lg = lane >> 4, h = hp * 2 + wn;
    bf16x8 qa0, qa1, qb0, qb1;
    lchar* gs = smem + AR0 + wn * 16384;
    {
        f32x4 acc[6][4], acch[2];
        float nm[4], rs[4], csv[2];
        f32x4 cs[4], nmv[4], rsv[4];
        const lchar* sa = smem + SNM + 64 + wm * 256 + l15 * 4;
        const lchar* sb = smem + SNM + 64 + wm * 256 + lg * 16;
        const lchar* sc = smem + SCOL + wn * 128;
        const int un = ((lg & 1) << 1) | (lg >> 1);
        const int ko = l15 * 64 + ((un ^ (l15 >> 2)) << 4), vo = l15 * 64 + ((lg ^ (l15 >> 2)) << 4);
        const char* kptr = (const char*)kv + ((size_t)h * SEQ + g * 128 + wm * 64) * 64 + ko;
        const char* vptr = (const char*)kv + ((size_t)(NH + h) * SEQ + g * 128 + wm * 64) * 64 + vo;
        lchar* kl = gs + wm * 8192 + ko;
        lchar* vl = gs + wm * 8192 + 4096 + vo;
        asm volatile("" : "+v"(kptr), "+v"(vptr));
        qkv_main<false>(smem, acc, acch, lane, wm, wn, false, [](int) {}, [&](int j) {
            if (j < 4) cs[j] = *(const LAS f32x4*)(sc + (j >> 1) * 256 + (j & 1) * 64 + lg * 16);
            else if (j < 8) { nm[j - 4] = *(const LAS float*)(sa + (j - 4) * 64); rs[j - 4] = *(const LAS float*)(sa + (j - 4) * 64 + 576); }
            else if (j < 10) csv[j - 8] = *(const LAS float*)(sc + 512 + (j - 8) * 64 + l15 * 4);
            else if (j < 18) {
                if (j & 1) rsv[(j - 10) >> 1] = *(const LAS f32x4*)(sb + ((j - 10) >> 1) * 64 + 576);
                else nmv[(j - 10) >> 1] = *(const LAS f32x4*)(sb + ((j - 10) >> 1) * 64);
            }
        } STPPASS(0));
        STAMP(2);
        auto tile = [&](int ni, int ti, float sc_) { return pk4((acc[ni][ti] + nm[ti] * cs[ni]) * sc_); };
        auto tilev = [&](int h_, int ti) { return pk4((acc[4 + h_][ti] + nmv[ti] * csv[h_]) * rsv[ti]); };
#define KOUT(ti) do { const u32x4 v = unit16(tile(2, ti, rs[ti]), tile(3, ti, rs[ti])); st16wt<ti * 1024>(kptr, v); *(LAS u32x4*)(kl + ti * 1024) = v; } while (0)
#define VOUT(j) do { const u32x4 v = __builtin_bit_cast(u32x4, cat(tilev(j & 1, j & 2), tilev(j & 1, (j & 2) + 1))); st16wt<j * 1024>(vptr, v); *(LAS u32x4*)(vl + j * 1024) = v; } while (0)
        KOUT(0); KOUT(1); KOUT(2); KOUT(3);
        VOUT(0); VOUT(1); VOUT(2); VOUT(3);
        if (EF & 4) raise(fkv + fi(wn * 32 + wm * 16 + g), tag);
        SB(); BARL(); SB();
#pragma unroll
        for (int ti = 0; ti < 4; ++ti) rs[ti] *= QSCALE * LOG2E;
        qa0 = __builtin_bit_cast(bf16x8, unit16(tile(0, 0, rs[0]), tile(0, 1, rs[1])));
        qa1 = __builtin_bit_cast(bf16x8, unit16(tile(1, 0, rs[0]), tile(1, 1, rs[1])));
        qb0 = __builtin_bit_cast(bf16x8, unit16(tile(0, 2, rs[2]), tile(0, 3, rs[3])));
        qb1 = __builtin_bit_cast(bf16x8, unit16(tile(1, 2, rs[2]), tile(1, 3, rs[3])));
    }
    STAMP(3);
    const int r = lane & 31, hh = lane >> 5;
    const int kro = ((r & 4) * 4 + (r >> 3) * 4 + (r & 3)) * 64 + ((hh ^ (r >> 3)) << 4), vro = r * 64 + ((hh ^ ((r >> 2) & 3)) << 4);
    const lchar* kb0 = gs + kro;
    const lchar* kb1 = gs + (kro ^ 32);
    const lchar* vb0 = gs + 4096 + vro;
    const lchar* vb1 = gs + 4096 + (vro ^ 32);
    const short se = (l15 & 1) ? (short)0 : (short)0x3f80, so = se ^ 0x3f80;
    const bf16x8 sel = {se, se, se, se, so, so, so, so};
    int hi = 0xdddd;
    const char* op = (const char*)O + ((size_t)wn * SEQ + g * 128 + wm * 64 + l15) * 64 + (((((lg & 1) << 1) | (lg >> 1)) ^ fz(l15)) << 4);
    const lchar* wb0 = wo_wb(smem, lane, wn * 2);
    const lchar* wb1 = wo_wb(smem, lane, wn * 2 + 1);
    asm volatile("" : "+v"(hi), "+v"(op), "+v"(wb0), "+v"(wb1));
    f32x16 oA, oB, cA, cB, sA, sB;
    f32x4 lA = {0.f, 0.f, 0.f, 0.f}, lB = {0.f, 0.f, 0.f, 0.f};
#pragma unroll
    for (int i = 0; i < 16; ++i) { oA[i] = 0.f; oB[i] = 0.f; }
    bf16x8 kf0, kf1, vf0, vf1[2];
    f32x8 p1 = {0.f, 0.f, 0.f, 0.f, 0.f, 0.f, 0.f, 0.f};
    auto nop = [&]() {};
    auto tilestep = [&](int par, int nx, bool bar) {
        block(sA, sB, kf0, kf1, qb0, qb1, cB, oB, lB, vf1[par ^ 1], p1, oA, lA, vf0, sel, hi, [&]() { if (bar) BAR(); },
              [&]() { kf0 = ldk(kb0 + nx); }, [&]() { __builtin_amdgcn_s_waitcnt(0xc17f); }, [&]() { LGKM0(); SB(); kf1 = ldk(kb1 + nx); });
        block(sB, sA, kf0, kf1, qa0, qa1, cA, oA, lA, vf1[par], p1, oB, lB, vf0, sel, hi, nop,
              [&]() { LGKM0(); SB(); vf1[par ^ 1] = ldk(vb1 + nx); }, nop, [&]() { vf0 = ldk(vb0 + nx); });
    };
    {
        const int own = wm * 8192, oth = own ^ 8192;
        kf0 = ldk(kb0 + own); kf1 = ldk(kb1 + own);
        vf0 = ldk(vb0 + own); vf1[0] = ldk(vb1 + own); vf1[1] = sel;
        asm volatile("" : "+v"(qa0), "+v"(qa1), "+v"(qb0), "+v"(qb1)); SB();
        if (!(EF & 4)) publish(fkv + fi(wn * 32 + wm * 16 + g), lane, tag);
        SB();
        sA = MFMA32(kf0, qa0, oA); sB = MFMA32(kf0, qb0, oA);
        sA = MFMA32(kf1, qa1, sA); sB = MFMA32(kf1, qb1, sB);
        const float mA = rowmax32(sA), mB = rowmax32(sB);
#pragma unroll
        for (int i = 0; i < 16; ++i) { cA[i] = -mA; cB[i] = -mB; sA[i] -= mA; }
        asm volatile("" : "+v"(cA), "+v"(cB));
        SB();
        STAMP(5);
        tilestep(0, own + 2048, false);
        tilestep(1, oth, false);
        tilestep(0, oth + 2048, false);
        tilestep(1, AR1 + 8192, true);
        STAMP(6);
    }
#pragma unroll 1
    for (int it = 0; it < 5; ++it) {
#pragma unroll
        for (int s = 0; s < 3; ++s) {
            const int cur = s == 0 ? AR1 : s == 1 ? AR2 : 0, nxt = s == 0 ? AR2 : s == 1 ? 0 : AR1;
            tilestep(0, cur + 10240, false);
            tilestep(1, cur, false);
            tilestep(0, cur + 2048, false);
            tilestep(1, nxt + 8192, true);
        }
    }
    oB = SMF32(vf1[1], p1, oB);
    lB = SMF16(sel, p1, lB);
    SB();
    STAMP(9);
    TSD(); TS(0);
    int wj = 0;
    auto fin = [&](const f32x16& o, float l, u32x4& u0, u32x4& u1, bool rd) {
        const float inv = __builtin_amdgcn_rcpf(l);
        unsigned y[8];
        if (rd) { WRD1(); WRD1(); }
#pragma unroll
        for (int i = 0; i < 8; ++i) {
            const f32x2 t = (f32x2){o[2 * i], o[2 * i + 1]} * inv;
            if (rd) WRD1();
            y[i] = cvtpk(t[0], t[1]);
        }
        const u32x2 a = sw32(y[0], y[2]), b = sw32(y[1], y[3]);
        if (rd) WRD1();
        const u32x2 c = sw32(y[4], y[6]), d = sw32(y[5], y[7]);
        if (rd) WRD1();
        const u32x2 e = sw16(a[0], c[0]), f = sw16(b[0], d[0]);
        if (rd) WRD1();
        const u32x2 i = sw16(a[1], c[1]), j = sw16(b[1], d[1]);
        if (rd) WRD1();
        u0 = (u32x4){e[0], f[0], i[0], j[0]};
        u1 = (u32x4){e[1], f[1], i[1], j[1]};
    };
    u32x4 u0, u1;
    float la = (lane & 16) ? lA[1] : lA[0];
#pragma unroll
    for (int i = 0; i < WB; ++i) WRD1();
    fin(oA, la, u0, u1, false);
    float lb = (lane & 16) ? lB[1] : lB[0];
    if (__builtin_expect(__any(!(la <= 1073741824.0f) || !(lb <= 1073741824.0f)), 0)) {
        const char* kh = (const char*)kv + (size_t)h * SEQ * 64;
#pragma unroll 1
        for (int ps = 0; ps < 2; ++ps) {
            fin(oB, lb, u0, u1, false);
            attn_slow(kh + kro, kh + (size_t)NH * SEQ * 64 + vro, ps ? qb0 : qa0, ps ? qb1 : qa1, oB, lb, hi);
        }
    }
    TS(1);
    if (EO >= 1) { SB(); raise(fo, tag); SB(); }
    st16wt<0>(op, u0); TS(2); st16wt<1024>(op, u1);
    if (WB == 48) { SB(); LGKM0(); BAR(); SB(); }
    TS(3);
    fin(oB, lb, u0, u1, false);
    TS(4);
    st16wt<2048>(op, u0); TS(5); st16wt<3072>(op, u1);
    TS(6);
    if (EO == 0) raise(fo, tag);
    TS(7);
    STAMP(10);
#pragma unroll
    for (int i = 0; i < 56; ++i) if (wj < 56) WRD1();
    TS(8);
    if (WB < 48) { __builtin_amdgcn_s_waitcnt(0xc87f); BAR(); SB(); }
    TS(9);
    TSW(10);
}

DEV constexpr int ko(int s) { return (int)(KORD >> (4 * s)) & 15; }
constexpr u64 kinv() { u64 r = 0; for (int s = 0; s < 16; ++s) r |= (u64)s << (4 * ((KORD >> (4 * s)) & 15)); return r; }
DEV constexpr unsigned wo_half(int i) { return (unsigned)(ko(i >> 1) * 2 + (i & 1)) * (32 * 2048); }
DEV constexpr int wo_unit(int t) { return t >= 7 && t < 10 ? t + 9 : t; }
DEV unsigned wo_voff(int lane, int cb, int i) {
    const int k = 8 * i + (lane >> 3), pc = lane & 7;
    return (k * 1024 + cb * 64 + ((((pc >> 1) ^ fswz(k)) << 1) + (pc & 1)) * 8) * 2 - i * 1024;
}
DEV void wo_compute(lchar* smem, u64* fo, u32x4 (&rr)[4], WReg& wr, u16* C, char* stats, u64* fx, u64 tag, int g, int cb, int lane,
                    int wid STPARG) {
    OPAQUE(lane, wid, g, cb);
    const int wm = wid >> 1, wn = wid & 1, l15 = lane & 15, lg = lane >> 4;
    const lchar* wb0 = wo_wb(smem + WQ0, lane, wn * 2);
    const lchar* wb1 = wo_wb(smem + WQ0, lane, wn * 2 + 1);
    asm volatile("" : "+v"(wb0), "+v"(wb1));
    bf16x8 xf[2][8];
    s16x4 wh[16][8];
    auto rdw = [&](int t, int j) { wh[t][j] = ldtr4(((j & 2) ? wb1 : wb0) + wo_unit(t) * 8192 - WQ0 + (j >> 2) * 4096 + (j & 1) * 512); };
#pragma unroll
    for (int j = 0; j < NRW * 8; ++j) wh[j >> 3][j & 7] = wr[j];
    WAITV(5);
    STAMP(5);
    STAMPW(6 + wid);
    asm volatile("" : "+v"(lane), "+v"(rr[0]), "+v"(rr[1]), "+v"(rr[2]), "+v"(rr[3]));
    const int un = ((lg & 1) << 1) | (lg >> 1), row = g * 128 + wm * 64 + l15, ch = cb * 2 + wn;
    const unsigned ocm = (ch * SEQ + row) * 64 + ((un ^ fz(l15)) << 4), orm = row * 2048 + ch * 64 + un * 16;
    const char* cp = (const char*)C + (stats ? ocm : orm);
    const unsigned cs = stats ? 1024 : 32768;
    const char* sp = stats + ((g * 2 + wm) * 32 + ch) * 512 + lane * 8;
    const lchar* xb = smem + (wm * 64 + l15) * 64 + ((lg ^ fz(l15)) << 4);
    asm volatile("" : "+v"(cp), "+v"(sp), "+v"(xb));
    f32x4 acc[2][4];
#pragma unroll
    for (int ti = 0; ti < 4; ++ti) {
        const u32x2 x = sw16(rr[ti][0], rr[ti][2]), y = sw16(rr[ti][1], rr[ti][3]);
#pragma unroll
        for (int ni = 0; ni < 2; ++ni) acc[ni][ti] = (f32x4){lo16(x[ni]), hi16(x[ni]), lo16(y[ni]), hi16(y[ni])};
    }
    auto rdx = [&](int t, int j) { xf[t & 1][j] = ldk(xb + ((t + 4) % 5) * 16384 + (j >> 2) * 8192 + (j & 3) * 1024); };
    auto mm = [&](int t, int kk, int j) {
        const int ti = j >> 1, ni = j & 1;
        mfma16t(acc[ni][ti], cat8(wh[t][kk * 4 + ni * 2], wh[t][kk * 4 + ni * 2 + 1]), xf[t & 1][kk * 4 + ti]);
    };
    SB();
    LGKM0(); SB();
#pragma unroll
    for (int t = 0; t < 16; ++t) {
        BAR(); SB();
        if ((EF & 2) && EXP && t == 16 - EXP && stats) { raise(fx + fi((wn * 32 + g * 2 + wm) * 16 + cb), tag); SB(); }
        STAMPB(t);
        if (t == 0) {
            STAMP(4);
#pragma unroll
            for (int j = 0; j < 8; ++j) rdx(0, j);
            SB();
        } else {
#pragma unroll
            for (int j = 0; j < 8; ++j) { rdx(t, j); SB(); mm(t - 1, 1, j); SB(); }
        }
        const bool more = t + 1 >= NRW && t < 15;
        if (WS) {
            __builtin_amdgcn_s_waitcnt(0xc47f); SB();
#pragma unroll
            for (int j = 0; j < 8; ++j) {
                if (more && (WS == 1 || j)) { rdw(t + 1, WS == 1 ? j : j - 1); SB(); }
                if (more && WS == 2 && j == 7) { rdw(t + 1, 7); SB(); }
                mm(t, 0, j); SB();
            }
        } else
#pragma unroll
        for (int j = 0; j < 8; ++j) { mm(t, 0, j); SB(); if (more) { rdw(t + 1, j); SB(); } }
        if (more) __builtin_amdgcn_s_waitcnt(0xc87f); else LGKM0();
        SB();
    }
    u32x4 (&ov)[4] = rr;
    auto out = [&](int ti) {
        asm volatile("" : "+v"(acc[0][ti]), "+v"(acc[1][ti]));
        ov[ti] = unit16(pk4(acc[0][ti]), pk4(acc[1][ti]));
        asm volatile("global_store_dwordx4 %0, %1, off sc1\n\ts_nop 1" ::"v"(cp + ti * cs), "v"(ov[ti]) : "memory");
        SB();
    };
#pragma unroll
    for (int j = 0; j < 6; ++j) mm(15, 1, j);
    SB();
    out(0);
    mm(15, 1, 6); mm(15, 1, 7); SB();
    out(1); out(2); out(3);
    STAMP(2);
    if (stats) {
        if ((EF & 2) && !EXP) { raise(fx + fi((wn * 32 + g * 2 + wm) * 16 + cb), tag); SB(); }
        float s[4], q[4];
        const bf16x2_t one2 = __builtin_bit_cast(bf16x2_t, 0x3f803f80u);
#pragma unroll
        for (int ti = 0; ti < 4; ++ti) {
            s[ti] = 0.f; q[ti] = 0.f;
#pragma unroll
            for (int e = 0; e < 4; ++e) {
                const unsigned u = ov[ti][e];
                const bf16x2_t v = __builtin_bit_cast(bf16x2_t, u);
                s[ti] = __builtin_amdgcn_fdot2_f32_bf16(v, one2, s[ti], false);
                q[ti] = __builtin_amdgcn_fdot2_f32_bf16(v, v, q[ti], false);
            }
        }
        auto red = [&](const float (&v)[4]) {
            const f32x2 a = sw16f(v[0], v[1]), b = sw16f(v[2], v[3]);
            const f32x2 c = sw32f(a[0] + a[1], b[0] + b[1]);
            return c[0] + c[1];
        };
        st8wt(sp, (u32x2){__float_as_uint(red(s)), __float_as_uint(red(q))});
        STAMP(10);
        if (!(EF & 2)) publish<1>(fx + fi((wn * 32 + g * 2 + wm) * 16 + cb), lane, tag);
        STAMP(11);
        STAMPW(12 + wid);
        publish<(EF & 2) ? 0 : 1>(fx + fi(FL_S - FL_X + (wn * 32 + g * 2 + wm) * 16 + cb), lane, tag);
    } else {
        STAMP(10);
    }
}

DEV unsigned ln0_stats(const u32x4 (&x)[4]) {
    const bf16x2_t one2 = __builtin_bit_cast(bf16x2_t, 0x3f803f80u);
    float s[2] = {0.f, 0.f}, q[2] = {0.f, 0.f};
#pragma unroll
    for (int i = 0; i < 16; ++i) {
        const unsigned u = x[i >> 2][i & 3];
        const bf16x2_t v = __builtin_bit_cast(bf16x2_t, u);
        s[i & 1] = __builtin_amdgcn_fdot2_f32_bf16(v, one2, s[i & 1], false);
        q[i & 1] = __builtin_amdgcn_fdot2_f32_bf16(v, v, q[i & 1], false);
    }
    const f32x2 a = sw32f(s[0] + s[1], q[0] + q[1]);
    float v = dppadd<0x128>(dppadd<0x4E>(dppadd<0xB1>(a[0] + a[1])));
    const f32x2 b = sw16f(v, v);
    v = (b[0] + b[1]) * (1.0f / DM);
    const f32x2 c = sw32f(v, v);
    return cvtpk(c[0], __builtin_amdgcn_sqrtf(bfr(bfr(fmx(c[1] - c[0] * c[0], 0.f)) + 1e-5f)));
}
typedef f32x2 Raw[4];
DEV void unpk(Raw& v, u32x4 x) {
#pragma unroll
    for (int i = 0; i < 4; ++i) { v[i] = (f32x2){lo16(x[i]), hi16(x[i])}; asm volatile("" : "+v"(v[i])); }
}
DEV u32x4 ln0_apply(const Raw& v, f32x2 st) {
    u32x4 x;
#pragma unroll
    for (int i = 0; i < 4; ++i) {
        const f32x2 e = v[i] - st[0];
        f32x2 d;
        asm("v_cvt_pk_bf16_f32 %0, 0, %1" : "=v"(d[0]) : "v"(e[0]));
        asm("v_cvt_pk_bf16_f32 %0, 0, %1" : "=v"(d[1]) : "v"(e[1]));
        d *= st[1];
        x[i] = cvtpk(d[0], d[1]);
    }
    return x;
}
DEV void ln0_rows(u32x4 (&x)[4]) {
    auto sum32 = [&](float v) {
        v = dppadd<0x128>(dppadd<0x4E>(dppadd<0xB1>(v)));
        const f32x2 a = sw16f(v, v);
        v = a[0] + a[1];
        const f32x2 b = sw32f(v, v);
        return b[0] + b[1];
    };
    f32x2 v[16];
#pragma unroll
    for (int i = 0; i < 16; ++i) v[i] = (f32x2){lo16(x[i >> 2][i & 3]), hi16(x[i >> 2][i & 3])};
    f32x2 s = v[0];
#pragma unroll
    for (int i = 1; i < 16; ++i) s += v[i];
    const float mean = sum32(s[0] + s[1]) * (1.0f / DM);
    f32x2 q = {0.f, 0.f};
#pragma unroll
    for (int i = 0; i < 16; ++i) { const f32x2 d = v[i] - mean; q += d * d; }
    const float var = sum32(q[0] + q[1]) * (1.0f / DM);
    const float mb = bfr(mean);
    const float inv = rsd(var);
#pragma unroll
    for (int i = 0; i < 16; ++i) {
        const f32x2 e = v[i] - mb;
        const unsigned d = cvtpk(e[0], e[1]);
        const f32x2 y = (f32x2){lo16(d), hi16(d)} * inv;
        x[i >> 2][i & 3] = cvtpk(y[0], y[1]);
    }
}

struct HW { unsigned wq[2][3], wo[4], vo16, lds, po, px, er, ec; int lw; };
typedef u32x4 Cols[24];
template <int N> DEV void wq_issue(const HW& h, const u16* W, int t, int r0) {
    const char* src = (const char*)W + (unsigned)(t * 8 + r0) * (8 * 6144);
    const unsigned l = h.lds + WQ0 + ((t + 2) % 3) * WQS + r0 * 3072;
#pragma unroll
    for (int i = 0; i < N; ++i) { glds3(src, l + i * 3072, h.wq[i & 1][0], h.wq[i & 1][1], h.wq[i & 1][2]); src += 8 * 6144; }
}
DEV void wo_issue(const HW& h, const u16* W, int n, int lim, int a, int b) {
#pragma unroll 1
    for (int j = h.lw; j < n; j += 4) {
        const int i = j + (j < lim ? a : b);
        glds4((const char*)W + wo_half(i), h.lds + wo_unit(i >> 1) * 8192 + (i & 1) * 4096, h.wo[0], h.wo[1], h.wo[2], h.wo[3]);
    }
}
DEV void fill(const void* sb, unsigned vo) {
    asm volatile("s_nop 4\n\tglobal_store_dwordx4 %0, %1, %2 sc1\n\tglobal_store_dwordx4 %0, %1, %2 offset:1024 sc1\n\t"
                 "global_store_dwordx4 %0, %1, %2 offset:2048 sc1\n\tglobal_store_dwordx4 %0, %1, %2 offset:3072 sc1\n\ts_nop 1"
                 ::"v"(vo), "v"((u32x4){~0u, ~0u, ~0u, ~0u}), "s"(sb) : "memory");
}
DEV void filled(const HW& h, const u64* fe, u64 tag) {
    asm volatile("s_mov_b64 exec, 3\n\ts_nop 3\n\tglobal_atomic_swap_x2 %0, %1, %2 sc1\n\ts_mov_b64 exec, -1\n\ts_nop 0" ::"v"(h.er), "v"(tag), "s"(fe) : "memory");
}
DEV u64 eload(const HW& h, const u64* fe) {
    u64 e;
    asm volatile("s_nop 4\n\tglobal_load_dwordx2 %0, %1, %2 sc1" : "=&v"(e) : "v"(h.ec), "s"(fe) : "memory");
    return e;
}
template <int N> DEV void echeck(const HW& h, const u64* fe, u64 e, u64 tag) {
    unsigned bud = POLLMAX;
    asm volatile("s_waitcnt vmcnt(%6)\n1:\n\tv_cmp_ne_u64 vcc, %5, %0\n\ts_sub_u32 %1, %1, 1\n\ts_cbranch_vccz 2f\n\ts_cbranch_scc1 2f\n\ts_sleep 1\n\t"
                 "global_atomic_add_x2 %0, %2, %3, %4 sc0 sc1\n\ts_waitcnt vmcnt(0)\n\ts_branch 1b\n2:"
                 : "+v"(e), "+s"(bud) : "v"(h.ec), "v"(0ull), "s"(fe), "s"(tag), "n"(N) : "memory", "scc", "vcc");
}
#define GL "global_load_lds_dwordx4 "
#define GL4(a, b, s) GL a ", " s " sc1\n\t" GL b ", " s " offset:1024 sc1\n\t" GL b ", " s " offset:2048 sc1\n\t" GL b ", " s " offset:3072 sc1\n\t"
#define XRD(c) "s_setprio 2\n\tds_read2st64_b32 v[240:241], " c " offset1:4\n\tds_read2st64_b32 v[242:243], " c " offset0:8 offset1:12\n\t" \
    "ds_read2st64_b32 v[244:245], " c " offset0:16 offset1:20\n\tds_read2st64_b32 v[246:247], " c " offset0:24 offset1:28\n\t" \
    ".if %[L]\n\tds_read_b32 v248, " c " offset:8192\n\t.endif\n\t"
#define XCMP "s_waitcnt lgkmcnt(0)\n\tv_max3_u32 v240, v240, v241, v242\n\tv_max3_u32 v243, v243, v244, v245\n\tv_max3_u32 v240, v240, v246, v247\n\t" \
    ".if %[L]\n\tv_max3_u32 v240, v240, v243, v248\n\t.else\n\tv_max_u32 v240, v240, v243\n\t.endif\n\tv_cmp_eq_u32 %[bad], -1, v240\n\ts_setprio 0"
#define XCLOB "memory", "v240", "v241", "v242", "v243", "v244", "v245", "v246", "v247", "v248"
template <bool L> DEV bool stale(unsigned c) {
    u64 bad;
    asm volatile(XRD("%[c]") XCMP : [bad] "=s"(bad) : [c] "v"(c), [L] "n"((int)L) : XCLOB);
    return bad != 0;
}
template <bool L, bool CK> DEV bool xstep(const HW& h, const void* sw, unsigned lw, const void* sx, unsigned lx, unsigned vo0, unsigned c) {
    u64 bad;
    asm volatile("s_mov_b32 m0, %[lw]\n\ts_nop 1\n\ts_waitcnt vmcnt(11+%[L])\n\ts_barrier\n\t" GL "%[w0], %[sw] sc1\n\t" GL "%[w1], %[sw] offset:1024 sc1\n\t" GL "%[w2], %[sw] offset:2048 sc1\n\t"
                 "s_mov_b32 m0, %[lx]\n\ts_nop 0\n\t" GL4("%[v0]", "%[va]", "%[sx]")
                 ".if %[CK]\n\ts_waitcnt vmcnt(7)\n\t" XRD("%[c]") ".endif\n\t"
                 "s_add_u32 m0, %[lx], 4096\n\ts_nop 0\n\t" GL4("%[vb]", "%[vb]", "%[sx]")
                 ".if %[L]\n\ts_add_u32 m0, %[lx], 8192\n\ts_nop 0\n\t" GL "%[vc], %[sx] sc1\n\t.endif\n\t"
                 ".if %[CK]\n\t" XCMP "\n\t.endif"
                 : [bad] "=s"(bad)
                 : [lw] "s"(lw), [sw] "s"(sw), [w0] "v"(h.wq[0][0]), [w1] "v"(h.wq[0][1]), [w2] "v"(h.wq[0][2]), [lx] "s"(lx), [sx] "s"(sx), [v0] "v"(vo0),
                   [va] "v"(h.vo16), [vb] "v"(h.vo16 + 4096), [vc] "v"(h.vo16 + 8192), [c] "v"(c), [L] "n"((int)L), [CK] "n"((int)CK) : XCLOB, "scc");
    return CK && bad != 0;
}
DEV void warm(const HW& h, const u16* Wo, int lane) {
    asm volatile("s_mov_b32 m0, %2\n\ts_nop 4\n\tglobal_load_lds_dword %0, %1\n\tglobal_load_lds_dword %3, %1" ::"v"(lane * 64),
                 "s"(Wo + (size_t)blockIdx.x * 4096), "s"(h.lds + DUMP), "v"(lane * 64 + 4096) : "memory");
}
template <int ST> DEV void ld8(f32x4 (&d)[8], const char* sb, unsigned vo) {
    asm volatile("s_nop 4\n\tglobal_load_dwordx4 %0, %8, %9 offset:%10 sc1\n\tglobal_load_dwordx4 %1, %8, %9 offset:%11 sc1\n\t"
                 "global_load_dwordx4 %2, %8, %9 offset:%12 sc1\n\tglobal_load_dwordx4 %3, %8, %9 offset:%13 sc1\n\t"
                 "global_load_dwordx4 %4, %8, %9 sc1\n\tglobal_load_dwordx4 %5, %8, %9 offset:%14 sc1\n\t"
                 "global_load_dwordx4 %6, %8, %9 offset:%15 sc1\n\tglobal_load_dwordx4 %7, %8, %9 offset:%16 sc1"
                 : "=&v"(d[0]), "=&v"(d[1]), "=&v"(d[2]), "=&v"(d[3]), "=&v"(d[4]), "=&v"(d[5]), "=&v"(d[6]), "=&v"(d[7])
                 : "v"(vo), "s"(sb + 4 * ST), "n"(-4 * ST), "n"(-3 * ST), "n"(-2 * ST), "n"(-ST), "n"(ST), "n"(2 * ST), "n"(3 * ST) : "memory");
}

template <class F> DEV void colsum(Cols& d, const void* out, int lane, F next) {
    const char* dst = (const char*)out + (lane & 7) * 32 + (lane >> 4) * 16;
    bf16x8 sel;
#pragma unroll
    for (int e = 0; e < 8; ++e) sel[e] = (lane & 7) == e ? (short)0x3f80 : (short)0;
#pragma unroll
    for (int i = 0; i < 24; ++i) asm volatile("" : "+v"(d[i]));
    f32x4 c;
#pragma unroll
    for (int k = 0; k < 6; ++k) {
        if (!(k & 1)) c = (f32x4){0.f, 0.f, 0.f, 0.f};
#pragma unroll
        for (int i = 0; i < 4; ++i) c = MFMA16(sel, __builtin_bit_cast(bf16x8, d[k * 4 + i]), c);
        if (k & 1) {
#pragma unroll
            for (int e = 0; e < 4; ++e) c[e] = dppadd<0x128>(c[e]);
            if (lane < 32 && !(lane & 8)) st16wt<0>(dst + (k >> 1) * 256, __builtin_bit_cast(u32x4, c));
        }
        SB();
        next(k);
    }
}

template <bool LOCAL>
DEV void qkv_helper(lchar* smem, HW& h, Cols& cw, char* ws, int layer, const u16* X, const u16* W, const u16* W2, const u16* Wo, const char* stats, float* csum,
                    u64* fl, u64 tag, int g, int hp, int lane, int lw STPARG) {
    OPAQUE(lane, lw, g, hp);
    constexpr int NX = LOCAL ? 9 : 8, HO = LOCAL ? 16 : 0;
    const int m0 = g * 128;
    const bool halo = LOCAL && g > 0;
    const u64* fx = fl + fi(FL_X + (layer - 1) * 1024 + (g * 2 - (halo ? 1 : 0)) * 16 + (lane < (halo ? 48 : 32) ? lane : 0));
    const bool fon = lane < (halo ? 48 : 32);
    auto fold = [&](u64 m) { return (unsigned)(m & (m >> 16) & (halo ? m >> 32 : m)); };
    auto first = [&](int r0) {
#pragma unroll 1
        for (int t = 0; t < 3; ++t) wq_issue<4>(h, W, t, r0);
    };
    auto colld = [&](Cols& d, const u16* M) {
        const unsigned vo = ((g * 64 + (lane >> 3)) * 3072 + (hp ^ CJ) * 64 + (lane & 7) * 8) * 2 + 2048;
#pragma unroll
        for (int i = 0; i < 8; ++i)
            asm volatile("s_nop 4\n\tglobal_load_dwordx4 %0, %3, %4 offset:-2048\n\tglobal_load_dwordx4 %1, %3, %4\n\t"
                         "global_load_dwordx4 %2, %3, %4 offset:2048" : "=&v"(d[i]), "=&v"(d[8 + i]), "=&v"(d[16 + i])
                         : "v"(vo), "s"((const char*)M + i * (8 * 6144)) : "memory");
    };
    if (lw < 2) {
        const unsigned vo0 = (LOCAL && g == 0) ? h.vo16 + 1024 : h.vo16;
        const char* xsrc = (const char*)X + ((size_t)lw * SEQ + m0 - HO) * 64;
        const unsigned ldx = h.lds + lw * (NX * 1024), xv = ldx + lane * 16 + (lane >> 4) * 4;
        auto fetch = [&](int t, int so) {
            const char* p = xsrc + t * (2 * SEQ * 64);
            glds4(p, ldx + so, vo0, h.vo16, h.vo16, h.vo16);
            glds4(p + 4096, ldx + so + 4096, h.vo16, h.vo16, h.vo16, h.vo16);
            if (LOCAL) glds1(p + 8192, ldx + so + 8192, h.vo16);
        };
        int bud = POLLMAX;
        auto redo = [&](int t, int so) {
#pragma unroll 1
            do { --bud; fetch(t, so); WAITV(0); } while (stale<LOCAL>(xv + so) && bud > 0);
        };
        STAMP(0);
        if (LOCAL && layer == 0) {
            const u64* f0 = fl + fi(FL_L + g * 16 - (halo ? 2 : 0)) + (lane & 31);
            asm volatile("" : "+v"(f0));
            BAR();
            if (lw == 0) poll(f0, lane < (halo ? 18 : 16), tag, ~0u, [](u64 m) { return (unsigned)m; });
            BAR();
        } else poll(fx + fi(lw * 512), fon, tag, PQ, fold);
        fetch(0, 0);
        STAMP(1);
        if (lw == 1) STAMPW(11);
        fetch(1, XS);
        if (Q2) fetch(2, 2 * XS);
        if (LOCAL && layer == 0) BAR();
        WAITV(Q2 ? 2 * NX : NX);
        if (__builtin_expect(stale<LOCAL>(xv), 0)) redo(0, 0);
        BAR();
        if (!Q2) fetch(2, 2 * XS);
        WAITV(NX);
        if (__builtin_expect(stale<LOCAL>(xv + XS), 0)) redo(1, XS);
        const char* wsrc = (const char*)W + (unsigned)(24 + lw) * (8 * 6144);
        const unsigned ldw = h.lds + WQ0 + lw * 3072;
        int so = 0, sv = 2 * XS, sw = 2 * WQS, t = 3;
        auto next = [&]() {
            wsrc += 64 * 6144;
            sv = so;
            so = so == 2 * XS ? 0 : so + XS;
            sw = sw == 2 * WQS ? 0 : sw + WQS;
        };
#pragma unroll 1
        for (; t <= NCK; ++t) {
            if (__builtin_expect((xstep<LOCAL, true>(h, wsrc, ldw + sw, xsrc + t * (2 * SEQ * 64), ldx + so, vo0, xv + sv)), 0)) redo(t - 1, sv);
            next();
        }
#pragma unroll 1
        for (; t < 16; ++t) {
            xstep<LOCAL, false>(h, wsrc, ldw + sw, xsrc + t * (2 * SEQ * 64), ldx + so, vo0, 0);
            next();
        }
        WAITB(NX + 3);
    } else if (lw == 2) {
        if (LOCAL && layer == 0) {
            BAR();
            first(0);
            STAMPW(6);
            BAR(); BAR();
        }
        WAITB(0);
        { HT0();
        if (layer) wq_issue<4>(h, W, 2, 0);
        HT1(0); }
        BAR();
#pragma unroll 1
        for (int t = 3; t < 16; ++t) {
            HT0();
            wq_issue<6>(h, W, t, 2);
            HT1(t - 2);
            WAITB(18);
        }
        { HT0(); HT1(14); }
    } else {
        if (LOCAL && layer == 0) {
            BAR();
            first(4);
            BAR(); BAR();
            Cols c0;
            colld(c0, W);
            warm(h, Wo, lane);
            WAITB(26);
            auto fill0 = [&](int k) { fill(ws + (k < 4 ? WS_O : WS_X + 4 * MB) + ((k >> 1) & 1) * (SEQ * 64) + (k & 1) * 4096, k < 4 ? h.po : h.px); };
            auto job = [&](int i) {
#pragma unroll
                for (int k = 0; k < 8; ++k) if (FE + k / FN == i) fill0(k);
                constexpr int L = FE + 7 / FN, R = L + FD;
                if (i == R) { WAITV((L < 6 && R >= 6) + (L < 8 && R >= 8) + (L < 10 && R >= 10)); filled(h, fl + fi(FL_E), tag); }
                BAR();
            };
            job(1); job(2); job(3); job(4);
            WAITV(FE > 4 ? 0 : (5 - FE) * FN >= 8 ? 32 : (5 - FE) * FN * 4);
            colsum(c0, (const char*)csum + ((hp ^ CJ) * 16 + g) * 768, lane, [&](int k) { job(5 + k); });
            job(11); job(12); job(13); job(14);
        } else {
            auto fin = [&](float s, float q, int o) {
                const float mean = s * (1.0f / DM);
                *(LAS float*)(smem + SNM + o) = -bfr(mean);
                *(LAS float*)(smem + SRS + o) = rsd(fmx(q * (1.0f / DM) - mean * mean, 0.f));
            };
            f32x4 sp[3][8], a = {0.f, 0.f, 0.f, 0.f};
            auto add8 = [&](f32x4 (&d)[8]) {
#pragma unroll
                for (int i = 0; i < 8; ++i) {
                    asm volatile("" : "+v"(d[i]));
#pragma unroll
                    for (int e = 0; e < 4; ++e) asm volatile("v_add_f32 %0, %0, %1" : "+v"(a[e]) : "v"(d[i][e]));
                }
            };
            const u64* fc = fl + fi(FL_C + hp * 16 + (lane & 15));
            const u64* fs = fx + fi(FL_S - FL_X);
            const char* sb = stats;
            const char* cb = (const char*)csum + (LOCAL ? 0 : 256 * 768);
            const unsigned so = (layer - 1) * (unsigned)(MB / 2) + g * 32768;
            const unsigned vo = so + (lane >> 5) * 16384 + (lane & 31) * 16, vc = hp * (16 * 768) + (lane < 48 ? lane * 16 : 0);
            WAITB(0);
            wq_issue<4>(h, W, 2, 4);
            BAR(); WAITB(0);
            u64 f0, f1, f2;
            asm volatile("global_load_dwordx2 %0, %3, off sc1\n\tglobal_load_dwordx2 %1, %4, off sc1\n\tglobal_load_dwordx2 %2, %5, off sc1"
                         : "=&v"(f0), "=&v"(f1), "=&v"(f2) : "v"(fs), "v"(fs + fi(512)), "v"(fc) : "memory");
            BAR();
            WAITV(0);
            asm volatile("" : "+v"(f0), "+v"(f1), "+v"(f2));
            if (!__all(f0 == tag && f1 == tag && f2 == tag)) {
                poll(fs, fon, tag, 0xffffu, fold);
                poll(fs + fi(512), fon, tag, 0xffffu, fold);
                poll(fc, lane < 16, tag, 0xffffu, [](u64 m) { return (unsigned)m; });
            }
            ld8<512>(sp[0], sb, vo);
            BAR();
            ld8<512>(sp[1], sb + 4096, vo);
            BAR();
            ld8<512>(sp[2], sb + 8192, vo);
            WAITV(16);
            add8(sp[0]);
            BAR();
            ld8<512>(sp[0], sb + 12288, vo);
            WAITV(16);
            add8(sp[1]);
            BAR();
            WAITV(8);
            ld8<768>(sp[1], cb, vc);
            add8(sp[2]);
            BAR();
            ld8<768>(sp[2], cb + 6144, vc);
            WAITV(16);
            add8(sp[0]);
            BAR();
            if (LOCAL) ld8<512>(sp[0], sb, so - (halo ? 16384 : 0) + (lane >> 4) * 4096 + 384 + (lane & 7) * 16);
            fin(a[0], a[1], 64 + lane * 8);
            fin(a[2], a[3], 68 + lane * 8);
            SB();
            BAR();
            WAITV(LOCAL ? 8 : 0);
            a = (f32x4){0.f, 0.f, 0.f, 0.f};
            add8(sp[1]);
            BAR();
            add8(sp[2]);
            if (lane < 48) *(LAS f32x4*)(smem + SCOL + lane * 16) = a;
            SB();
            BAR();
            if (LOCAL) {
                WAITV(0);
                a = (f32x4){0.f, 0.f, 0.f, 0.f};
                add8(sp[0]);
            }
            BAR();
            if (LOCAL) {
#pragma unroll
                for (int e = 0; e < 4; ++e) {
                    const f32x2 x = sw16f(a[e], a[e]);
                    const float v = x[0] + x[1];
                    const f32x2 y = sw32f(v, v);
                    a[e] = y[0] + y[1];
                }
                fin(a[0], a[1], (lane & 7) * 8);
                fin(a[2], a[3], (lane & 7) * 8 + 4);
            }
            BARL();
        }
    }
    if (LOCAL) wo_issue(h, Wo, 8, 2, 6, 18);
    WAITV(LOCAL ? 8 : 0);
    BAR();
    { HT0(); HT1(15); }
    if (LOCAL) wo_issue(h, Wo, 12, 6, 8, 20);
    BAR();
    if (LOCAL) {
        wo_issue(h, Wo, 12, 6, 0, 8);
        if (layer == 0 && lw == 3) {
            const u64 e = eload(h, fl + fi(FL_E));
            colld(cw, W2);
            WAITB(29);
            echeck<24>(h, fl + fi(FL_E), e, tag);
        } else WAITB(4);
    } else BAR();
}

DEV u64 probe(const u64* f) {
    u64 v;
    asm volatile("s_mov_b64 exec, 1\n\t" FLD("%0", "%1", "%2") "\n\ts_mov_b64 exec, -1" : "=&v"(v) : "v"(f), "v"(0ull) : "memory");
    return v;
}
DEV void attn_helper(const HW& h, char* ws, int nf, const u16* kv, const u16* Wo, const u64* fe, const u64* fkv, u64 tag, int g, int hp, int lane, int lw STPARG) {
    __builtin_amdgcn_s_setprio(2);
    OPAQUE(lane, lw, g, hp);
    const int rh = lw & 1, hd = lw >> 1;
    const char* src = (const char*)kv + (unsigned)((hp * 2 + hd) * SEQ * 64 + rh * 4096);
    const unsigned ld = h.lds + AR0 + hd * 16384 + rh * 8192;
    const u64* f = fkv + fi(lw * 16 + (lane & 15));
    unsigned rdy = 0u;
    int reg = AR1;
    auto issue = [&](int c) {
        const int blk = g ^ c;
        if (!((rdy >> blk) & 1)) rdy = poll(f, lane < 16, tag, 1u << blk, [](u64 m) { return (unsigned)m; });
        glds4(src + blk * 8192, ld + reg, h.vo16, h.vo16, h.vo16, h.vo16);
        glds4(src + blk * 8192 + NH * SEQ * 64, ld + reg + 4096, h.vo16, h.vo16, h.vo16, h.vo16);
        reg = reg == AR1 ? AR2 : reg == AR2 ? 0 : AR1;
    };
    issue(1);
    STAMP(7);
#pragma unroll 1
    for (int c = 0; c < 14; ++c) {
        WAITB(0);
        issue(c + 2);
        if (c == 1 && lw == 3) warm(h, Wo, lane);
        if ((c >> 2) == (FA == 4 ? 2 : 1)) wo_issue(h, Wo, min(4, 14 - 4 * (c & 3)), 4, 4 * (c & 3), 0);
        if (c >= FA && c < FA + nf)
            fill(ws + (c == FA ? WS_O + 4 * MB : c == FA + 1 ? WS_X + 8 * MB : c == FA + 2 ? WS_O : WS_X + 12 * MB) + hd * (SEQ * 64) + rh * 4096, (c & 1) ? h.px : h.po);
        if (c == FA + 4 && lw == 3) filled(h, fe, tag);
    }
    WAITB(0);
    wo_issue(h, Wo, 8, 6, 14, 24);
    if (lw == 3) echeck<0>(h, fe, eload(h, fe), tag);
    BAR();
}

#define TM0
#define TM1(t)
#define CNT(n)
DEV void wo_helper(lchar* smem, HW& h, Cols& cw, const char* csum, const u16* O, const u16* Wo, int nw, const u16* Wn, u64* fc, const u64* fo, u64 tag, int g, int lane, int lw STPARG) {
    int dummy = 0;
    OPAQUE(lane, lw, g, dummy);
    const int p = lw & 1, rh = lw >> 1;
    const char* xsrc = (const char*)O + ((size_t)p * SEQ + g * 128 + rh * 64) * 64;
    const unsigned ldx = h.lds + p * 8192 + rh * 4096, c0 = ldx + lane * 16 + (lane >> 4) * 4;
    const u64* f = fo + fi((p * 32 + g * 2 + rh) * 16 + (lane & 15));
    unsigned rdy = 0;
    auto need = [&](unsigned m) { if ((rdy & m) != m) rdy = poll(f, lane < 16, tag, m, [](u64 m) { return (unsigned)m; }); };
    STAMP(0);
    {
        u64 v = probe(f);
        wo_issue(h, Wo, nw, 10, 20, 0);
        if (LP) __builtin_amdgcn_s_setprio(0);
        asm volatile("s_barrier\n\ts_cmp_lg_u32 %1, 0\n\ts_waitcnt vmcnt(8)\n\ts_cbranch_scc1 1f\n\ts_waitcnt vmcnt(0)\n1:" : "+v"(v) : "s"(nw) : "memory", "scc");
        rdy = (unsigned)__ballot(v == tag) & 1;
    }
    if (PW) need(1);
    if (NP) rdy = ~0u;
    if (!LP) __builtin_amdgcn_s_setprio(HPW);
    int so = 65536;
    unsigned np = 0;
#pragma unroll 1
    for (int t = 0; t < 4; ++t) {
        if (!((rdy >> t) & 1)) ++np;
        need(1u << t);
        glds4(xsrc + t * (2 * SEQ * 64), ldx + so, h.vo16, h.vo16, h.vo16, h.vo16);
        if (t == 0) STAMP(1);
        so = t * 16384;
    }
    if (LP) __builtin_amdgcn_s_setprio(HPW);
    if (fc) { WAITV(16); colsum(cw, csum, lane, [](int) {}); }
    unsigned vo = h.vo16 + 4 * (2 * SEQ * 64), sa, sb, sm;
    int bud = POLLMAX;
    u64 ret;
    unsigned tv = 0, cn = 0;
    const char* wn = (const char*)Wn + lw * (8 * 6144);
    unsigned w0 = h.wq[lw == 3][0], w1 = h.wq[lw == 3][1], w2 = h.wq[lw == 3][2];
    asm volatile(
        ".macro WRD t\n.if (\\t %% 5) == 0\n"
        "ds_read2st64_b32 v[240:241], %[c4] offset1:4\nds_read2st64_b32 v[242:243], %[c4] offset0:8 offset1:12\n.else\n"
        "ds_read2st64_b32 v[240:241], %[c0] offset0:(\\t %% 5)*64-64 offset1:(\\t %% 5)*64-60\n"
        "ds_read2st64_b32 v[242:243], %[c0] offset0:(\\t %% 5)*64-56 offset1:(\\t %% 5)*64-52\n.endif\n.endm\n"
        ".macro WCMP\ns_waitcnt lgkmcnt(0)\nv_max3_u32 v240, v240, v241, v242\nv_max_u32 v240, v240, v243\nv_cmp_eq_u32 vcc, -1, v240\n.endm\n"
        ".macro WTS t, x\nWCMP\n" TM1("(\\t+63)&63") "s_cbranch_vccnz .Lwr%=_\\x\\t\n.Lwb%=_\\x\\t:\n.endm\n"
        ".macro WNX t\ns_bitcmp1_b32 %[rdy], \\t+4\ns_barrier\n" TM0 "s_cbranch_scc0 .Lwp%=_\\t\n.Lwq%=_\\t:\n.endm\n"
        ".macro WST t\n" GL "%[vo], %[src] sc1\n" GL "%[vo], %[src] offset:1024 sc1\n"
        "s_waitcnt vmcnt(10)\nWRD \\t\n"
        GL "%[vo], %[src] offset:2048 sc1\n" GL "%[vo], %[src] offset:3072 sc1\n"
        "s_add_u32 m0, %[ldx], ((\\t+3) %% 5)*16384\nv_add_u32 %[vo], 0x40000, %[vo]\n"
        "WTS \\t, a\n"
        ".endm\n"
        ".macro WTL t, n, x\ns_waitcnt vmcnt(\\n)\nWRD \\t\nWTS \\t, \\x\ns_barrier\n" TM0 ".endm\n"
        ".macro WWN o\ns_add_u32 m0, %[wl], \\o\ns_nop 0\n" GL "%[w0], %[wn] sc1\n" GL "%[w1], %[wn] offset:1024 sc1\n" GL "%[w2], %[wn] offset:2048 sc1\n"
        "v_add_u32 %[w0], 0x30000, %[w0]\nv_add_u32 %[w1], 0x30000, %[w1]\nv_add_u32 %[w2], 0x30000, %[w2]\n.endm\n"
        ".macro WCR t, x\n.Lwr%=_\\x\\t:\ns_mov_b32 %[sa], ((\\t+4) %% 5)*16384\ns_mov_b32 %[sb], \\t*0x40000\ns_call_b64 %[ret], .Lwretry%=\ns_branch .Lwb%=_\\x\\t\n.endm\n"
        ".macro WCP t\n.Lwp%=_\\t:\ns_mov_b32 %[sa], \\t+4\ns_call_b64 %[ret], .Lwpoll%=\ns_branch .Lwq%=_\\t\n.endm\n"
        "s_add_u32 m0, %[ldx], 49152\ns_waitcnt vmcnt(12)\nWRD 0\nWTS 0, a\nWNX 0\n"
        ".irp t,1,2,3,4,5,6,7,8,9,10,11\nWST \\t\nWNX \\t\n.endr\n"
        "WST 12\ns_cmp_eq_u64 %[Wn], 0\ns_barrier\n" TM0 "s_cbranch_scc1 .Lwlast%=\n"
        "WWN 2*24576\nWTL 13, 11, a\nWWN 2*24576+12288\nWTL 14, 10, a\nWWN 0\nWTL 15, 9, a\nWWN 12288\n"
        "s_branch .Lwend%=\n"
        ".irp t,0,1,2,3,4,5,6,7,8,9,10,11,12,13,14,15\nWCR \\t, a\n.endr\n"
        ".irp t,13,14,15\nWCR \\t, b\n.endr\n"
        ".irp t,0,1,2,3,4,5,6,7,8,9,10,11\nWCP \\t\n.endr\n"
        ".Lwretry%=:\ns_mov_b32 %[sm], m0\ns_add_u32 m0, %[ldx], %[sa]\nv_add_u32 v244, %[sb], %[v0]\nv_add_u32 v245, %[sa], %[c0]\n"
        "1:\n" GL "v244, %[src] sc1\n" GL "v244, %[src] offset:1024 sc1\n" GL "v244, %[src] offset:2048 sc1\n" GL "v244, %[src] offset:3072 sc1\n"
        "s_waitcnt vmcnt(0)\nds_read2st64_b32 v[240:241], v245 offset1:4\nds_read2st64_b32 v[242:243], v245 offset0:8 offset1:12\n"
        "WCMP\n" CNT("0x10000") "s_sub_i32 %[bud], %[bud], 1\ns_cmp_lt_i32 %[bud], 0\ns_cbranch_scc1 2f\ns_cbranch_vccnz 1b\n"
        "2:\ns_mov_b32 m0, %[sm]\ns_setpc_b64 %[ret]\n"
        ".Lwpoll%=:\ns_mov_b64 exec, 0xffff\n"
        "1:\n" FLD("v[240:241]", "%[f]", "%[z]") "\ns_waitcnt vmcnt(0)\nv_cmp_eq_u64 vcc, %[tag], v[240:241]\n" CNT("1")
        "s_sub_i32 %[bud], %[bud], 1\ns_cmp_lt_i32 %[bud], 0\ns_cselect_b32 %[rdy], -1, vcc_lo\ns_bitcmp1_b32 %[rdy], %[sa]\n"
        "s_cbranch_scc1 2f\ns_sleep 1\ns_branch 1b\n"
        "2:\ns_mov_b64 exec, -1\ns_setpc_b64 %[ret]\n"
        ".Lwlast%=:\nWTL 13, 8, b\nWTL 14, 4, b\nWTL 15, 0, b\n"
        ".Lwend%=:\n"
        ".purgem WRD\n.purgem WCMP\n.purgem WTS\n.purgem WNX\n.purgem WST\n.purgem WTL\n.purgem WWN\n.purgem WCR\n.purgem WCP"
        : [vo] "+v"(vo), [rdy] "+s"(rdy), [bud] "+s"(bud), [sa] "=&s"(sa), [sb] "=&s"(sb), [sm] "=&s"(sm), [ret] "=&s"(ret),
          [w0] "+v"(w0), [w1] "+v"(w1), [w2] "+v"(w2)
        : [src] "s"(xsrc), [ldx] "s"(ldx), [c0] "v"(c0), [c4] "v"(c0 + 65536), [v0] "v"(h.vo16), [f] "v"(f), [z] "v"(0ull), [tag] "s"(tag),
          [wn] "s"(wn), [Wn] "s"(Wn), [wl] "s"(h.lds + WQ0 + lw * 3072)
        : "memory", "scc", "vcc", "v240", "v241", "v242", "v243", "v244", "v245"
        );
    __builtin_amdgcn_s_setprio(HPQ);
    if (fc && lane == 0) stflag(fc, tag);
}

__global__ __launch_bounds__(512) void mega(const u16* xin, const u16* wqa, char* ws, const u16* __restrict__ woa,
                                            const u16* __restrict__ wqb, const u16* __restrict__ wob, u16* __restrict__ out) {
    __shared__ __attribute__((aligned(16))) char smem_[LDS_BYTES];
    asm volatile("s_load_dwordx2 %0, %3, 0\n\ts_load_dwordx2 %1, %3, 8\n\ts_load_dwordx2 %2, %3, 16"
                 : "=&s"(xin), "=&s"(wqa), "=&s"(ws) : "s"(__builtin_amdgcn_kernarg_segment_ptr()));
    u64 pc;
    if (CT) asm volatile("s_getpc_b64 %0" : "=s"(pc));
    SB();
    lchar* smem = (lchar*)smem_;
    const int bid = blockIdx.x, xcd = bid & 7, idx = bid >> 3;
    const int lane = threadIdx.x & 63, wid = __builtin_amdgcn_readfirstlane(threadIdx.x >> 6);
    const int g = (xcd >> 2) * 8 + (idx >> 2), hp = (xcd & 3) * 4 + (idx & 3);
    const int g2 = (xcd >> 1) * 4 + (idx >> 3), cb = (xcd & 1) * 8 + ((idx & 7) ^ CX);
    const int row = g * 128 + hp * 8 + (wid & 3) * 2 + ((lane >> 2) & 1);
    unsigned xo = row * 2048 + (lane >> 3) * 64 + (lane & 3) * 16, eo = WS_FL + fi(FL_EP) * 8;
    asm volatile("" : "+v"(xo), "+v"(eo));
    asm volatile("s_waitcnt lgkmcnt(0)" : "+s"(xin), "+s"(wqa), "+s"(ws));
    SB();
    u64* fl = (u64*)(ws + WS_FL);
    const int pos = (int)(kinv() >> (4 * hp)) & 15;
    auto mktag = [](u64 e) {
        e += 1;
        return ((u64)(unsigned)__builtin_amdgcn_readfirstlane((int)(e >> 32)) << 32) | (unsigned)__builtin_amdgcn_readfirstlane((int)e);
    };
    const unsigned lds0 = (unsigned)(uintptr_t)smem;
    LAS unsigned* cnt = (LAS unsigned*)(smem + DUMP + 1024);
    auto warmup = [&]() {
        *cnt = 0u;
        if (CT == 1 && wid == 4) touch((const char*)(pc & ~255ull), lds0 + DUMP, (idx * 24 + (lane < 24 ? lane : 0)) * 64);
        BARL();
        if (CT == 2 && wid == 4) touch((const char*)(pc & ~255ull), lds0 + DUMP, (idx * 24 + (lane < 24 ? lane : 0)) * 64);
        if (TL && wid == 4) touch(ws + WS_X, lds0 + DUMP, (lane & 3) * (unsigned)MB + row * 64);
        if (wid > 4) touch((const char*)wqa + (wid - 5) * 2048, lds0 + DUMP, (idx + (lane >> 3) * 32) * 6144 + (xcd & 3) * 512 + (lane & 7) * 64);
        else if (wid == 4) {
            const int f = lane < 4 ? (bid < 240 ? bid : 0) * 64 + lane * 16 : FL_L + (g & 7) * 16 + (lane & 1) * 128;
            asm volatile("s_mov_b64 exec, 63\n\tglobal_atomic_add_x2 %0, %1, %2 sc1\n\ts_mov_b64 exec, -1" ::"v"(fi(f) * 8), "v"(0ull), "s"(fl) : "memory");
        }
    };
    u64 tag;
    if (wid < 4) {
        u32x4 x[4];
        u64 e;
        asm volatile("global_load_dwordx4 %0, %5, %6" NTX "\n\tglobal_load_dwordx4 %1, %5, %6 offset:512" NTX "\n\tglobal_load_dwordx4 %2, %5, %6 offset:1024" NTX "\n\t"
                     "global_load_dwordx4 %3, %5, %6 offset:1536" NTX "\n\tglobal_load_dwordx2 %4, %7, %8 sc1"
                     : "=&v"(x[0]), "=&v"(x[1]), "=&v"(x[2]), "=&v"(x[3]), "=&v"(e)
                     : "v"(xo), "s"(xin), "v"(eo), "s"(ws) : "memory");
        SB();
        warmup();
        unsigned d = ((lane >> 3) * SEQ + row) * 64 + (((lane & 3) ^ fz(row)) << 4);
        asm volatile("" : "+v"(d));
        WAITV(0);
        asm volatile("" : "+v"(x[0]), "+v"(x[1]), "+v"(x[2]), "+v"(x[3]), "+v"(e));
        if (wid == 0) STAMP(14);
        if (FL0) {
            const unsigned s0 = ln0_stats(x);
            const f32x2 st = {lo16(s0), __builtin_amdgcn_rcpf(hi16(s0))};
#pragma unroll
            for (int i = 0; i < 4; ++i) {
                Raw v;
                unpk(v, x[i]);
                asm volatile("global_store_dwordx4 %0, %1, %2 sc1\n\ts_nop 1" ::"v"(d), "v"(ln0_apply(v, st)), "s"(ws + WS_X + i * MB) : "memory");
            }
        } else {
        ln0_rows(x);
#pragma unroll
        for (int i = 0; i < 4; ++i)
            asm volatile("global_store_dwordx4 %0, %1, %2 sc1\n\ts_nop 1" ::"v"(d), "v"(x[i]), "s"(ws + WS_X + i * MB) : "memory");
        }
        tag = mktag(e);
        if (wid == 0) STAMP(15);
        WAITV(0);
        if (lane == 0 && __hip_atomic_fetch_add(cnt, 1u, __ATOMIC_RELAXED, __HIP_MEMORY_SCOPE_WORKGROUP) == 3)
            stflag(fl + fi(FL_L + g * 16 + hp), tag);
    } else {
        warmup();
        tag = mktag(ldep(fl + fi(FL_EP)));
    }
    if (wid < 4) {
        BAR();
        if (wid == 0) STAMP(9);
        __builtin_amdgcn_s_setprio(1);
        u32x4 res[4];
        {
            const unsigned ro = (g2 * 128 + (wid >> 1) * 64 + (lane & 15)) * 2048 + (cb * 2 + (wid & 1)) * 64 + ((lane & 16) << 1) + ((lane & 32) >> 1);
#pragma unroll
            for (int i = 0; i < 4; ++i) res[i] = (u32x4){ro, ro, ro, ro};
            if (!PB) { BAR(); BAR(); }
        }
#pragma unroll 1
        for (int layer = 0; layer < 4; ++layer) {
            u64* fo = fl + fi(FL_O + layer * 1024 + ((wid & 1) * 32 + g * 2 + (wid >> 1)) * 16 + pos);
            u16* ob = (u16*)(ws + WS_O + pos * (2 * SEQ * 64));
            WReg wr;
            if (layer & 1) qb_compute(smem, (u16*)(ws + WS_KV + (layer >> 1) * 8 * MB), wr, ob + 2 * MB, fo, fl + fi(FL_KV + (layer >> 1) * 1024 + hp * 64),
                                      tag, g, hp, lane, wid STPPASS(layer * 32));
            else qa_compute(smem, layer > 0, xin, res, wr, ob, fo, tag, g, hp, lane, wid STPPASS(layer * 32));
            const bool last = layer == 3;
            wo_compute(smem, fo, res, wr,
                       last ? out : (u16*)(ws + WS_X + (layer + 1) * 4 * MB), last ? nullptr : ws + WS_ST + layer * (MB / 2),
                       fl + fi(FL_X + layer * 1024), tag, g2, cb, lane, wid STPPASS(layer * 32 + 16));
        }
        if (bid == 0 && threadIdx.x == 0) stflag(fl + fi(FL_EP), tag);
    } else {
        const int lw = wid - 4;
        HW h;
        h.lds = lds0;
        h.lw = lw;
        h.vo16 = lane * 16;
        h.po = (pos * 2 * SEQ + g * 128) * 64 + lane * 16;
        h.px = (cb * 2 * SEQ + g2 * 128) * 64 + lane * 16;
        h.er = fi(lane ? 256 + g2 * 16 + cb : g * 16 + hp) * 8;
        h.ec = fi((lane < 16 ? g2 : lane < 32 || g == 0 ? 16 + g : 15 + g) * 16 + (lane & 15)) * 8;
#pragma unroll
        for (int i = 0; i < 6; ++i) {
            const int k = (lane >> 3) + (((i / 3) ^ (lw == 1)) << 3), pc = lane & 7;
            h.wq[i / 3][i % 3] = (lane >> 3) * 6144 + (i % 3) * 1024 + hp * 128 + ((((pc >> 1) ^ fswz(k)) << 1) + (pc & 1)) * 16;
        }
#pragma unroll
        for (int i = 0; i < 4; ++i) h.wo[i] = wo_voff(lane, cb, i);
#pragma unroll 1
        for (int layer = 0; layer < 4; ++layer) {
            if (OQ) asm volatile("" : "+s"(wqa), "+s"(wqb), "+s"(ws));
            const u16* X = (const u16*)(ws + WS_X + layer * 4 * MB);
            const char* st = ws + WS_ST;
            const u16* wo = (layer & 1) ? wob : woa;
            Cols cw;
            const u64* fe = fl + fi(FL_E + ((layer + 1) >> 1) * 512);
            if (layer & 1) {
                qkv_helper<false>(smem, h, cw, ws, layer, X, wqb, wqb, wo, st, (float*)(ws + WS_CS), fl, tag, g, hp, lane, lw STPPASS(layer * 32));
                attn_helper(h, ws, layer == 1 ? 4 : 1, (const u16*)(ws + WS_KV + (layer >> 1) * 8 * MB), wo, fe, fl + fi(FL_KV + (layer >> 1) * 1024 + hp * 64), tag, g, hp, lane,
                            lw STPPASS(layer * 32));
            } else {
                qkv_helper<true>(smem, h, cw, ws, layer, X, wqa, wqb, wo, st, (float*)(ws + WS_CS), fl, tag, g, hp, lane, lw STPPASS(layer * 32));
            }
            wo_helper(smem, h, cw, ws + WS_CS + (256 + (hp ^ CJ) * 16 + g) * 768, (const u16*)(ws + WS_O + (layer & 1) * 4 * MB), wo, (layer & 1) * 10, layer == 3 ? nullptr : (layer & 1) ? wqa : wqb, (layer == 0 && lw == 3) ? fl + fi(FL_C + (hp ^ CJ) * 16 + g) : nullptr,
                      fl + fi(FL_O + layer * 1024), tag, g2, lane, lw STPPASS(layer * 32 + 16));
        }
    }
}

extern "C" void kernel_launch(void* const* d_in, const int* in_sizes, int n_in, void* d_out, int out_size, void* d_ws,
                              size_t ws_size, hipStream_t stream) {
    mega<<<256, 512, 0, stream>>>((const u16*)d_in[0], (const u16*)d_in[1], (char*)d_ws, (const u16*)d_in[2], (const u16*)d_in[4],
                                  (const u16*)d_in[5], (u16*)d_out);
}
```

```cpp
#include <hip/hip_runtime.h>
#include <stdint.h>

typedef unsigned short u16;
typedef unsigned long long u64;
typedef __attribute__((ext_vector_type(8))) short bf16x8;
typedef __attribute__((ext_vector_type(4))) short s16x4;
typedef __attribute__((ext_vector_type(4))) float f32x4;
typedef __attribute__((ext_vector_type(16))) float f32x16;
typedef __attribute__((ext_vector_type(4))) unsigned u32x4;
typedef __attribute__((ext_vector_type(2))) unsigned u32x2;
typedef __attribute__((ext_vector_type(2))) __bf16 bf16x2_t;
typedef __attribute__((ext_vector_type(2))) float f32x2;

#define LAS __attribute__((address_space(3)))
#define GAS __attribute__((address_space(1)))
#define DEV __device__ __forceinline__
typedef LAS char lchar;

constexpr int SEQ = 2048, DM = 1024, NH = 32;
constexpr float QSCALE = 0.17677669529663687f, LOG2E = 1.4426950408889634f;
#define POLLMAX (1 << 16)
#define NTX " nt"
#define CT 1
#define CT0 0
#define OQ 1
#define PW 2
#define VF 1
#define AL 0
#define PB 1
#define FL0 1
#define TW 8
#define K0 1
#define BW 0
#define RS 0
#define TC 0
#define TD 0
#define LP 1
#define WA 1
#define WB 48
#define EF 3
#define EO 0
#define EXP 1
#define WS 2
#define HPW 2
#define HPQ 0
#define NP 1
#define PRE 0
#define F0 0
#define FE 5
#define FN 2
#define FD 2
#define FA 4
#define E1 1
#define AW 0
#define PQ 1
#define Q2 1
#define SF 6
#define NCK (SF + 3)
#define L2 1
#define CI 2
#define RI 4
#define KORD 0xb3fedc7654a98210ull
#define CJ 2
#define CX 1
#define EMU 1
#define PUB 2
#define XP 0
#define WE 1
#define EA 0
#define EB 0
#define P2 6
#define SP 1
#define GM 0
#define MO 5
#define MW 0

DEV unsigned cvtpk(float lo, float hi) {
    f32x2 v = {lo, hi};
    bf16x2_t b = __builtin_convertvector(v, bf16x2_t);
    return __builtin_bit_cast(unsigned, b);
}
DEV float lo16(unsigned v) { return __uint_as_float(v << 16); }
DEV float hi16(unsigned v) { return __uint_as_float(v & 0xffff0000u); }
DEV float bfr(float f) { return lo16(cvtpk(f, f)); }
DEV u32x2 pk4(f32x4 a) { return (u32x2){cvtpk(a[0], a[1]), cvtpk(a[2], a[3])}; }
DEV bf16x8 cat(u32x2 a, u32x2 b) { return __builtin_bit_cast(bf16x8, (u32x4){a[0], a[1], b[0], b[1]}); }
DEV u32x2 sw16(unsigned a, unsigned b) { auto r = __builtin_amdgcn_permlane16_swap(a, b, false, false); return (u32x2){r[0], r[1]}; }
DEV u32x2 sw32(unsigned a, unsigned b) { auto r = __builtin_amdgcn_permlane32_swap(a, b, false, false); return (u32x2){r[0], r[1]}; }
DEV f32x2 sw16f(float a, float b) { return __builtin_bit_cast(f32x2, sw16(__float_as_uint(a), __float_as_uint(b))); }
DEV f32x2 sw32f(float a, float b) { return __builtin_bit_cast(f32x2, sw32(__float_as_uint(a), __float_as_uint(b))); }
DEV u32x4 unit16(u32x2 a, u32x2 b) {
    const u32x2 x = sw16(a[0], b[0]), y = sw16(a[1], b[1]);
    return (u32x4){x[0], y[0], x[1], y[1]};
}
template <int OFF> DEV void st16wt(const void* p, u32x4 v) {
    asm volatile("global_store_dwordx4 %0, %1, off offset:%2 sc1\n\ts_nop 1" ::"v"(p), "v"(v), "n"(OFF) : "memory");
}
DEV void st8wt(const void* p, u32x2 v) { asm volatile("global_store_dwordx2 %0, %1, off sc1\n\ts_nop 1" ::"v"(p), "v"(v) : "memory"); }
template <int CTRL> DEV float dppadd(float v) {
    return v + __builtin_bit_cast(float, __builtin_amdgcn_update_dpp(0, __builtin_bit_cast(int, v), CTRL, 0xf, 0xf, true));
}
DEV int fswz(int k) { return ((k >> 1) & 1) | (((k >> 3) & 1) << 1); }
DEV int fz(int tok) { return (-(tok >> 2)) & 3; }
DEV s16x4 ldtr4(const lchar* p) { return __builtin_bit_cast(s16x4, __builtin_amdgcn_ds_read_tr16_b64_v4i16((LAS s16x4*)p)); }
DEV bf16x8 cat8(s16x4 a, s16x4 b) { return (bf16x8){a[0], a[1], a[2], a[3], b[0], b[1], b[2], b[3]}; }
DEV bf16x8 ldk(const lchar* p) { return *(const LAS bf16x8*)p; }

#define STAMP(i)
#define STAMPW(i)
#define STPARG
#define STPPASS(o)
#define STAMPB(i)
#define SM(w, i)
#define SMD()
#define POLL(f, on, nd) poll(f, on, tag, nd, [](u64 m) { return (unsigned)m; })
#define HT0()
#define HT1(i)
#define TSD()
#define TS(i)
#define TSW(n)
#define BAR() asm volatile("s_barrier" ::: "memory")
#define BARL() asm volatile("s_waitcnt lgkmcnt(0)\n\ts_barrier" ::: "memory")
#define WAITB(n) asm volatile("s_waitcnt vmcnt(%0)\n\ts_barrier" ::"n"(n) : "memory")
#define WAITV(n) asm volatile("s_waitcnt vmcnt(%0)" ::"n"(n) : "memory")
#define SB() __builtin_amdgcn_sched_barrier(0)
#define LGKM0() __builtin_amdgcn_s_waitcnt(0xc07f)
#define MFMA32(a, b, c) __builtin_amdgcn_mfma_f32_32x32x16_bf16(a, b, c, 0, 0, 0)
#define MFMA16(a, b, c) __builtin_amdgcn_mfma_f32_16x16x32_bf16(a, b, c, 0, 0, 0)

DEV void mfma16t(f32x4& c, const bf16x8& a, const bf16x8& b) { asm volatile("v_mfma_f32_16x16x32_bf16 %0, %1, %2, %0" : "+v"(c) : "v"(a), "v"(b)); }
DEV void mfma16z(f32x4& c, const bf16x8& a, const bf16x8& b) { asm volatile("v_mfma_f32_16x16x32_bf16 %0, %1, %2, 0" : "=&v"(c) : "v"(a), "v"(b)); }
#define MFMA_DONE() do { SB(); asm volatile("s_nop 7\n\ts_nop 3"); SB(); } while (0)
DEV float fmx(float a, float b) { return __builtin_elementwise_maximum(a, b); }
DEV float rsd(float var) { return __builtin_amdgcn_rcpf(bfr(__builtin_amdgcn_sqrtf(bfr(bfr(var) + 1e-5f)))); }

constexpr int XS = 18432;
constexpr int WQ0 = 81920, WQS = 24576;
constexpr int STO = 65536;
constexpr int EXO = 57344;
constexpr int AR0 = 57344, AR1 = 65536, AR2 = 32768;
constexpr int SNM = 155648, SRS = SNM + 576, SCOL = SNM + 1280, HXO = SNM + 2048, DUMP = SNM + 4096, SCB = SNM + 4352;
constexpr int LDS_BYTES = 163840;
constexpr size_t MB = 1u << 20;
constexpr size_t WS_ST = 0, WS_GO = MB / 4, WS_CS = 3 * MB / 2, WS_FL = 15 * MB / 8, WS_X = 2 * MB, WS_O = 18 * MB, WS_KV = 26 * MB;
constexpr int FL_KV = 8192;
constexpr int FL_L = 10240;
constexpr int FL_C = 7168;
constexpr int FL_E = 10496;
constexpr int FL_S = 12288;
constexpr int FL_EP = 16368;
#define FLS 16
#define FS 1
DEV int fi(int i) { return (i >> 4) * FLS + (i & 15) * FS; }

#define GL_HEAD "s_mov_b32 m0, %1\n\ts_nop 4\n\tglobal_load_lds_dwordx4 %2, %0 sc1"
DEV void glds1(const void* sb, unsigned lds, unsigned v0) { asm volatile(GL_HEAD ::"s"(sb), "s"(lds), "v"(v0) : "memory"); }
DEV void glds3(const void* sb, unsigned lds, unsigned v0, unsigned v1, unsigned v2) {
    asm volatile(GL_HEAD "\n\tglobal_load_lds_dwordx4 %3, %0 offset:1024 sc1\n\tglobal_load_lds_dwordx4 %4, %0 offset:2048 sc1"
                 ::"s"(sb), "s"(lds), "v"(v0), "v"(v1), "v"(v2) : "memory");
}
DEV void glds4(const void* sb, unsigned lds, unsigned v0, unsigned v1, unsigned v2, unsigned v3) {
    asm volatile(GL_HEAD "\n\tglobal_load_lds_dwordx4 %3, %0 offset:1024 sc1\n\tglobal_load_lds_dwordx4 %4, %0 offset:2048 sc1\n\t"
                 "global_load_lds_dwordx4 %5, %0 offset:3072 sc1" ::"s"(sb), "s"(lds), "v"(v0), "v"(v1), "v"(v2), "v"(v3) : "memory");
}
DEV void touch(const void* sb, unsigned lds, unsigned v0) {
    asm volatile("s_mov_b32 m0, %1\n\ts_nop 4\n\tglobal_load_lds_dword %2, %0 sc1" ::"s"(sb), "s"(lds), "v"(v0) : "memory");
}
DEV u64 ldep(const u64* p) { return __hip_atomic_load((const GAS u64*)p, __ATOMIC_RELAXED, __HIP_MEMORY_SCOPE_AGENT); }
#define FLD(d, a, z) "global_atomic_add_x2 " d ", " a ", " z ", off sc0 sc1"
DEV u64 ldflag(const u64* p) {
    u64 v;
    asm volatile(FLD("%0", "%1", "%2") "\n\ts_waitcnt vmcnt(0)" : "=&v"(v) : "v"(p), "v"(0ull) : "memory");
    return v;
}
DEV void stflag(u64* p, u64 v) { asm volatile("global_atomic_swap_x2 %0, %1, off sc1\n\ts_nop 1" ::"v"(p), "v"(v) : "memory"); }
template <class F> DEV unsigned poll(const u64* p, bool on, u64 tag, unsigned need, F fold) {
    unsigned m = ~0u;
#pragma unroll 1
    for (int it = 0; it < POLLMAX; ++it) {
        u64 v = tag;
        if (on) v = ldflag(p);
        const unsigned r = fold(__ballot(v == tag));
        if ((r & need) == need) { m = r; break; }
        __builtin_amdgcn_s_sleep(1);
    }
    asm volatile("" ::: "memory");
    return m;
}
template <int B> DEV void poll2(const u64* f, unsigned m, u64 tag, u64& a, u64& b) {
    unsigned bud = POLLMAX;
    asm volatile("s_mov_b32 exec_lo, %5\n\ts_mov_b32 exec_hi, 0\n\t.if %8\n\t" FLD("%0", "%3", "%4") "\n\ts_waitcnt vmcnt(%8)\n\ts_barrier\n\t.else\n\t"
                 "v_cmp_ne_u64 vcc, %6, %0\n\ts_cbranch_vccz 2f\n\t" FLD("%0", "%3", "%4") "\n\ts_sleep %7\n\t.endif\n"
                 "1:\n\t" FLD("%1", "%3", "%4") "\n\ts_waitcnt vmcnt(1)\n\tv_cmp_ne_u64 vcc, %6, %0\n\ts_cbranch_vccz 2f\n\t"
                 FLD("%0", "%3", "%4") "\n\ts_waitcnt vmcnt(1)\n\tv_cmp_ne_u64 vcc, %6, %1\n\ts_sub_u32 %2, %2, 1\n\ts_cbranch_vccz 2f\n\ts_cbranch_scc0 1b\n"
                 "2:\n\ts_mov_b64 exec, -1"
                 : "+v"(a), "=&v"(b), "+s"(bud) : "v"(f), "v"(0ull), "s"(m), "s"(tag), "n"(P2), "n"(B) : "memory", "scc", "vcc");
}
template <int N = 0> DEV void publish(u64* f, int lane, u64 tag) {
    asm volatile("s_mov_b64 exec, 1\n\ts_waitcnt vmcnt(%2)\n\tglobal_atomic_swap_x2 %0, %1, off sc1\n\ts_mov_b64 exec, -1\n\ts_nop 0" ::"v"(f), "v"(tag), "n"(N) : "memory");
}
DEV void raise(u64* f, u64 tag) {
    asm volatile("s_mov_b64 exec, 1\n\tglobal_atomic_swap_x2 %0, %1, off sc1\n\ts_mov_b64 exec, -1\n\ts_nop 0" ::"v"(f), "v"(tag) : "memory");
}
DEV unsigned go_off(int g, int lane, int wm, int wn) { return (g * 16 + (lane & 31)) * 1024 + (wn * 8 + ((lane & 16) ? 2 : wm)) * 8; }
DEV void go(const char* b, unsigned o, unsigned m, u64 tag) {
    asm volatile("s_cmp_eq_u32 %3, 0\n\ts_cbranch_scc1 1f\n\ts_mov_b32 exec_lo, %3\n\ts_mov_b32 exec_hi, 0\n\tglobal_atomic_swap_x2 %0, %1, %2 sc1\n\ts_mov_b64 exec, -1\n1:"
                 ::"v"(o), "v"(tag), "s"(b), "s"(m) : "memory", "scc");
}
#define OPAQUE(l, a, b, c) do { asm volatile("" : "+v"(l)); asm volatile("" : "+s"(a), "+s"(b), "+s"(c)); } while (0)

template <bool LOCAL, class F, class G>
DEV void qkv_main(lchar* smem, f32x4 (&acc)[6][4], f32x4 (&acch)[2], int lane, int wm, int wn, int layer, G mid, F tailread STPARG) {
    constexpr int XC_B = LOCAL ? 9216 : 8192, HO = LOCAL ? 1024 : 0, NR = LOCAL ? 17 : 16;
    const int l15 = lane & 15, lg = lane >> 4, hs = LOCAL ? wm : 0;
    const lchar* xhb = smem + l15 * 64 + ((lg ^ fz(l15)) << 4);
    const lchar* xb = xhb + HO + wm * 4096;
    const int k0 = 8 * lg + (l15 >> 2);
    const lchar* wb0 = smem + WQ0 + lg * 3072 + (l15 >> 2) * 128 + (((wn * 2 + hs) ^ fswz(k0)) << 5) + (l15 & 3) * 8;
    const lchar* wb1 = smem + WQ0 + lg * 3072 + (l15 >> 2) * 128 + (((wn * 2 + (hs ^ 1)) ^ fswz(k0)) << 5) + (l15 & 3) * 8;
    lchar* sto = smem + STO + (wm * 2 + wn) * 4096 + lane * 16;
    acch[0] = acch[1] = (f32x4){0.f, 0.f, 0.f, 0.f};
    if (LOCAL) asm volatile("" : "+v"(acch[0]), "+v"(acch[1]), "+v"(xhb), "+v"(sto));
    asm volatile("" : "+v"(xb), "+v"(wb0), "+v"(wb1));
    bf16x8 xf[2][5], hf[6];
    s16x4 wh[2][6][2];
    auto rd = [&](int b, int j, int xo, int wo) {
        if (j < 4) xf[b][j] = ldk(xb + xo + j * 1024);
        else if (j < 16) wh[b][(j - 4) >> 1][j & 1] = ldtr4(((j & 2) ? wb1 : wb0) + wo + ((j - 4) >> 2) * 1024 + (j & 1) * 512);
        else xf[b][4] = ldk(xhb + xo);
    };
    auto aside = [&](int b) {
        *(LAS bf16x8*)(sto + b * 2048) = cat8(wh[b][2][0], wh[b][2][1]);
        *(LAS bf16x8*)(sto + b * 2048 + 1024) = cat8(wh[b][4][0], wh[b][4][1]);
    };
    auto kstep = [&](int b, int xo, int wo, int md) __attribute__((always_inline)) {
        int j = 0;
        auto fillr = [&]() __attribute__((always_inline)) {
            SB();
            if (md == 2) {
                if (!LOCAL) tailread(j);
                else if (j < 6) hf[j] = j < 2 ? ldk(xhb + HXO + j * 1024) : ldk(sto + (j - 2) * 1024);
                else tailread(j - 6);
            } else if (j < (md ? NR : 16)) rd(b ^ 1, j, xo, wo);
            else if (LOCAL && md == 0 && j == 16) aside(0);
            SB();
            ++j;
        };
        SB();
#pragma unroll
        for (int v = 0; v < 34; ++v) {
            const int u = v < 15 ? v : v - 4;
            const int ni = u < 20 ? u / 5 : 4 + (((u >> 1) ^ u) & 1), ti = u >= 20 ? (u - 20) / 2 : (ni & 1) ? 4 - u % 5 : u % 5;
            const bf16x8 w = cat8(wh[b][ni][0], wh[b][ni][1]);
            if (v >= 15 && v < 19) {
                if (!LOCAL || md != 2) continue;
                const int k = (v - 15) >> 1;
                if (v & 1) mfma16t(acch[1], hf[k], hf[3 + 2 * k]);
                else mfma16t(acch[0], hf[2 + 2 * k], hf[k]);
            } else if (ti == 4) {
                if (!LOCAL || md == 0 || (ni != 2 && ni != 4)) continue;
                if (ni == 2) mfma16t(acch[0], w, xf[b][4]);
                else mfma16t(acch[1], xf[b][4], w);
            } else if (md == 0) {
                if (ni >= 4) mfma16z(acc[ni][ti], xf[b][ti], w);
                else mfma16z(acc[ni][ti], w, xf[b][ti]);
            } else {
                if (ni >= 4) mfma16t(acc[ni][ti], xf[b][ti], w);
                else mfma16t(acc[ni][ti], w, xf[b][ti]);
            }
            fillr();
        }
        if (md < 2) { LGKM0(); SB(); }
    };
    if (LOCAL && PB) asm volatile("s_cmp_lg_u32 %0, 0\n\ts_cbranch_scc1 1f\n\ts_barrier\n\ts_barrier\n\ts_barrier\n1:" ::"s"(layer) : "memory", "scc");
    SB();
    STAMP(4);
#pragma unroll
    for (int j = 0; j < 16; ++j) rd(0, j, 0, 2 * WQS);
    SB();
    LGKM0(); SB();
    kstep(0, XC_B, 2 * WQS + WQS / 2, 0);
    if (LOCAL) {
        aside(1);
        xf[1][4] = (bf16x8){0, 0, 0, 0, 0, 0, 0, 0};
        asm volatile("" : "+v"(xf[1][4]));
    }
    SM(1, 11);
    WAITV(0);
    SM(2, 12);
    SB();
#pragma unroll 1
    for (int it = 0; it < 5; ++it) {
        mid(it);
#pragma unroll
        for (int s = 0; s < 3; ++s) {
            BAR(); SB();
            kstep(1, ((s + 1) % 3) * XS, s * WQS, 1);
            kstep(0, ((s + 1) % 3) * XS + XC_B, s * WQS + WQS / 2, 1);
        }
    }
    BAR(); SB();
    kstep(1, 0, 0, 2);
    MFMA_DONE();
}

constexpr int NRW = 7;
typedef s16x4 WReg[NRW * 8];
DEV const lchar* wo_wb(const lchar* smem, int lane, int c) {
    const int k0 = 8 * (lane >> 4) + ((lane & 15) >> 2);
    return smem + k0 * 128 + ((c ^ fswz(k0)) << 5) + (lane & 3) * 8;
}
#define PIN2(a, b) asm volatile("" : "+v"(a), "+v"(b))
#define WRD1() do { SB(); wr[wj] = ldtr4(((wj & 2) ? wb1 : wb0) + (wj >> 2) * 4096 + (wj & 1) * 512); ++wj; SB(); } while (0)

DEV void qa_compute(lchar* smem, int fold, const u16* xin, u32x4 (&rr)[4], WReg& wr, u16* O, const char* fo, unsigned gm, u64 tag, int g, int hp, int lane, int wid STPARG) {
    OPAQUE(lane, wid, g, hp);
    const int wm = wid >> 1, wn = wid & 1, l15 = lane & 15, lg = lane >> 4;
    f32x4 acc[6][4], acch[2];
    float nm[5], rs[5], csv[2];
    f32x4 nmv[5], rsv[5], cs[4];
    const lchar* sa = smem + SNM + l15 * 4;
    const lchar* sb = smem + SNM + lg * 16;
    const lchar* sc = smem + SCOL + wn * 128;
    const int ro = 64 + wm * 256;
    f32x4 cprev, ccur;
#pragma unroll
    for (int j = 0; j < 4; ++j) {
        const int d = l15 - 4 * lg - j;
        ccur[j] = (d >= 0 && !(d & 1)) ? 0.f : -3.0e38f;
        cprev[j] = (d <= -2 && !(d & 1)) ? 0.f : -3.0e38f;
    }
    const char* optr = (const char*)O + ((size_t)wn * SEQ + g * 128 + wm * 64 + l15) * 64 +
                       (((((lg & 1) << 1) | (lg >> 1)) ^ (wm << 1) ^ fz(l15)) << 4);
    unsigned xo = rr[0][0];
    asm volatile("" : "+v"(cprev), "+v"(ccur), "+v"(optr), "+v"(xo));
    qkv_main<true>(smem, acc, acch, lane, wm, wn, fold, [&](int it) {
        if (RS == 1) {
#pragma unroll
            for (int k = 0; k < 4; ++k)
                asm volatile("s_cmp_lg_u32 %3, %4\n\ts_cbranch_scc1 1f\n\tglobal_load_dwordx4 %0, %1, %2\n\tv_add_u32 %1, 0x8000, %1\n1:"
                             : "+v"(rr[k]), "+v"(xo) : "s"(xin), "s"(it + fold * 8), "n"(k + 1) : "memory", "scc");
        } else if (RS == 2) {
#pragma unroll
            for (int k = 0; k < 2; ++k)
                asm volatile("s_cmp_lg_u32 %4, %5\n\ts_cbranch_scc1 1f\n\tglobal_load_dwordx4 %0, %2, %3\n\tv_add_u32 %2, 0x8000, %2\n\t"
                             "global_load_dwordx4 %1, %2, %3\n\tv_add_u32 %2, 0x8000, %2\n1:"
                             : "+v"(rr[2 * k]), "+v"(rr[2 * k + 1]), "+v"(xo) : "s"(xin), "s"(it + fold * 8), "n"(k + 3) : "memory", "scc");
        } else
        asm volatile("s_cmp_lg_u32 %6, %7\n\ts_cbranch_scc1 1f\n\tglobal_load_dwordx4 %0, %4, %5\n\tv_add_u32 %4, 0x8000, %4\n\t"
                     "global_load_dwordx4 %1, %4, %5\n\tv_add_u32 %4, 0x8000, %4\n\tglobal_load_dwordx4 %2, %4, %5\n\tv_add_u32 %4, 0x8000, %4\n\t"
                     "global_load_dwordx4 %3, %4, %5\n1:" : "+v"(rr[0]), "+v"(rr[1]), "+v"(rr[2]), "+v"(rr[3]), "+v"(xo)
                     : "s"(xin), "s"(it + fold * 8), "n"(RI) : "memory", "scc");
    }, [&](int j) {
        if (j < 4) cs[j] = *(const LAS f32x4*)(sc + (j >> 1) * 256 + (((j & 1) ^ wm) << 6) + lg * 16);
        else if (j < 9) { const int o = j < 8 ? ro + (j - 4) * 64 : 0; nm[j - 4] = *(const LAS float*)(sa + o); rs[j - 4] = *(const LAS float*)(sa + o + 576); }
        else if (j < 11) csv[j - 9] = *(const LAS float*)(sc + 512 + (((j - 9) ^ wm) << 6) + l15 * 4);
        else if (j < 21) {
            const int t = (j - 11) >> 1, o = t < 4 ? ro + t * 64 : 0;
            if ((j - 11) & 1) rsv[t] = *(const LAS f32x4*)(sb + o + 576);
            else nmv[t] = *(const LAS f32x4*)(sb + o);
        }
    } STPPASS(0));
    STAMP(2);
    TSD(); TS(0);
    if (fold) {
#pragma unroll
        for (int ti = 0; ti < 4; ++ti) {
#pragma unroll
            for (int ni = 0; ni < 4; ++ni) acc[ni][ti] = (acc[ni][ti] + nm[ti] * cs[ni]) * rs[ti];
#pragma unroll
            for (int h = 0; h < 2; ++h) acc[4 + h][ti] = (acc[4 + h][ti] + nmv[ti] * csv[h]) * rsv[ti];
        }
        acch[0] = (acch[0] + nm[4] * cs[2]) * rs[4];
        acch[1] = (acch[1] + nmv[4] * csv[0]) * rsv[4];
    }
    typedef __attribute__((ext_vector_type(10))) unsigned u32x10;
    u32x2 kp[5][2];
    u32x10 vp[2];
    auto setv = [&](int h, int i, u32x2 a) { vp[h][2 * i] = a[0]; vp[h][2 * i + 1] = a[1]; };
    lchar* ex = smem + EXO + wn * 4096 + lane * 16;
    {
        const u32x2 a = pk4(acch[0]), b = pk4(acch[1]);
        *(LAS u32x4*)(ex + wm * 1024) = (u32x4){a[0], a[1], b[0], b[1]};
    }
#pragma unroll
    for (int h = 0; h < 2; ++h) { kp[4][h] = pk4(acc[2 + h][3]); setv(h, 4, pk4(acc[4 + h][3])); }
    if (wm == 0) {
#pragma unroll
        for (int h = 0; h < 2; ++h) *(LAS u32x4*)(ex + 2048 + h * 1024) = (u32x4){kp[4][h][0], kp[4][h][1], vp[h][8], vp[h][9]};
    }
    bf16x8 qf[4];
#pragma unroll
    for (int ti = 0; ti < 4; ++ti) {
        qf[ti] = cat(pk4(acc[0][ti]), pk4(acc[1][ti]));
        if (ti < 3) {
#pragma unroll
            for (int h = 0; h < 2; ++h) { kp[ti + 1][h] = pk4(acc[2 + h][ti]); setv(h, ti + 1, pk4(acc[4 + h][ti])); }
        }
    }
    const lchar* wb0 = wo_wb(smem, lane, wn * 2);
    const lchar* wb1 = wo_wb(smem, lane, wn * 2 + 1);
    const lchar* e0 = ex + wm * 3072;
    const lchar* e1 = ex + wm * 1024 + 1024;
    f32x4 cp0 = cprev;
    if (g == 0 && wm == 0) cp0 = (f32x4){-3.0e38f, -3.0e38f, -3.0e38f, -3.0e38f};
    unsigned sel0 = (lane & 1) ? 0x01000c0cu : 0x0c0c0100u, sel1 = (lane & 1) ? 0x03020c0cu : 0x0c0c0302u;
    bf16x8 ones = {0x3f80, 0x3f80, 0x3f80, 0x3f80, 0x3f80, 0x3f80, 0x3f80, 0x3f80};
    unsigned gof = go_off(g, lane, wm, wn);
    lchar* ol = smem + 65536 + wn * 8192 + wm * 4096 + l15 * 64 + (((((lg & 1) << 1) | (lg >> 1)) ^ (wm << 1) ^ fz(l15)) << 4);
    asm volatile("" : "+v"(wb0), "+v"(wb1), "+v"(e0), "+v"(e1), "+v"(cp0), "+v"(sel0), "+v"(sel1), "+v"(ones), "+v"(vp[0]), "+v"(vp[1]), "+v"(ol), "+v"(gof));
    SB();
    TS(1);
    BARL();
    TS(2);
    kp[0][0] = *(const LAS u32x2*)e0; SB(); kp[0][1] = *(const LAS u32x2*)e1; SB();
    setv(0, 0, *(const LAS u32x2*)(e0 + 8)); SB(); setv(1, 0, *(const LAS u32x2*)(e1 + 8));
    SB();
    int wj = 0;
    f32x4 s0[4], s1[4];
    f32x2 sv[4][2];
    float mx[4];
#pragma unroll
    for (int ti = 3; ti >= 0; --ti) {
        s1[ti] = MFMA16(cat(kp[ti + 1][0], kp[ti + 1][1]), qf[ti], ccur);
        WRD1();
        s0[ti] = MFMA16(cat(kp[ti][0], kp[ti][1]), qf[ti], ti ? cprev : cp0);
        WRD1();
    }
#pragma unroll
    for (int i = 0; i < 48; ++i) if (wj < 48) WRD1();
#pragma unroll
    for (int i = 7; i >= 0; --i) {
        const f32x4& t = (i & 1) ? s1[i >> 1] : s0[i >> 1];
        sv[i >> 1][i & 1] = (f32x2){fmx(t[0], t[1]), fmx(t[2], t[3])};
        asm volatile("" : "+v"(sv[i >> 1][i & 1]));
    }
    if (WA == 0) { SB(); LGKM0(); BAR(); SB(); }
    if (EMU) {
        unsigned r[8];
#pragma unroll
        for (int ps = 0; ps < 2; ++ps) {
#pragma unroll
            for (int i = 7; i >= 0; --i) r[i] = cvtpk(sv[i >> 1][i & 1][0], sv[i >> 1][i & 1][1]);
#pragma unroll
            for (int i = 7; i >= 0; --i) {
                sv[i >> 1][i & 1] = (f32x2){lo16(r[i]), hi16(r[i])};
                if (ps == 0) sv[i >> 1][i & 1] *= QSCALE;
            }
        }
    }
#pragma unroll
    for (int ti = 3; ti >= 0; --ti) mx[ti] = fmx(fmx(fmx(sv[ti][0][0], sv[ti][0][1]), sv[ti][1][0]), sv[ti][1][1]);
    TS(3);
    {
        const f32x2 a = sw16f(mx[0], mx[1]), b = sw16f(mx[2], mx[3]);
        const f32x2 c = sw32f(fmx(a[0], a[1]), fmx(b[0], b[1]));
        const float r = fmx(c[0], c[1]) * -(EMU ? LOG2E : QSCALE * LOG2E);
        const f32x2 d = sw32f(r, r), e = sw16f(d[0], d[0]), f = sw16f(d[1], d[1]);
        mx[0] = e[0]; mx[1] = e[1]; mx[2] = f[0]; mx[3] = f[1];
    }
    SB();
    TS(4);
    if (WA == 1) { LGKM0(); BAR(); SB(); }
    TS(5);
    const f32x4 z = {0.f, 0.f, 0.f, 0.f};
    f32x4 l[4], o0[4], o1[4];
    auto pv = [&](int ti, u32x4 v0, u32x4 v1) {
        const f32x2 x = sv[ti][0] * (EMU ? LOG2E : QSCALE * LOG2E) + mx[ti], y = sv[ti][1] * (EMU ? LOG2E : QSCALE * LOG2E) + mx[ti];
        const unsigned c0 = cvtpk(__builtin_amdgcn_exp2f(x[0]), __builtin_amdgcn_exp2f(x[1]));
        const unsigned c1 = cvtpk(__builtin_amdgcn_exp2f(y[0]), __builtin_amdgcn_exp2f(y[1]));
        const bf16x8 p = __builtin_bit_cast(bf16x8, (u32x4){__builtin_amdgcn_perm(c0, c0, sel0), __builtin_amdgcn_perm(c0, c0, sel1),
                                                            __builtin_amdgcn_perm(c1, c1, sel0), __builtin_amdgcn_perm(c1, c1, sel1)});
        SB();
        l[ti] = MFMA16(ones, p, z);
        WRD1();
        o0[ti] = MFMA16(__builtin_bit_cast(bf16x8, v0), p, z);
        WRD1();
        o1[ti] = MFMA16(__builtin_bit_cast(bf16x8, v1), p, z);
        SB();
    };
    auto fin = [&](int ti) {
        const float inv = __builtin_amdgcn_rcpf(l[ti][0]);
        return unit16(pk4(o0[ti] * inv), pk4(o1[ti] * inv));
    };
#define PV(ti) pv(ti, __builtin_shufflevector(vp[0], vp[0], 2 * ti, 2 * ti + 1, 2 * ti + 2, 2 * ti + 3), \
                      __builtin_shufflevector(vp[1], vp[1], 2 * ti, 2 * ti + 1, 2 * ti + 2, 2 * ti + 3))
    PV(0); PV(1);
    TS(6);
#define OOUT(ti) do { const u32x4 v = fin(ti); st16wt<ti * 1024>(optr, v); *(LAS u32x4*)(ol + ti * 1024) = v; SB(); } while (0)
    if (EA == 4) { go(fo, gof, gm, tag); SB(); }
    OOUT(0);
    if (EA == 3) { go(fo, gof, gm, tag); SB(); }
    if (WA == 2) { __builtin_amdgcn_s_waitcnt(0xc47f); BAR(); SB(); }
    TS(7);
    PV(2);
    OOUT(1);
    if (EA == 2) { go(fo, gof, gm, tag); SB(); }
    if (WA == 3) { __builtin_amdgcn_s_waitcnt(0xc67f); BAR(); SB(); }
    TS(8);
    PV(3);
    OOUT(2);
    if (EA == 1) { go(fo, gof, gm, tag); SB(); }
    if (WA == 4) { __builtin_amdgcn_s_waitcnt(0xc87f); BAR(); SB(); }
    TS(9);
    OOUT(3);
    TS(10);
    if (EA == 0) go(fo, gof, gm, tag);
    SM(0, 2); SMD();
    TS(11);
    TSW(12);
    STAMP(10);
}

#define MX3(a, b, c) fmx(fmx((a), (b)), (c))
DEV float rowmax32(const f32x16& s0) {
    float ma = MX3(s0[0], s0[1], s0[2]), mb = MX3(s0[3], s0[4], s0[5]);
    ma = MX3(ma, s0[6], s0[7]); mb = MX3(mb, s0[8], s0[9]);
    ma = MX3(ma, s0[10], s0[11]); mb = MX3(mb, s0[12], s0[13]);
    ma = fmx(MX3(ma, s0[14], s0[15]), mb);
    const f32x2 b = sw32f(ma, ma);
    return fmx(b[0], b[1]);
}
#define EX(i) do { S[i] = __builtin_amdgcn_exp2f(S[i]); SB(); } while (0)
#define EQ(i) do { p1[i] = __builtin_amdgcn_exp2f(S[8 + i]); SB(); } while (0)
typedef __attribute__((ext_vector_type(16))) __bf16 bf16x16_t;
typedef __attribute__((ext_vector_type(8))) __bf16 bf16x8_t;
typedef __attribute__((ext_vector_type(8))) float f32x8;
template <int H> DEV f32x8 half8(const f32x16& s) { return __builtin_shufflevector(s, s, H, H + 1, H + 2, H + 3, H + 4, H + 5, H + 6, H + 7); }
#define SMF32(a, b, c) __builtin_amdgcn_smfmac_f32_32x32x32_bf16(__builtin_bit_cast(bf16x8_t, a), __builtin_bit_cast(bf16x16_t, b), c, hi, 0, 0)
#define SMF16(a, b, c) __builtin_amdgcn_smfmac_f32_16x16x64_bf16(__builtin_bit_cast(bf16x8_t, a), __builtin_bit_cast(bf16x16_t, b), c, hi, 0, 0)
template <class G0, class F1, class F2, class F3>
DEV void block(f32x16& S, f32x16& N, const bf16x8& kf0, const bf16x8& kf1, const bf16x8& q0, const bf16x8& q1, const f32x16& c,
               f32x16& oP, f32x4& lP, const bf16x8& vf1, f32x8& p1, f32x16& oC, f32x4& lC, const bf16x8& vf0,
               const bf16x8& sel, int hi, G0 fb, F1 fc, F2 fd, F3 ff) {
    N = MFMA32(kf0, q0, c); SB();
    EX(0); EX(1); EX(2);
    oP = SMF32(vf1, p1, oP); SB();
    fb(); SB();
    EX(3); EX(4); EX(5);
    lP = SMF16(sel, p1, lP); SB();
    fc(); SB();
    EX(6);
    N = MFMA32(kf1, q1, N); SB();
    fd(); SB();
    EX(7); EQ(0); EQ(1);
    oC = SMF32(vf0, half8<0>(S), oC); SB();
    EQ(2); EQ(3); EQ(4);
    lC = SMF16(sel, half8<0>(S), lC); SB();
    ff(); SB();
    EQ(5); EQ(6); EQ(7);
}
DEV void attn_slow(const char* kh, const char* vh, const bf16x8& q0, const bf16x8& q1, f32x16& o, float& l, int hi) {
    f32x16 c;
#pragma unroll
    for (int i = 0; i < 16; ++i) { o[i] = 0.f; c[i] = 0.f; }
    float m = 0.f;
    l = 0.f;
#pragma unroll 1
    for (int t = 0; t < SEQ / 32; ++t) {
        bf16x8 k0, k1, v0, v1;
        const char* kp = kh + t * 2048;
        const char* vp = vh + t * 2048;
        asm volatile("global_load_dwordx4 %0, %4, off sc1\n\tglobal_load_dwordx4 %1, %5, off sc1\n\tglobal_load_dwordx4 %2, %6, off sc1\n\t"
                     "global_load_dwordx4 %3, %7, off sc1\n\ts_waitcnt vmcnt(0)" : "=&v"(k0), "=&v"(k1), "=&v"(v0), "=&v"(v1)
                     : "v"(kp), "v"((const char*)((uintptr_t)kp ^ 32)), "v"(vp), "v"((const char*)((uintptr_t)vp ^ 32)) : "memory");
        f32x16 s = MFMA32(k0, q0, c);
        s = MFMA32(k1, q1, s);
        const float mx = rowmax32(s);
        const float dl = t == 0 ? mx : fmx(mx, 0.f);
        const float alpha = t == 0 ? 0.f : __builtin_amdgcn_exp2f(-dl);
        m += dl;
        l *= alpha;
#pragma unroll
        for (int i = 0; i < 16; ++i) {
            o[i] *= alpha; c[i] = -m;
            s[i] = __builtin_amdgcn_exp2f(s[i] - dl);
            l += hi16(__float_as_uint(s[i]));
        }
        o = SMF32(v0, half8<0>(s), o);
        o = SMF32(v1, half8<8>(s), o);
    }
    const f32x2 b = sw32f(l, l);
    l = b[0] + b[1];
}

DEV void qb_compute(lchar* smem, u16* kv, WReg& wr, u16* O, const char* fo, unsigned gm, u64* fkv, u64 tag, int g, int hp, int lane, int wid STPARG) {
    OPAQUE(lane, wid, g, hp);
    const int wm = wid >> 1, wn = wid & 1, l15 = lane & 15, lg = lane >> 4, h = hp * 2 + wn;
    bf16x8 qa0, qa1, qb0, qb1;
    lchar* gs = smem + AR0 + wn * 16384;
    {
        f32x4 acc[6][4], acch[2];
        float nm[4], rs[4], csv[2];
        f32x4 cs[4], nmv[4], rsv[4];
        const lchar* sa = smem + SNM + 64 + wm * 256 + l15 * 4;
        const lchar* sb = smem + SNM + 64 + wm * 256 + lg * 16;
        const lchar* sc = smem + SCB + wn * 128;
        const int un = ((lg & 1) << 1) | (lg >> 1);
        const int ko = l15 * 64 + ((un ^ (l15 >> 2)) << 4), vo = l15 * 64 + ((lg ^ (l15 >> 2)) << 4);
        const char* kptr = (const char*)kv + ((size_t)h * SEQ + g * 128 + wm * 64) * 64 + ko;
        const char* vptr = (const char*)kv + ((size_t)(NH + h) * SEQ + g * 128 + wm * 64) * 64 + vo;
        lchar* kl = gs + wm * 8192 + ko;
        lchar* vl = gs + wm * 8192 + 4096 + vo;
        asm volatile("" : "+v"(kptr), "+v"(vptr));
        qkv_main<false>(smem, acc, acch, lane, wm, wn, 1, [](int) {}, [&](int j) {
            if (j < 4) cs[j] = *(const LAS f32x4*)(sc + (j >> 1) * 256 + (j & 1) * 64 + lg * 16);
            else if (j < 8) { nm[j - 4] = *(const LAS float*)(sa + (j - 4) * 64); rs[j - 4] = *(const LAS float*)(sa + (j - 4) * 64 + 576); }
            else if (j < 10) csv[j - 8] = *(const LAS float*)(sc + 512 + (j - 8) * 64 + l15 * 4);
            else if (j < 18) {
                if (j & 1) rsv[(j - 10) >> 1] = *(const LAS f32x4*)(sb + ((j - 10) >> 1) * 64 + 576);
                else nmv[(j - 10) >> 1] = *(const LAS f32x4*)(sb + ((j - 10) >> 1) * 64);
            }
        } STPPASS(0));
        STAMP(2);
        auto tile = [&](int ni, int ti, float sc_) { return pk4((acc[ni][ti] + nm[ti] * cs[ni]) * sc_); };
        auto tilev = [&](int h_, int ti) { return pk4((acc[4 + h_][ti] + nmv[ti] * csv[h_]) * rsv[ti]); };
#define KOUT(ti) do { const u32x4 v = unit16(tile(2, ti, rs[ti]), tile(3, ti, rs[ti])); st16wt<ti * 1024>(kptr, v); *(LAS u32x4*)(kl + ti * 1024) = v; } while (0)
#define VOUT(j) do { const u32x4 v = __builtin_bit_cast(u32x4, cat(tilev(j & 1, j & 2), tilev(j & 1, (j & 2) + 1))); st16wt<j * 1024>(vptr, v); *(LAS u32x4*)(vl + j * 1024) = v; } while (0)
        KOUT(0); KOUT(1); KOUT(2); KOUT(3);
        VOUT(0); VOUT(1); VOUT(2); VOUT(3);
        if (EF & 4) raise(fkv + fi(wn * 32 + wm * 16 + g), tag);
        SB(); BARL(); SB();
#pragma unroll
        for (int ti = 0; ti < 4; ++ti) rs[ti] *= QSCALE * LOG2E;
        qa0 = __builtin_bit_cast(bf16x8, unit16(tile(0, 0, rs[0]), tile(0, 1, rs[1])));
        qa1 = __builtin_bit_cast(bf16x8, unit16(tile(1, 0, rs[0]), tile(1, 1, rs[1])));
        qb0 = __builtin_bit_cast(bf16x8, unit16(tile(0, 2, rs[2]), tile(0, 3, rs[3])));
        qb1 = __builtin_bit_cast(bf16x8, unit16(tile(1, 2, rs[2]), tile(1, 3, rs[3])));
    }
    STAMP(3);
    const int r = lane & 31, hh = lane >> 5;
    const int kro = ((r & 4) * 4 + (r >> 3) * 4 + (r & 3)) * 64 + ((hh ^ (r >> 3)) << 4), vro = r * 64 + ((hh ^ ((r >> 2) & 3)) << 4);
    const lchar* kb0 = gs + kro;
    const lchar* kb1 = gs + (kro ^ 32);
    const lchar* vb0 = gs + 4096 + vro;
    const lchar* vb1 = gs + 4096 + (vro ^ 32);
    const short se = (l15 & 1) ? (short)0 : (short)0x3f80, so = se ^ 0x3f80;
    const bf16x8 sel = {se, se, se, se, so, so, so, so};
    int hi = 0xdddd;
    const char* op = (const char*)O + ((size_t)wn * SEQ + g * 128 + wm * 64 + l15) * 64 + (((((lg & 1) << 1) | (lg >> 1)) ^ fz(l15)) << 4);
    const lchar* wb0 = wo_wb(smem, lane, wn * 2);
    const lchar* wb1 = wo_wb(smem, lane, wn * 2 + 1);
    unsigned gof = go_off(g, lane, wm, wn);
    lchar* ol = smem + 65536 + wn * 8192 + wm * 4096 + l15 * 64 + (((((lg & 1) << 1) | (lg >> 1)) ^ fz(l15)) << 4);
    asm volatile("" : "+v"(hi), "+v"(op), "+v"(wb0), "+v"(wb1), "+v"(ol), "+v"(gof));
    f32x16 oA, oB, cA, cB, sA, sB;
    f32x4 lA = {0.f, 0.f, 0.f, 0.f}, lB = {0.f, 0.f, 0.f, 0.f};
#pragma unroll
    for (int i = 0; i < 16; ++i) { oA[i] = 0.f; oB[i] = 0.f; }
    bf16x8 kf0, kf1, vf0, vf1[2];
    f32x8 p1 = {0.f, 0.f, 0.f, 0.f, 0.f, 0.f, 0.f, 0.f};
    auto nop = [&]() {};
    auto tilestep = [&](int par, int nx, bool bar) {
        block(sA, sB, kf0, kf1, qb0, qb1, cB, oB, lB, vf1[par ^ 1], p1, oA, lA, vf0, sel, hi, [&]() { if (bar) BAR(); },
              [&]() { kf0 = ldk(kb0 + nx); }, [&]() { __builtin_amdgcn_s_waitcnt(0xc17f); }, [&]() { LGKM0(); SB(); kf1 = ldk(kb1 + nx); });
        block(sB, sA, kf0, kf1, qa0, qa1, cA, oA, lA, vf1[par], p1, oB, lB, vf0, sel, hi, nop,
              [&]() { LGKM0(); SB(); vf1[par ^ 1] = ldk(vb1 + nx); }, nop, [&]() { vf0 = ldk(vb0 + nx); });
    };
    {
        const int own = wm * 8192, oth = own ^ 8192;
        kf0 = ldk(kb0 + own); kf1 = ldk(kb1 + own);
        vf0 = ldk(vb0 + own); vf1[0] = ldk(vb1 + own); vf1[1] = sel;
        asm volatile("" : "+v"(qa0), "+v"(qa1), "+v"(qb0), "+v"(qb1)); SB();
        if (!(EF & 4)) publish(fkv + fi(wn * 32 + wm * 16 + g), lane, tag);
        SB();
        sA = MFMA32(kf0, qa0, oA); sB = MFMA32(kf0, qb0, oA);
        sA = MFMA32(kf1, qa1, sA); sB = MFMA32(kf1, qb1, sB);
        const float mA = rowmax32(sA), mB = rowmax32(sB);
#pragma unroll
        for (int i = 0; i < 16; ++i) { cA[i] = -mA; cB[i] = -mB; sA[i] -= mA; }
        asm volatile("" : "+v"(cA), "+v"(cB));
        SB();
        STAMP(5);
        tilestep(0, own + 2048, false);
        tilestep(1, oth, false);
        tilestep(0, oth + 2048, false);
        tilestep(1, AR1 + 8192, true);
        STAMP(6);
    }
#pragma unroll 1
    for (int it = 0; it < 5; ++it) {
#pragma unroll
        for (int s = 0; s < 3; ++s) {
            const int cur = s == 0 ? AR1 : s == 1 ? AR2 : 0, nxt = s == 0 ? AR2 : s == 1 ? 0 : AR1;
            tilestep(0, cur + 10240, false);
            tilestep(1, cur, false);
            tilestep(0, cur + 2048, false);
            tilestep(1, nxt + 8192, true);
        }
    }
    oB = SMF32(vf1[1], p1, oB);
    lB = SMF16(sel, p1, lB);
    SB();
    STAMP(9);
    TSD(); TS(0);
    int wj = 0;
    auto fin = [&](const f32x16& o, float l, u32x4& u0, u32x4& u1, bool rd) {
        const float inv = __builtin_amdgcn_rcpf(l);
        unsigned y[8];
        if (rd) { WRD1(); WRD1(); }
#pragma unroll
        for (int i = 0; i < 8; ++i) {
            const f32x2 t = (f32x2){o[2 * i], o[2 * i + 1]} * inv;
            if (rd) WRD1();
            y[i] = cvtpk(t[0], t[1]);
        }
        const u32x2 a = sw32(y[0], y[2]), b = sw32(y[1], y[3]);
        if (rd) WRD1();
        const u32x2 c = sw32(y[4], y[6]), d = sw32(y[5], y[7]);
        if (rd) WRD1();
        const u32x2 e = sw16(a[0], c[0]), f = sw16(b[0], d[0]);
        if (rd) WRD1();
        const u32x2 i = sw16(a[1], c[1]), j = sw16(b[1], d[1]);
        if (rd) WRD1();
        u0 = (u32x4){e[0], f[0], i[0], j[0]};
        u1 = (u32x4){e[1], f[1], i[1], j[1]};
    };
    if (EB == 4) { SB(); go(fo, gof, gm, tag); SB(); }
    u32x4 u0, u1;
    float la = (lane & 16) ? lA[1] : lA[0];
#pragma unroll
    for (int i = 0; i < WB; ++i) WRD1();
    fin(oA, la, u0, u1, false);
    float lb = (lane & 16) ? lB[1] : lB[0];
    if (__builtin_expect(__any(!(la <= 1073741824.0f) || !(lb <= 1073741824.0f)), 0)) {
        const char* kh = (const char*)kv + (size_t)h * SEQ * 64;
#pragma unroll 1
        for (int ps = 0; ps < 2; ++ps) {
            fin(oB, lb, u0, u1, false);
            attn_slow(kh + kro, kh + (size_t)NH * SEQ * 64 + vro, ps ? qb0 : qa0, ps ? qb1 : qa1, oB, lb, hi);
        }
    }
    TS(1);
    if (EB == 3) { SB(); go(fo, gof, gm, tag); SB(); }
    st16wt<0>(op, u0); TS(2); st16wt<1024>(op, u1);
    if (EB == 2) { SB(); go(fo, gof, gm, tag); SB(); }
    SB(); LGKM0(); BAR(); SB();
    if (EB == 1) { go(fo, gof, gm, tag); SB(); }
    *(LAS u32x4*)ol = u0; *(LAS u32x4*)(ol + 1024) = u1;
    SB();
    TS(3);
    fin(oB, lb, u0, u1, false);
    TS(4);
    st16wt<2048>(op, u0); TS(5); st16wt<3072>(op, u1);
    TS(6);
    if (EB == 0) go(fo, gof, gm, tag);
    SM(0, 2); SMD();
    *(LAS u32x4*)(ol + 2048) = u0; *(LAS u32x4*)(ol + 3072) = u1;
    TS(7);
    STAMP(10);
#pragma unroll
    for (int i = 0; i < 56; ++i) if (wj < 56) WRD1();
    TS(8);
    TS(9);
    TSW(10);
}

constexpr u64 kinv() { u64 r = 0; for (int s = 0; s < 16; ++s) r |= (u64)s << (4 * ((KORD >> (4 * s)) & 15)); return r; }
DEV constexpr int wo_unit(int t) { return t >= 7 && t < 10 ? t + 9 : t; }
DEV unsigned wo_voff(int lane, int cb, int i) {
    const int k = 8 * i + (lane >> 3), pc = lane & 7;
    return (k * 1024 + cb * 64 + ((((pc >> 1) ^ fswz(k)) << 1) + (pc & 1)) * 8) * 2 - i * 1024;
}
DEV void wo_compute(lchar* smem, u32x4 (&rr)[4], WReg& wr, u16* C, bool stats, const char* fx, unsigned gm, int nl, u64 tag, int g, int cb, int lane,
                    int wid STPARG) {
    OPAQUE(lane, wid, g, cb);
    const int wm = wid >> 1, wn = wid & 1, l15 = lane & 15, lg = lane >> 4;
    const lchar* wb0 = wo_wb(smem + WQ0, lane, wn * 2);
    const lchar* wb1 = wo_wb(smem + WQ0, lane, wn * 2 + 1);
    asm volatile("" : "+v"(wb0), "+v"(wb1));
    bf16x8 xf[2][8];
    s16x4 wh[16][8];
    auto rdw = [&](int t, int j) { wh[t][j] = ldtr4(((j & 2) ? wb1 : wb0) + wo_unit(t) * 8192 - WQ0 + (j >> 2) * 4096 + (j & 1) * 512); };
#pragma unroll
    for (int j = 0; j < NRW * 8; ++j) wh[j >> 3][j & 7] = wr[j];
    asm volatile("s_cmp_gt_u32 %0, 1\n\ts_waitcnt vmcnt(5)\n\ts_cbranch_scc0 1f\n\ts_waitcnt vmcnt(4)\n1:" ::"s"(cb) : "memory", "scc");
    STAMP(5);
    STAMPW(6 + wid);
    asm volatile("" : "+v"(lane), "+v"(rr[0]), "+v"(rr[1]), "+v"(rr[2]), "+v"(rr[3]));
    const int un = ((lg & 1) << 1) | (lg >> 1), row = g * 128 + wm * 64 + l15, ch = cb * 2 + wn;
    const unsigned ocm = (ch * SEQ + row) * 64 + ((un ^ fz(l15)) << 4), orm = row * 2048 + ch * 64 + un * 16;
    const char* cp = (const char*)C + (stats ? ocm : orm);
    const unsigned cs = stats ? 1024 : 32768;
    const lchar* xb = smem + (wm * 64 + l15) * 64 + ((lg ^ fz(l15)) << 4);
    lchar* xl = smem + wn * 8192 + (wn + 1) * nl + (wm * 64 + l15) * 64 + ((un ^ fz(l15)) << 4);
    unsigned gof = go_off(g, lane, wm, wn);
    gm *= !stats ? 0 : (nl && wm && g < 15) ? 0x10001 : 1;
    asm volatile("" : "+v"(cp), "+v"(xb), "+v"(xl), "+v"(gof));
    asm volatile("" : "+s"(gm));
    f32x4 acc[2][4];
#pragma unroll
    for (int ti = 0; ti < 4; ++ti) {
        const u32x2 x = sw16(rr[ti][0], rr[ti][2]), y = sw16(rr[ti][1], rr[ti][3]);
#pragma unroll
        for (int ni = 0; ni < 2; ++ni) acc[ni][ti] = (f32x4){lo16(x[ni]), hi16(x[ni]), lo16(y[ni]), hi16(y[ni])};
    }
    auto rdx = [&](int t, int j) { xf[t & 1][j] = ldk(xb + ((t + 4) % 5) * 16384 + (j >> 2) * 8192 + (j & 3) * 1024); };
    auto mm = [&](int t, int kk, int j) {
        const bool alt = MW && !(t == 15 && kk == 1);
        const int ni = !alt ? (MW ? ((j >> 1) ^ j) & 1 : j & 1) : MW == 1 ? j >> 2 : ((j >> 1) ^ j) & 1;
        const int ti = (!alt || MW == 2) ? j >> 1 : ni ? 7 - j : j;
        mfma16t(acc[ni][ti], cat8(wh[t][kk * 4 + ni * 2], wh[t][kk * 4 + ni * 2 + 1]), xf[t & 1][kk * 4 + ti]);
    };
    SB();
    LGKM0(); SB();
#pragma unroll
    for (int t = 0; t < 16; ++t) {
        BAR(); SB();
        if (t == 1) SM(0, 4);
        if (EXP && t == 16 - EXP) { go(fx, gof, gm, tag); SB(); }
        if (t == 0) {
            STAMP(4);
#pragma unroll
            for (int j = 0; j < 8; ++j) rdx(0, j);
            SB();
        } else {
#pragma unroll
            for (int j = 0; j < 8; ++j) { rdx(t, j); SB(); mm(t - 1, 1, j); SB(); }
        }
        const bool more = t + 1 >= NRW && t < 15;
        if (WS) {
            __builtin_amdgcn_s_waitcnt(0xc47f); SB();
#pragma unroll
            for (int j = 0; j < 8; ++j) {
                if (more && (WS == 1 || j)) { rdw(t + 1, WS == 1 ? j : j - 1); SB(); }
                if (more && WS == 2 && j == 7) { rdw(t + 1, 7); SB(); }
                mm(t, 0, j); SB();
            }
        } else
#pragma unroll
        for (int j = 0; j < 8; ++j) { mm(t, 0, j); SB(); if (more) { rdw(t + 1, j); SB(); } }
        if (more) __builtin_amdgcn_s_waitcnt(0xc87f); else LGKM0();
        SB();
    }
    u32x4 (&ov)[4] = rr;
    auto out = [&](int ti) {
        asm volatile("" : "+v"(acc[0][ti]), "+v"(acc[1][ti]));
        ov[ti] = unit16(pk4(acc[0][ti]), pk4(acc[1][ti]));
        asm volatile("global_store_dwordx4 %0, %1, off sc1\n\ts_nop 1" ::"v"(cp + ti * cs), "v"(ov[ti]) : "memory");
        SB();
    };
#pragma unroll
    for (int j = 0; j < 6; ++j) mm(15, 1, j);
    SB();
    out(0);
    mm(15, 1, 6); mm(15, 1, 7); SB();
    out(1); out(2); out(3);
    STAMP(2);
    if (stats) {
        if (!EXP) go(fx, gof, gm, tag);
        SM(0, 2); SMD(); SB();
#pragma unroll
        for (int ti = 0; ti < 4; ++ti) *(LAS u32x4*)(xl + ti * 1024) = ov[ti];
        SB(); BARL(); SB();
        STAMP(10);
        STAMP(11);
        STAMPW(12 + wid);
    } else {
        STAMP(10);
    }
}

DEV unsigned ln0_stats(const u32x4 (&x)[4]) {
    const bf16x2_t one2 = __builtin_bit_cast(bf16x2_t, 0x3f803f80u);
    float s[2] = {0.f, 0.f}, q[2] = {0.f, 0.f};
#pragma unroll
    for (int i = 0; i < 16; ++i) {
        const unsigned u = x[i >> 2][i & 3];
        const bf16x2_t v = __builtin_bit_cast(bf16x2_t, u);
        s[i & 1] = __builtin_amdgcn_fdot2_f32_bf16(v, one2, s[i & 1], false);
        q[i & 1] = __builtin_amdgcn_fdot2_f32_bf16(v, v, q[i & 1], false);
    }
    const f32x2 a = sw32f(s[0] + s[1], q[0] + q[1]);
    float v = dppadd<0x128>(dppadd<0x4E>(dppadd<0xB1>(a[0] + a[1])));
    const f32x2 b = sw16f(v, v);
    v = (b[0] + b[1]) * (1.0f / DM);
    const f32x2 c = sw32f(v, v);
    return cvtpk(c[0], __builtin_amdgcn_sqrtf(bfr(bfr(fmx(c[1] - c[0] * c[0], 0.f)) + 1e-5f)));
}
typedef f32x2 Raw[4];
DEV void unpk(Raw& v, u32x4 x) {
#pragma unroll
    for (int i = 0; i < 4; ++i) { v[i] = (f32x2){lo16(x[i]), hi16(x[i])}; asm volatile("" : "+v"(v[i])); }
}
DEV u32x4 ln0_apply(const Raw& v, f32x2 st) {
    u32x4 x;
#pragma unroll
    for (int i = 0; i < 4; ++i) {
        const f32x2 e = v[i] - st[0];
        f32x2 d;
        asm("v_cvt_pk_bf16_f32 %0, 0, %1" : "=v"(d[0]) : "v"(e[0]));
        asm("v_cvt_pk_bf16_f32 %0, 0, %1" : "=v"(d[1]) : "v"(e[1]));
        d *= st[1];
        x[i] = cvtpk(d[0], d[1]);
    }
    return x;
}

struct HW { unsigned wq[2][3], wo[4], vo16, lds, po, px, er, ec; int lw, pos; u64 kord; LAS float* st; const u64* go; };
typedef u32x4 Cols[24];
template <int N> DEV void wq_issue(const HW& h, const u16* W, int t, int at, int r0) {
    const char* src = (const char*)W + (unsigned)(at * 8 + r0) * (8 * 6144);
    const unsigned l = h.lds + WQ0 + ((t + 2) % 3) * WQS + r0 * 3072;
#pragma unroll
    for (int i = 0; i < N; ++i) { glds3(src, l + i * 3072, h.wq[i & 1][0], h.wq[i & 1][1], h.wq[i & 1][2]); src += 8 * 6144; }
}
DEV void wo_issue(const HW& h, const u16* W, int n, int lim, int a, int b) {
#pragma unroll 1
    for (int j = h.lw; j < n; j += 4) {
        const int i = j + (j < lim ? a : b);
        glds4((const char*)W + (((unsigned)(h.kord >> (2 * (i & ~1))) & 15) * 2 + (i & 1)) * (32 * 2048), h.lds + wo_unit(i >> 1) * 8192 + (i & 1) * 4096, h.wo[0], h.wo[1], h.wo[2], h.wo[3]);
    }
}
DEV void fill(const void* sb, unsigned vo) {
    asm volatile("s_nop 4\n\tglobal_store_dwordx4 %0, %1, %2 sc1\n\tglobal_store_dwordx4 %0, %1, %2 offset:1024 sc1\n\t"
                 "global_store_dwordx4 %0, %1, %2 offset:2048 sc1\n\tglobal_store_dwordx4 %0, %1, %2 offset:3072 sc1\n\ts_nop 1"
                 ::"v"(vo), "v"((u32x4){~0u, ~0u, ~0u, ~0u}), "s"(sb) : "memory");
}
DEV void filled(const HW& h, const u64* fe, u64 tag) {
    asm volatile("s_mov_b64 exec, 3\n\ts_nop 3\n\tglobal_atomic_swap_x2 %0, %1, %2 sc1\n\ts_mov_b64 exec, -1\n\ts_nop 0" ::"v"(h.er), "v"(tag), "s"(fe) : "memory");
}
DEV u64 eload(const HW& h, const u64* fe) {
    u64 e;
    asm volatile("s_nop 4\n\tglobal_load_dwordx2 %0, %1, %2 sc1" : "=&v"(e) : "v"(h.ec), "s"(fe) : "memory");
    return e;
}
template <int N> DEV void echeck(const HW& h, const u64* fe, u64 e, u64 tag) {
    unsigned bud = POLLMAX;
    asm volatile("s_waitcnt vmcnt(%6)\n1:\n\tv_cmp_ne_u64 vcc, %5, %0\n\ts_sub_u32 %1, %1, 1\n\ts_cbranch_vccz 2f\n\ts_cbranch_scc1 2f\n\ts_sleep 1\n\t"
                 "global_atomic_add_x2 %0, %2, %3, %4 sc0 sc1\n\ts_waitcnt vmcnt(0)\n\ts_branch 1b\n2:"
                 : "+v"(e), "+s"(bud) : "v"(h.ec), "v"(0ull), "s"(fe), "s"(tag), "n"(N) : "memory", "scc", "vcc");
}
#define GL "global_load_lds_dwordx4 "
#define GL4(a, b, s) GL a ", " s " sc1\n\t" GL b ", " s " offset:1024 sc1\n\t" GL b ", " s " offset:2048 sc1\n\t" GL b ", " s " offset:3072 sc1\n\t"
#define XRD(c) "s_setprio 2\n\tds_read2st64_b32 v[240:241], " c " offset1:4\n\tds_read2st64_b32 v[242:243], " c " offset0:8 offset1:12\n\t" \
    "ds_read2st64_b32 v[244:245], " c " offset0:16 offset1:20\n\tds_read2st64_b32 v[246:247], " c " offset0:24 offset1:28\n\t" \
    ".if %[L]\n\tds_read_b32 v248, " c " offset:8192\n\t.endif\n\t"
#define XCMP "s_waitcnt lgkmcnt(0)\n\tv_max3_u32 v240, v240, v241, v242\n\tv_max3_u32 v243, v243, v244, v245\n\tv_max3_u32 v240, v240, v246, v247\n\t" \
    ".if %[L]\n\tv_max3_u32 v240, v240, v243, v248\n\t.else\n\tv_max_u32 v240, v240, v243\n\t.endif\n\tv_cmp_eq_u32 %[bad], -1, v240\n\ts_setprio 0"
#define XCLOB "memory", "v240", "v241", "v242", "v243", "v244", "v245", "v246", "v247", "v248"
template <bool L> DEV bool stale(unsigned c) {
    u64 bad;
    asm volatile(XRD("%[c]") XCMP : [bad] "=s"(bad) : [c] "v"(c), [L] "n"((int)L) : XCLOB);
    return bad != 0;
}
template <bool L, bool CK> DEV bool xstep(const HW& h, const void* sw, unsigned lw, const void* sx, unsigned lx, unsigned vo0, unsigned c, int wt) {
    u64 bad;
    asm volatile("s_mov_b32 m0, %[lw]\n\t.if %[CK]\n\ts_nop 2\n\t.else\n\ts_cmp_eq_u32 %[wt], 0\n\ts_cbranch_scc1 5f\n\ts_waitcnt vmcnt(11+%[L])\n5:\n\ts_nop 0\n\t.endif\n\ts_barrier\n\t" GL "%[w0], %[sw] sc1\n\t" GL "%[w1], %[sw] offset:1024 sc1\n\t" GL "%[w2], %[sw] offset:2048 sc1\n\t"
                 "s_mov_b32 m0, %[lx]\n\ts_nop 0\n\t" GL4("%[v0]", "%[va]", "%[sx]")
                 ".if %[CK]\n\ts_waitcnt vmcnt(7)\n\t" XRD("%[c]") ".endif\n\t"
                 "s_add_u32 m0, %[lx], 4096\n\ts_nop 0\n\t" GL4("%[vb]", "%[vb]", "%[sx]")
                 ".if %[L]\n\ts_add_u32 m0, %[lx], 8192\n\ts_nop 0\n\t" GL "%[vc], %[sx] sc1\n\t.endif\n\t"
                 ".if %[CK]\n\t" XCMP "\n\t.endif"
                 : [bad] "=s"(bad)
                 : [lw] "s"(lw), [sw] "s"(sw), [w0] "v"(h.wq[0][0]), [w1] "v"(h.wq[0][1]), [w2] "v"(h.wq[0][2]), [lx] "s"(lx), [sx] "s"(sx), [v0] "v"(vo0),
                   [va] "v"(h.vo16), [vb] "v"(h.vo16 + 4096), [vc] "v"(h.vo16 + 8192), [c] "v"(c), [L] "n"((int)L), [CK] "n"((int)CK), [wt] "s"(wt) : XCLOB, "scc");
    return CK && bad != 0;
}
DEV void warm(const HW& h, const u16* Wo, int lane) {
    asm volatile("s_mov_b32 m0, %2\n\ts_nop 4\n\tglobal_load_lds_dword %0, %1\n\tglobal_load_lds_dword %3, %1" ::"v"(lane * 64),
                 "s"(Wo + (size_t)blockIdx.x * 4096), "s"(h.lds + DUMP), "v"(lane * 64 + 4096) : "memory");
}
template <int ST> DEV void ld8(f32x4 (&d)[8], const char* sb, unsigned vo) {
    asm volatile("s_nop 4\n\tglobal_load_dwordx4 %0, %8, %9 offset:%10 sc1\n\tglobal_load_dwordx4 %1, %8, %9 offset:%11 sc1\n\t"
                 "global_load_dwordx4 %2, %8, %9 offset:%12 sc1\n\tglobal_load_dwordx4 %3, %8, %9 offset:%13 sc1\n\t"
                 "global_load_dwordx4 %4, %8, %9 sc1\n\tglobal_load_dwordx4 %5, %8, %9 offset:%14 sc1\n\t"
                 "global_load_dwordx4 %6, %8, %9 offset:%15 sc1\n\tglobal_load_dwordx4 %7, %8, %9 offset:%16 sc1"
                 : "=&v"(d[0]), "=&v"(d[1]), "=&v"(d[2]), "=&v"(d[3]), "=&v"(d[4]), "=&v"(d[5]), "=&v"(d[6]), "=&v"(d[7])
                 : "v"(vo), "s"(sb + 4 * ST), "n"(-4 * ST), "n"(-3 * ST), "n"(-2 * ST), "n"(-ST), "n"(ST), "n"(2 * ST), "n"(3 * ST) : "memory");
}

template <class F> DEV void colsum(Cols& d, const void* out, int lane, F next) {
    const char* dst = (const char*)out + (lane & 7) * 32 + (lane >> 4) * 16;
    bf16x8 sel;
#pragma unroll
    for (int e = 0; e < 8; ++e) sel[e] = (lane & 7) == e ? (short)0x3f80 : (short)0;
#pragma unroll
    for (int i = 0; i < 24; ++i) asm volatile("" : "+v"(d[i]));
    f32x4 c;
#pragma unroll
    for (int k = 0; k < 6; ++k) {
        if (!(k & 1)) c = (f32x4){0.f, 0.f, 0.f, 0.f};
#pragma unroll
        for (int i = 0; i < 4; ++i) c = MFMA16(sel, __builtin_bit_cast(bf16x8, d[k * 4 + i]), c);
        if (k & 1) {
#pragma unroll
            for (int e = 0; e < 4; ++e) c[e] = dppadd<0x128>(c[e]);
            if (lane < 32 && !(lane & 8)) st16wt<0>(dst + (k >> 1) * 256, __builtin_bit_cast(u32x4, c));
        }
        SB();
        next(k);
    }
}

template <bool LOCAL>
DEV void qkv_helper(lchar* smem, HW& h, Cols& cw, char* ws, int layer, const u16* X, const u16* W, const u16* W2, const u16* Wo, const char* stats, float* csum,
                    u64* fl, u64 tag, int g, int hp, int lane, int lw STPARG) {
    OPAQUE(lane, lw, g, hp);
    constexpr int NX = LOCAL ? 9 : 8, HO = LOCAL ? 16 : 0;
    const int m0 = g * 128;
    const bool halo = LOCAL && g > 0;
    const int ps = (LOCAL && layer == 0) ? 0 : h.pos;
    auto at = [&](int t) { return t - 1 + (t > ps); };
    auto first = [&](int r0) {
#pragma unroll 1
        for (int t = 0; t < 3; ++t) wq_issue<4>(h, W, t, t, r0);
    };
    auto colld = [&](Cols& d, const u16* M) {
        const unsigned vo = ((g * 64 + (lane >> 3)) * 3072 + (hp ^ CJ) * 64 + (lane & 7) * 8) * 2 + 2048;
#pragma unroll
        for (int i = 0; i < 8; ++i)
            asm volatile("s_nop 4\n\tglobal_load_dwordx4 %0, %3, %4 offset:-2048\n\tglobal_load_dwordx4 %1, %3, %4\n\t"
                         "global_load_dwordx4 %2, %3, %4 offset:2048" : "=&v"(d[i]), "=&v"(d[8 + i]), "=&v"(d[16 + i])
                         : "v"(vo), "s"((const char*)M + i * (8 * 6144)) : "memory");
    };
    f32x4 sp[4];
    float hs, hq;
    auto any4 = [](f32x4 (&d)[4]) { asm volatile("" : "=v"(d[0]), "=v"(d[1]), "=v"(d[2]), "=v"(d[3])); };
    const unsigned so = (layer - 1) * (unsigned)(MB / 2) + g * 16384 + (lane >> 4) * 4096 + (lane & 15) * 16;
    auto ld4 = [&](f32x4 (&d)[4], unsigned vo, int a, int b) {
        asm volatile("s_cmp_lg_u32 %6, %7\n\ts_cbranch_scc1 1f\n\tglobal_load_dwordx4 %0, %4, %5 sc1\n\tglobal_load_dwordx4 %1, %4, %5 offset:1024 sc1\n\t"
                     "global_load_dwordx4 %2, %4, %5 offset:2048 sc1\n\tglobal_load_dwordx4 %3, %4, %5 offset:3072 sc1\n1:"
                     : "+v"(d[0]), "+v"(d[1]), "+v"(d[2]), "+v"(d[3]) : "v"(vo), "s"(stats), "s"(a), "s"(b) : "memory", "scc");
    };
    auto add1 = [](f32x4& a, const f32x4& d, int e0) {
#pragma unroll
        for (int e = e0; e < 4; ++e) asm volatile("v_add_f32 %0, %0, %1" : "+v"(a[e]) : "v"(d[e]));
    };
    auto cols = [&](int src, int dst, auto next) {
        f32x4 sc[2][8];
        const unsigned vc = hp * (16 * 768) + (lane < 48 ? lane * 16 : 0);
        ld8<768>(sc[0], (const char*)csum + src, vc);
        next(0);
        ld8<768>(sc[1], (const char*)csum + src + 6144, vc);
        next(1);
        WAITV(8);
#pragma unroll
        for (int i = 1; i < 8; ++i) add1(sc[0][0], sc[0][i], 0);
        next(2);
        WAITV(0);
#pragma unroll
        for (int i = 0; i < 8; ++i) add1(sc[0][0], sc[1][i], 0);
        if (lane < 48) *(LAS f32x4*)(smem + dst + lane * 16) = sc[0][0];
        SB();
    };
    auto sum4 = [&]() {
#pragma unroll
        for (int i = 1; i < 4; ++i) add1(sp[0], sp[i], 0);
    };
    if (lw < 2) {
        unsigned vo0 = (LOCAL && g == 0) ? h.vo16 + 1024 : h.vo16;
        const char* xsrc = (const char*)X + ((size_t)lw * SEQ + m0 - HO) * 64;
        unsigned ldx = h.lds + lw * (NX * 1024), xv = ldx + lane * 16 + (lane >> 4) * 4;
        auto fetch = [&](const char* p, int so) {
            glds4(p, ldx + so, vo0, h.vo16, h.vo16, h.vo16);
            glds4(p + 4096, ldx + so + 4096, h.vo16, h.vo16, h.vo16, h.vo16);
            if (LOCAL) glds1(p + 8192, ldx + so + 8192, h.vo16);
        };
        int bud = POLLMAX;
        auto redo = [&](int t, int so) {
#pragma unroll 1
            do { --bud; fetch(xsrc + (unsigned)at(t) * (2 * SEQ * 64), so); WAITV(0); } while (stale<LOCAL>(xv + so) && bud > 0);
        };
        STAMP(0);
        u64 v, v2;
        asm volatile("" : "=v"(v), "=v"(v2));
        const char* p1 = xsrc + (unsigned)at(1) * (2 * SEQ * 64);
        const char* p2 = xsrc + (unsigned)at(2) * (2 * SEQ * 64);
        asm volatile("" : "+s"(p1), "+s"(p2), "+s"(ldx));
        asm volatile("" : "+v"(xv), "+v"(vo0));
        if (LOCAL && layer == 0) {
            const u64* f0 = fl + fi(FL_L + g * 16 - (halo ? 2 : 0)) + (lane & 31);
            asm volatile("" : "+v"(f0));
            BAR();
            if (lw == 0) poll(f0, lane < (halo ? 18 : 16), tag, ~0u, [](u64 m) { return (unsigned)m; });
            BAR();
            fetch(xsrc, 0);
        } else poll2<7>(h.go + (layer * 2 - 1) * 16 + lw * 8 + (lane & 3), halo ? 7 : 3, tag, v, v2);
        STAMP(1);
        if (lw == 1) STAMPW(11);
        fetch(p1, XS);
        SM(4, 1);
        fetch(p2, 2 * XS);
        if (LOCAL && layer == 0) { BAR(); WAITB(2 * NX); }
        WAITV(NX);
        asm volatile("" ::"v"(v), "v"(v2));
        SM(5, 13);
        if (__builtin_expect(stale<LOCAL>(xv + XS), 0)) redo(1, XS);
        const char* wsrc = (const char*)W + (unsigned)lw * (8 * 6144);
        const unsigned ldw = h.lds + WQ0 + lw * 3072;
        int xo = 0, sv = 2 * XS, sw = 2 * WQS, t = 3;
        auto next = [&]() {
            sv = xo;
            xo = xo == 2 * XS ? 0 : xo + XS;
            sw = sw == 2 * WQS ? 0 : sw + WQS;
        };
#pragma unroll 1
        for (; t <= (K0 && LOCAL && layer == 0 ? 0 : NCK); ++t) {
            const unsigned a = at(t);
            if (__builtin_expect((xstep<LOCAL, true>(h, wsrc + a * (64 * 6144), ldw + sw, xsrc + a * (2 * SEQ * 64), ldx + xo, vo0, xv + sv, 0)), 0)) redo(t - 1, sv);
            next();
        }
        any4(sp);
        ld4(sp, so + lw * 256, t, NCK + 1);
        const int t0 = t;
#pragma unroll 1
        for (; t < 16; ++t) {
            const unsigned a = at(t);
            xstep<LOCAL, false>(h, wsrc + a * (64 * 6144), ldw + sw, xsrc + a * (2 * SEQ * 64), ldx + xo, vo0, 0, t - t0);
            next();
        }
        if (LOCAL) glds1(xsrc + (unsigned)ps * (2 * SEQ * 64), h.lds + HXO + lw * 1024, vo0);
        WAITB(NX + 3 + LOCAL);
        sum4();
        asm volatile("" : "=v"(hs), "=v"(hq));
    } else if (lw == 2) {
        if (LOCAL && layer == 0) {
            if (BW & 1) first(0);
            BAR();
            if (!(BW & 1)) first(0);
            STAMPW(6);
            BAR(); BAR();
            WAITB(0);
        }
        { HT0(); HT1(0); }
        WAITB(14);
        SM(6, 4);
        any4(sp);
#pragma unroll 1
        for (int t = 3; t < 16; ++t) {
            HT0();
            wq_issue<6>(h, W, t, at(t), 2);
            HT1(t - 2);
            WAITV(18);
            ld4(sp, so + 512, t, layer ? NCK + L2 : 0);
            BAR();
        }
        sum4();
        asm volatile("" : "=v"(hs), "=v"(hq));
    } else {
        if (LOCAL && layer == 0) {
            if (BW & 1) first(4);
            BAR();
            if (!(BW & 1)) first(4);
            if (!(BW & 2)) { BAR(); BAR(); }
            Cols c0;
            colld(c0, W);
            warm(h, Wo, lane);
            if (BW & 2) { BAR(); BAR(); }
            WAITB(26);
            auto fill0 = [&](int k) { fill(ws + (k < 4 ? WS_O : WS_X + 4 * MB) + ((k >> 1) & 1) * (SEQ * 64) + (k & 1) * 4096, k < 4 ? h.po : h.px); };
            auto job = [&](int i) {
#pragma unroll
                for (int k = 0; k < 8; ++k) if (FE + k / FN == i) fill0(k);
                constexpr int L = FE + 7 / FN, R = L + FD;
                if (i == R) { WAITV((L < 6 && R >= 6) + (L < 8 && R >= 8) + (L < 10 && R >= 10)); filled(h, fl + fi(FL_E), tag); }
                BAR();
            };
            job(1); job(2); job(3); job(4);
            WAITV(FE > 4 ? 0 : (5 - FE) * FN >= 8 ? 32 : (5 - FE) * FN * 4);
            colsum(c0, (const char*)csum + ((hp ^ CJ) * 16 + g) * 768, lane, [&](int k) { job(5 + k); });
            job(11); job(12); job(13); job(14);
            asm volatile("" : "=v"(hs), "=v"(hq), "=v"(sp[0]));
        } else {
            f32x4 sh[4];
            u64* fs = fl + fi(FL_S + (layer - 1) * 256 + g * 16);
            u64 f0;
            if (!LOCAL && layer == 1) {
                const u64* fc = fl + fi(FL_C + hp * 16 + (lane & 15));
                asm volatile("global_load_dwordx2 %0, %1, off sc1" : "=&v"(f0) : "v"(fc) : "memory");
                WAITB(15);
                WAITV(0);
                asm volatile("" : "+v"(f0));
                if (!__all(f0 == tag)) poll(fc, lane < 16, tag, 0xffffu, [](u64 m) { return (unsigned)m; });
                cols(256 * 768, SCB, [&](int k) { BAR(); if (k == 0) raise(fs + h.pos, tag); });
            } else {
                WAITB(14); WAITB(0);
                raise(fs + h.pos, tag);
                BAR(); BAR();
            }
#pragma unroll
            for (int i = 5; i < SF; ++i) BAR();
            fs += (halo && lane >= 16) ? (lane & 15) - 16 : lane & 15;
            asm volatile("global_load_dwordx2 %0, %1, off sc1" : "=&v"(f0) : "v"(fs) : "memory");
            BAR();
            WAITV(0);
            asm volatile("" : "+v"(f0));
            if (!__all(f0 == tag)) poll(fs, lane < 32, tag, ~0u, [](u64 m) { return (unsigned)m; });
            any4(sp);
            ld4(sp, so + 768, 0, 0);
            if (LOCAL) {
                any4(sh);
                ld4(sh, so + 768 - (halo ? 16384 : 0), 0, 0);
            }
            BAR(); BAR();
            WAITV(0);
            if (LOCAL) {
#pragma unroll
                for (int i = 1; i < 4; ++i) add1(sh[0], sh[i], 2);
                const f32x2 x = sw16f(sh[0][2], sh[0][2]), y = sw16f(sh[0][3], sh[0][3]);
                hs = x[0] + x[1]; hq = y[0] + y[1];
                asm volatile("" : "+v"(hs), "+v"(hq));
            } else asm volatile("" : "=v"(hs), "=v"(hq));
#pragma unroll
            for (int i = SF + 3; i < 15; ++i) BAR();
            sum4();
        }
    }
    { HT0(); HT1(14); }
    if (LOCAL) wo_issue(h, Wo, 8, 2, 6, 18);
    if (!LOCAL || layer) {
        const f32x2 x = sw16f(sp[0][0], sp[0][2]), y = sw16f(sp[0][1], sp[0][3]);
        const f32x2 z = sw32f(x[0] + x[1], hs), u = sw32f(y[0] + y[1], hq);
        const float mean = (z[0] + z[1]) * (1.0f / DM);
        const float r = rsd(fmx((u[0] + u[1]) * (1.0f / DM) - mean * mean, 0.f));
        if (lane < 32 || lw == 3) { h.st[0] = -bfr(mean); h.st[(SRS - SNM) / 4] = r; }
    }
    WAITV(LOCAL ? 8 : 0);
    BARL();
    { HT0(); HT1(15); }
    if (LOCAL) wo_issue(h, Wo, 12, 6, 8, 20);
    if (!LOCAL && layer == 1 && lw == 3) cols(0, SCOL, [](int k) { if (k == 1) BAR(); });
    else BAR();
    if (LOCAL) {
        wo_issue(h, Wo, 12, 6, 0, 8);
        if (layer == 0 && lw == 3) {
            const u64 e = eload(h, fl + fi(FL_E));
            colld(cw, W2);
            WAITB(29);
            echeck<24>(h, fl + fi(FL_E), e, tag);
        } else WAITB(4);
    } else BAR();
}

DEV u64 probe(const u64* f) {
    u64 v;
    asm volatile("s_mov_b64 exec, 1\n\t" FLD("%0", "%1", "%2") "\n\ts_mov_b64 exec, -1" : "=&v"(v) : "v"(f), "v"(0ull) : "memory");
    return v;
}
DEV void attn_helper(const HW& h, char* ws, int nf, const u16* kv, const u16* Wo, const u64* fe, const u64* fkv, u64 tag, int g, int hp, int lane, int lw STPARG) {
    __builtin_amdgcn_s_setprio(2);
    OPAQUE(lane, lw, g, hp);
    const int rh = lw & 1, hd = lw >> 1;
    const char* src = (const char*)kv + (unsigned)((hp * 2 + hd) * SEQ * 64 + rh * 4096);
    const unsigned ld = h.lds + AR0 + hd * 16384 + rh * 8192;
    const u64* f = fkv + fi(lw * 16 + (lane & 15));
    unsigned rdy = 0u;
    int reg = AR1;
    auto issue = [&](int c) {
        const int blk = g ^ c;
        if (!((rdy >> blk) & 1)) rdy = poll(f, lane < 16, tag, 1u << blk, [](u64 m) { return (unsigned)m; });
        glds4(src + blk * 8192, ld + reg, h.vo16, h.vo16, h.vo16, h.vo16);
        glds4(src + blk * 8192 + NH * SEQ * 64, ld + reg + 4096, h.vo16, h.vo16, h.vo16, h.vo16);
        reg = reg == AR1 ? AR2 : reg == AR2 ? 0 : AR1;
    };
    issue(1);
    STAMP(7);
#pragma unroll 1
    for (int c = 0; c < 14; ++c) {
        WAITB(0);
        issue(c + 2);
        if (c == 1 && lw == 3) warm(h, Wo, lane);
        if ((c >> 2) == (FA == 4 ? 2 : 1)) wo_issue(h, Wo, min(4, 14 - 4 * (c & 3)), 4, 4 * (c & 3), 0);
        if (c >= FA && c < FA + nf)
            fill(ws + (c == FA ? WS_O + 4 * MB : c == FA + 1 ? WS_X + 8 * MB : c == FA + 2 ? WS_O : WS_X + 12 * MB) + hd * (SEQ * 64) + rh * 4096, (c & 1) ? h.px : h.po);
        if (c == FA + 4 && lw == 3) filled(h, fe, tag);
    }
    WAITB(0);
    wo_issue(h, Wo, 8, 6, 14, 24);
    if (lw == 3) echeck<0>(h, fe, eload(h, fe), tag);
    BAR();
}

#define TM0
#define TM1(t)
#define CNT(n)
DEV void wo_helper(lchar* smem, HW& h, Cols& cw, const char* csum, const char* stats, int nl, const u16* O, const u16* Wo, int nw, const u16* Wn, u64* fc, const u64* fo, u64 tag, int g, int lane, int lw STPARG) {
    int dummy = 0;
    OPAQUE(lane, lw, g, dummy);
    const int p = lw & 1, rh = lw >> 1;
    const char* xsrc = (const char*)O + ((size_t)p * SEQ + g * 128 + rh * 64) * 64;
    const unsigned ldx = h.lds + p * 8192 + rh * 4096, c0 = ldx + lane * 16 + (lane >> 4) * 4;
    const u64* f = fo + p * 8 + rh;
    STAMP(0);
    bool rdy;
    u64 v, v2;
    {
        v = probe(f);
        wo_issue(h, Wo, nw, 10, 20, 0);
        __builtin_amdgcn_s_setprio(0);
        asm volatile("s_barrier\n\ts_cmp_lg_u32 %1, 0\n\ts_waitcnt vmcnt(8)\n\ts_cbranch_scc1 1f\n\ts_waitcnt vmcnt(0)\n1:" : "+v"(v) : "s"(nw) : "memory", "scc");
        rdy = __ballot(v == tag) & 1;
        if (XP && !rdy && nw == 0) {
            v = probe(f);
            asm volatile("s_waitcnt vmcnt(0)" : "+v"(v) :: "memory");
            rdy = __ballot(v == tag) & 1;
        }
        if (!rdy) {
            if (WE == 1) {
                v = probe(f);
                asm volatile("s_barrier\n\ts_waitcnt vmcnt(0)" : "+v"(v) :: "memory");
            }
            if (!P2 && !(__ballot(v == tag) & 1)) POLL(f, lane == 0, 1);
        }
        if (P2) poll2<0>(f, 1, tag, v, v2);
    }
#pragma unroll 1
    for (int t = 0; t < 3; ++t) {
        glds4(xsrc + (unsigned)(t + (t >= h.pos)) * (2 * SEQ * 64), ldx + t * 16384, h.vo16, h.vo16, h.vo16, h.vo16);
        if (t == 0) { STAMP(1); SM(4, 1); SM(5, 5); SM(6, 6); SM(7, 7); }
        if (WE == 2 && t == (rdy ? 2 : 0)) BAR();
    }
    if (WE == 1 ? rdy : WE == 0) BAR();
    __builtin_amdgcn_s_setprio(HPW);
    if (fc) { WAITV(12); colsum(cw, csum, lane, [](int) {}); }
    unsigned vo = h.vo16 + (3 + (3 >= h.pos)) * (2 * SEQ * 64), sa, sb, sm;
    int bud = POLLMAX;
    u64 ret;
    unsigned tv = 0, cn = 0, np = 0;
    const char* wn = (const char*)Wn + lw * (8 * 6144);
    const unsigned wa = h.pos * 0x60000, wd = (h.pos == 0) * 0x60000 - wa - 0x30000;
    unsigned w0 = h.wq[lw == 3][0] + wa, w1 = h.wq[lw == 3][1] + wa, w2 = h.wq[lw == 3][2] + wa;
    asm volatile(
        ".macro WRD t\n.if (\\t %% 5) == 0\n"
        "ds_read2st64_b32 v[240:241], %[c4] offset1:4\nds_read2st64_b32 v[242:243], %[c4] offset0:8 offset1:12\n.else\n"
        "ds_read2st64_b32 v[240:241], %[c0] offset0:(\\t %% 5)*64-64 offset1:(\\t %% 5)*64-60\n"
        "ds_read2st64_b32 v[242:243], %[c0] offset0:(\\t %% 5)*64-56 offset1:(\\t %% 5)*64-52\n.endif\n.endm\n"
        ".macro WCMP\ns_waitcnt lgkmcnt(0)\nv_max3_u32 v240, v240, v241, v242\nv_max_u32 v240, v240, v243\nv_cmp_eq_u32 vcc, -1, v240\n.endm\n"
        ".macro WTS t, x\nWCMP\n" TM1("(\\t+63)&63") "s_cbranch_vccnz .Lwr%=_\\x\\t\n.Lwb%=_\\x\\t:\n.endm\n"
        ".macro WST t\n" GL "%[vo], %[src] sc1\n" GL "%[vo], %[src] offset:1024 sc1\n"
        "s_waitcnt vmcnt(10)\nWRD \\t\n"
        GL "%[vo], %[src] offset:2048 sc1\n" GL "%[vo], %[src] offset:3072 sc1\n"
        "s_add_u32 m0, %[ldx], ((\\t+3) %% 5)*16384\ns_cmp_eq_u32 %[pos], \\t+3\ns_cselect_b32 %[sa], 2, 1\nv_lshl_add_u32 %[vo], %[sa], 18, %[vo]\n"
        "WTS \\t, a\n"
        ".endm\n"
        ".macro WTL t, n, x\ns_waitcnt vmcnt(\\n)\nWRD \\t\nWTS \\t, \\x\ns_barrier\n" TM0 ".endm\n"
        ".macro WWN o, d\ns_add_u32 m0, %[wl], \\o\ns_nop 0\n" GL "%[w0], %[wn] sc1\n" GL "%[w1], %[wn] offset:1024 sc1\n" GL "%[w2], %[wn] offset:2048 sc1\n"
        "v_add_u32 %[w0], \\d, %[w0]\nv_add_u32 %[w1], \\d, %[w1]\nv_add_u32 %[w2], \\d, %[w2]\n.endm\n"
        ".macro WCR t, x\n.Lwr%=_\\x\\t:\ns_mov_b32 %[sa], ((\\t+4) %% 5)*16384\ns_mov_b32 %[sb], \\t-1\ns_call_b64 %[ret], .Lwretry%=\ns_branch .Lwb%=_\\x\\t\n.endm\n"
        "s_add_u32 m0, %[ldx], 49152\ns_nop 0\n" TM0
        ".irp t,1,2,3,4,5,6,7,8,9,10,11\nWST \\t\n.if \\t == 4\ns_cmp_eq_u64 %[fc], 0\ns_cbranch_scc1 3f\ns_mov_b64 exec, 1\n"
        "global_atomic_swap_x2 %[v0], %[tg], %[fc] sc1\ns_mov_b64 exec, -1\n3:\n.endif\ns_barrier\n" TM0 ".endr\n"
        "WST 12\ns_cmp_eq_u64 %[Wn], 0\ns_barrier\n" TM0 "s_cbranch_scc1 .Lwlast%=\n"
        "WWN 2*24576, 0x30000\nWTL 13, 11, a\nWWN 2*24576+12288, %[wd]\nWTL 14, 10, a\nWWN 0, 0x30000\nWTL 15, 9, a\nWWN 12288, 0\n"
        "s_branch .Lwend%=\n"
        ".irp t,1,2,3,4,5,6,7,8,9,10,11,12,13,14,15\nWCR \\t, a\n.endr\n"
        ".irp t,13,14,15\nWCR \\t, b\n.endr\n"
        ".Lwretry%=:\ns_cmp_ge_u32 %[sb], %[pos]\ns_addc_u32 %[sb], %[sb], 0\ns_mov_b32 %[sm], m0\ns_add_u32 m0, %[ldx], %[sa]\n"
        "v_lshl_add_u32 v244, %[sb], 18, %[v0]\nv_add_u32 v245, %[sa], %[c0]\n"
        "1:\n" GL "v244, %[src] sc1\n" GL "v244, %[src] offset:1024 sc1\n" GL "v244, %[src] offset:2048 sc1\n" GL "v244, %[src] offset:3072 sc1\n"
        "s_waitcnt vmcnt(0)\nds_read2st64_b32 v[240:241], v245 offset1:4\nds_read2st64_b32 v[242:243], v245 offset0:8 offset1:12\n"
        "WCMP\n" CNT("0x10000") "s_sub_i32 %[bud], %[bud], 1\ns_cmp_lt_i32 %[bud], 0\ns_cbranch_scc1 2f\ns_cbranch_vccnz 1b\n"
        "2:\ns_mov_b32 m0, %[sm]\ns_setpc_b64 %[ret]\n"
        ".Lwlast%=:\nWTL 13, 8, b\nWTL 14, 4, b\nWTL 15, 0, b\n"
        ".Lwend%=:\n"
        ".purgem WRD\n.purgem WCMP\n.purgem WTS\n.purgem WST\n.purgem WTL\n.purgem WWN\n.purgem WCR"
        : [vo] "+v"(vo), [bud] "+s"(bud), [sa] "=&s"(sa), [sb] "=&s"(sb), [sm] "=&s"(sm), [ret] "=&s"(ret),
          [w0] "+v"(w0), [w1] "+v"(w1), [w2] "+v"(w2)
        : [src] "s"(xsrc), [ldx] "s"(ldx), [c0] "v"(c0), [c4] "v"(c0 + 65536), [v0] "v"(h.vo16), [pos] "s"(h.pos), [wd] "s"(wd), [fc] "s"(fc), [tg] "v"(tag),
          [wn] "s"(wn), [Wn] "s"(Wn), [wl] "s"(h.lds + WQ0 + lw * 3072)
        : "memory", "scc", "vcc", "v240", "v241", "v242", "v243", "v244", "v245"
        );
    asm volatile("" ::"v"(v), "v"(v2));
    if (!SP) __builtin_amdgcn_s_setprio(HPQ);
    if (Wn && lw >= 2) {
        {
            WAITB(6);
            wq_issue<4>(h, Wn, 2, 1 + (2 > h.pos), lw * 4 - 8);
            SM(6, 31);
#pragma unroll 1
            for (int r = lw - 2; r < 4; r += 2) {
                const lchar* xs = smem + nl + r * 2048 + lane * 16;
                u32x4 y[4];
#pragma unroll
                for (int i = 0; i < 4; ++i) y[i] = *(const LAS u32x4*)(xs + (i & 1) * (8192 + nl) + (i >> 1) * 1024);
                float a[4] = {0.f, 0.f, 0.f, 0.f};
                const bf16x2_t one2 = __builtin_bit_cast(bf16x2_t, 0x3f803f80u);
#pragma unroll
                for (int e = 0; e < 8; ++e)
#pragma unroll
                    for (int t = 0; t < 2; ++t) {
                        const unsigned u = y[t * 2 + (e >> 2)][e & 3];
                        const bf16x2_t x = __builtin_bit_cast(bf16x2_t, u);
                        a[2 * t] = __builtin_amdgcn_fdot2_f32_bf16(x, one2, a[2 * t], false);
                        a[2 * t + 1] = __builtin_amdgcn_fdot2_f32_bf16(x, x, a[2 * t + 1], false);
                    }
#pragma unroll
                for (int i = 0; i < 4; ++i) a[i] = dppadd<0x4E>(dppadd<0xB1>(a[i]));
                asm volatile("s_mov_b64 exec, %2\n\tglobal_store_dwordx4 %0, %1, %3 sc1\n\ts_mov_b64 exec, -1\n\ts_nop 0"
                             ::"v"((g * 16 + h.pos) * 1024 + r * 256 + lane * 4), "v"((f32x4){a[0], a[1], a[2], a[3]}), "s"(0x1111111111111111ull), "s"(stats) : "memory");
            }
            SM(6, 30);
        }
    }
    if (SP) __builtin_amdgcn_s_setprio(HPQ);
}

__global__ __launch_bounds__(512) void mega(const u16* xin, char* ws, const u16* wqa, const u16* __restrict__ woa,
                                            const u16* __restrict__ wqb, const u16* __restrict__ wob, u16* __restrict__ out) {
    __shared__ __attribute__((aligned(16))) char smem_[LDS_BYTES];
    u32x4 a01;
    {
        u64 pc;
        unsigned t, v;
        asm volatile("s_load_dwordx4 %0, %5, 0\n\ts_getpc_b64 %2\n1:\n\ts_mov_b32 m0, %8\n\ts_lshr_b32 %3, %6, 3\n\ts_mulk_i32 %3, 28*64\n\t"
                     "v_min_u32 %4, 27, %7\n\tv_lshl_add_u32 %4, %4, 6, %3\n\tv_min_u32 %4, .Lmega_last-1b, %4\n\tglobal_load_lds_dword %4, %2\n\t"
                     "s_load_dwordx2 %1, %5, 16"
                     : "=&s"(a01), "=&s"(wqa), "=&s"(pc), "=&s"(t), "=&v"(v)
                     : "s"(__builtin_amdgcn_kernarg_segment_ptr()), "s"(blockIdx.x), "v"(threadIdx.x), "n"(DUMP) : "memory", "scc");
    }
    SB();
    lchar* smem = (lchar*)smem_;
    const int bid = blockIdx.x, xcd = bid & 7, idx = bid >> 3;
    const int lane = threadIdx.x & 63, wid = __builtin_amdgcn_readfirstlane(threadIdx.x >> 6);
    const int rg = (bid & 4) * 32 + idx * 4 + (bid & 3), row = rg * 8 + (wid & 3) * 2 + ((lane >> 2) & 1);
    u32x4 x[4];
    u64 e;
    {
        unsigned xo = row * 2048 + (lane >> 3) * 64 + (lane & 3) * 16, eo = WS_FL + fi(FL_EP) * 8;
        asm volatile("" : "+v"(xo), "+v"(eo));
        asm volatile("s_waitcnt lgkmcnt(0)" : "+s"(a01), "+s"(wqa));
        xin = (const u16*)(((u64)a01[1] << 32) | a01[0]);
        ws = (char*)(((u64)a01[3] << 32) | a01[2]);
        asm volatile("s_cmp_gt_u32 %9, 3\n\ts_cbranch_scc1 1f\n\t"
                     "global_load_dwordx4 %0, %5, %6" NTX "\n\tglobal_load_dwordx4 %1, %5, %6 offset:512" NTX "\n\tglobal_load_dwordx4 %2, %5, %6 offset:1024" NTX "\n\t"
                     "global_load_dwordx4 %3, %5, %6 offset:1536" NTX "\n1:\n\tglobal_load_dwordx2 %4, %7, %8 sc1"
                     : "=&v"(x[0]), "=&v"(x[1]), "=&v"(x[2]), "=&v"(x[3]), "=&v"(e)
                     : "v"(xo), "s"(xin), "v"(eo), "s"(ws), "s"(wid) : "memory", "scc");
    }
    SB();
    const int g = (xcd >> 2) * 8 + (idx >> 2), hp = (xcd & 3) * 4 + (idx & 3);
    const int pos = (int)(kinv() >> (4 * hp)) & 15, g2 = g, cb = pos;
    u64* fl = (u64*)(ws + WS_FL);
    auto mktag = [](u64 e) {
        e += 1;
        return ((u64)(unsigned)__builtin_amdgcn_readfirstlane((int)(e >> 32)) << 32) | (unsigned)__builtin_amdgcn_readfirstlane((int)e);
    };
    const unsigned lds0 = (unsigned)(uintptr_t)smem;
    LAS unsigned* cnt = (LAS unsigned*)(smem + SNM + 1152);
    *cnt = 0u;
    BARL();
    u64 tag;
    if (wid < 4) {
        unsigned d = ((lane >> 3) * SEQ + row) * 64 + (((lane & 3) ^ fz(row)) << 4);
        asm volatile("" : "+v"(d));
        WAITV(0);
        asm volatile("" : "+v"(x[0]), "+v"(x[1]), "+v"(x[2]), "+v"(x[3]), "+v"(e));
        if (wid == 0) STAMP(14);
        const unsigned s0 = ln0_stats(x);
        const f32x2 st = {lo16(s0), __builtin_amdgcn_rcpf(hi16(s0))};
#pragma unroll
        for (int i = 0; i < 4; ++i) {
            Raw v;
            unpk(v, x[i]);
            asm volatile("global_store_dwordx4 %0, %1, %2 sc1\n\ts_nop 1" ::"v"(d), "v"(ln0_apply(v, st)), "s"(ws + WS_X + i * MB) : "memory");
        }
        tag = mktag(e);
        if (wid == 0) STAMP(15);
        WAITV(0);
        if (lane == 0 && __hip_atomic_fetch_add(cnt, 1u, __ATOMIC_RELAXED, __HIP_MEMORY_SCOPE_WORKGROUP) == 3)
            stflag(fl + fi(FL_L + rg), tag);
    } else {
        if (TD) WAITV(0);
        if (wid > 4) touch((const char*)wqa + (wid - 5) * 2048, lds0 + DUMP, (idx + ((lane >> 3) & (TW - 1)) * 32) * 6144 + (xcd & 3) * 512 + (lane & 7) * 64);
        else {
            const unsigned f = lane < 7 ? WS_GO + (g * 16 + pos) * 1024 + lane * 128
                                        : WS_FL + (lane < 9 ? FL_C + bid * 32 + (lane & 1) * 16 : FL_L + (g & 7) * 16 + (lane & 1) * 128) * 8;
            asm volatile("s_mov_b64 exec, 0x7ff\n\tglobal_atomic_add_x2 %0, %1, %2 sc1\n\ts_mov_b64 exec, -1" ::"v"(f), "v"(0ull), "s"(ws) : "memory");
        }
        auto tc = [&]() {
            const unsigned v = (g * 64 + (lane >> 1)) * 6144 + (hp ^ CJ) * 128 + (lane & 1) * 64;
            const char* b = (const char*)wqa + (wid == 4 ? 0 : wid - 5) * 2048;
            touch(b, lds0 + DUMP, wid == 4 ? 0 : v);
            touch(b + 32 * 6144, lds0 + DUMP, wid == 4 ? 0 : v);
        };
        if (TC == 1) tc();
        unsigned lo, hi;
        asm volatile("s_waitcnt vmcnt(%4)\n\tv_readfirstlane_b32 %0, %2\n\tv_readfirstlane_b32 %1, %3" : "=&s"(lo), "=s"(hi) : "v"((unsigned)e), "v"((unsigned)(e >> 32)), "n"(TC == 1 ? 3 : 1) : "memory");
        if (TC == 2) tc();
        if (TC == 3) { WAITV(0); tc(); }
        tag = (((u64)hi << 32) | lo) + 1;
    }
    if (wid < 4) {
        BAR();
        if (wid == 0) STAMP(9);
        __builtin_amdgcn_s_setprio(1);
        u32x4 res[4];
        {
            const unsigned ro = (g2 * 128 + (wid >> 1) * 64 + (lane & 15)) * 2048 + (cb * 2 + (wid & 1)) * 64 + ((lane & 16) << 1) + ((lane & 32) >> 1);
#pragma unroll
            for (int i = 0; i < 4; ++i) res[i] = (u32x4){ro, ro, ro, ro};
            if (!PB) { BAR(); BAR(); }
        }
#pragma unroll 1
        for (int layer = 0; layer < 4; ++layer) {
            const char* fo = ws + WS_GO + layer * 256;
            const unsigned gm = __builtin_amdgcn_readfirstlane(pos == 0 ? 0xfffe : pos == 1);
            u16* ob = (u16*)(ws + WS_O + pos * (2 * SEQ * 64));
            WReg wr;
            if (layer & 1) qb_compute(smem, (u16*)(ws + WS_KV + (layer >> 1) * 8 * MB), wr, ob + 2 * MB, fo, gm, fl + fi(FL_KV + (layer >> 1) * 1024 + hp * 64),
                                      tag, g, hp, lane, wid STPPASS(layer * 32));
            else qa_compute(smem, layer, xin, res, wr, ob, fo, gm, tag, g, hp, lane, wid STPPASS(layer * 32));
            const bool last = layer == 3;
            wo_compute(smem, res, wr,
                       last ? out : (u16*)(ws + WS_X + (layer + 1) * 4 * MB), !last,
                       fo + 128, gm, (layer & 1) * 1024, tag, g2, cb, lane, wid STPPASS(layer * 32 + 16));
        }
        asm volatile(".Lmega_last:");
        if (bid == 0 && threadIdx.x == 0) stflag(fl + fi(FL_EP), tag);
    } else {
        const int lw = wid - 4;
        HW h;
        h.lds = lds0;
        h.lw = lw;
        h.pos = pos;
        h.go = (const u64*)(ws + WS_GO + (unsigned)(g * 16 + pos) * 1024);
        h.st = (LAS float*)(smem + SNM + (lane < 32 ? 64 + lw * 128 + lane * 4 : (lane & 15) * 4));
        asm volatile("" : "+v"(h.st));
        h.kord = ((KORD & ((1ull << (4 * pos)) - 1)) << 4) | hp | (KORD & ~((16ull << (4 * pos)) - 1));
        h.vo16 = lane * 16;
        h.po = (pos * 2 * SEQ + g * 128) * 64 + lane * 16;
        h.px = (cb * 2 * SEQ + g2 * 128) * 64 + lane * 16;
        h.er = fi(lane ? 256 + g2 * 16 + cb : g * 16 + hp) * 8;
        h.ec = fi((lane < 16 ? g2 : lane < 32 || g == 0 ? 16 + g : 15 + g) * 16 + (lane & 15)) * 8;
#pragma unroll
        for (int i = 0; i < 6; ++i) {
            const int k = (lane >> 3) + (((i / 3) ^ (lw == 1)) << 3), pc = lane & 7;
            h.wq[i / 3][i % 3] = (lane >> 3) * 6144 + (i % 3) * 1024 + hp * 128 + ((((pc >> 1) ^ fswz(k)) << 1) + (pc & 1)) * 16;
        }
#pragma unroll
        for (int i = 0; i < 4; ++i) h.wo[i] = wo_voff(lane, cb, i);
#pragma unroll 1
        for (int layer = 0; layer < 4; ++layer) {
            if (OQ) asm volatile("" : "+s"(wqa), "+s"(wqb), "+s"(ws));
            const u16* X = (const u16*)(ws + WS_X + layer * 4 * MB);
            const char* st = ws + WS_ST;
            const u16* wo = (layer & 1) ? wob : woa;
            Cols cw;
            const u64* fe = fl + fi(FL_E + ((layer + 1) >> 1) * 512);
            if (layer & 1) {
                qkv_helper<false>(smem, h, cw, ws, layer, X, wqb, wqb, wo, st, (float*)(ws + WS_CS), fl, tag, g, hp, lane, lw STPPASS(layer * 32));
                attn_helper(h, ws, layer == 1 ? 4 : 1, (const u16*)(ws + WS_KV + (layer >> 1) * 8 * MB), wo, fe, fl + fi(FL_KV + (layer >> 1) * 1024 + hp * 64), tag, g, hp, lane,
                            lw STPPASS(layer * 32));
            } else {
                qkv_helper<true>(smem, h, cw, ws, layer, X, wqa, wqb, wo, st, (float*)(ws + WS_CS), fl, tag, g, hp, lane, lw STPPASS(layer * 32));
            }
            wo_helper(smem, h, cw, ws + WS_CS + (256 + (hp ^ CJ) * 16 + g) * 768, st + layer * (MB / 2), (layer & 1) * 1024, (const u16*)(ws + WS_O + (layer & 1) * 4 * MB), wo, (layer & 1) * 10, layer == 3 ? nullptr : (layer & 1) ? wqa : wqb, (layer == 0 && lw == 3) ? fl + fi(FL_C + (hp ^ CJ) * 16 + g) : nullptr,
                      h.go + layer * 32, tag, g2, lane, lw STPPASS(layer * 32 + 16));
        }
    }
}

extern "C" void kernel_launch(void* const* d_in, const int* in_sizes, int n_in, void* d_out, int out_size, void* d_ws,
                              size_t ws_size, hipStream_t stream) {
    mega<<<256, 512, 0, stream>>>((const u16*)d_in[0], (char*)d_ws, (const u16*)d_in[1], (const u16*)d_in[2], (const u16*)d_in[4],
                                  (const u16*)d_in[5], (u16*)d_out);
}
```

```cpp
#include <hip/hip_runtime.h>
#include <stdint.h>

typedef unsigned short u16;
typedef unsigned long long u64;
typedef __attribute__((ext_vector_type(8))) short bf16x8;
typedef __attribute__((ext_vector_type(4))) short s16x4;
typedef __attribute__((ext_vector_type(4))) float f32x4;
typedef __attribute__((ext_vector_type(16))) float f32x16;
typedef __attribute__((ext_vector_type(4))) unsigned u32x4;
typedef __attribute__((ext_vector_type(2))) unsigned u32x2;
typedef __attribute__((ext_vector_type(2))) __bf16 bf16x2_t;
typedef __attribute__((ext_vector_type(2))) float f32x2;

#define LAS __attribute__((address_space(3)))
#define GAS __attribute__((address_space(1)))
#define DEV __device__ __forceinline__
typedef LAS char lchar;

constexpr int SEQ = 2048, DM = 1024, NH = 32;
constexpr float QSCALE = 0.17677669529663687f, LOG2E = 1.4426950408889634f;
#define POLLMAX (1 << 16)
#define NTX " nt"
#define CT 1
#define CT0 0
#define OQ 1
#define PW 2
#define VF 1
#define AL 0
#define PB 1
#define FL0 1
#define TW 8
#define K0 1
#define BW 1
#define RS 0
#define TC 0
#define TD 0
#define LP 1
#define WA 1
#define WB 48
#define EF 3
#define EO 0
#define EXP 1
#define WS 2
#define HPW 2
#define HPQ 0
#define NP 1
#define PRE 0
#define F0 0
#define FE 5
#define FN 2
#define FD 2
#define FA 4
#define E1 1
#define AW 0
#define PQ 1
#define Q2 1
#define SF 6
#define NCK (SF + 3)
#define L2 1
#define CI 2
#define RI 4
#define KORD 0x3bfedc7654a98210ull
#define CJ 2
#define CX 1
#define EMU 1
#define PUB 2
#define XP 0
#define WE 1
#define EA 0
#define EB 0
#define P2 6
#define SP 1
#define GM 0
#define MO 5
#define MW 0
#define TL 0
#define LG 1
#define WN 4
#define T2 5
#define CW 1
#define RM 0
#define CE 0
#define CS 4
#define RC 1
#define FX 1
#define RT -1
#define PE 4
#define DW 1
#define DS 9
#define BR 1
#define PF 0
#define S1 2
#define S2 4
#define X3 0
#define QB 1
#define SA 3

DEV unsigned cvtpk(float lo, float hi) {
    f32x2 v = {lo, hi};
    bf16x2_t b = __builtin_convertvector(v, bf16x2_t);
    return __builtin_bit_cast(unsigned, b);
}
DEV float lo16(unsigned v) { return __uint_as_float(v << 16); }
DEV float hi16(unsigned v) { return __uint_as_float(v & 0xffff0000u); }
DEV float bfr(float f) { return lo16(cvtpk(f, f)); }
DEV u32x2 pk4(f32x4 a) { return (u32x2){cvtpk(a[0], a[1]), cvtpk(a[2], a[3])}; }
DEV bf16x8 cat(u32x2 a, u32x2 b) { return __builtin_bit_cast(bf16x8, (u32x4){a[0], a[1], b[0], b[1]}); }
DEV u32x2 sw16(unsigned a, unsigned b) { auto r = __builtin_amdgcn_permlane16_swap(a, b, false, false); return (u32x2){r[0], r[1]}; }
DEV u32x2 sw32(unsigned a, unsigned b) { auto r = __builtin_amdgcn_permlane32_swap(a, b, false, false); return (u32x2){r[0], r[1]}; }
DEV f32x2 sw16f(float a, float b) { return __builtin_bit_cast(f32x2, sw16(__float_as_uint(a), __float_as_uint(b))); }
DEV f32x2 sw32f(float a, float b) { return __builtin_bit_cast(f32x2, sw32(__float_as_uint(a), __float_as_uint(b))); }
DEV u32x4 unit16(u32x2 a, u32x2 b) {
    const u32x2 x = sw16(a[0], b[0]), y = sw16(a[1], b[1]);
    return (u32x4){x[0], y[0], x[1], y[1]};
}
template <int OFF> DEV void st16wt(const void* p, u32x4 v) {
    asm volatile("global_store_dwordx4 %0, %1, off offset:%2 sc1\n\ts_nop 1" ::"v"(p), "v"(v), "n"(OFF) : "memory");
}
DEV void st8wt(const void* p, u32x2 v) { asm volatile("global_store_dwordx2 %0, %1, off sc1\n\ts_nop 1" ::"v"(p), "v"(v) : "memory"); }
template <int CTRL> DEV float dppadd(float v) {
    return v + __builtin_bit_cast(float, __builtin_amdgcn_update_dpp(0, __builtin_bit_cast(int, v), CTRL, 0xf, 0xf, true));
}
DEV int fswz(int k) { return ((k >> 1) & 1) | (((k >> 3) & 1) << 1); }
DEV int fz(int tok) { return (-(tok >> 2)) & 3; }
DEV s16x4 ldtr4(const lchar* p) { return __builtin_bit_cast(s16x4, __builtin_amdgcn_ds_read_tr16_b64_v4i16((LAS s16x4*)p)); }
DEV bf16x8 cat8(s16x4 a, s16x4 b) { return (bf16x8){a[0], a[1], a[2], a[3], b[0], b[1], b[2], b[3]}; }
DEV bf16x8 ldk(const lchar* p) { return *(const LAS bf16x8*)p; }

#define STAMP(i)
#define STAMPW(i)
#define STPARG
#define STPPASS(o)
#define STAMPB(i)
#define SM(w, i)
#define SMD()
#define POLL(f, on, nd) poll(f, on, tag, nd, [](u64 m) { return (unsigned)m; })
#define HT0()
#define HT1(i)
#define TSD()
#define TS(i)
#define TSW(n)
#define BAR() asm volatile("s_barrier" ::: "memory")
#define BARL() asm volatile("s_waitcnt lgkmcnt(0)\n\ts_barrier" ::: "memory")
#define WAITB(n) asm volatile("s_waitcnt vmcnt(%0)\n\ts_barrier" ::"n"(n) : "memory")
#define WAITV(n) asm volatile("s_waitcnt vmcnt(%0)" ::"n"(n) : "memory")
#define SB() __builtin_amdgcn_sched_barrier(0)
#define LGKM0() __builtin_amdgcn_s_waitcnt(0xc07f)
#define MFMA32(a, b, c) __builtin_amdgcn_mfma_f32_32x32x16_bf16(a, b, c, 0, 0, 0)
#define MFMA16(a, b, c) __builtin_amdgcn_mfma_f32_16x16x32_bf16(a, b, c, 0, 0, 0)

DEV void mfma16t(f32x4& c, const bf16x8& a, const bf16x8& b) { asm volatile("v_mfma_f32_16x16x32_bf16 %0, %1, %2, %0" : "+v"(c) : "v"(a), "v"(b)); }
DEV void mfma16z(f32x4& c, const bf16x8& a, const bf16x8& b) { asm volatile("v_mfma_f32_16x16x32_bf16 %0, %1, %2, 0" : "=&v"(c) : "v"(a), "v"(b)); }
#define MFMA_DONE() do { SB(); asm volatile("s_nop 7\n\ts_nop 3"); SB(); } while (0)
DEV float fmx(float a, float b) { return __builtin_elementwise_maximum(a, b); }
DEV float rsd(float var) { return __builtin_amdgcn_rcpf(bfr(__builtin_amdgcn_sqrtf(bfr(bfr(var) + 1e-5f)))); }

constexpr int XS = 18432;
constexpr int WQ0 = 81920, WQS = 24576;
constexpr int STO = 65536;
constexpr int EXO = 57344;
constexpr int AR0 = 57344, AR1 = 65536, AR2 = 32768;
constexpr int SNM = 155648, SRS = SNM + 576, SCOL = SNM + 1280, HXO = SNM + 2048, DUMP = SNM + 4096, SCB = SNM + 4352;
constexpr int LDS_BYTES = 163840;
constexpr size_t MB = 1u << 20;
constexpr size_t WS_ST = 0, WS_GO = MB / 4, WS_L = 3 * MB / 4, WS_CS = 3 * MB / 2, WS_FL = 15 * MB / 8, WS_X = 2 * MB, WS_O = 18 * MB, WS_KV = 26 * MB, WS_R = 42 * MB;
constexpr int FL_KV = 8192;
constexpr int FL_L = 10240;
constexpr int FL_C = 7168;
constexpr int FL_E = 10496;
constexpr int FL_S = 12288;
constexpr int FL_B = 13312;
constexpr int FL_EP = 16368;
#define FLS 16
#define FS 1
DEV int fi(int i) { return (i >> 4) * FLS + (i & 15) * FS; }

#define GL_HEAD "s_mov_b32 m0, %1\n\ts_nop 4\n\tglobal_load_lds_dwordx4 %2, %0 sc1"
DEV void glds1(const void* sb, unsigned lds, unsigned v0) { asm volatile(GL_HEAD ::"s"(sb), "s"(lds), "v"(v0) : "memory"); }
DEV void glds3(const void* sb, unsigned lds, unsigned v0, unsigned v1, unsigned v2) {
    asm volatile(GL_HEAD "\n\tglobal_load_lds_dwordx4 %3, %0 offset:1024 sc1\n\tglobal_load_lds_dwordx4 %4, %0 offset:2048 sc1"
                 ::"s"(sb), "s"(lds), "v"(v0), "v"(v1), "v"(v2) : "memory");
}
DEV void glds4(const void* sb, unsigned lds, unsigned v0, unsigned v1, unsigned v2, unsigned v3) {
    asm volatile(GL_HEAD "\n\tglobal_load_lds_dwordx4 %3, %0 offset:1024 sc1\n\tglobal_load_lds_dwordx4 %4, %0 offset:2048 sc1\n\t"
                 "global_load_lds_dwordx4 %5, %0 offset:3072 sc1" ::"s"(sb), "s"(lds), "v"(v0), "v"(v1), "v"(v2), "v"(v3) : "memory");
}
DEV void touch(const void* sb, unsigned lds, unsigned v0) {
    asm volatile("s_mov_b32 m0, %1\n\ts_nop 4\n\tglobal_load_lds_dword %2, %0 sc1" ::"s"(sb), "s"(lds), "v"(v0) : "memory");
}
DEV u64 ldep(const u64* p) { return __hip_atomic_load((const GAS u64*)p, __ATOMIC_RELAXED, __HIP_MEMORY_SCOPE_AGENT); }
#define FLD(d, a, z) "global_atomic_add_x2 " d ", " a ", " z ", off sc0 sc1"
DEV u64 ldflag(const u64* p) {
    u64 v;
    asm volatile(FLD("%0", "%1", "%2") "\n\ts_waitcnt vmcnt(0)" : "=&v"(v) : "v"(p), "v"(0ull) : "memory");
    return v;
}
DEV void stflag(u64* p, u64 v) { asm volatile("global_atomic_swap_x2 %0, %1, off sc1\n\ts_nop 1" ::"v"(p), "v"(v) : "memory"); }
template <class F> DEV unsigned poll(const u64* p, bool on, u64 tag, unsigned need, F fold) {
    unsigned m = ~0u;
#pragma unroll 1
    for (int it = 0; it < POLLMAX; ++it) {
        u64 v = tag;
        if (on) v = ldflag(p);
        const unsigned r = fold(__ballot(v == tag));
        if ((r & need) == need) { m = r; break; }
        __builtin_amdgcn_s_sleep(1);
    }
    asm volatile("" ::: "memory");
    return m;
}
template <int B> DEV void poll2(const u64* f, unsigned m, u64 tag, u64& a, u64& b) {
    unsigned bud = POLLMAX;
    asm volatile("s_mov_b32 exec_lo, %5\n\ts_mov_b32 exec_hi, 0\n\t.if %8\n\t" FLD("%0", "%3", "%4") "\n\ts_waitcnt vmcnt(%8)\n\ts_barrier\n\t.else\n\t"
                 "v_cmp_ne_u64 vcc, %6, %0\n\ts_cbranch_vccz 2f\n\t" FLD("%0", "%3", "%4") "\n\ts_sleep %7\n\t.endif\n"
                 "1:\n\t" FLD("%1", "%3", "%4") "\n\ts_waitcnt vmcnt(1)\n\tv_cmp_ne_u64 vcc, %6, %0\n\ts_cbranch_vccz 2f\n\t"
                 FLD("%0", "%3", "%4") "\n\ts_waitcnt vmcnt(1)\n\tv_cmp_ne_u64 vcc, %6, %1\n\ts_sub_u32 %2, %2, 1\n\ts_cbranch_vccz 2f\n\ts_cbranch_scc0 1b\n"
                 "2:\n\ts_mov_b64 exec, -1"
                 : "+v"(a), "=&v"(b), "+s"(bud) : "v"(f), "v"(0ull), "s"(m), "s"(tag), "n"(P2), "n"(B) : "memory", "scc", "vcc");
}
template <int N = 0> DEV void publish(u64* f, int lane, u64 tag) {
    asm volatile("s_mov_b64 exec, 1\n\ts_waitcnt vmcnt(%2)\n\tglobal_atomic_swap_x2 %0, %1, off sc1\n\ts_mov_b64 exec, -1\n\ts_nop 0" ::"v"(f), "v"(tag), "n"(N) : "memory");
}
DEV void raise(u64* f, u64 tag) {
    asm volatile("s_mov_b64 exec, 1\n\tglobal_atomic_swap_x2 %0, %1, off sc1\n\ts_mov_b64 exec, -1\n\ts_nop 0" ::"v"(f), "v"(tag) : "memory");
}
DEV unsigned go_off(int g, int lane, int wm, int wn) { return (g * 16 + (lane & 31)) * 1024 + (wn * 8 + ((lane & 16) ? 2 : wm)) * 8; }
DEV void go(const char* b, unsigned o, unsigned m, u64 tag) {
    asm volatile("s_cmp_eq_u32 %3, 0\n\ts_cbranch_scc1 1f\n\ts_mov_b32 exec_lo, %3\n\ts_mov_b32 exec_hi, 0\n\tglobal_atomic_swap_x2 %0, %1, %2 sc1\n\ts_mov_b64 exec, -1\n1:"
                 ::"v"(o), "v"(tag), "s"(b), "s"(m) : "memory", "scc");
}
#define OPAQUE(l, a, b, c) do { asm volatile("" : "+v"(l)); asm volatile("" : "+s"(a), "+s"(b), "+s"(c)); } while (0)

template <bool LOCAL, class F, class G>
DEV void qkv_main(lchar* smem, f32x4 (&acc)[6][4], f32x4 (&acch)[2], int lane, int wm, int wn, int layer, G mid, F tailread STPARG) {
    constexpr int XC_B = LOCAL ? 9216 : 8192, HO = LOCAL ? 1024 : 0, NR = LOCAL ? 17 : 16;
    const int l15 = lane & 15, lg = lane >> 4, hs = LOCAL ? wm : 0;
    const lchar* xhb = smem + l15 * 64 + ((lg ^ fz(l15)) << 4);
    const lchar* xb = xhb + HO + wm * 4096;
    const int k0 = 8 * lg + (l15 >> 2);
    const lchar* wb0 = smem + WQ0 + lg * 3072 + (l15 >> 2) * 128 + (((wn * 2 + hs) ^ fswz(k0)) << 5) + (l15 & 3) * 8;
    const lchar* wb1 = smem + WQ0 + lg * 3072 + (l15 >> 2) * 128 + (((wn * 2 + (hs ^ 1)) ^ fswz(k0)) << 5) + (l15 & 3) * 8;
    lchar* sto = smem + STO + (wm * 2 + wn) * 4096 + lane * 16;
    acch[0] = acch[1] = (f32x4){0.f, 0.f, 0.f, 0.f};
    if (LOCAL) asm volatile("" : "+v"(acch[0]), "+v"(acch[1]), "+v"(xhb), "+v"(sto));
    asm volatile("" : "+v"(xb), "+v"(wb0), "+v"(wb1));
    bf16x8 xf[2][5], hf[6];
    s16x4 wh[2][6][2];
    auto rd = [&](int b, int j, int xo, int wo) {
        if (j < 4) xf[b][j] = ldk(xb + xo + j * 1024);
        else if (j < 16) wh[b][(j - 4) >> 1][j & 1] = ldtr4(((j & 2) ? wb1 : wb0) + wo + ((j - 4) >> 2) * 1024 + (j & 1) * 512);
        else xf[b][4] = ldk(xhb + xo);
    };
    auto aside = [&](int b) {
        *(LAS bf16x8*)(sto + b * 2048) = cat8(wh[b][2][0], wh[b][2][1]);
        *(LAS bf16x8*)(sto + b * 2048 + 1024) = cat8(wh[b][4][0], wh[b][4][1]);
    };
    auto kstep = [&](int b, int xo, int wo, int md) __attribute__((always_inline)) {
        int j = 0;
        auto fillr = [&]() __attribute__((always_inline)) {
            SB();
            if (md == 2) {
                if (!LOCAL) tailread(j);
                else if (j < 6) hf[j] = j < 2 ? ldk(xhb + HXO + j * 1024) : ldk(sto + (j - 2) * 1024);
                else tailread(j - 6);
            } else if (j < (md ? NR : 16)) rd(b ^ 1, j, xo, wo);
            else if (LOCAL && md == 0 && j == 16) aside(0);
            SB();
            ++j;
        };
        SB();
#pragma unroll
        for (int v = 0; v < 34; ++v) {
            const int u = v < 15 ? v : v - 4;
            const int ni = u < 20 ? u / 5 : 4 + (((u >> 1) ^ u) & 1), ti = u >= 20 ? (u - 20) / 2 : (ni & 1) ? 4 - u % 5 : u % 5;
            const bf16x8 w = cat8(wh[b][ni][0], wh[b][ni][1]);
            if (v >= 15 && v < 19) {
                if (!LOCAL || md != 2) continue;
                const int k = (v - 15) >> 1;
                if (v & 1) mfma16t(acch[1], hf[k], hf[3 + 2 * k]);
                else mfma16t(acch[0], hf[2 + 2 * k], hf[k]);
            } else if (ti == 4) {
                if (!LOCAL || md == 0 || (ni != 2 && ni != 4)) continue;
                if (ni == 2) mfma16t(acch[0], w, xf[b][4]);
                else mfma16t(acch[1], xf[b][4], w);
            } else if (md == 0) {
                if (ni >= 4) mfma16z(acc[ni][ti], xf[b][ti], w);
                else mfma16z(acc[ni][ti], w, xf[b][ti]);
            } else {
                if (ni >= 4) mfma16t(acc[ni][ti], xf[b][ti], w);
                else mfma16t(acc[ni][ti], w, xf[b][ti]);
            }
            fillr();
        }
        if (md < 2) { LGKM0(); SB(); }
    };
    if (LOCAL && PB) asm volatile("s_cmp_lg_u32 %0, 0\n\ts_cbranch_scc1 1f\n\t.if %1\n\ts_waitcnt vmcnt(0)\n\t.endif\n\ts_barrier\n1:" ::"s"(layer), "n"(BR == 0) : "memory", "scc");
    SB();
    STAMP(4);
#pragma unroll
    for (int j = 0; j < 16; ++j) rd(0, j, 0, 2 * WQS);
    SB();
    LGKM0(); SB();
    kstep(0, XC_B, 2 * WQS + WQS / 2, 0);
    if (LOCAL) {
        aside(1);
        xf[1][4] = (bf16x8){0, 0, 0, 0, 0, 0, 0, 0};
        asm volatile("" : "+v"(xf[1][4]));
    }
    SM(1, 11);
    if (LOCAL && BR == 1) asm volatile("s_cmp_lg_u32 %0, 0\n\ts_cbranch_scc1 1f\n\ts_waitcnt vmcnt(0)\n1:" ::"s"(layer) : "memory", "scc");
    SM(2, 12);
    SB();
#pragma unroll 1
    for (int it = 0; it < 5; ++it) {
        if (!RM) mid(it);
#pragma unroll
        for (int s = 0; s < 3; ++s) {
            if (s == DW) WAITV(0);
            BAR(); SB();
            kstep(1, ((s + 1) % 3) * XS, s * WQS, 1);
            if (RM == s + 1) { mid(it); SB(); }
            kstep(0, ((s + 1) % 3) * XS + XC_B, s * WQS + WQS / 2, 1);
        }
    }
    BAR(); SB();
    kstep(1, 0, 0, 2);
    MFMA_DONE();
}

constexpr int NRW = 7;
typedef s16x4 WReg[NRW * 8];
DEV const lchar* wo_wb(const lchar* smem, int lane, int c) {
    const int k0 = 8 * (lane >> 4) + ((lane & 15) >> 2);
    return smem + k0 * 128 + ((c ^ fswz(k0)) << 5) + (lane & 3) * 8;
}
#define PIN2(a, b) asm volatile("" : "+v"(a), "+v"(b))
#define WRD1() do { SB(); wr[wj] = ldtr4(((wj & 2) ? wb1 : wb0) + (wj >> 2) * 4096 + (wj & 1) * 512); ++wj; SB(); } while (0)

DEV void qa_compute(lchar* smem, int fold, const u16* xin, unsigned cb, u32x4 (&rr)[4], WReg& wr, u16* O, const char* fo, unsigned gm, u64 tag, int g, int hp, int lane, int wid STPARG) {
    OPAQUE(lane, wid, g, hp);
    const int wm = wid >> 1, wn = wid & 1, l15 = lane & 15, lg = lane >> 4;
    f32x4 acc[6][4], acch[2];
    float nm[5], rs[5], csv[2];
    f32x4 nmv[5], rsv[5], cs[4];
    const lchar* sa = smem + SNM + l15 * 4;
    const lchar* sb = smem + SNM + lg * 16;
    const lchar* sc = smem + SCOL + wn * 128;
    const int ro = 64 + wm * 256;
    f32x4 cprev, ccur;
#pragma unroll
    for (int j = 0; j < 4; ++j) {
        const int d = l15 - 4 * lg - j;
        ccur[j] = (d >= 0 && !(d & 1)) ? 0.f : -3.0e38f;
        cprev[j] = (d <= -2 && !(d & 1)) ? 0.f : -3.0e38f;
    }
    const char* optr = (const char*)O + ((size_t)wn * SEQ + g * 128 + wm * 64 + l15) * 64 +
                       (((((lg & 1) << 1) | (lg >> 1)) ^ (wm << 1) ^ fz(l15)) << 4);
    unsigned xo = rr[0][0], xd = 0;
    if (RT >= 0) {
        const int row = (g * 16 + (hp & 3) * 4 + (hp >> 2)) * 8 + wid * 2 + ((lane >> 2) & 1);
        xd = ((lane >> 3) * SEQ + row) * 64 + (((lane & 3) ^ fz(row)) << 4);
        xo = ((cb * 2 + wn) * SEQ + g * 128 + wm * 64 + l15) * 64 + (((((lg & 1) << 1) | (lg >> 1)) ^ fz(l15)) << 4);
        asm volatile("" : "+v"(xd));
    }
    asm volatile("" : "+v"(cprev), "+v"(ccur), "+v"(optr), "+v"(xo));
    qkv_main<true>(smem, acc, acch, lane, wm, wn, fold, [&](int it) {
        if (RT >= 0)
        asm volatile("s_cmp_eq_u32 %6, %7+1\n\ts_cbranch_scc0 2f\n\ts_waitcnt vmcnt(0)\n2:\n\ts_cmp_lg_u32 %6, %7\n\ts_cbranch_scc1 1f\n\tglobal_store_dwordx4 %4, %0, %5 sc1\n\tv_add_u32 %4, 0x100000, %4\n\t"
                     "global_store_dwordx4 %4, %1, %5 sc1\n\tv_add_u32 %4, 0x100000, %4\n\tglobal_store_dwordx4 %4, %2, %5 sc1\n\tv_add_u32 %4, 0x100000, %4\n\t"
                     "global_store_dwordx4 %4, %3, %5 sc1\n1:" : "+v"(rr[0]), "+v"(rr[1]), "+v"(rr[2]), "+v"(rr[3]), "+v"(xd)
                     : "s"(xin), "s"(it + fold * 8), "n"(RT) : "memory", "scc");
        if (RS == 1) {
#pragma unroll
            for (int k = 0; k < 4; ++k)
                asm volatile("s_cmp_lg_u32 %3, %4\n\ts_cbranch_scc1 1f\n\tglobal_load_dwordx4 %0, %1, %2\n\tv_add_u32 %1, 0x8000, %1\n1:"
                             : "+v"(rr[k]), "+v"(xo) : "s"(xin), "s"(it + fold * 8), "n"(k + 1) : "memory", "scc");
        } else if (RS == 2) {
#pragma unroll
            for (int k = 0; k < 2; ++k)
                asm volatile("s_cmp_lg_u32 %4, %5\n\ts_cbranch_scc1 1f\n\tglobal_load_dwordx4 %0, %2, %3\n\tv_add_u32 %2, 0x8000, %2\n\t"
                             "global_load_dwordx4 %1, %2, %3\n\tv_add_u32 %2, 0x8000, %2\n1:"
                             : "+v"(rr[2 * k]), "+v"(rr[2 * k + 1]), "+v"(xo) : "s"(xin), "s"(it + fold * 8), "n"(k + 3) : "memory", "scc");
        } else if (RC)
        asm volatile("s_cmp_lg_u32 %6, %7\n\ts_cbranch_scc1 1f\n\tglobal_load_dwordx4 %0, %4, %5 sc1\n\tglobal_load_dwordx4 %1, %4, %5 offset:1024 sc1\n\t"
                     "global_load_dwordx4 %2, %4, %5 offset:2048 sc1\n\tglobal_load_dwordx4 %3, %4, %5 offset:3072 sc1\n1:"
                     : "+v"(rr[0]), "+v"(rr[1]), "+v"(rr[2]), "+v"(rr[3]), "+v"(xo) : "s"(xin), "s"(it + fold * 8), "n"(RI) : "memory", "scc");
        else
        asm volatile("s_cmp_lg_u32 %6, %7\n\ts_cbranch_scc1 1f\n\tglobal_load_dwordx4 %0, %4, %5\n\tv_add_u32 %4, 0x8000, %4\n\t"
                     "global_load_dwordx4 %1, %4, %5\n\tv_add_u32 %4, 0x8000, %4\n\tglobal_load_dwordx4 %2, %4, %5\n\tv_add_u32 %4, 0x8000, %4\n\t"
                     "global_load_dwordx4 %3, %4, %5\n1:" : "+v"(rr[0]), "+v"(rr[1]), "+v"(rr[2]), "+v"(rr[3]), "+v"(xo)
                     : "s"(xin), "s"(it + fold * 8), "n"(RI) : "memory", "scc");
    }, [&](int j) {
        if (j < 4) cs[j] = *(const LAS f32x4*)(sc + (j >> 1) * 256 + (((j & 1) ^ wm) << 6) + lg * 16);
        else if (j < 9) { const int o = j < 8 ? ro + (j - 4) * 64 : 0; nm[j - 4] = *(const LAS float*)(sa + o); rs[j - 4] = *(const LAS float*)(sa + o + 576); }
        else if (j < 11) csv[j - 9] = *(const LAS float*)(sc + 512 + (((j - 9) ^ wm) << 6) + l15 * 4);
        else if (j < 21) {
            const int t = (j - 11) >> 1, o = t < 4 ? ro + t * 64 : 0;
            if ((j - 11) & 1) rsv[t] = *(const LAS f32x4*)(sb + o + 576);
            else nmv[t] = *(const LAS f32x4*)(sb + o);
        }
    } STPPASS(0));
    STAMP(2);
    TSD(); TS(0);
    if (fold) {
#pragma unroll
        for (int ti = 0; ti < 4; ++ti) {
#pragma unroll
            for (int ni = 0; ni < 4; ++ni) acc[ni][ti] = (acc[ni][ti] + nm[ti] * cs[ni]) * rs[ti];
#pragma unroll
            for (int h = 0; h < 2; ++h) acc[4 + h][ti] = (acc[4 + h][ti] + nmv[ti] * csv[h]) * rsv[ti];
        }
        acch[0] = (acch[0] + nm[4] * cs[2]) * rs[4];
        acch[1] = (acch[1] + nmv[4] * csv[0]) * rsv[4];
    }
    typedef __attribute__((ext_vector_type(10))) unsigned u32x10;
    u32x2 kp[5][2];
    u32x10 vp[2];
    auto setv = [&](int h, int i, u32x2 a) { vp[h][2 * i] = a[0]; vp[h][2 * i + 1] = a[1]; };
    lchar* ex = smem + EXO + wn * 4096 + lane * 16;
    {
        const u32x2 a = pk4(acch[0]), b = pk4(acch[1]);
        *(LAS u32x4*)(ex + wm * 1024) = (u32x4){a[0], a[1], b[0], b[1]};
    }
#pragma unroll
    for (int h = 0; h < 2; ++h) { kp[4][h] = pk4(acc[2 + h][3]); setv(h, 4, pk4(acc[4 + h][3])); }
    if (wm == 0) {
#pragma unroll
        for (int h = 0; h < 2; ++h) *(LAS u32x4*)(ex + 2048 + h * 1024) = (u32x4){kp[4][h][0], kp[4][h][1], vp[h][8], vp[h][9]};
    }
    bf16x8 qf[4];
#pragma unroll
    for (int ti = 0; ti < 4; ++ti) {
        qf[ti] = cat(pk4(acc[0][ti]), pk4(acc[1][ti]));
        if (ti < 3) {
#pragma unroll
            for (int h = 0; h < 2; ++h) { kp[ti + 1][h] = pk4(acc[2 + h][ti]); setv(h, ti + 1, pk4(acc[4 + h][ti])); }
        }
    }
    const lchar* wb0 = wo_wb(smem, lane, wn * 2);
    const lchar* wb1 = wo_wb(smem, lane, wn * 2 + 1);
    const lchar* e0 = ex + wm * 3072;
    const lchar* e1 = ex + wm * 1024 + 1024;
    f32x4 cp0 = cprev;
    if (g == 0 && wm == 0) cp0 = (f32x4){-3.0e38f, -3.0e38f, -3.0e38f, -3.0e38f};
    unsigned sel0 = (lane & 1) ? 0x01000c0cu : 0x0c0c0100u, sel1 = (lane & 1) ? 0x03020c0cu : 0x0c0c0302u;
    bf16x8 ones = {0x3f80, 0x3f80, 0x3f80, 0x3f80, 0x3f80, 0x3f80, 0x3f80, 0x3f80};
    unsigned gof = go_off(g, lane, wm, wn);
    lchar* ol = smem + 65536 + wn * 8192 + wm * 4096 + l15 * 64 + (((((lg & 1) << 1) | (lg >> 1)) ^ (wm << 1) ^ fz(l15)) << 4);
    asm volatile("" : "+v"(wb0), "+v"(wb1), "+v"(e0), "+v"(e1), "+v"(cp0), "+v"(sel0), "+v"(sel1), "+v"(ones), "+v"(vp[0]), "+v"(vp[1]), "+v"(ol), "+v"(gof));
    SB();
    TS(1);
    BARL();
    TS(2);
    kp[0][0] = *(const LAS u32x2*)e0; SB(); kp[0][1] = *(const LAS u32x2*)e1; SB();
    setv(0, 0, *(const LAS u32x2*)(e0 + 8)); SB(); setv(1, 0, *(const LAS u32x2*)(e1 + 8));
    SB();
    int wj = 0;
    f32x4 s0[4], s1[4];
    f32x2 sv[4][2];
    float mx[4];
#pragma unroll
    for (int ti = 3; ti >= 0; --ti) {
        s1[ti] = MFMA16(cat(kp[ti + 1][0], kp[ti + 1][1]), qf[ti], ccur);
        WRD1();
        s0[ti] = MFMA16(cat(kp[ti][0], kp[ti][1]), qf[ti], ti ? cprev : cp0);
        WRD1();
    }
#pragma unroll
    for (int i = 0; i < 48; ++i) if (wj < 48) WRD1();
#pragma unroll
    for (int i = 7; i >= 0; --i) {
        const f32x4& t = (i & 1) ? s1[i >> 1] : s0[i >> 1];
        sv[i >> 1][i & 1] = (f32x2){fmx(t[0], t[1]), fmx(t[2], t[3])};
        asm volatile("" : "+v"(sv[i >> 1][i & 1]));
    }
    if (WA == 0) { SB(); LGKM0(); BAR(); SB(); }
    if (EMU) {
        unsigned r[8];
#pragma unroll
        for (int ps = 0; ps < 2; ++ps) {
#pragma unroll
            for (int i = 7; i >= 0; --i) r[i] = cvtpk(sv[i >> 1][i & 1][0], sv[i >> 1][i & 1][1]);
#pragma unroll
            for (int i = 7; i >= 0; --i) {
                sv[i >> 1][i & 1] = (f32x2){lo16(r[i]), hi16(r[i])};
                if (ps == 0) sv[i >> 1][i & 1] *= QSCALE;
            }
        }
    }
#pragma unroll
    for (int ti = 3; ti >= 0; --ti) mx[ti] = fmx(fmx(fmx(sv[ti][0][0], sv[ti][0][1]), sv[ti][1][0]), sv[ti][1][1]);
    TS(3);
    {
        const f32x2 a = sw16f(mx[0], mx[1]), b = sw16f(mx[2], mx[3]);
        const f32x2 c = sw32f(fmx(a[0], a[1]), fmx(b[0], b[1]));
        const float r = fmx(c[0], c[1]) * -(EMU ? LOG2E : QSCALE * LOG2E);
        const f32x2 d = sw32f(r, r), e = sw16f(d[0], d[0]), f = sw16f(d[1], d[1]);
        mx[0] = e[0]; mx[1] = e[1]; mx[2] = f[0]; mx[3] = f[1];
    }
    SB();
    TS(4);
    if (WA == 1) { LGKM0(); BAR(); SB(); }
    TS(5);
    const f32x4 z = {0.f, 0.f, 0.f, 0.f};
    f32x4 l[4], o0[4], o1[4];
    auto pv = [&](int ti, u32x4 v0, u32x4 v1) {
        const f32x2 x = sv[ti][0] * (EMU ? LOG2E : QSCALE * LOG2E) + mx[ti], y = sv[ti][1] * (EMU ? LOG2E : QSCALE * LOG2E) + mx[ti];
        const unsigned c0 = cvtpk(__builtin_amdgcn_exp2f(x[0]), __builtin_amdgcn_exp2f(x[1]));
        const unsigned c1 = cvtpk(__builtin_amdgcn_exp2f(y[0]), __builtin_amdgcn_exp2f(y[1]));
        const bf16x8 p = __builtin_bit_cast(bf16x8, (u32x4){__builtin_amdgcn_perm(c0, c0, sel0), __builtin_amdgcn_perm(c0, c0, sel1),
                                                            __builtin_amdgcn_perm(c1, c1, sel0), __builtin_amdgcn_perm(c1, c1, sel1)});
        SB();
        l[ti] = MFMA16(ones, p, z);
        WRD1();
        o0[ti] = MFMA16(__builtin_bit_cast(bf16x8, v0), p, z);
        WRD1();
        o1[ti] = MFMA16(__builtin_bit_cast(bf16x8, v1), p, z);
        SB();
    };
    auto fin = [&](int ti) {
        const float inv = __builtin_amdgcn_rcpf(l[ti][0]);
        return unit16(pk4(o0[ti] * inv), pk4(o1[ti] * inv));
    };
#define PV(ti) pv(ti, __builtin_shufflevector(vp[0], vp[0], 2 * ti, 2 * ti + 1, 2 * ti + 2, 2 * ti + 3), \
                      __builtin_shufflevector(vp[1], vp[1], 2 * ti, 2 * ti + 1, 2 * ti + 2, 2 * ti + 3))
    PV(0); PV(1);
    TS(6);
#define OOUT(ti) do { const u32x4 v = fin(ti); st16wt<ti * 1024>(optr, v); *(LAS u32x4*)(ol + ti * 1024) = v; SB(); } while (0)
    if (EA == 4) { go(fo, gof, gm, tag); SB(); }
    OOUT(0);
    if (EA == 3) { go(fo, gof, gm, tag); SB(); }
    if (WA == 2) { __builtin_amdgcn_s_waitcnt(0xc47f); BAR(); SB(); }
    TS(7);
    PV(2);
    OOUT(1);
    if (EA == 2) { go(fo, gof, gm, tag); SB(); }
    if (WA == 3) { __builtin_amdgcn_s_waitcnt(0xc67f); BAR(); SB(); }
    TS(8);
    PV(3);
    OOUT(2);
    if (EA == 1) { go(fo, gof, gm, tag); SB(); }
    if (WA == 4) { __builtin_amdgcn_s_waitcnt(0xc87f); BAR(); SB(); }
    TS(9);
    OOUT(3);
    TS(10);
    if (EA == 0) go(fo, gof, gm, tag);
    SM(0, 2); SMD();
    TS(11);
    TSW(12);
    STAMP(10);
}

#define MX3(a, b, c) fmx(fmx((a), (b)), (c))
DEV float rowmax32(const f32x16& s0) {
    float ma = MX3(s0[0], s0[1], s0[2]), mb = MX3(s0[3], s0[4], s0[5]);
    ma = MX3(ma, s0[6], s0[7]); mb = MX3(mb, s0[8], s0[9]);
    ma = MX3(ma, s0[10], s0[11]); mb = MX3(mb, s0[12], s0[13]);
    ma = fmx(MX3(ma, s0[14], s0[15]), mb);
    const f32x2 b = sw32f(ma, ma);
    return fmx(b[0], b[1]);
}
#define EX(i) do { S[i] = __builtin_amdgcn_exp2f(S[i]); SB(); } while (0)
#define EQ(i) do { p1[i] = __builtin_amdgcn_exp2f(S[8 + i]); SB(); } while (0)
typedef __attribute__((ext_vector_type(16))) __bf16 bf16x16_t;
typedef __attribute__((ext_vector_type(8))) __bf16 bf16x8_t;
typedef __attribute__((ext_vector_type(8))) float f32x8;
template <int H> DEV f32x8 half8(const f32x16& s) { return __builtin_shufflevector(s, s, H, H + 1, H + 2, H + 3, H + 4, H + 5, H + 6, H + 7); }
#define SMF32(a, b, c) __builtin_amdgcn_smfmac_f32_32x32x32_bf16(__builtin_bit_cast(bf16x8_t, a), __builtin_bit_cast(bf16x16_t, b), c, hi, 0, 0)
#define SMF16(a, b, c) __builtin_amdgcn_smfmac_f32_16x16x64_bf16(__builtin_bit_cast(bf16x8_t, a), __builtin_bit_cast(bf16x16_t, b), c, hi, 0, 0)
template <class G0, class F1, class F2, class F3>
DEV void block(f32x16& S, f32x16& N, const bf16x8& kf0, const bf16x8& kf1, const bf16x8& q0, const bf16x8& q1, const f32x16& c,
               f32x16& oP, f32x4& lP, const bf16x8& vf1, f32x8& p1, f32x16& oC, f32x4& lC, const bf16x8& vf0,
               const bf16x8& sel, int hi, G0 fb, F1 fc, F2 fd, F3 ff) {
    N = MFMA32(kf0, q0, c); SB();
    EX(0); EX(1); EX(2);
    oP = SMF32(vf1, p1, oP); SB();
    fb(); SB();
    EX(3); EX(4); EX(5);
    lP = SMF16(sel, p1, lP); SB();
    fc(); SB();
    EX(6);
    N = MFMA32(kf1, q1, N); SB();
    fd(); SB();
    EX(7); EQ(0); EQ(1);
    oC = SMF32(vf0, half8<0>(S), oC); SB();
    EQ(2); EQ(3); EQ(4);
    lC = SMF16(sel, half8<0>(S), lC); SB();
    ff(); SB();
    EQ(5); EQ(6); EQ(7);
}
DEV void attn_slow(const char* kh, const char* vh, const bf16x8& q0, const bf16x8& q1, f32x16& o, float& l, int hi) {
    f32x16 c;
#pragma unroll
    for (int i = 0; i < 16; ++i) { o[i] = 0.f; c[i] = 0.f; }
    float m = 0.f;
    l = 0.f;
#pragma unroll 1
    for (int t = 0; t < SEQ / 32; ++t) {
        bf16x8 k0, k1, v0, v1;
        const char* kp = kh + t * 2048;
        const char* vp = vh + t * 2048;
        asm volatile("global_load_dwordx4 %0, %4, off sc1\n\tglobal_load_dwordx4 %1, %5, off sc1\n\tglobal_load_dwordx4 %2, %6, off sc1\n\t"
                     "global_load_dwordx4 %3, %7, off sc1\n\ts_waitcnt vmcnt(0)" : "=&v"(k0), "=&v"(k1), "=&v"(v0), "=&v"(v1)
                     : "v"(kp), "v"((const char*)((uintptr_t)kp ^ 32)), "v"(vp), "v"((const char*)((uintptr_t)vp ^ 32)) : "memory");
        f32x16 s = MFMA32(k0, q0, c);
        s = MFMA32(k1, q1, s);
        const float mx = rowmax32(s);
        const float dl = t == 0 ? mx : fmx(mx, 0.f);
        const float alpha = t == 0 ? 0.f : __builtin_amdgcn_exp2f(-dl);
        m += dl;
        l *= alpha;
#pragma unroll
        for (int i = 0; i < 16; ++i) {
            o[i] *= alpha; c[i] = -m;
            s[i] = __builtin_amdgcn_exp2f(s[i] - dl);
            l += hi16(__float_as_uint(s[i]));
        }
        o = SMF32(v0, half8<0>(s), o);
        o = SMF32(v1, half8<8>(s), o);
    }
    const f32x2 b = sw32f(l, l);
    l = b[0] + b[1];
}

DEV void qb_compute(lchar* smem, u16* kv, WReg& wr, u16* O, const char* fo, unsigned gm, u64* fkv, u64 tag, int g, int hp, int lane, int wid STPARG) {
    OPAQUE(lane, wid, g, hp);
    const int wm = wid >> 1, wn = wid & 1, l15 = lane & 15, lg = lane >> 4, h = hp * 2 + wn;
    bf16x8 qa0, qa1, qb0, qb1;
    lchar* gs = smem + AR0 + wn * 16384;
    {
        f32x4 acc[6][4], acch[2];
        float nm[4], rs[4], csv[2];
        f32x4 cs[4], nmv[4], rsv[4];
        const lchar* sa = smem + SNM + 64 + wm * 256 + l15 * 4;
        const lchar* sb = smem + SNM + 64 + wm * 256 + lg * 16;
        const lchar* sc = smem + SCB + wn * 128;
        const int un = ((lg & 1) << 1) | (lg >> 1);
        const int ko = l15 * 64 + ((un ^ (l15 >> 2)) << 4), vo = l15 * 64 + ((lg ^ (l15 >> 2)) << 4);
        const char* kptr = (const char*)kv + ((size_t)h * SEQ + g * 128 + wm * 64) * 64 + ko;
        const char* vptr = (const char*)kv + ((size_t)(NH + h) * SEQ + g * 128 + wm * 64) * 64 + vo;
        lchar* kl = gs + wm * 8192 + ko;
        lchar* vl = gs + wm * 8192 + 4096 + vo;
        asm volatile("" : "+v"(kptr), "+v"(vptr));
        qkv_main<false>(smem, acc, acch, lane, wm, wn, 1, [](int) {}, [&](int j) {
            if (j < 4) cs[j] = *(const LAS f32x4*)(sc + (j >> 1) * 256 + (j & 1) * 64 + lg * 16);
            else if (j < 8) { nm[j - 4] = *(const LAS float*)(sa + (j - 4) * 64); rs[j - 4] = *(const LAS float*)(sa + (j - 4) * 64 + 576); }
            else if (j < 10) csv[j - 8] = *(const LAS float*)(sc + 512 + (j - 8) * 64 + l15 * 4);
            else if (j < 18) {
                if (j & 1) rsv[(j - 10) >> 1] = *(const LAS f32x4*)(sb + ((j - 10) >> 1) * 64 + 576);
                else nmv[(j - 10) >> 1] = *(const LAS f32x4*)(sb + ((j - 10) >> 1) * 64);
            }
        } STPPASS(0));
        STAMP(2);
        auto tile = [&](int ni, int ti, float sc_) { return pk4((acc[ni][ti] + nm[ti] * cs[ni]) * sc_); };
        auto tilev = [&](int h_, int ti) { return pk4((acc[4 + h_][ti] + nmv[ti] * csv[h_]) * rsv[ti]); };
#define KOUT(ti) do { const u32x4 v = unit16(tile(2, ti, rs[ti]), tile(3, ti, rs[ti])); st16wt<ti * 1024>(kptr, v); *(LAS u32x4*)(kl + ti * 1024) = v; } while (0)
#define VOUT(j) do { const u32x4 v = __builtin_bit_cast(u32x4, cat(tilev(j & 1, j & 2), tilev(j & 1, (j & 2) + 1))); st16wt<j * 1024>(vptr, v); *(LAS u32x4*)(vl + j * 1024) = v; } while (0)
        KOUT(0); KOUT(1); KOUT(2); KOUT(3);
        VOUT(0); VOUT(1); VOUT(2); VOUT(3);
        if (EF & 4) raise(fkv + fi(wn * 32 + wm * 16 + g), tag);
        SB(); BARL(); SB();
#pragma unroll
        for (int ti = 0; ti < 4; ++ti) rs[ti] *= QSCALE * LOG2E;
        qa0 = __builtin_bit_cast(bf16x8, unit16(tile(0, 0, rs[0]), tile(0, 1, rs[1])));
        qa1 = __builtin_bit_cast(bf16x8, unit16(tile(1, 0, rs[0]), tile(1, 1, rs[1])));
        qb0 = __builtin_bit_cast(bf16x8, unit16(tile(0, 2, rs[2]), tile(0, 3, rs[3])));
        qb1 = __builtin_bit_cast(bf16x8, unit16(tile(1, 2, rs[2]), tile(1, 3, rs[3])));
    }
    STAMP(3);
    const int r = lane & 31, hh = lane >> 5;
    const int kro = ((r & 4) * 4 + (r >> 3) * 4 + (r & 3)) * 64 + ((hh ^ (r >> 3)) << 4), vro = r * 64 + ((hh ^ ((r >> 2) & 3)) << 4);
    const lchar* kb0 = gs + kro;
    const lchar* kb1 = gs + (kro ^ 32);
    const lchar* vb0 = gs + 4096 + vro;
    const lchar* vb1 = gs + 4096 + (vro ^ 32);
    const short se = (l15 & 1) ? (short)0 : (short)0x3f80, so = se ^ 0x3f80;
    const bf16x8 sel = {se, se, se, se, so, so, so, so};
    int hi = 0xdddd;
    const char* op = (const char*)O + ((size_t)wn * SEQ + g * 128 + wm * 64 + l15) * 64 + (((((lg & 1) << 1) | (lg >> 1)) ^ fz(l15)) << 4);
    const lchar* wb0 = wo_wb(smem, lane, wn * 2);
    const lchar* wb1 = wo_wb(smem, lane, wn * 2 + 1);
    unsigned gof = go_off(g, lane, wm, wn);
    lchar* ol = smem + 65536 + wn * 8192 + wm * 4096 + l15 * 64 + (((((lg & 1) << 1) | (lg >> 1)) ^ fz(l15)) << 4);
    asm volatile("" : "+v"(hi), "+v"(op), "+v"(wb0), "+v"(wb1), "+v"(ol), "+v"(gof));
    f32x16 oA, oB, cA, cB, sA, sB;
    f32x4 lA = {0.f, 0.f, 0.f, 0.f}, lB = {0.f, 0.f, 0.f, 0.f};
#pragma unroll
    for (int i = 0; i < 16; ++i) { oA[i] = 0.f; oB[i] = 0.f; }
    bf16x8 kf0, kf1, vf0, vf1[2];
    f32x8 p1 = {0.f, 0.f, 0.f, 0.f, 0.f, 0.f, 0.f, 0.f};
    auto nop = [&]() {};
    auto tilestep = [&](int par, int nx, bool bar) {
        block(sA, sB, kf0, kf1, qb0, qb1, cB, oB, lB, vf1[par ^ 1], p1, oA, lA, vf0, sel, hi, [&]() { if (bar) BAR(); },
              [&]() { kf0 = ldk(kb0 + nx); }, [&]() { __builtin_amdgcn_s_waitcnt(0xc17f); }, [&]() { LGKM0(); SB(); kf1 = ldk(kb1 + nx); });
        block(sB, sA, kf0, kf1, qa0, qa1, cA, oA, lA, vf1[par], p1, oB, lB, vf0, sel, hi, nop,
              [&]() { LGKM0(); SB(); vf1[par ^ 1] = ldk(vb1 + nx); }, nop, [&]() { vf0 = ldk(vb0 + nx); });
    };
    {
        const int own = wm * 8192, oth = own ^ 8192;
        kf0 = ldk(kb0 + own); kf1 = ldk(kb1 + own);
        vf0 = ldk(vb0 + own); vf1[0] = ldk(vb1 + own); vf1[1] = sel;
        asm volatile("" : "+v"(qa0), "+v"(qa1), "+v"(qb0), "+v"(qb1)); SB();
        if (!(EF & 4)) publish(fkv + fi(wn * 32 + wm * 16 + g), lane, tag);
        SB();
        sA = MFMA32(kf0, qa0, oA); sB = MFMA32(kf0, qb0, oA);
        sA = MFMA32(kf1, qa1, sA); sB = MFMA32(kf1, qb1, sB);
        const float mA = rowmax32(sA), mB = rowmax32(sB);
#pragma unroll
        for (int i = 0; i < 16; ++i) { cA[i] = -mA; cB[i] = -mB; sA[i] -= mA; }
        asm volatile("" : "+v"(cA), "+v"(cB));
        SB();
        STAMP(5);
        tilestep(0, own + 2048, false);
        tilestep(1, oth, false);
        tilestep(0, oth + 2048, false);
        tilestep(1, AR1 + 8192, true);
        STAMP(6);
    }
#pragma unroll 1
    for (int it = 0; it < 5; ++it) {
#pragma unroll
        for (int s = 0; s < 3; ++s) {
            const int cur = s == 0 ? AR1 : s == 1 ? AR2 : 0, nxt = s == 0 ? AR2 : s == 1 ? 0 : AR1;
            tilestep(0, cur + 10240, false);
            tilestep(1, cur, false);
            tilestep(0, cur + 2048, false);
            tilestep(1, nxt + 8192, true);
        }
    }
    oB = SMF32(vf1[1], p1, oB);
    lB = SMF16(sel, p1, lB);
    SB();
    STAMP(9);
    TSD(); TS(0);
    int wj = 0;
    auto fin = [&](const f32x16& o, float l, u32x4& u0, u32x4& u1, bool rd) {
        const float inv = __builtin_amdgcn_rcpf(l);
        unsigned y[8];
        if (rd) { WRD1(); WRD1(); }
#pragma unroll
        for (int i = 0; i < 8; ++i) {
            const f32x2 t = (f32x2){o[2 * i], o[2 * i + 1]} * inv;
            if (rd) WRD1();
            y[i] = cvtpk(t[0], t[1]);
        }
        const u32x2 a = sw32(y[0], y[2]), b = sw32(y[1], y[3]);
        if (rd) WRD1();
        const u32x2 c = sw32(y[4], y[6]), d = sw32(y[5], y[7]);
        if (rd) WRD1();
        const u32x2 e = sw16(a[0], c[0]), f = sw16(b[0], d[0]);
        if (rd) WRD1();
        const u32x2 i = sw16(a[1], c[1]), j = sw16(b[1], d[1]);
        if (rd) WRD1();
        u0 = (u32x4){e[0], f[0], i[0], j[0]};
        u1 = (u32x4){e[1], f[1], i[1], j[1]};
    };
    if (EB == 4) { SB(); go(fo, gof, gm, tag); SB(); }
    u32x4 u0, u1;
    float la = (lane & 16) ? lA[1] : lA[0];
#pragma unroll
    for (int i = 0; i < WB; ++i) WRD1();
    fin(oA, la, u0, u1, false);
    float lb = (lane & 16) ? lB[1] : lB[0];
    if (__builtin_expect(__any(!(la <= 1073741824.0f) || !(lb <= 1073741824.0f)), 0)) {
        const char* kh = (const char*)kv + (size_t)h * SEQ * 64;
#pragma unroll 1
        for (int ps = 0; ps < 2; ++ps) {
            fin(oB, lb, u0, u1, false);
            attn_slow(kh + kro, kh + (size_t)NH * SEQ * 64 + vro, ps ? qb0 : qa0, ps ? qb1 : qa1, oB, lb, hi);
        }
    }
    TS(1);
    if (EB == 3) { SB(); go(fo, gof, gm, tag); SB(); }
    st16wt<0>(op, u0); TS(2); st16wt<1024>(op, u1);
    if (EB == 2) { SB(); go(fo, gof, gm, tag); SB(); }
    SB(); LGKM0(); BAR(); SB();
    if (EB == 1) { go(fo, gof, gm, tag); SB(); }
    *(LAS u32x4*)ol = u0; *(LAS u32x4*)(ol + 1024) = u1;
    SB();
    TS(3);
    fin(oB, lb, u0, u1, false);
    TS(4);
    st16wt<2048>(op, u0); TS(5); st16wt<3072>(op, u1);
    TS(6);
    if (EB == 0) go(fo, gof, gm, tag);
    SM(0, 2); SMD();
    *(LAS u32x4*)(ol + 2048) = u0; *(LAS u32x4*)(ol + 3072) = u1;
    TS(7);
    STAMP(10);
#pragma unroll
    for (int i = 0; i < 56; ++i) if (wj < 56) WRD1();
    TS(8);
    TS(9);
    TSW(10);
}

constexpr u64 kinv() { u64 r = 0; for (int s = 0; s < 16; ++s) r |= (u64)s << (4 * ((KORD >> (4 * s)) & 15)); return r; }
DEV constexpr int wo_unit(int t) { return t >= 7 && t < 10 ? t + 9 : t; }
DEV unsigned wo_voff(int lane, int cb, int i) {
    const int k = 8 * i + (lane >> 3), pc = lane & 7;
    return (k * 1024 + cb * 64 + ((((pc >> 1) ^ fswz(k)) << 1) + (pc & 1)) * 8) * 2 - i * 1024;
}
DEV void wo_compute(lchar* smem, u32x4 (&rr)[4], WReg& wr, u16* C, bool stats, const char* fx, unsigned gm, int nl, u64 tag, int g, int cb, int lane,
                    int wid STPARG) {
    OPAQUE(lane, wid, g, cb);
    const int wm = wid >> 1, wn = wid & 1, l15 = lane & 15, lg = lane >> 4;
    const lchar* wb0 = wo_wb(smem + WQ0, lane, wn * 2);
    const lchar* wb1 = wo_wb(smem + WQ0, lane, wn * 2 + 1);
    asm volatile("" : "+v"(wb0), "+v"(wb1));
    bf16x8 xf[2][8];
    s16x4 wh[16][8];
    auto rdw = [&](int t, int j) { wh[t][j] = ldtr4(((j & 2) ? wb1 : wb0) + wo_unit(t) * 8192 - WQ0 + (j >> 2) * 4096 + (j & 1) * 512); };
#pragma unroll
    for (int j = 0; j < NRW * 8; ++j) wh[j >> 3][j & 7] = wr[j];
    asm volatile("s_cmp_gt_u32 %0, 1\n\ts_waitcnt vmcnt(5)\n\ts_cbranch_scc0 1f\n\ts_waitcnt vmcnt(4)\n1:" ::"s"(cb) : "memory", "scc");
    STAMP(5);
    STAMPW(6 + wid);
    asm volatile("" : "+v"(lane), "+v"(rr[0]), "+v"(rr[1]), "+v"(rr[2]), "+v"(rr[3]));
    const int un = ((lg & 1) << 1) | (lg >> 1), row = g * 128 + wm * 64 + l15, ch = cb * 2 + wn;
    const unsigned ocm = (ch * SEQ + row) * 64 + ((un ^ fz(l15)) << 4), orm = row * 2048 + ch * 64 + un * 16;
    const char* cp = (const char*)C + (stats ? ocm : orm);
    const unsigned cs = stats ? 1024 : 32768;
    const lchar* xb = smem + (wm * 64 + l15) * 64 + ((lg ^ fz(l15)) << 4);
    lchar* xl = smem + wn * 8192 + (wn + 1) * nl + (wm * 64 + l15) * 64 + ((un ^ fz(l15)) << 4);
    unsigned gof = go_off(g, lane, wm, wn);
    gm *= !stats ? 0 : (nl && wm && g < 15) ? 0x10001 : 1;
    asm volatile("" : "+v"(cp), "+v"(xb), "+v"(xl), "+v"(gof));
    int late = stats ? cb : 0;
    asm volatile("" : "+s"(gm), "+s"(late));
    f32x4 acc[2][4];
#pragma unroll
    for (int ti = 0; ti < 4; ++ti) {
        const u32x2 x = sw16(rr[ti][0], rr[ti][2]), y = sw16(rr[ti][1], rr[ti][3]);
#pragma unroll
        for (int ni = 0; ni < 2; ++ni) acc[ni][ti] = (f32x4){lo16(x[ni]), hi16(x[ni]), lo16(y[ni]), hi16(y[ni])};
    }
    auto rdx = [&](int t, int j) { xf[t & 1][j] = ldk(xb + ((t + 4) % 5) * 16384 + (j >> 2) * 8192 + (j & 3) * 1024); };
    auto mm = [&](int t, int kk, int j) {
        const bool alt = MW && !(t == 15 && kk == 1);
        const int ni = !alt ? (MW ? ((j >> 1) ^ j) & 1 : j & 1) : MW == 1 ? j >> 2 : ((j >> 1) ^ j) & 1;
        const int ti = (!alt || MW == 2) ? j >> 1 : ni ? 7 - j : j;
        mfma16t(acc[ni][ti], cat8(wh[t][kk * 4 + ni * 2], wh[t][kk * 4 + ni * 2 + 1]), xf[t & 1][kk * 4 + ti]);
    };
    SB();
    LGKM0(); SB();
#pragma unroll
    for (int t = 0; t < 16; ++t) {
        BAR(); SB();
        if (t == 1) SM(0, 4);
        if (EXP && t == 16 - EXP) { go(fx, gof, gm, tag); SB(); }
        if (t == 0) {
            STAMP(4);
#pragma unroll
            for (int j = 0; j < 8; ++j) rdx(0, j);
            SB();
        } else {
#pragma unroll
            for (int j = 0; j < 8; ++j) { rdx(t, j); SB(); mm(t - 1, 1, j); SB(); }
        }
        const bool more = t + 1 >= NRW && t < 15;
        if (WS) {
            __builtin_amdgcn_s_waitcnt(0xc47f); SB();
#pragma unroll
            for (int j = 0; j < 8; ++j) {
                if (more && (WS == 1 || j)) { rdw(t + 1, WS == 1 ? j : j - 1); SB(); }
                if (more && WS == 2 && j == 7) { rdw(t + 1, 7); SB(); }
                mm(t, 0, j); SB();
            }
        } else
#pragma unroll
        for (int j = 0; j < 8; ++j) { mm(t, 0, j); SB(); if (more) { rdw(t + 1, j); SB(); } }
        if (more) __builtin_amdgcn_s_waitcnt(0xc87f); else LGKM0();
        SB();
    }
    u32x4 (&ov)[4] = rr;
    auto out = [&](int ti) {
        asm volatile("" : "+v"(acc[0][ti]), "+v"(acc[1][ti]));
        ov[ti] = unit16(pk4(acc[0][ti]), pk4(acc[1][ti]));
        asm volatile("s_cmp_ge_u32 %2, %3\n\ts_cbranch_scc1 1f\n\tglobal_store_dwordx4 %0, %1, off sc1\n\ts_nop 1\n1:" ::"v"(cp + ti * cs), "v"(ov[ti]), "s"(late), "n"(PE) : "memory", "scc");
        SB();
    };
#pragma unroll
    for (int j = 0; j < 6; ++j) mm(15, 1, j);
    SB();
    out(0);
    mm(15, 1, 6); mm(15, 1, 7); SB();
    out(1); out(2); out(3);
    STAMP(2);
    if (stats) {
        if (!EXP) go(fx, gof, gm, tag);
        SM(0, 2); SMD(); SB();
#pragma unroll
        for (int ti = 0; ti < 4; ++ti) *(LAS u32x4*)(xl + ti * 1024) = ov[ti];
        SB(); BARL(); SB();
        STAMP(10);
        STAMP(11);
        STAMPW(12 + wid);
    } else {
        STAMP(10);
    }
}

DEV unsigned ln0_stats(const u32x4 (&x)[4]) {
    const bf16x2_t one2 = __builtin_bit_cast(bf16x2_t, 0x3f803f80u);
    float s[2] = {0.f, 0.f}, q[2] = {0.f, 0.f};
#pragma unroll
    for (int i = 0; i < 16; ++i) {
        const unsigned u = x[i >> 2][i & 3];
        const bf16x2_t v = __builtin_bit_cast(bf16x2_t, u);
        s[i & 1] = __builtin_amdgcn_fdot2_f32_bf16(v, one2, s[i & 1], false);
        q[i & 1] = __builtin_amdgcn_fdot2_f32_bf16(v, v, q[i & 1], false);
    }
    const f32x2 a = sw32f(s[0] + s[1], q[0] + q[1]);
    float v = dppadd<0x128>(dppadd<0x4E>(dppadd<0xB1>(a[0] + a[1])));
    const f32x2 b = sw16f(v, v);
    v = (b[0] + b[1]) * (1.0f / DM);
    const f32x2 c = sw32f(v, v);
    return cvtpk(c[0], __builtin_amdgcn_sqrtf(bfr(bfr(fmx(c[1] - c[0] * c[0], 0.f)) + 1e-5f)));
}
typedef f32x2 Raw[4];
DEV void unpk(Raw& v, u32x4 x) {
#pragma unroll
    for (int i = 0; i < 4; ++i) { v[i] = (f32x2){lo16(x[i]), hi16(x[i])}; asm volatile("" : "+v"(v[i])); }
}
DEV u32x4 ln0_apply(const Raw& v, f32x2 st) {
    u32x4 x;
#pragma unroll
    for (int i = 0; i < 4; ++i) {
        const f32x2 e = v[i] - st[0];
        f32x2 d;
        asm("v_cvt_pk_bf16_f32 %0, 0, %1" : "=v"(d[0]) : "v"(e[0]));
        asm("v_cvt_pk_bf16_f32 %0, 0, %1" : "=v"(d[1]) : "v"(e[1]));
        d *= st[1];
        x[i] = cvtpk(d[0], d[1]);
    }
    return x;
}

struct HW { unsigned wq[2][3], wo[4], vo16, lds, po, px, er, ec; int lw, pos; u64 kord; LAS float* st; const u64* go; const void* out; };
typedef u32x4 Cols[24];
template <int N> DEV void wq_issue(const HW& h, const u16* W, int t, int at, int r0) {
    const char* src = (const char*)W + (unsigned)(at * 8 + r0) * (8 * 6144);
    const unsigned l = h.lds + WQ0 + ((t + 2) % 3) * WQS + r0 * 3072;
#pragma unroll
    for (int i = 0; i < N; ++i) { glds3(src, l + i * 3072, h.wq[i & 1][0], h.wq[i & 1][1], h.wq[i & 1][2]); src += 8 * 6144; }
}
DEV void wo_issue(const HW& h, const u16* W, int n, int lim, int a, int b) {
#pragma unroll 1
    for (int j = h.lw; j < n; j += 4) {
        const int i = j + (j < lim ? a : b);
        glds4((const char*)W + (((unsigned)(h.kord >> (2 * (i & ~1))) & 15) * 2 + (i & 1)) * (32 * 2048), h.lds + wo_unit(i >> 1) * 8192 + (i & 1) * 4096, h.wo[0], h.wo[1], h.wo[2], h.wo[3]);
    }
}
DEV void fill(const void* sb, unsigned vo) {
    asm volatile("s_nop 4\n\tglobal_store_dwordx4 %0, %1, %2 sc1\n\tglobal_store_dwordx4 %0, %1, %2 offset:1024 sc1\n\t"
                 "global_store_dwordx4 %0, %1, %2 offset:2048 sc1\n\tglobal_store_dwordx4 %0, %1, %2 offset:3072 sc1\n\ts_nop 1"
                 ::"v"(vo), "v"((u32x4){~0u, ~0u, ~0u, ~0u}), "s"(sb) : "memory");
}
DEV void filled(const HW& h, const u64* fe, u64 tag) {
    asm volatile("s_mov_b64 exec, 3\n\ts_nop 3\n\tglobal_atomic_swap_x2 %0, %1, %2 sc1\n\ts_mov_b64 exec, -1\n\ts_nop 0" ::"v"(h.er), "v"(tag), "s"(fe) : "memory");
}
DEV u64 eload(const HW& h, const u64* fe) {
    u64 e;
    asm volatile("s_nop 4\n\tglobal_load_dwordx2 %0, %1, %2 sc1" : "=&v"(e) : "v"(h.ec), "s"(fe) : "memory");
    return e;
}
template <int N> DEV void echeck(const HW& h, const u64* fe, u64 e, u64 tag) {
    unsigned bud = POLLMAX;
    asm volatile("s_waitcnt vmcnt(%6)\n1:\n\tv_cmp_ne_u64 vcc, %5, %0\n\ts_sub_u32 %1, %1, 1\n\ts_cbranch_vccz 2f\n\ts_cbranch_scc1 2f\n\ts_sleep 1\n\t"
                 "global_atomic_add_x2 %0, %2, %3, %4 sc0 sc1\n\ts_waitcnt vmcnt(0)\n\ts_branch 1b\n2:"
                 : "+v"(e), "+s"(bud) : "v"(h.ec), "v"(0ull), "s"(fe), "s"(tag), "n"(N) : "memory", "scc", "vcc");
}
#define GL "global_load_lds_dwordx4 "
#define GL4(a, b, s) GL a ", " s " sc1\n\t" GL b ", " s " offset:1024 sc1\n\t" GL b ", " s " offset:2048 sc1\n\t" GL b ", " s " offset:3072 sc1\n\t"
#define XRD(c) "s_setprio 2\n\tds_read2st64_b32 v[240:241], " c " offset1:4\n\tds_read2st64_b32 v[242:243], " c " offset0:8 offset1:12\n\t" \
    "ds_read2st64_b32 v[244:245], " c " offset0:16 offset1:20\n\tds_read2st64_b32 v[246:247], " c " offset0:24 offset1:28\n\t" \
    ".if %[L]\n\tds_read_b32 v248, " c " offset:8192\n\t.endif\n\t"
#define XCMP "s_waitcnt lgkmcnt(0)\n\tv_max3_u32 v240, v240, v241, v242\n\tv_max3_u32 v243, v243, v244, v245\n\tv_max3_u32 v240, v240, v246, v247\n\t" \
    ".if %[L]\n\tv_max3_u32 v240, v240, v243, v248\n\t.else\n\tv_max_u32 v240, v240, v243\n\t.endif\n\tv_cmp_eq_u32 %[bad], -1, v240\n\ts_setprio 0"
#define XCLOB "memory", "v240", "v241", "v242", "v243", "v244", "v245", "v246", "v247", "v248"
template <bool L> DEV bool stale(unsigned c) {
    u64 bad;
    asm volatile(XRD("%[c]") XCMP : [bad] "=s"(bad) : [c] "v"(c), [L] "n"((int)L) : XCLOB);
    return bad != 0;
}
template <bool L, bool CK> DEV bool xstep(const HW& h, const void* sw, unsigned lw, const void* sx, unsigned lx, unsigned vo0, unsigned c, int wt, int t) {
    u64 bad;
    asm volatile("s_mov_b32 m0, %[lw]\n\t.if %[CK]\n\ts_nop 1\n\t.else\n\ts_cmp_eq_u32 %[wt], 0\n\ts_cbranch_scc1 5f\n\ts_waitcnt vmcnt(11+%[L])\n5:\n\t.endif\n\t"
                 "s_cmp_eq_u32 %[t], %[B1]\n\ts_cbranch_scc1 6f\n\ts_barrier\n6:\n\t" GL "%[w0], %[sw] sc1\n\t" GL "%[w1], %[sw] offset:1024 sc1\n\t" GL "%[w2], %[sw] offset:2048 sc1\n\t"
                 "s_mov_b32 m0, %[lx]\n\ts_nop 0\n\t" GL4("%[v0]", "%[va]", "%[sx]")
                 ".if %[CK]\n\ts_waitcnt vmcnt(7)\n\t" XRD("%[c]") ".endif\n\t"
                 "s_add_u32 m0, %[lx], 4096\n\ts_nop 0\n\t" GL4("%[vb]", "%[vb]", "%[sx]")
                 ".if %[L]\n\ts_add_u32 m0, %[lx], 8192\n\ts_nop 0\n\t" GL "%[vc], %[sx] sc1\n\t.endif\n\t"
                 ".if %[CK]\n\t" XCMP "\n\t.endif"
                 : [bad] "=s"(bad)
                 : [lw] "s"(lw), [sw] "s"(sw), [w0] "v"(h.wq[0][0]), [w1] "v"(h.wq[0][1]), [w2] "v"(h.wq[0][2]), [lx] "s"(lx), [sx] "s"(sx), [v0] "v"(vo0),
                   [va] "v"(h.vo16), [vb] "v"(h.vo16 + 4096), [vc] "v"(h.vo16 + 8192), [c] "v"(c), [L] "n"((int)L), [CK] "n"((int)CK), [wt] "s"(wt), [t] "s"(t), [B1] "n"(QB ? 3 : 0) : XCLOB, "scc");
    return CK && bad != 0;
}
DEV void warm(const HW& h, const u16* Wo, int lane) {
    asm volatile("s_mov_b32 m0, %2\n\ts_nop 4\n\tglobal_load_lds_dword %0, %1\n\tglobal_load_lds_dword %3, %1" ::"v"(lane * 64),
                 "s"(Wo + (size_t)blockIdx.x * 4096), "s"(h.lds + DUMP), "v"(lane * 64 + 4096) : "memory");
}
template <int ST> DEV void ld8(f32x4 (&d)[8], const char* sb, unsigned vo) {
    asm volatile("s_nop 4\n\tglobal_load_dwordx4 %0, %8, %9 offset:%10 sc1\n\tglobal_load_dwordx4 %1, %8, %9 offset:%11 sc1\n\t"
                 "global_load_dwordx4 %2, %8, %9 offset:%12 sc1\n\tglobal_load_dwordx4 %3, %8, %9 offset:%13 sc1\n\t"
                 "global_load_dwordx4 %4, %8, %9 sc1\n\tglobal_load_dwordx4 %5, %8, %9 offset:%14 sc1\n\t"
                 "global_load_dwordx4 %6, %8, %9 offset:%15 sc1\n\tglobal_load_dwordx4 %7, %8, %9 offset:%16 sc1"
                 : "=&v"(d[0]), "=&v"(d[1]), "=&v"(d[2]), "=&v"(d[3]), "=&v"(d[4]), "=&v"(d[5]), "=&v"(d[6]), "=&v"(d[7])
                 : "v"(vo), "s"(sb + 4 * ST), "n"(-4 * ST), "n"(-3 * ST), "n"(-2 * ST), "n"(-ST), "n"(ST), "n"(2 * ST), "n"(3 * ST) : "memory");
}

template <class F> DEV void colsum(Cols& d, const void* out, int lane, F next) {
    const char* dst = (const char*)out + (lane & 7) * 32 + (lane >> 4) * 16;
    bf16x8 sel;
#pragma unroll
    for (int e = 0; e < 8; ++e) sel[e] = (lane & 7) == e ? (short)0x3f80 : (short)0;
#pragma unroll
    for (int i = 0; i < 24; ++i) asm volatile("" : "+v"(d[i]));
    f32x4 c;
#pragma unroll
    for (int k = 0; k < 6; ++k) {
        if (!(k & 1)) c = (f32x4){0.f, 0.f, 0.f, 0.f};
#pragma unroll
        for (int i = 0; i < 4; ++i) c = MFMA16(sel, __builtin_bit_cast(bf16x8, d[k * 4 + i]), c);
        if (k & 1) {
#pragma unroll
            for (int e = 0; e < 4; ++e) c[e] = dppadd<0x128>(c[e]);
            if (lane < 32 && !(lane & 8)) st16wt<0>(dst + (k >> 1) * 256, __builtin_bit_cast(u32x4, c));
        }
        SB();
        next(k);
    }
}

template <bool LOCAL>
DEV void qkv_helper(lchar* smem, HW& h, Cols& cw, char* ws, int layer, const u16* X, const u16* W, const u16* W2, const u16* Wo, const char* stats, float* csum,
                    u64* fl, u64 tag, int g, int hp, int lane, int lw STPARG) {
    OPAQUE(lane, lw, g, hp);
    constexpr int NX = LOCAL ? 9 : 8, HO = LOCAL ? 16 : 0;
    const int m0 = g * 128;
    const bool halo = LOCAL && g > 0;
    const int ps = (LOCAL && layer == 0) ? 0 : h.pos;
    auto at = [&](int t) { return t - 1 + (t > ps); };
    auto first = [&](int r0) {
        BAR();
#pragma unroll 1
        for (int t = 0; t < 3; ++t) {
            wq_issue<4>(h, W, t, t, r0);
            if (t == WN - 1) BAR();
        }
    };
    auto colld = [&](Cols& d, const u16* M, int i0 = 0, int i1 = 8) {
        const unsigned vo = ((g * 64 + (lane >> 3)) * 3072 + (hp ^ CJ) * 64 + (lane & 7) * 8) * 2 + 2048;
#pragma unroll
        for (int i = i0; i < i1; ++i)
            asm volatile("s_nop 4\n\tglobal_load_dwordx4 %0, %3, %4 offset:-2048\n\tglobal_load_dwordx4 %1, %3, %4\n\t"
                         "global_load_dwordx4 %2, %3, %4 offset:2048" : "=&v"(d[i]), "=&v"(d[8 + i]), "=&v"(d[16 + i])
                         : "v"(vo), "s"((const char*)M + i * (8 * 6144)) : "memory");
    };
    f32x4 sp[4];
    float hs, hq;
    auto any4 = [](f32x4 (&d)[4]) { asm volatile("" : "=v"(d[0]), "=v"(d[1]), "=v"(d[2]), "=v"(d[3])); };
    const unsigned so = (layer - 1) * (unsigned)(MB / 2) + g * 16384 + (lane >> 4) * 4096 + (lane & 15) * 16;
    auto ld4 = [&](f32x4 (&d)[4], unsigned vo, int a, int b) {
        asm volatile("s_cmp_lg_u32 %6, %7\n\ts_cbranch_scc1 1f\n\tglobal_load_dwordx4 %0, %4, %5 sc1\n\tglobal_load_dwordx4 %1, %4, %5 offset:1024 sc1\n\t"
                     "global_load_dwordx4 %2, %4, %5 offset:2048 sc1\n\tglobal_load_dwordx4 %3, %4, %5 offset:3072 sc1\n1:"
                     : "+v"(d[0]), "+v"(d[1]), "+v"(d[2]), "+v"(d[3]) : "v"(vo), "s"(stats), "s"(a), "s"(b) : "memory", "scc");
    };
    auto add1 = [](f32x4& a, const f32x4& d, int e0) {
#pragma unroll
        for (int e = e0; e < 4; ++e) asm volatile("v_add_f32 %0, %0, %1" : "+v"(a[e]) : "v"(d[e]));
    };
    auto cols = [&](int src, int dst, auto next) {
        f32x4 sc[2][8];
        const unsigned vc = hp * (16 * 768) + (lane < 48 ? lane * 16 : 0);
        ld8<768>(sc[0], (const char*)csum + src, vc);
        next(0);
        ld8<768>(sc[1], (const char*)csum + src + 6144, vc);
        next(1);
        WAITV(8);
#pragma unroll
        for (int i = 1; i < 8; ++i) add1(sc[0][0], sc[0][i], 0);
        next(2);
        WAITV(0);
#pragma unroll
        for (int i = 0; i < 8; ++i) add1(sc[0][0], sc[1][i], 0);
        if (lane < 48) *(LAS f32x4*)(smem + dst + lane * 16) = sc[0][0];
        SB();
    };
    auto sum4 = [&]() {
#pragma unroll
        for (int i = 1; i < 4; ++i) add1(sp[0], sp[i], 0);
    };
    unsigned vo0 = (LOCAL && g == 0) ? h.vo16 + 1024 : h.vo16;
    const char* xsrc = (const char*)X + ((size_t)lw * SEQ + m0 - HO) * 64;
    unsigned ldx = h.lds + lw * (NX * 1024), xv = ldx + lane * 16 + (lane >> 4) * 4;
    auto fetch = [&](const char* p, int so) {
        glds4(p, ldx + so, vo0, h.vo16, h.vo16, h.vo16);
        glds4(p + 4096, ldx + so + 4096, h.vo16, h.vo16, h.vo16, h.vo16);
        if (LOCAL) glds1(p + 8192, ldx + so + 8192, h.vo16);
    };
    u64 v, v2;
    asm volatile("" : "=v"(v), "=v"(v2));
    if (lw < 2) {
        STAMP(0);
        const char* p1 = xsrc + (unsigned)at(1) * (2 * SEQ * 64);
        const char* p2 = xsrc + (unsigned)at(2) * (2 * SEQ * 64);
        asm volatile("" : "+s"(p1), "+s"(p2));
        if (LOCAL && layer == 0) {
            const u64* f0 = (const u64*)(ws + WS_L + (g * 16 + hp) * 256) + (lane & 31);
            asm volatile("" : "+v"(f0));
            BAR();
            if (lw == 1) STAMPW(5);
            v = ~tag;
            poll2<0>(f0, halo ? 0x3ffff : 0xffff, tag, v, v2);
            fetch(xsrc, 0);
        } else poll2<7>(h.go + (layer * 2 - 1) * 16 + lw * 8 + (lane & 3), halo ? 7 : 3, tag, v, v2);
        STAMP(1);
        if (lw == 1) STAMPW(11);
        fetch(p1, XS);
        SM(4, 1);
        fetch(p2, 2 * XS);
    } else if (!LOCAL || layer) {
        WAITB(6);
        wq_issue<4>(h, W, 2, at(2), lw * 4 - 8);
        SM(6, 15);
        WAITV(12);
    }
    if (!LOCAL || layer) {
        const unsigned fo = (lane & 15) * 64 + (((lane >> 4) ^ fz(lane & 15)) << 4);
        const lchar* xs = smem + HO * 64 + lw * 2048 + fo;
        bf16x8 y[4];
#pragma unroll
        for (int i = 0; i < 4; ++i) y[i] = ldk(xs + (i & 1) * (NX * 1024) + (i >> 1) * 1024);
        const bf16x8 ones = {0x3f80, 0x3f80, 0x3f80, 0x3f80, 0x3f80, 0x3f80, 0x3f80, 0x3f80};
        f32x4 a[4] = {};
#pragma unroll
        for (int i = 0; i < 8; ++i) {
            const bf16x8& x = y[(i & 2) + (i >> 2)];
            a[i & 3] = MFMA16((i & 1) ? x : ones, x, a[i & 3]);
        }
        SB();
        if (PE < 16)
        asm volatile("s_cmp_lt_u32 %7, %8\n\ts_cbranch_scc1 1f\n\tglobal_store_dwordx4 %0, %1, %5 sc1\n\tglobal_store_dwordx4 %0, %2, %6 sc1\n\t"
                     "global_store_dwordx4 %0, %3, %5 offset:1024 sc1\n\tglobal_store_dwordx4 %0, %4, %6 offset:1024 sc1\n\ts_nop 1\n1:"
                     ::"v"(h.px - h.vo16 + fo), "v"(y[0]), "v"(y[1]), "v"(y[2]), "v"(y[3]), "s"((const char*)X + lw * 2048), "s"((const char*)X + lw * 2048 + SEQ * 64),
                     "s"(h.pos), "n"(PE) : "memory", "scc");
        auto diag = [&](const f32x4& d) { return (lane & 2) ? ((lane & 1) ? d[3] : d[2]) : ((lane & 1) ? d[1] : d[0]); };
        asm volatile("s_mov_b64 exec, %2\n\tglobal_store_dwordx4 %0, %1, %3 sc1\n\ts_mov_b64 exec, -1\n\ts_nop 0"
                     ::"v"((layer - 1) * (unsigned)(MB / 2) + (g * 16 + h.pos) * 1024 + lw * 256 + (lane & 15) * 16), "v"((f32x4){a[0][0], diag(a[1]), a[2][0], diag(a[3])}),
                     "s"(0xf0000f0000f0000full), "s"(stats) : "memory");
        SM(6, 14);
        __builtin_amdgcn_s_setprio(HPQ);
    }
    if (lw < 2) {
        int bud = POLLMAX;
        auto redo = [&](int t, int so) {
#pragma unroll 1
            do { --bud; fetch(xsrc + (unsigned)at(t) * (2 * SEQ * 64), so); WAITV(0); } while (stale<LOCAL>(xv + so) && bud > 0);
        };
        unsigned x1 = xv + XS;
        asm volatile("" : "+v"(x1));
        if (LOCAL && layer == 0) { WAITB(2 * NX); WAITV(NX); }
        asm volatile("s_cmp_ge_u32 %0, %1\n\ts_waitcnt vmcnt(%2+5)\n\ts_cbranch_scc1 1f\n\ts_waitcnt vmcnt(%2+1)\n1:" ::"s"(h.pos), "n"(PE), "n"(NX) : "memory", "scc");
        asm volatile("" ::"v"(v), "v"(v2));
        SM(5, 13);
        if (__builtin_expect(stale<LOCAL>(x1), 0)) redo(1, XS);
        if (QB) { BAR(); SB(); }
        const char* wsrc = (const char*)W + (unsigned)lw * (8 * 6144);
        const unsigned ldw = h.lds + WQ0 + lw * 3072;
        int xo = 0, sv = 2 * XS, sw = 2 * WQS, t = 3;
        auto next = [&]() {
            sv = xo;
            xo = xo == 2 * XS ? 0 : xo + XS;
            sw = sw == 2 * WQS ? 0 : sw + WQS;
        };
#pragma unroll 1
        for (; t <= (K0 && LOCAL && layer == 0 ? 0 : NCK); ++t) {
            const unsigned a = at(t);
            if (__builtin_expect((xstep<LOCAL, true>(h, wsrc + a * (64 * 6144), ldw + sw, xsrc + a * (2 * SEQ * 64), ldx + xo, vo0, xv + sv, 0, t)), 0)) redo(t - 1, sv);
            next();
        }
        any4(sp);
        ld4(sp, so + lw * 256, t, NCK + 1);
        const int t0 = t;
#pragma unroll 1
        for (; t < 16; ++t) {
            const unsigned a = at(t);
            xstep<LOCAL, false>(h, wsrc + a * (64 * 6144), ldw + sw, xsrc + a * (2 * SEQ * 64), ldx + xo, vo0, 0, t - t0, t);
            next();
        }
        if (LOCAL) glds1(xsrc + (unsigned)ps * (2 * SEQ * 64), h.lds + HXO + lw * 1024, vo0);
        WAITB(NX + 3 + LOCAL);
        sum4();
        asm volatile("" : "=v"(hs), "=v"(hq));
    } else if (lw == 2) {
        if (LOCAL && layer == 0) {
            first(0);
            STAMPW(6);
            WAITB(24);
            WAITV(12);
        }
        { HT0(); HT1(0); }
        BAR();
        SM(6, 4);
        any4(sp);
#pragma unroll 1
        for (int t = 3; t < 16; ++t) {
            HT0();
            wq_issue<6>(h, W, t, at(t), 2);
            HT1(t - 2);
            WAITV(18);
            ld4(sp, so + 512, t, layer ? NCK + L2 : 0);
            BAR();
        }
        sum4();
        asm volatile("" : "=v"(hs), "=v"(hq));
    } else {
        if (LOCAL && layer == 0) {
            first(4);
            Cols c0;
            WAITB(24);
            u64* fr = fl + fi(FL_B + g * 16 + (hp & 3) * 4 + (hp >> 2));
            if (BR == 0) raise(fr, tag);
            colld(c0, W);
            warm(h, Wo, lane);
            WAITV(38 + (BR == 0));
            auto fill0 = [&](int k) { fill(ws + (k < 4 ? WS_O : WS_X + 4 * MB) + ((k >> 1) & 1) * (SEQ * 64) + (k & 1) * 4096, k < 4 ? h.po : h.px); };
            Cols c1;
            constexpr int NG[16] = {0, CE == 1 ? 8 : 0, 0, 0, 0, 0, 0, 0, 0, CE == 2 ? 8 : 0, CE == 3 ? 8 : 0, CE == 4 ? 8 : 0};
            int ng = 0;
            auto job = [&](int i) {
#pragma unroll
                for (int k = 0; k < 8; ++k) if (!F0 && FE + k / FN == i && (!FX || k < 4 || h.pos < NCK)) fill0(k);
                if (!F0 && i == 10) { WAITV(1 + (CE ? 3 * (NG[8] + NG[9]) : 0)); filled(h, fl + fi(FL_E), tag); }
                if (CE && NG[i]) { if (i > 8) __builtin_amdgcn_s_sleep(CS); colld(c1, W2, ng, ng + NG[i]); ng += NG[i]; }
                BAR();
            };
            job(1);
            if (BR == 1) raise(fr, tag);
            WAITV(26 + (BR < 2));
            job(2);
            if (BR == 2) raise(fr, tag);
            job(3); job(4);
            WAITV(0);
            const u64* fb = fl + fi(FL_B + g * 16 - (halo ? 2 : 0)) + min(lane, halo ? 17 : 15);
            u64 f0;
            asm volatile("global_load_dwordx2 %0, %1, off sc1" : "=&v"(f0) : "v"(fb) : "memory");
            colsum(c0, (const char*)csum + ((hp ^ CJ) * 16 + g) * 768, lane, [&](int k) {
                if (k == 1) {
                    WAITV(F0 ? 1 : 9 + (CE ? 3 * NG[5] : 0));
                    asm volatile("" : "+v"(f0));
                    if (!__all(f0 == tag)) poll(fb, lane < 18, tag, ~0u, [](u64 m) { return (unsigned)m; });
                }
                job(5 + k);
            });
            job(11);
            if (CE) {
                if (CE == 4) BAR();
                WAITV(0);
                colsum(c1, (const char*)csum + (256 + (hp ^ CJ) * 16 + g) * 768, lane, [&](int k) { if (CE == 4 ? k % 3 == 2 : k & 1) BAR(); });
            } else { BAR(); BAR(); BAR(); }
            asm volatile("" : "=v"(hs), "=v"(hq), "=v"(sp[0]));
        } else {
            f32x4 sh[4];
            u64* fs = fl + fi(FL_S + (layer - 1) * 256 + g * 16);
            u64 f0;
            if (!LOCAL && layer == 1) {
                const u64* fc = fl + fi(FL_C + hp * 16 + (lane & 15));
                asm volatile("global_load_dwordx2 %0, %1, off sc1" : "=&v"(f0) : "v"(fc) : "memory");
                BAR();
                WAITV(0);
                asm volatile("" : "+v"(f0));
                if (!__all(f0 == tag)) poll(fc, lane < 16, tag, 0xffffu, [](u64 m) { return (unsigned)m; });
                cols(256 * 768, SCB, [&](int k) { BAR(); if (k == DW - 1) raise(fs + h.pos, tag); });
            } else {
                BAR(); WAITB(0);
                if (DW == 2) BAR();
                raise(fs + h.pos, tag);
                BAR();
                if (DW == 1) BAR();
            }
#pragma unroll
            for (int i = 5; i < SF; ++i) BAR();
            fs += (halo && lane >= 16) ? (lane & 15) - 16 : lane & 15;
            asm volatile("global_load_dwordx2 %0, %1, off sc1" : "=&v"(f0) : "v"(fs) : "memory");
            BAR();
            WAITV(0);
            asm volatile("" : "+v"(f0));
            if (!__all(f0 == tag)) poll(fs, lane < 32, tag, ~0u, [](u64 m) { return (unsigned)m; });
            any4(sp);
            ld4(sp, so + 768, 0, 0);
            if (LOCAL) {
                any4(sh);
                ld4(sh, so + 768 - (halo ? 16384 : 0), 0, 0);
            }
            BAR(); BAR();
            WAITV(0);
            if (LOCAL) {
#pragma unroll
                for (int i = 1; i < 4; ++i) add1(sh[0], sh[i], 2);
                const f32x2 x = sw16f(sh[0][2], sh[0][2]), y = sw16f(sh[0][3], sh[0][3]);
                hs = x[0] + x[1]; hq = y[0] + y[1];
                asm volatile("" : "+v"(hs), "+v"(hq));
            } else asm volatile("" : "=v"(hs), "=v"(hq));
#pragma unroll
            for (int i = SF + 3; i < 15; ++i) BAR();
            sum4();
        }
    }
    { HT0(); HT1(14); }
    if (LOCAL) wo_issue(h, Wo, 8, 2, 6, 18);
    if (!LOCAL || layer) {
        const f32x2 x = sw16f(sp[0][0], sp[0][2]), y = sw16f(sp[0][1], sp[0][3]);
        const f32x2 z = sw32f(x[0] + x[1], hs), u = sw32f(y[0] + y[1], hq);
        const float mean = (z[0] + z[1]) * (1.0f / DM);
        const float r = rsd(fmx((u[0] + u[1]) * (1.0f / DM) - mean * mean, 0.f));
        if (lane < 32 || lw == 3) { h.st[0] = -bfr(mean); h.st[(SRS - SNM) / 4] = r; }
    }
    WAITV(LOCAL ? 8 : 0);
    BARL();
    { HT0(); HT1(15); }
    if (LOCAL) wo_issue(h, Wo, 12, 6, 8, 20);
    if (!LOCAL && layer == 1 && lw == 3) cols(0, SCOL, [](int k) { if (k == 1) BAR(); });
    else BAR();
    if (LOCAL) {
        wo_issue(h, Wo, 12, 6, 0, 8);
        if (layer == 0 && lw == 3) {
            if (F0) { colld(cw, W2); WAITB(28); }
            else {
            const u64 e = eload(h, fl + fi(FL_E));
            colld(cw, W2);
            WAITB(29);
            echeck<24>(h, fl + fi(FL_E), e, tag);
            }
        } else WAITB(4);
    } else BAR();
}

DEV u64 probe(const u64* f) {
    u64 v;
    asm volatile("s_mov_b64 exec, 1\n\t" FLD("%0", "%1", "%2") "\n\ts_mov_b64 exec, -1" : "=&v"(v) : "v"(f), "v"(0ull) : "memory");
    return v;
}
DEV void attn_helper(const HW& h, char* ws, int nf, const u16* kv, const u16* Wo, const u64* fe, const u64* fkv, u64 tag, int g, int hp, int lane, int lw STPARG) {
    __builtin_amdgcn_s_setprio(2);
    OPAQUE(lane, lw, g, hp);
    const int rh = lw & 1, hd = lw >> 1;
    const char* src = (const char*)kv + (unsigned)((hp * 2 + hd) * SEQ * 64 + rh * 4096);
    const unsigned ld = h.lds + AR0 + hd * 16384 + rh * 8192;
    const u64* f = fkv + fi(lw * 16 + (lane & 15));
    unsigned rdy = 0u;
    int reg = AR1;
    auto issue = [&](int c) {
        const int blk = g ^ c;
        if (!((rdy >> blk) & 1)) rdy = poll(f, lane < 16, tag, 1u << blk, [](u64 m) { return (unsigned)m; });
        glds4(src + blk * 8192, ld + reg, h.vo16, h.vo16, h.vo16, h.vo16);
        glds4(src + blk * 8192 + NH * SEQ * 64, ld + reg + 4096, h.vo16, h.vo16, h.vo16, h.vo16);
        reg = reg == AR1 ? AR2 : reg == AR2 ? 0 : AR1;
    };
    issue(1);
    STAMP(7);
#pragma unroll 1
    for (int c = 0; c < 14; ++c) {
        WAITB(0);
        issue(c + 2);
        if (c == 1 && lw == 3) warm(h, Wo, lane);
        if ((TL & 4) && c == 2 && lw == 3) touch(h.out, h.lds + DUMP, (lane & 3) * MB + blockIdx.x * 4096);
        if ((c >> 2) == (FA == 4 ? 2 : 1)) wo_issue(h, Wo, min(4, 14 - 4 * (c & 3)), 4, 4 * (c & 3), 0);
        if (c >= FA && c < FA + nf && (!FX || !(c & 1) || h.pos < NCK))
            fill(ws + (c == FA ? WS_O + 4 * MB : c == FA + 1 ? WS_X + 8 * MB : c == FA + 2 ? WS_O : WS_X + 12 * MB) + hd * (SEQ * 64) + rh * 4096, (c & 1) ? h.px : h.po);
        if (c == FA + 4 && lw == 3) filled(h, fe, tag);
    }
    WAITB(0);
    wo_issue(h, Wo, 8, 6, 14, 24);
    if (lw == 3) echeck<0>(h, fe, eload(h, fe), tag);
    BAR();
}

#define TM0
#define TM1(t)
#define CNT(n)
DEV void wo_helper(lchar* smem, HW& h, Cols& cw, const char* csum, const char* stats, int nl, const u16* O, const u16* Wo, int nw, const u16* Wn, u64* fc, const u64* fo, u64 tag, int g, int lane, int lw STPARG) {
    int dummy = 0;
    OPAQUE(lane, lw, g, dummy);
    const int p = lw & 1, rh = lw >> 1;
    const char* xsrc = (const char*)O + ((size_t)p * SEQ + g * 128 + rh * 64) * 64;
    const unsigned ldx = h.lds + p * 8192 + rh * 4096, c0 = ldx + lane * 16 + (lane >> 4) * 4;
    const u64* f = fo + p * 8 + rh;
    STAMP(0);
    unsigned rdy;
    u64 v, v2;
    u64 pa, pb;
    if (PF) pa = probe(f);
    wo_issue(h, Wo, nw, 10, 20, 0);
    __builtin_amdgcn_s_setprio(0);
    if (!PF) { __builtin_amdgcn_s_sleep(S1); pa = probe(f); }
    asm volatile("s_sleep %7\n\ts_mov_b64 exec, 1\n\t" FLD("%1", "%4", "%5") "\n\ts_mov_b32 %3, 1\n\ts_barrier\n\t" FLD("%2", "%4", "%5") "\n\t"
                 ".if %9\n\ts_cmp_lg_u32 %10, 0\n\ts_waitcnt vmcnt(10)\n\ts_cbranch_scc1 2f\n\t.endif\n\t"
                 "s_waitcnt vmcnt(2)\n2:\n\tv_cmp_ne_u64 vcc, %6, %0\n\ts_cbranch_vccz 1f\n\t"
                 "s_waitcnt vmcnt(1)\n\tv_cmp_ne_u64 vcc, %6, %1\n\ts_cbranch_vccz 1f\n\t"
                 ".if %8\n\ts_cmp_ge_u32 %10, %8\n\ts_cbranch_scc1 3f\n\ts_waitcnt vmcnt(0)\n\tv_cmp_ne_u64 vcc, %6, %2\n\ts_cbranch_vccz 1f\n\t" FLD("%2", "%4", "%5") "\n3:\n\t.endif\n\t"
                 "s_mov_b32 %3, 0\n\ts_barrier\n1:\n\ts_mov_b64 exec, -1"
                 : "+v"(pa), "=&v"(pb), "=&v"(v), "=&s"(rdy) : "v"(f), "v"(0ull), "s"(tag), "n"(S2), "n"(X3), "n"(PF), "s"(nw) : "memory", "scc", "vcc");
    auto issue = [&]() {
#pragma unroll 1
        for (int t = 0; t < 3; ++t) {
            glds4(xsrc + (unsigned)(t + (t >= h.pos)) * (2 * SEQ * 64), ldx + t * 16384, h.vo16, h.vo16, h.vo16, h.vo16);
            if (t == 0) { STAMP(1); SM(4, 1); SM(5, 5); SM(6, 6); SM(7, 7); }
        }
    };
    if (!CE && CW == 2 && fc) colsum(cw, csum, lane, [](int) {});
    if (rdy) issue();
    if (!CE && CW == 1 && fc) colsum(cw, csum, lane, [](int) {});
    asm volatile("s_cmp_lg_u32 %1, 0\n\ts_cbranch_scc0 1f\n\ts_barrier\n\ts_branch 2f\n1:\n\ts_cmp_lg_u64 %2, 0\n\ts_waitcnt vmcnt(3)\n\ts_cbranch_scc1 2f\n\ts_waitcnt vmcnt(0)\n2:"
                 : "+v"(v) : "s"(rdy), "s"(!CE && CW == 1 ? fc : nullptr) : "memory", "scc");
    poll2<0>(f, rdy ^ 1, tag, v, v2);
    if (!rdy) issue();
    __builtin_amdgcn_s_setprio(HPW);
    if (!CE && CW == 0 && fc) colsum(cw, csum, lane, [](int) {});
    unsigned vo = h.vo16 + (3 + (3 >= h.pos)) * (2 * SEQ * 64), sa, sb, sm;
    int bud = POLLMAX;
    u64 ret;
    unsigned tv = 0, cn = 0, np = 0;
    const char* wn = (const char*)Wn + lw * (8 * 6144);
    const unsigned wa = h.pos * 0x60000, wd = (h.pos == 0) * 0x60000 - wa - 0x30000;
    unsigned w0 = h.wq[lw == 3][0] + wa, w1 = h.wq[lw == 3][1] + wa, w2 = h.wq[lw == 3][2] + wa;
    asm volatile(
        ".macro WRD t\n.if (\\t %% 5) == 0\n"
        "ds_read2st64_b32 v[240:241], %[c4] offset1:4\nds_read2st64_b32 v[242:243], %[c4] offset0:8 offset1:12\n.else\n"
        "ds_read2st64_b32 v[240:241], %[c0] offset0:(\\t %% 5)*64-64 offset1:(\\t %% 5)*64-60\n"
        "ds_read2st64_b32 v[242:243], %[c0] offset0:(\\t %% 5)*64-56 offset1:(\\t %% 5)*64-52\n.endif\n.endm\n"
        ".macro WCMP\ns_waitcnt lgkmcnt(0)\nv_max3_u32 v240, v240, v241, v242\nv_max_u32 v240, v240, v243\nv_cmp_eq_u32 vcc, -1, v240\n.endm\n"
        ".macro WTS t, x\nWCMP\n" TM1("(\\t+63)&63") "s_cbranch_vccnz .Lwr%=_\\x\\t\n.Lwb%=_\\x\\t:\n.endm\n"
        ".macro WST t\n" GL "%[vo], %[src] sc1\n" GL "%[vo], %[src] offset:1024 sc1\n"
        "s_waitcnt vmcnt(10)\nWRD \\t\n"
        GL "%[vo], %[src] offset:2048 sc1\n" GL "%[vo], %[src] offset:3072 sc1\n"
        "s_add_u32 m0, %[ldx], ((\\t+3) %% 5)*16384\ns_cmp_eq_u32 %[pos], \\t+3\ns_cselect_b32 %[sa], 2, 1\nv_lshl_add_u32 %[vo], %[sa], 18, %[vo]\n"
        "WTS \\t, a\n"
        ".endm\n"
        ".macro WTL t, n, x\ns_waitcnt vmcnt(\\n)\nWRD \\t\nWTS \\t, \\x\ns_barrier\n" TM0 ".endm\n"
        ".macro WWN o, d\ns_add_u32 m0, %[wl], \\o\ns_nop 0\n" GL "%[w0], %[wn] sc1\n" GL "%[w1], %[wn] offset:1024 sc1\n" GL "%[w2], %[wn] offset:2048 sc1\n"
        "v_add_u32 %[w0], \\d, %[w0]\nv_add_u32 %[w1], \\d, %[w1]\nv_add_u32 %[w2], \\d, %[w2]\n.endm\n"
        ".macro WCR t, x\n.Lwr%=_\\x\\t:\ns_mov_b32 %[sa], ((\\t+4) %% 5)*16384\ns_mov_b32 %[sb], \\t-1\ns_call_b64 %[ret], .Lwretry%=\ns_branch .Lwb%=_\\x\\t\n.endm\n"
        "s_add_u32 m0, %[ldx], 49152\ns_nop 0\n" TM0
        ".irp t,1,2,3,4,5,6,7,8,9,10,11\nWST \\t\n.if \\t == 4\ns_cmp_eq_u64 %[fc], 0\ns_cbranch_scc1 3f\ns_mov_b64 exec, 1\n"
        "global_atomic_swap_x2 %[v0], %[tg], %[fc] sc1\ns_mov_b64 exec, -1\n3:\n.endif\ns_barrier\n" TM0 ".endr\n"
        "WST 12\ns_cmp_eq_u64 %[Wn], 0\ns_barrier\n" TM0 "s_cbranch_scc1 .Lwlast%=\n"
        "WWN 2*24576, 0x30000\nWTL 13, 11, a\nWWN 2*24576+12288, %[wd]\nWTL 14, 10, a\nWWN 0, 0x30000\nWTL 15, 9, a\nWWN 12288, 0\n"
        "s_branch .Lwend%=\n"
        ".irp t,1,2,3,4,5,6,7,8,9,10,11,12,13,14,15\nWCR \\t, a\n.endr\n"
        ".irp t,13,14,15\nWCR \\t, b\n.endr\n"
        ".Lwretry%=:\ns_cmp_ge_u32 %[sb], %[pos]\ns_addc_u32 %[sb], %[sb], 0\ns_mov_b32 %[sm], m0\ns_add_u32 m0, %[ldx], %[sa]\n"
        "v_lshl_add_u32 v244, %[sb], 18, %[v0]\nv_add_u32 v245, %[sa], %[c0]\n"
        "1:\n" GL "v244, %[src] sc1\n" GL "v244, %[src] offset:1024 sc1\n" GL "v244, %[src] offset:2048 sc1\n" GL "v244, %[src] offset:3072 sc1\n"
        "s_waitcnt vmcnt(0)\nds_read2st64_b32 v[240:241], v245 offset1:4\nds_read2st64_b32 v[242:243], v245 offset0:8 offset1:12\n"
        "WCMP\n" CNT("0x10000") "s_sub_i32 %[bud], %[bud], 1\ns_cmp_lt_i32 %[bud], 0\ns_cbranch_scc1 2f\ns_cbranch_vccnz 1b\n"
        "2:\ns_mov_b32 m0, %[sm]\ns_setpc_b64 %[ret]\n"
        ".Lwlast%=:\nWTL 13, 8, b\nWTL 14, 4, b\nWTL 15, 0, b\n"
        ".Lwend%=:\n"
        ".purgem WRD\n.purgem WCMP\n.purgem WTS\n.purgem WST\n.purgem WTL\n.purgem WWN\n.purgem WCR"
        : [vo] "+v"(vo), [bud] "+s"(bud), [sa] "=&s"(sa), [sb] "=&s"(sb), [sm] "=&s"(sm), [ret] "=&s"(ret),
          [w0] "+v"(w0), [w1] "+v"(w1), [w2] "+v"(w2)
        : [src] "s"(xsrc), [ldx] "s"(ldx), [c0] "v"(c0), [c4] "v"(c0 + 65536), [v0] "v"(h.vo16), [pos] "s"(h.pos), [wd] "s"(wd), [fc] "s"(fc), [tg] "v"(tag),
          [wn] "s"(wn), [Wn] "s"(Wn), [wl] "s"(h.lds + WQ0 + lw * 3072)
        : "memory", "scc", "vcc", "v240", "v241", "v242", "v243", "v244", "v245"
        );
    asm volatile("" ::"v"(v), "v"(v2), "v"(pb));
}

__global__ __launch_bounds__(512) void mega(const u16* xin, char* ws, const u16* wqa, const u16* __restrict__ woa,
                                            const u16* __restrict__ wqb, const u16* __restrict__ wob, u16* __restrict__ out) {
    __shared__ __attribute__((aligned(16))) char smem_[LDS_BYTES];
    u32x4 a01;
    {
        u64 pc;
        unsigned t, v;
        asm volatile("s_load_dwordx4 %0, %5, 0\n\ts_getpc_b64 %2\n1:\n\ts_mov_b32 m0, %8\n\ts_lshr_b32 %3, %6, 3\n\ts_mulk_i32 %3, 28*64\n\t"
                     "v_min_u32 %4, 27, %7\n\tv_lshl_add_u32 %4, %4, 6, %3\n\tv_min_u32 %4, .Lmega_last-1b, %4\n\tglobal_load_lds_dword %4, %2\n\t"
                     "s_load_dwordx2 %1, %5, 16"
                     : "=&s"(a01), "=&s"(wqa), "=&s"(pc), "=&s"(t), "=&v"(v)
                     : "s"(__builtin_amdgcn_kernarg_segment_ptr()), "s"(blockIdx.x), "v"(threadIdx.x), "n"(DUMP) : "memory", "scc");
    }
    SB();
    lchar* smem = (lchar*)smem_;
    const int bid = blockIdx.x, xcd = bid & 7, idx = bid >> 3;
    const int lane = threadIdx.x & 63, wid = __builtin_amdgcn_readfirstlane(threadIdx.x >> 6);
    const int rg = (bid & 4) * 32 + idx * 4 + (bid & 3), row = rg * 8 + (wid & 3) * 2 + ((lane >> 2) & 1);
    u32x4 x[4];
    u64 e;
    {
        unsigned xo = row * 2048 + (lane >> 3) * 64 + (lane & 3) * 16, eo = WS_FL + fi(FL_EP) * 8;
        asm volatile("" : "+v"(xo), "+v"(eo));
        asm volatile("s_waitcnt lgkmcnt(0)" : "+s"(a01), "+s"(wqa));
        xin = (const u16*)(((u64)a01[1] << 32) | a01[0]);
        ws = (char*)(((u64)a01[3] << 32) | a01[2]);
        asm volatile("s_cmp_gt_u32 %9, 3\n\ts_cbranch_scc1 1f\n\t"
                     "global_load_dwordx4 %0, %5, %6" NTX "\n\tglobal_load_dwordx4 %1, %5, %6 offset:512" NTX "\n\tglobal_load_dwordx4 %2, %5, %6 offset:1024" NTX "\n\t"
                     "global_load_dwordx4 %3, %5, %6 offset:1536" NTX "\n1:\n\tglobal_load_dwordx2 %4, %7, %8 sc1"
                     : "=&v"(x[0]), "=&v"(x[1]), "=&v"(x[2]), "=&v"(x[3]), "=&v"(e)
                     : "v"(xo), "s"(xin), "v"(eo), "s"(ws), "s"(wid) : "memory", "scc");
    }
    SB();
    const int g = (xcd >> 2) * 8 + (idx >> 2), hp = (xcd & 3) * 4 + (idx & 3);
    const int pos = (int)(kinv() >> (4 * hp)) & 15, g2 = g, cb = pos;
    u64* fl = (u64*)(ws + WS_FL);
    auto mktag = [](u64 e) {
        e += 1;
        return ((u64)(unsigned)__builtin_amdgcn_readfirstlane((int)(e >> 32)) << 32) | (unsigned)__builtin_amdgcn_readfirstlane((int)e);
    };
    const unsigned lds0 = (unsigned)(uintptr_t)smem;
    LAS unsigned* cnt = (LAS unsigned*)(smem + SNM + 1152);
    *cnt = 0u;
    BARL();
    u64 tag;
    if (wid < 4) {
        unsigned d = ((lane >> 3) * SEQ + row) * 64 + (((lane & 3) ^ fz(row)) << 4);
        unsigned lo_ = (g * 16 + (lane & 31)) * 256 + (rg & 15) * 8 + (lane & 16), lm = ((rg & 15) >= 14 && g < 15) ? ~0u : 0xffffu;
        asm volatile("" : "+v"(d), "+v"(lo_));
        asm volatile("" : "+s"(lm));
        WAITV(0);
        asm volatile("" : "+v"(x[0]), "+v"(x[1]), "+v"(x[2]), "+v"(x[3]), "+v"(e));
        if (wid == 0) STAMP(14);
        const unsigned s0 = ln0_stats(x);
        const f32x2 st = {lo16(s0), __builtin_amdgcn_rcpf(hi16(s0))};
        u32x4 y[4];
        auto put = [&](int i) { asm volatile("global_store_dwordx4 %0, %1, %2 sc1\n\ts_nop 1" ::"v"(d), "v"(i < 4 ? y[i] : x[i - 4]), "s"(ws + (i < 4 ? WS_X : WS_R - 4 * MB) + i * MB) : "memory"); };
#pragma unroll
        for (int i = 0; i < 4; ++i) {
            Raw v;
            unpk(v, x[i]);
            y[i] = ln0_apply(v, st);
            if (SA < 2 || i < SA - 1) put(i);
            SB();
        }
        tag = mktag(e);
        if (wid == 0) STAMP(15);
        WAITV(SA == 1 ? 3 : 0);
        if (LG) {
            unsigned n = 0;
            if (lane == 0) n = __hip_atomic_fetch_add(cnt, 1u, __ATOMIC_RELAXED, __HIP_MEMORY_SCOPE_WORKGROUP);
            n = __builtin_amdgcn_readfirstlane(n);
            go(ws + WS_L, lo_, n == 3 ? lm : 0, tag);
            if (n == 3) STAMPW(3);
        } else
        if (lane == 0 && __hip_atomic_fetch_add(cnt, 1u, __ATOMIC_RELAXED, __HIP_MEMORY_SCOPE_WORKGROUP) == 3)
        {
            stflag(fl + fi(FL_L + rg), tag);
            STAMPW(3);
        }
        if (SA >= 2) {
            BAR();
#pragma unroll
            for (int i = SA - 1; i < (RC && RT < 0 ? 8 : 4); ++i) {
                put(i);
            }
            if (F0) {
                if (F0 > 1) __builtin_amdgcn_s_sleep(F0);
                const unsigned o = ((pos * 2 + (wid & 1)) * SEQ + g * 128 + (wid >> 1) * 64) * 64 + lane * 16;
                fill(ws + WS_O, o);
                if (pos < NCK) fill(ws + WS_X + 4 * MB, o);
            }
        }
    } else {
        if (TD) WAITV(0);
        if (wid > 4) touch((const char*)wqa + (wid - 5) * 2048, lds0 + DUMP, (idx + ((lane >> 3) & (TW - 1)) * 32) * 6144 + (xcd & 3) * 512 + (lane & 7) * 64);
        else {
            const unsigned f = lane < 7 ? WS_GO + (g * 16 + pos) * 1024 + lane * 128
                             : (LG && lane >= 9) ? WS_L + (g * 16 + hp) * 256 + (lane & 1) * 128
                                        : WS_FL + (lane < 9 ? FL_C + bid * 32 + (lane & 1) * 16 : FL_L + (g & 7) * 16 + (lane & 1) * 128) * 8;
            asm volatile("s_mov_b64 exec, %3\n\tglobal_atomic_add_x2 %0, %1, %2 sc1\n\ts_mov_b64 exec, -1"
                         ::"v"(lane < 11 ? f : (unsigned)(WS_X + (lane - 11) * MB + rg * 512)), "v"(0ull), "s"(ws), "n"((TL & 1) ? 0x7fff : 0x7ff) : "memory");
        }
        auto tc = [&]() {
            const unsigned v = (g * 64 + (lane >> 1)) * 6144 + (hp ^ CJ) * 128 + (lane & 1) * 64;
            const char* b = (const char*)wqa + (wid == 4 ? 0 : wid - 5) * 2048;
            touch(b, lds0 + DUMP, wid == 4 ? 0 : v);
            touch(b + 32 * 6144, lds0 + DUMP, wid == 4 ? 0 : v);
        };
        if (TC == 1) tc();
        unsigned lo, hi;
        asm volatile("s_waitcnt vmcnt(%4)\n\tv_readfirstlane_b32 %0, %2\n\tv_readfirstlane_b32 %1, %3" : "=&s"(lo), "=s"(hi) : "v"((unsigned)e), "v"((unsigned)(e >> 32)), "n"(TC == 1 ? 3 : 1) : "memory");
        if (T2 && wid > 4) {
            if (T2 & 4) WAITV(0);
#pragma unroll
            for (int i = 1; i <= (T2 & 3); ++i)
                touch((const char*)wqa + (wid - 5) * 2048 + i * 256 * 6144, lds0 + DUMP, (idx + (lane >> 3) * 32) * 6144 + (xcd & 3) * 512 + (lane & 7) * 64);
        }
        if (TC == 2) tc();
        if (TC == 3) { WAITV(0); tc(); }
        tag = (((u64)hi << 32) | lo) + 1;
    }
    if (wid < 4) {
        if (SA < 2) BAR();
        if (wid == 0) STAMP(9);
        __builtin_amdgcn_s_setprio(1);
        u32x4 res[4];
        {
            const unsigned ro = RC ? ((cb * 2 + (wid & 1)) * SEQ + g2 * 128 + (wid >> 1) * 64 + (lane & 15)) * 64 + ((((lane & 16) << 1) + ((lane & 32) >> 1)) ^ (fz(lane & 15) << 4))
                                   : (g2 * 128 + (wid >> 1) * 64 + (lane & 15)) * 2048 + (cb * 2 + (wid & 1)) * 64 + ((lane & 16) << 1) + ((lane & 32) >> 1);
#pragma unroll
            for (int i = 0; i < 4; ++i) res[i] = (u32x4){ro, ro, ro, ro};
            if (RI == 9 || RT >= 0) for (int i = 0; i < 4; ++i) res[i] = x[i];
            if (!PB) { BAR(); BAR(); }
        }
#pragma unroll 1
        for (int layer = 0; layer < 4; ++layer) {
            const char* fo = ws + WS_GO + layer * 256;
            const unsigned gm = __builtin_amdgcn_readfirstlane(pos == 0 ? 0xfffe : pos == 1);
            u16* ob = (u16*)(ws + WS_O + pos * (2 * SEQ * 64));
            WReg wr;
            if (layer & 1) qb_compute(smem, (u16*)(ws + WS_KV + (layer >> 1) * 8 * MB), wr, ob + 2 * MB, fo, gm, fl + fi(FL_KV + (layer >> 1) * 1024 + hp * 64),
                                      tag, g, hp, lane, wid STPPASS(layer * 32));
            else qa_compute(smem, layer, RC ? (const u16*)(ws + WS_R) : xin, cb, res, wr, ob, fo, gm, tag, g, hp, lane, wid STPPASS(layer * 32));
            const bool last = layer == 3;
            wo_compute(smem, res, wr,
                       last ? out : (u16*)(ws + WS_X + (layer + 1) * 4 * MB), !last,
                       fo + 128, gm, (layer & 1) * 1024, tag, g2, cb, lane, wid STPPASS(layer * 32 + 16));
        }
        asm volatile(".Lmega_last:");
        if (bid == 0 && threadIdx.x == 0) stflag(fl + fi(FL_EP), tag);
    } else {
        const int lw = wid - 4;
        HW h;
        h.lds = lds0;
        h.out = out;
        h.lw = lw;
        h.pos = pos;
        h.go = (const u64*)(ws + WS_GO + (unsigned)(g * 16 + pos) * 1024);
        h.st = (LAS float*)(smem + SNM + (lane < 32 ? 64 + lw * 128 + lane * 4 : (lane & 15) * 4));
        asm volatile("" : "+v"(h.st));
        h.kord = ((KORD & ((1ull << (4 * pos)) - 1)) << 4) | hp | (KORD & ~((16ull << (4 * pos)) - 1));
        h.vo16 = lane * 16;
        h.po = (pos * 2 * SEQ + g * 128) * 64 + lane * 16;
        h.px = (cb * 2 * SEQ + g2 * 128) * 64 + lane * 16;
        h.er = fi(lane ? 256 + g2 * 16 + cb : g * 16 + hp) * 8;
        h.ec = fi((lane < 16 ? g2 : lane < 32 || g == 0 ? 16 + g : 15 + g) * 16 + (lane & 15)) * 8;
#pragma unroll
        for (int i = 0; i < 6; ++i) {
            const int k = (lane >> 3) + (((i / 3) ^ (lw == 1)) << 3), pc = lane & 7;
            h.wq[i / 3][i % 3] = (lane >> 3) * 6144 + (i % 3) * 1024 + hp * 128 + ((((pc >> 1) ^ fswz(k)) << 1) + (pc & 1)) * 16;
        }
#pragma unroll
        for (int i = 0; i < 4; ++i) h.wo[i] = wo_voff(lane, cb, i);
#pragma unroll 1
        for (int layer = 0; layer < 4; ++layer) {
            if (OQ) asm volatile("" : "+s"(wqa), "+s"(wqb), "+s"(ws));
            const u16* X = (const u16*)(ws + WS_X + layer * 4 * MB);
            const char* st = ws + WS_ST;
            const u16* wo = (layer & 1) ? wob : woa;
            Cols cw;
            const u64* fe = fl + fi(FL_E + ((layer + 1) >> 1) * 512);
            if (layer & 1) {
                qkv_helper<false>(smem, h, cw, ws, layer, X, wqb, wqb, wo, st, (float*)(ws + WS_CS), fl, tag, g, hp, lane, lw STPPASS(layer * 32));
                attn_helper(h, ws, layer == 1 ? 4 : 1, (const u16*)(ws + WS_KV + (layer >> 1) * 8 * MB), wo, fe, fl + fi(FL_KV + (layer >> 1) * 1024 + hp * 64), tag, g, hp, lane,
                            lw STPPASS(layer * 32));
            } else {
                qkv_helper<true>(smem, h, cw, ws, layer, X, wqa, wqb, wo, st, (float*)(ws + WS_CS), fl, tag, g, hp, lane, lw STPPASS(layer * 32));
            }
            wo_helper(smem, h, cw, ws + WS_CS + (256 + (hp ^ CJ) * 16 + g) * 768, st + layer * (MB / 2), (layer & 1) * 1024, (const u16*)(ws + WS_O + (layer & 1) * 4 * MB), wo, (layer & 1) * 10, layer == 3 ? nullptr : (layer & 1) ? wqa : wqb, (layer == 0 && lw == 3) ? fl + fi(FL_C + (hp ^ CJ) * 16 + g) : nullptr,
                      h.go + layer * 32, tag, g2, lane, lw STPPASS(layer * 32 + 16));
        }
    }
}

extern "C" void kernel_launch(void* const* d_in, const int* in_sizes, int n_in, void* d_out, int out_size, void* d_ws,
                              size_t ws_size, hipStream_t stream) {
    mega<<<256, 512, 0, stream>>>((const u16*)d_in[0], (char*)d_ws, (const u16*)d_in[1], (const u16*)d_in[2], (const u16*)d_in[4],
                                  (const u16*)d_in[5], (u16*)d_out);
}
```

```cpp
#include <hip/hip_runtime.h>
#include <stdint.h>

typedef unsigned short u16;
typedef unsigned long long u64;
typedef __attribute__((ext_vector_type(8))) short bf16x8;
typedef __attribute__((ext_vector_type(4))) short s16x4;
typedef __attribute__((ext_vector_type(4))) float f32x4;
typedef __attribute__((ext_vector_type(16))) float f32x16;
typedef __attribute__((ext_vector_type(4))) unsigned u32x4;
typedef __attribute__((ext_vector_type(2))) unsigned u32x2;
typedef __attribute__((ext_vector_type(2))) __bf16 bf16x2_t;
typedef __attribute__((ext_vector_type(2))) float f32x2;

#define LAS __attribute__((address_space(3)))
#define GAS __attribute__((address_space(1)))
#define DEV __device__ __forceinline__
typedef LAS char lchar;

constexpr int SEQ = 2048, DM = 1024, NH = 32;
constexpr float QSCALE = 0.17677669529663687f, LOG2E = 1.4426950408889634f;
#define POLLMAX (1 << 16)
#define NTX " nt"
#define CT 1
#define CT0 0
#define OQ 1
#define PW 2
#define VF 1
#define AL 0
#define PB 1
#define FL0 1
#define TW 8
#define K0 1
#define BW 1
#define RS 0
#define TC 0
#define TD 0
#define LP 1
#define WA 1
#define WB 48
#define EF 3
#define EO 0
#define EXP 1
#define WS 2
#define HPW 2
#define HPQ 0
#define NP 1
#define PRE 0
#define F0 0
#define FE 5
#define FN 2
#define FD 2
#define FA 4
#define E1 1
#define AW 0
#define PQ 1
#define Q2 1
#define SF 6
#define NCK (SF + 3)
#define L2 1
#define CI 2
#define RI 4
#define KORD 0x3bfedc7654a98210ull
#define CJ 2
#define CX 1
#define EMU 1
#define PUB 2
#define XP 0
#define WE 1
#define EA 0
#define EB 0
#define P2 6
#define SP 1
#define GM 0
#define MO 5
#define MW 0
#define TL 0
#define LG 1
#define WN 4
#define T2 5
#define CW 1
#define RM 0
#define CE 0
#define CS 4
#define RC 1
#define FX 1
#define RT -1
#define PE 4
#define DW 1
#define DS 9
#define BR 1
#define PF 0
#define S1 2
#define S2 4
#define X3 0
#define QB 1
#define HN 12
#define BP 1
#define BT 1
#define SX 0
#define ZP 0
#define PO 0
#define SA 3

DEV unsigned cvtpk(float lo, float hi) {
    f32x2 v = {lo, hi};
    bf16x2_t b = __builtin_convertvector(v, bf16x2_t);
    return __builtin_bit_cast(unsigned, b);
}
DEV float lo16(unsigned v) { return __uint_as_float(v << 16); }
DEV float hi16(unsigned v) { return __uint_as_float(v & 0xffff0000u); }
DEV float bfr(float f) { return lo16(cvtpk(f, f)); }
DEV u32x2 pk4(f32x4 a) { return (u32x2){cvtpk(a[0], a[1]), cvtpk(a[2], a[3])}; }
DEV bf16x8 cat(u32x2 a, u32x2 b) { return __builtin_bit_cast(bf16x8, (u32x4){a[0], a[1], b[0], b[1]}); }
DEV u32x2 sw16(unsigned a, unsigned b) { auto r = __builtin_amdgcn_permlane16_swap(a, b, false, false); return (u32x2){r[0], r[1]}; }
DEV u32x2 sw32(unsigned a, unsigned b) { auto r = __builtin_amdgcn_permlane32_swap(a, b, false, false); return (u32x2){r[0], r[1]}; }
DEV f32x2 sw16f(float a, float b) { return __builtin_bit_cast(f32x2, sw16(__float_as_uint(a), __float_as_uint(b))); }
DEV f32x2 sw32f(float a, float b) { return __builtin_bit_cast(f32x2, sw32(__float_as_uint(a), __float_as_uint(b))); }
DEV u32x4 unit16(u32x2 a, u32x2 b) {
    const u32x2 x = sw16(a[0], b[0]), y = sw16(a[1], b[1]);
    return (u32x4){x[0], y[0], x[1], y[1]};
}
template <int OFF> DEV void st16wt(const void* p, u32x4 v) {
    asm volatile("global_store_dwordx4 %0, %1, off offset:%2 sc1\n\ts_nop 1" ::"v"(p), "v"(v), "n"(OFF) : "memory");
}
DEV void st8wt(const void* p, u32x2 v) { asm volatile("global_store_dwordx2 %0, %1, off sc1\n\ts_nop 1" ::"v"(p), "v"(v) : "memory"); }
template <int CTRL> DEV float dppadd(float v) {
    return v + __builtin_bit_cast(float, __builtin_amdgcn_update_dpp(0, __builtin_bit_cast(int, v), CTRL, 0xf, 0xf, true));
}
DEV int fswz(int k) { return ((k >> 1) & 1) | (((k >> 3) & 1) << 1); }
DEV int fz(int tok) { return (-(tok >> 2)) & 3; }
DEV s16x4 ldtr4(const lchar* p) { return __builtin_bit_cast(s16x4, __builtin_amdgcn_ds_read_tr16_b64_v4i16((LAS s16x4*)p)); }
DEV bf16x8 cat8(s16x4 a, s16x4 b) { return (bf16x8){a[0], a[1], a[2], a[3], b[0], b[1], b[2], b[3]}; }
DEV bf16x8 ldk(const lchar* p) { return *(const LAS bf16x8*)p; }

#define STAMP(i)
#define STAMPW(i)
#define STPARG
#define STPPASS(o)
#define STAMPB(i)
#define SM(w, i)
#define SMD()
#define POLL(f, on, nd) poll(f, on, tag, nd, [](u64 m) { return (unsigned)m; })
#define HT0()
#define HT1(i)
#define TSD()
#define TS(i)
#define TSW(n)
#define BAR() asm volatile("s_barrier" ::: "memory")
#define BARL() asm volatile("s_waitcnt lgkmcnt(0)\n\ts_barrier" ::: "memory")
#define WAITB(n) asm volatile("s_waitcnt vmcnt(%0)\n\ts_barrier" ::"n"(n) : "memory")
#define WAITV(n) asm volatile("s_waitcnt vmcnt(%0)" ::"n"(n) : "memory")
#define SB() __builtin_amdgcn_sched_barrier(0)
#define LGKM0() __builtin_amdgcn_s_waitcnt(0xc07f)
#define MFMA32(a, b, c) __builtin_amdgcn_mfma_f32_32x32x16_bf16(a, b, c, 0, 0, 0)
#define MFMA16(a, b, c) __builtin_amdgcn_mfma_f32_16x16x32_bf16(a, b, c, 0, 0, 0)

DEV void mfma16t(f32x4& c, const bf16x8& a, const bf16x8& b) { asm volatile("v_mfma_f32_16x16x32_bf16 %0, %1, %2, %0" : "+v"(c) : "v"(a), "v"(b)); }
DEV void mfma16z(f32x4& c, const bf16x8& a, const bf16x8& b) { asm volatile("v_mfma_f32_16x16x32_bf16 %0, %1, %2, 0" : "=&v"(c) : "v"(a), "v"(b)); }
#define MFMA_DONE() do { SB(); asm volatile("s_nop 7\n\ts_nop 3"); SB(); } while (0)
DEV float fmx(float a, float b) { return __builtin_elementwise_maximum(a, b); }
DEV float rsd(float var) { return __builtin_amdgcn_rcpf(bfr(__builtin_amdgcn_sqrtf(bfr(bfr(var) + 1e-5f)))); }

constexpr int XS = 18432;
constexpr int WQ0 = 81920, WQS = 24576;
constexpr int STO = 65536;
constexpr int EXO = 57344;
constexpr int AR0 = 57344, AR1 = 65536, AR2 = 32768;
constexpr int SNM = 155648, SRS = SNM + 576, SCOL = SNM + 1280, HXO = SNM + 2048, DUMP = SNM + 4096, SCB = SNM + 4352;
constexpr int LDS_BYTES = 163840;
constexpr size_t MB = 1u << 20;
constexpr size_t WS_ST = 0, WS_GO = MB / 4, WS_L = 3 * MB / 4, WS_CS = 3 * MB / 2, WS_FL = 15 * MB / 8, WS_X = 2 * MB, WS_O = 18 * MB, WS_KV = 26 * MB, WS_R = 42 * MB;
constexpr int FL_KV = 8192;
constexpr int FL_L = 10240;
constexpr int FL_C = 7168;
constexpr int FL_E = 10496;
constexpr int FL_S = 12288;
constexpr int FL_B = 13312;
constexpr int FL_EP = 16368;
#define FLS 16
#define FS 1
DEV int fi(int i) { return (i >> 4) * FLS + (i & 15) * FS; }

#define GL_HEAD "s_mov_b32 m0, %1\n\ts_nop 4\n\tglobal_load_lds_dwordx4 %2, %0 sc1"
DEV void glds1(const void* sb, unsigned lds, unsigned v0) { asm volatile(GL_HEAD ::"s"(sb), "s"(lds), "v"(v0) : "memory"); }
DEV void glds3(const void* sb, unsigned lds, unsigned v0, unsigned v1, unsigned v2) {
    asm volatile(GL_HEAD "\n\tglobal_load_lds_dwordx4 %3, %0 offset:1024 sc1\n\tglobal_load_lds_dwordx4 %4, %0 offset:2048 sc1"
                 ::"s"(sb), "s"(lds), "v"(v0), "v"(v1), "v"(v2) : "memory");
}
DEV void glds4(const void* sb, unsigned lds, unsigned v0, unsigned v1, unsigned v2, unsigned v3) {
    asm volatile(GL_HEAD "\n\tglobal_load_lds_dwordx4 %3, %0 offset:1024 sc1\n\tglobal_load_lds_dwordx4 %4, %0 offset:2048 sc1\n\t"
                 "global_load_lds_dwordx4 %5, %0 offset:3072 sc1" ::"s"(sb), "s"(lds), "v"(v0), "v"(v1), "v"(v2), "v"(v3) : "memory");
}
DEV void touch(const void* sb, unsigned lds, unsigned v0) {
    asm volatile("s_mov_b32 m0, %1\n\ts_nop 4\n\tglobal_load_lds_dword %2, %0 sc1" ::"s"(sb), "s"(lds), "v"(v0) : "memory");
}
DEV u64 ldep(const u64* p) { return __hip_atomic_load((const GAS u64*)p, __ATOMIC_RELAXED, __HIP_MEMORY_SCOPE_AGENT); }
#define FLD(d, a, z) "global_atomic_add_x2 " d ", " a ", " z ", off sc0 sc1"
DEV u64 ldflag(const u64* p) {
    u64 v;
    asm volatile(FLD("%0", "%1", "%2") "\n\ts_waitcnt vmcnt(0)" : "=&v"(v) : "v"(p), "v"(0ull) : "memory");
    return v;
}
DEV void stflag(u64* p, u64 v) { asm volatile("global_atomic_swap_x2 %0, %1, off sc1\n\ts_nop 1" ::"v"(p), "v"(v) : "memory"); }
template <class F> DEV unsigned poll(const u64* p, bool on, u64 tag, unsigned need, F fold) {
    unsigned m = ~0u;
#pragma unroll 1
    for (int it = 0; it < POLLMAX; ++it) {
        u64 v = tag;
        if (on) v = ldflag(p);
        const unsigned r = fold(__ballot(v == tag));
        if ((r & need) == need) { m = r; break; }
        __builtin_amdgcn_s_sleep(1);
    }
    asm volatile("" ::: "memory");
    return m;
}
template <int B> DEV void poll2(const u64* f, unsigned m, u64 tag, u64& a, u64& b) {
    unsigned bud = POLLMAX;
    asm volatile("s_mov_b32 exec_lo, %5\n\ts_mov_b32 exec_hi, 0\n\t.if %8\n\t" FLD("%0", "%3", "%4") "\n\ts_waitcnt vmcnt(%8)\n\ts_barrier\n\t.else\n\t"
                 "v_cmp_ne_u64 vcc, %6, %0\n\ts_cbranch_vccz 2f\n\t" FLD("%0", "%3", "%4") "\n\ts_sleep %7\n\t.endif\n"
                 "1:\n\t" FLD("%1", "%3", "%4") "\n\ts_waitcnt vmcnt(1)\n\tv_cmp_ne_u64 vcc, %6, %0\n\ts_cbranch_vccz 2f\n\t"
                 FLD("%0", "%3", "%4") "\n\ts_waitcnt vmcnt(1)\n\tv_cmp_ne_u64 vcc, %6, %1\n\ts_sub_u32 %2, %2, 1\n\ts_cbranch_vccz 2f\n\ts_cbranch_scc0 1b\n"
                 "2:\n\ts_mov_b64 exec, -1"
                 : "+v"(a), "=&v"(b), "+s"(bud) : "v"(f), "v"(0ull), "s"(m), "s"(tag), "n"(P2), "n"(B) : "memory", "scc", "vcc");
}
template <int N = 0> DEV void publish(u64* f, int lane, u64 tag) {
    asm volatile("s_mov_b64 exec, 1\n\ts_waitcnt vmcnt(%2)\n\tglobal_atomic_swap_x2 %0, %1, off sc1\n\ts_mov_b64 exec, -1\n\ts_nop 0" ::"v"(f), "v"(tag), "n"(N) : "memory");
}
DEV void raise(u64* f, u64 tag) {
    asm volatile("s_mov_b64 exec, 1\n\tglobal_atomic_swap_x2 %0, %1, off sc1\n\ts_mov_b64 exec, -1\n\ts_nop 0" ::"v"(f), "v"(tag) : "memory");
}
DEV unsigned go_off(int g, int lane, int wm, int wn) { return (g * 16 + (lane & 31)) * 1024 + (wn * 8 + ((lane & 16) ? 2 : wm)) * 8; }
DEV void go(const char* b, unsigned o, unsigned m, u64 tag) {
    asm volatile("s_cmp_eq_u32 %3, 0\n\ts_cbranch_scc1 1f\n\ts_mov_b32 exec_lo, %3\n\ts_mov_b32 exec_hi, 0\n\tglobal_atomic_swap_x2 %0, %1, %2 sc1\n\ts_mov_b64 exec, -1\n1:"
                 ::"v"(o), "v"(tag), "s"(b), "s"(m) : "memory", "scc");
}
#define OPAQUE(l, a, b, c) do { asm volatile("" : "+v"(l)); asm volatile("" : "+s"(a), "+s"(b), "+s"(c)); } while (0)

template <bool LOCAL, class F, class G>
DEV void qkv_main(lchar* smem, f32x4 (&acc)[6][4], f32x4 (&acch)[2], int lane, int wm, int wn, int layer, G mid, F tailread STPARG) {
    constexpr int XC_B = LOCAL ? 9216 : 8192, HO = LOCAL ? 1024 : 0, NR = LOCAL ? 17 : 16;
    const int l15 = lane & 15, lg = lane >> 4, hs = LOCAL ? wm : 0;
    const lchar* xhb = smem + l15 * 64 + ((lg ^ fz(l15)) << 4);
    const lchar* xb = xhb + HO + wm * 4096;
    const int k0 = 8 * lg + (l15 >> 2);
    const lchar* wb0 = smem + WQ0 + lg * 3072 + (l15 >> 2) * 128 + (((wn * 2 + hs) ^ fswz(k0)) << 5) + (l15 & 3) * 8;
    const lchar* wb1 = smem + WQ0 + lg * 3072 + (l15 >> 2) * 128 + (((wn * 2 + (hs ^ 1)) ^ fswz(k0)) << 5) + (l15 & 3) * 8;
    lchar* sto = smem + STO + (wm * 2 + wn) * 4096 + lane * 16;
    acch[0] = acch[1] = (f32x4){0.f, 0.f, 0.f, 0.f};
    if (LOCAL) asm volatile("" : "+v"(acch[0]), "+v"(acch[1]), "+v"(xhb), "+v"(sto));
    asm volatile("" : "+v"(xb), "+v"(wb0), "+v"(wb1));
    bf16x8 xf[2][5], hf[6];
    s16x4 wh[2][6][2];
    auto rd = [&](int b, int j, int xo, int wo) {
        if (j < 4) xf[b][j] = ldk(xb + xo + j * 1024);
        else if (j < 16) wh[b][(j - 4) >> 1][j & 1] = ldtr4(((j & 2) ? wb1 : wb0) + wo + ((j - 4) >> 2) * 1024 + (j & 1) * 512);
        else xf[b][4] = ldk(xhb + xo);
    };
    auto aside = [&](int b) {
        *(LAS bf16x8*)(sto + b * 2048) = cat8(wh[b][2][0], wh[b][2][1]);
        *(LAS bf16x8*)(sto + b * 2048 + 1024) = cat8(wh[b][4][0], wh[b][4][1]);
    };
    auto kstep = [&](int b, int xo, int wo, int md) __attribute__((always_inline)) {
        int j = 0;
        auto fillr = [&]() __attribute__((always_inline)) {
            SB();
            if (md == 2) {
                if (!LOCAL) tailread(j);
                else if (j < 6) hf[j] = j < 2 ? ldk(xhb + HXO + j * 1024) : ldk(sto + (j - 2) * 1024);
                else tailread(j - 6);
            } else if (j < (md ? NR : 16)) rd(b ^ 1, j, xo, wo);
            else if (LOCAL && md == 0 && j == 16) aside(0);
            SB();
            ++j;
        };
        SB();
#pragma unroll
        for (int v = 0; v < 34; ++v) {
            const int u = v < 15 ? v : v - 4;
            const int ni = u < 20 ? u / 5 : 4 + (((u >> 1) ^ u) & 1), ti = u >= 20 ? (u - 20) / 2 : (ni & 1) ? 4 - u % 5 : u % 5;
            const bf16x8 w = cat8(wh[b][ni][0], wh[b][ni][1]);
            if (v >= 15 && v < 19) {
                if (!LOCAL || md != 2) continue;
                const int k = (v - 15) >> 1;
                if (v & 1) mfma16t(acch[1], hf[k], hf[3 + 2 * k]);
                else mfma16t(acch[0], hf[2 + 2 * k], hf[k]);
            } else if (ti == 4) {
                if (!LOCAL || md == 0 || (ni != 2 && ni != 4)) continue;
                if (ni == 2) mfma16t(acch[0], w, xf[b][4]);
                else mfma16t(acch[1], xf[b][4], w);
            } else if (md == 0) {
                if (ni >= 4) mfma16z(acc[ni][ti], xf[b][ti], w);
                else mfma16z(acc[ni][ti], w, xf[b][ti]);
            } else {
                if (ni >= 4) mfma16t(acc[ni][ti], xf[b][ti], w);
                else mfma16t(acc[ni][ti], w, xf[b][ti]);
            }
            fillr();
        }
        if (md < 2) { LGKM0(); SB(); }
    };
    if (LOCAL && PB) asm volatile("s_cmp_lg_u32 %0, 0\n\ts_cbranch_scc1 1f\n\t.if %1\n\ts_waitcnt vmcnt(0)\n\t.endif\n\ts_barrier\n1:" ::"s"(layer), "n"(BR == 0) : "memory", "scc");
    SB();
    STAMP(4);
#pragma unroll
    for (int j = 0; j < 16; ++j) rd(0, j, 0, 2 * WQS);
    SB();
    LGKM0(); SB();
    kstep(0, XC_B, 2 * WQS + WQS / 2, 0);
    if (LOCAL) {
        aside(1);
        xf[1][4] = (bf16x8){0, 0, 0, 0, 0, 0, 0, 0};
        asm volatile("" : "+v"(xf[1][4]));
    }
    SM(1, 11);
    if (LOCAL && BR == 1) asm volatile("s_cmp_lg_u32 %0, 0\n\ts_cbranch_scc1 1f\n\ts_waitcnt vmcnt(0)\n1:" ::"s"(layer) : "memory", "scc");
    SM(2, 12);
    SB();
#pragma unroll 1
    for (int it = 0; it < 5; ++it) {
        if (!RM) mid(it);
#pragma unroll
        for (int s = 0; s < 3; ++s) {
            if (s == DW) WAITV(0);
            BAR(); SB();
            kstep(1, ((s + 1) % 3) * XS, s * WQS, 1);
            if (RM == s + 1) { mid(it); SB(); }
            kstep(0, ((s + 1) % 3) * XS + XC_B, s * WQS + WQS / 2, 1);
        }
    }
    BAR(); SB();
    kstep(1, 0, 0, 2);
    MFMA_DONE();
}

constexpr int NRW = 7;
typedef s16x4 WReg[NRW * 8];
DEV const lchar* wo_wb(const lchar* smem, int lane, int c) {
    const int k0 = 8 * (lane >> 4) + ((lane & 15) >> 2);
    return smem + k0 * 128 + ((c ^ fswz(k0)) << 5) + (lane & 3) * 8;
}
#define PIN2(a, b) asm volatile("" : "+v"(a), "+v"(b))
#define WRD1() do { SB(); wr[wj] = ldtr4(((wj & 2) ? wb1 : wb0) + (wj >> 2) * 4096 + (wj & 1) * 512); ++wj; SB(); } while (0)

DEV void qa_compute(lchar* smem, int fold, const u16* xin, unsigned cb, u32x4 (&rr)[4], WReg& wr, u16* O, const char* fo, unsigned gm, u64 tag, int g, int hp, int lane, int wid STPARG) {
    OPAQUE(lane, wid, g, hp);
    const int wm = wid >> 1, wn = wid & 1, l15 = lane & 15, lg = lane >> 4;
    f32x4 acc[6][4], acch[2];
    float nm[5], rs[5], csv[2];
    f32x4 nmv[5], rsv[5], cs[4];
    const lchar* sa = smem + SNM + l15 * 4;
    const lchar* sb = smem + SNM + lg * 16;
    const lchar* sc = smem + SCOL + wn * 128;
    const int ro = 64 + wm * 256;
    f32x4 cprev, ccur;
#pragma unroll
    for (int j = 0; j < 4; ++j) {
        const int d = l15 - 4 * lg - j;
        ccur[j] = (d >= 0 && !(d & 1)) ? 0.f : -3.0e38f;
        cprev[j] = (d <= -2 && !(d & 1)) ? 0.f : -3.0e38f;
    }
    const char* optr = (const char*)O + ((size_t)wn * SEQ + g * 128 + wm * 64 + l15) * 64 +
                       (((((lg & 1) << 1) | (lg >> 1)) ^ (wm << 1) ^ fz(l15)) << 4);
    unsigned xo = rr[0][0], xd = 0;
    if (RT >= 0) {
        const int row = (g * 16 + (hp & 3) * 4 + (hp >> 2)) * 8 + wid * 2 + ((lane >> 2) & 1);
        xd = ((lane >> 3) * SEQ + row) * 64 + (((lane & 3) ^ fz(row)) << 4);
        xo = ((cb * 2 + wn) * SEQ + g * 128 + wm * 64 + l15) * 64 + (((((lg & 1) << 1) | (lg >> 1)) ^ fz(l15)) << 4);
        asm volatile("" : "+v"(xd));
    }
    asm volatile("" : "+v"(cprev), "+v"(ccur), "+v"(optr), "+v"(xo));
    qkv_main<true>(smem, acc, acch, lane, wm, wn, fold, [&](int it) {
        if (RT >= 0)
        asm volatile("s_cmp_eq_u32 %6, %7+1\n\ts_cbranch_scc0 2f\n\ts_waitcnt vmcnt(0)\n2:\n\ts_cmp_lg_u32 %6, %7\n\ts_cbranch_scc1 1f\n\tglobal_store_dwordx4 %4, %0, %5 sc1\n\tv_add_u32 %4, 0x100000, %4\n\t"
                     "global_store_dwordx4 %4, %1, %5 sc1\n\tv_add_u32 %4, 0x100000, %4\n\tglobal_store_dwordx4 %4, %2, %5 sc1\n\tv_add_u32 %4, 0x100000, %4\n\t"
                     "global_store_dwordx4 %4, %3, %5 sc1\n1:" : "+v"(rr[0]), "+v"(rr[1]), "+v"(rr[2]), "+v"(rr[3]), "+v"(xd)
                     : "s"(xin), "s"(it + fold * 8), "n"(RT) : "memory", "scc");
        if (RS == 1) {
#pragma unroll
            for (int k = 0; k < 4; ++k)
                asm volatile("s_cmp_lg_u32 %3, %4\n\ts_cbranch_scc1 1f\n\tglobal_load_dwordx4 %0, %1, %2\n\tv_add_u32 %1, 0x8000, %1\n1:"
                             : "+v"(rr[k]), "+v"(xo) : "s"(xin), "s"(it + fold * 8), "n"(k + 1) : "memory", "scc");
        } else if (RS == 2) {
#pragma unroll
            for (int k = 0; k < 2; ++k)
                asm volatile("s_cmp_lg_u32 %4, %5\n\ts_cbranch_scc1 1f\n\tglobal_load_dwordx4 %0, %2, %3\n\tv_add_u32 %2, 0x8000, %2\n\t"
                             "global_load_dwordx4 %1, %2, %3\n\tv_add_u32 %2, 0x8000, %2\n1:"
                             : "+v"(rr[2 * k]), "+v"(rr[2 * k + 1]), "+v"(xo) : "s"(xin), "s"(it + fold * 8), "n"(k + 3) : "memory", "scc");
        } else if (RC)
        asm volatile("s_cmp_lg_u32 %6, %7\n\ts_cbranch_scc1 1f\n\tglobal_load_dwordx4 %0, %4, %5 sc1\n\tglobal_load_dwordx4 %1, %4, %5 offset:1024 sc1\n\t"
                     "global_load_dwordx4 %2, %4, %5 offset:2048 sc1\n\tglobal_load_dwordx4 %3, %4, %5 offset:3072 sc1\n1:"
                     : "+v"(rr[0]), "+v"(rr[1]), "+v"(rr[2]), "+v"(rr[3]), "+v"(xo) : "s"(xin), "s"(it + fold * 8), "n"(RI) : "memory", "scc");
        else
        asm volatile("s_cmp_lg_u32 %6, %7\n\ts_cbranch_scc1 1f\n\tglobal_load_dwordx4 %0, %4, %5\n\tv_add_u32 %4, 0x8000, %4\n\t"
                     "global_load_dwordx4 %1, %4, %5\n\tv_add_u32 %4, 0x8000, %4\n\tglobal_load_dwordx4 %2, %4, %5\n\tv_add_u32 %4, 0x8000, %4\n\t"
                     "global_load_dwordx4 %3, %4, %5\n1:" : "+v"(rr[0]), "+v"(rr[1]), "+v"(rr[2]), "+v"(rr[3]), "+v"(xo)
                     : "s"(xin), "s"(it + fold * 8), "n"(RI) : "memory", "scc");
    }, [&](int j) {
        if (j < 4) cs[j] = *(const LAS f32x4*)(sc + (j >> 1) * 256 + (((j & 1) ^ wm) << 6) + lg * 16);
        else if (j < 9) { const int o = j < 8 ? ro + (j - 4) * 64 : 0; nm[j - 4] = *(const LAS float*)(sa + o); rs[j - 4] = *(const LAS float*)(sa + o + 576); }
        else if (j < 11) csv[j - 9] = *(const LAS float*)(sc + 512 + (((j - 9) ^ wm) << 6) + l15 * 4);
        else if (j < 21) {
            const int t = (j - 11) >> 1, o = t < 4 ? ro + t * 64 : 0;
            if ((j - 11) & 1) rsv[t] = *(const LAS f32x4*)(sb + o + 576);
            else nmv[t] = *(const LAS f32x4*)(sb + o);
        }
    } STPPASS(0));
    STAMP(2);
    TSD(); TS(0);
    if (fold) {
#pragma unroll
        for (int ti = 0; ti < 4; ++ti) {
#pragma unroll
            for (int ni = 0; ni < 4; ++ni) acc[ni][ti] = (acc[ni][ti] + nm[ti] * cs[ni]) * rs[ti];
#pragma unroll
            for (int h = 0; h < 2; ++h) acc[4 + h][ti] = (acc[4 + h][ti] + nmv[ti] * csv[h]) * rsv[ti];
        }
        acch[0] = (acch[0] + nm[4] * cs[2]) * rs[4];
        acch[1] = (acch[1] + nmv[4] * csv[0]) * rsv[4];
    }
    typedef __attribute__((ext_vector_type(10))) unsigned u32x10;
    u32x2 kp[5][2];
    u32x10 vp[2];
    auto setv = [&](int h, int i, u32x2 a) { vp[h][2 * i] = a[0]; vp[h][2 * i + 1] = a[1]; };
    lchar* ex = smem + EXO + wn * 4096 + lane * 16;
    {
        const u32x2 a = pk4(acch[0]), b = pk4(acch[1]);
        *(LAS u32x4*)(ex + wm * 1024) = (u32x4){a[0], a[1], b[0], b[1]};
    }
#pragma unroll
    for (int h = 0; h < 2; ++h) { kp[4][h] = pk4(acc[2 + h][3]); setv(h, 4, pk4(acc[4 + h][3])); }
    if (wm == 0) {
#pragma unroll
        for (int h = 0; h < 2; ++h) *(LAS u32x4*)(ex + 2048 + h * 1024) = (u32x4){kp[4][h][0], kp[4][h][1], vp[h][8], vp[h][9]};
    }
    bf16x8 qf[4];
#pragma unroll
    for (int ti = 0; ti < 4; ++ti) {
        qf[ti] = cat(pk4(acc[0][ti]), pk4(acc[1][ti]));
        if (ti < 3) {
#pragma unroll
            for (int h = 0; h < 2; ++h) { kp[ti + 1][h] = pk4(acc[2 + h][ti]); setv(h, ti + 1, pk4(acc[4 + h][ti])); }
        }
    }
    const lchar* wb0 = wo_wb(smem, lane, wn * 2);
    const lchar* wb1 = wo_wb(smem, lane, wn * 2 + 1);
    const lchar* e0 = ex + wm * 3072;
    const lchar* e1 = ex + wm * 1024 + 1024;
    f32x4 cp0 = cprev;
    if (g == 0 && wm == 0) cp0 = (f32x4){-3.0e38f, -3.0e38f, -3.0e38f, -3.0e38f};
    unsigned sel0 = (lane & 1) ? 0x01000c0cu : 0x0c0c0100u, sel1 = (lane & 1) ? 0x03020c0cu : 0x0c0c0302u;
    bf16x8 ones = {0x3f80, 0x3f80, 0x3f80, 0x3f80, 0x3f80, 0x3f80, 0x3f80, 0x3f80};
    unsigned gof = go_off(g, lane, wm, wn);
    lchar* ol = smem + 65536 + wn * 8192 + wm * 4096 + l15 * 64 + (((((lg & 1) << 1) | (lg >> 1)) ^ (wm << 1) ^ fz(l15)) << 4);
    asm volatile("" : "+v"(wb0), "+v"(wb1), "+v"(e0), "+v"(e1), "+v"(cp0), "+v"(sel0), "+v"(sel1), "+v"(ones), "+v"(vp[0]), "+v"(vp[1]), "+v"(ol), "+v"(gof));
    SB();
    TS(1);
    BARL();
    TS(2);
    kp[0][0] = *(const LAS u32x2*)e0; SB(); kp[0][1] = *(const LAS u32x2*)e1; SB();
    setv(0, 0, *(const LAS u32x2*)(e0 + 8)); SB(); setv(1, 0, *(const LAS u32x2*)(e1 + 8));
    SB();
    int wj = 0;
    f32x4 s0[4], s1[4];
    f32x2 sv[4][2];
    float mx[4];
#pragma unroll
    for (int ti = 3; ti >= 0; --ti) {
        s1[ti] = MFMA16(cat(kp[ti + 1][0], kp[ti + 1][1]), qf[ti], ccur);
        WRD1();
        s0[ti] = MFMA16(cat(kp[ti][0], kp[ti][1]), qf[ti], ti ? cprev : cp0);
        WRD1();
    }
#pragma unroll
    for (int i = 0; i < 48; ++i) if (wj < 48) WRD1();
#pragma unroll
    for (int i = 7; i >= 0; --i) {
        const f32x4& t = (i & 1) ? s1[i >> 1] : s0[i >> 1];
        sv[i >> 1][i & 1] = (f32x2){fmx(t[0], t[1]), fmx(t[2], t[3])};
        asm volatile("" : "+v"(sv[i >> 1][i & 1]));
    }
    if (WA == 0) { SB(); LGKM0(); BAR(); SB(); }
    if (EMU) {
        unsigned r[8];
#pragma unroll
        for (int ps = 0; ps < 2; ++ps) {
#pragma unroll
            for (int i = 7; i >= 0; --i) r[i] = cvtpk(sv[i >> 1][i & 1][0], sv[i >> 1][i & 1][1]);
#pragma unroll
            for (int i = 7; i >= 0; --i) {
                sv[i >> 1][i & 1] = (f32x2){lo16(r[i]), hi16(r[i])};
                if (ps == 0) sv[i >> 1][i & 1] *= QSCALE;
            }
        }
    }
#pragma unroll
    for (int ti = 3; ti >= 0; --ti) mx[ti] = fmx(fmx(fmx(sv[ti][0][0], sv[ti][0][1]), sv[ti][1][0]), sv[ti][1][1]);
    TS(3);
    {
        const f32x2 a = sw16f(mx[0], mx[1]), b = sw16f(mx[2], mx[3]);
        const f32x2 c = sw32f(fmx(a[0], a[1]), fmx(b[0], b[1]));
        const float r = fmx(c[0], c[1]) * -(EMU ? LOG2E : QSCALE * LOG2E);
        const f32x2 d = sw32f(r, r), e = sw16f(d[0], d[0]), f = sw16f(d[1], d[1]);
        mx[0] = e[0]; mx[1] = e[1]; mx[2] = f[0]; mx[3] = f[1];
    }
    SB();
    TS(4);
    if (WA == 1) { LGKM0(); BAR(); SB(); }
    TS(5);
    const f32x4 z = {0.f, 0.f, 0.f, 0.f};
    f32x4 l[4], o0[4], o1[4];
    auto pv = [&](int ti, u32x4 v0, u32x4 v1) {
        const f32x2 x = sv[ti][0] * (EMU ? LOG2E : QSCALE * LOG2E) + mx[ti], y = sv[ti][1] * (EMU ? LOG2E : QSCALE * LOG2E) + mx[ti];
        const unsigned c0 = cvtpk(__builtin_amdgcn_exp2f(x[0]), __builtin_amdgcn_exp2f(x[1]));
        const unsigned c1 = cvtpk(__builtin_amdgcn_exp2f(y[0]), __builtin_amdgcn_exp2f(y[1]));
        const bf16x8 p = __builtin_bit_cast(bf16x8, (u32x4){__builtin_amdgcn_perm(c0, c0, sel0), __builtin_amdgcn_perm(c0, c0, sel1),
                                                            __builtin_amdgcn_perm(c1, c1, sel0), __builtin_amdgcn_perm(c1, c1, sel1)});
        SB();
        l[ti] = MFMA16(ones, p, z);
        WRD1();
        o0[ti] = MFMA16(__builtin_bit_cast(bf16x8, v0), p, z);
        WRD1();
        o1[ti] = MFMA16(__builtin_bit_cast(bf16x8, v1), p, z);
        SB();
    };
    auto fin = [&](int ti) {
        const float inv = __builtin_amdgcn_rcpf(l[ti][0]);
        return unit16(pk4(o0[ti] * inv), pk4(o1[ti] * inv));
    };
#define PV(ti) pv(ti, __builtin_shufflevector(vp[0], vp[0], 2 * ti, 2 * ti + 1, 2 * ti + 2, 2 * ti + 3), \
                      __builtin_shufflevector(vp[1], vp[1], 2 * ti, 2 * ti + 1, 2 * ti + 2, 2 * ti + 3))
    PV(0); PV(1);
    TS(6);
#define OOUT(ti) do { const u32x4 v = fin(ti); st16wt<ti * 1024>(optr, v); *(LAS u32x4*)(ol + ti * 1024) = v; SB(); } while (0)
    if (EA == 4) { go(fo, gof, gm, tag); SB(); }
    OOUT(0);
    if (EA == 3) { go(fo, gof, gm, tag); SB(); }
    if (WA == 2) { __builtin_amdgcn_s_waitcnt(0xc47f); BAR(); SB(); }
    TS(7);
    PV(2);
    OOUT(1);
    if (EA == 2) { go(fo, gof, gm, tag); SB(); }
    if (WA == 3) { __builtin_amdgcn_s_waitcnt(0xc67f); BAR(); SB(); }
    TS(8);
    PV(3);
    OOUT(2);
    if (EA == 1) { go(fo, gof, gm, tag); SB(); }
    if (WA == 4) { __builtin_amdgcn_s_waitcnt(0xc87f); BAR(); SB(); }
    TS(9);
    OOUT(3);
    TS(10);
    if (EA == 0) go(fo, gof, gm, tag);
    SM(0, 2); SMD();
    TS(11);
    TSW(12);
    STAMP(10);
}

#define MX3(a, b, c) fmx(fmx((a), (b)), (c))
DEV float rowmax32(const f32x16& s0) {
    float ma = MX3(s0[0], s0[1], s0[2]), mb = MX3(s0[3], s0[4], s0[5]);
    ma = MX3(ma, s0[6], s0[7]); mb = MX3(mb, s0[8], s0[9]);
    ma = MX3(ma, s0[10], s0[11]); mb = MX3(mb, s0[12], s0[13]);
    ma = fmx(MX3(ma, s0[14], s0[15]), mb);
    const f32x2 b = sw32f(ma, ma);
    return fmx(b[0], b[1]);
}
#define EX(i) do { S[i] = __builtin_amdgcn_exp2f(S[i]); SB(); } while (0)
#define EQ(i) do { p1[i] = __builtin_amdgcn_exp2f(S[8 + i]); SB(); } while (0)
typedef __attribute__((ext_vector_type(16))) __bf16 bf16x16_t;
typedef __attribute__((ext_vector_type(8))) __bf16 bf16x8_t;
typedef __attribute__((ext_vector_type(8))) float f32x8;
template <int H> DEV f32x8 half8(const f32x16& s) { return __builtin_shufflevector(s, s, H, H + 1, H + 2, H + 3, H + 4, H + 5, H + 6, H + 7); }
#define SMF32(a, b, c) __builtin_amdgcn_smfmac_f32_32x32x32_bf16(__builtin_bit_cast(bf16x8_t, a), __builtin_bit_cast(bf16x16_t, b), c, hi, 0, 0)
#define SMF16(a, b, c) __builtin_amdgcn_smfmac_f32_16x16x64_bf16(__builtin_bit_cast(bf16x8_t, a), __builtin_bit_cast(bf16x16_t, b), c, hi, 0, 0)
template <class G0, class F1, class F2, class F3>
DEV void block(f32x16& S, f32x16& N, const bf16x8& kf0, const bf16x8& kf1, const bf16x8& q0, const bf16x8& q1, const f32x16& c,
               f32x16& oP, f32x4& lP, const bf16x8& vf1, f32x8& p1, f32x16& oC, f32x4& lC, const bf16x8& vf0,
               const bf16x8& sel, int hi, G0 fb, F1 fc, F2 fd, F3 ff) {
    N = MFMA32(kf0, q0, c); SB();
    EX(0); EX(1); EX(2);
    oP = SMF32(vf1, p1, oP); SB();
    fb(); SB();
    EX(3); EX(4); EX(5);
    lP = SMF16(sel, p1, lP); SB();
    fc(); SB();
    EX(6);
    N = MFMA32(kf1, q1, N); SB();
    fd(); SB();
    EX(7); EQ(0); EQ(1);
    oC = SMF32(vf0, half8<0>(S), oC); SB();
    EQ(2); EQ(3); EQ(4);
    lC = SMF16(sel, half8<0>(S), lC); SB();
    ff(); SB();
    EQ(5); EQ(6); EQ(7);
}
DEV void attn_slow(const char* kh, const char* vh, const bf16x8& q0, const bf16x8& q1, f32x16& o, float& l, int hi) {
    f32x16 c;
#pragma unroll
    for (int i = 0; i < 16; ++i) { o[i] = 0.f; c[i] = 0.f; }
    float m = 0.f;
    l = 0.f;
#pragma unroll 1
    for (int t = 0; t < SEQ / 32; ++t) {
        bf16x8 k0, k1, v0, v1;
        const char* kp = kh + t * 2048;
        const char* vp = vh + t * 2048;
        asm volatile("global_load_dwordx4 %0, %4, off sc1\n\tglobal_load_dwordx4 %1, %5, off sc1\n\tglobal_load_dwordx4 %2, %6, off sc1\n\t"
                     "global_load_dwordx4 %3, %7, off sc1\n\ts_waitcnt vmcnt(0)" : "=&v"(k0), "=&v"(k1), "=&v"(v0), "=&v"(v1)
                     : "v"(kp), "v"((const char*)((uintptr_t)kp ^ 32)), "v"(vp), "v"((const char*)((uintptr_t)vp ^ 32)) : "memory");
        f32x16 s = MFMA32(k0, q0, c);
        s = MFMA32(k1, q1, s);
        const float mx = rowmax32(s);
        const float dl = t == 0 ? mx : fmx(mx, 0.f);
        const float alpha = t == 0 ? 0.f : __builtin_amdgcn_exp2f(-dl);
        m += dl;
        l *= alpha;
#pragma unroll
        for (int i = 0; i < 16; ++i) {
            o[i] *= alpha; c[i] = -m;
            s[i] = __builtin_amdgcn_exp2f(s[i] - dl);
            l += hi16(__float_as_uint(s[i]));
        }
        o = SMF32(v0, half8<0>(s), o);
        o = SMF32(v1, half8<8>(s), o);
    }
    const f32x2 b = sw32f(l, l);
    l = b[0] + b[1];
}

DEV void qb_compute(lchar* smem, u16* kv, WReg& wr, u16* O, const char* fo, unsigned gm, u64* fkv, u64 tag, int g, int hp, int lane, int wid STPARG) {
    OPAQUE(lane, wid, g, hp);
    const int wm = wid >> 1, wn = wid & 1, l15 = lane & 15, lg = lane >> 4, h = hp * 2 + wn;
    bf16x8 qa0, qa1, qb0, qb1;
    lchar* gs = smem + AR0 + wn * 16384;
    {
        f32x4 acc[6][4], acch[2];
        float nm[4], rs[4], csv[2];
        f32x4 cs[4], nmv[4], rsv[4];
        const lchar* sa = smem + SNM + 64 + wm * 256 + l15 * 4;
        const lchar* sb = smem + SNM + 64 + wm * 256 + lg * 16;
        const lchar* sc = smem + SCB + wn * 128;
        const int un = ((lg & 1) << 1) | (lg >> 1);
        const int ko = l15 * 64 + ((un ^ (l15 >> 2)) << 4), vo = l15 * 64 + ((lg ^ (l15 >> 2)) << 4);
        const char* kptr = (const char*)kv + ((size_t)h * SEQ + g * 128 + wm * 64) * 64 + ko;
        const char* vptr = (const char*)kv + ((size_t)(NH + h) * SEQ + g * 128 + wm * 64) * 64 + vo;
        lchar* kl = gs + wm * 8192 + ko;
        lchar* vl = gs + wm * 8192 + 4096 + vo;
        asm volatile("" : "+v"(kptr), "+v"(vptr));
        qkv_main<false>(smem, acc, acch, lane, wm, wn, 1, [](int) {}, [&](int j) {
            if (j < 4) cs[j] = *(const LAS f32x4*)(sc + (j >> 1) * 256 + (j & 1) * 64 + lg * 16);
            else if (j < 8) { nm[j - 4] = *(const LAS float*)(sa + (j - 4) * 64); rs[j - 4] = *(const LAS float*)(sa + (j - 4) * 64 + 576); }
            else if (j < 10) csv[j - 8] = *(const LAS float*)(sc + 512 + (j - 8) * 64 + l15 * 4);
            else if (j < 18) {
                if (j & 1) rsv[(j - 10) >> 1] = *(const LAS f32x4*)(sb + ((j - 10) >> 1) * 64 + 576);
                else nmv[(j - 10) >> 1] = *(const LAS f32x4*)(sb + ((j - 10) >> 1) * 64);
            }
        } STPPASS(0));
        STAMP(2);
        auto tile = [&](int ni, int ti, float sc_) { return pk4((acc[ni][ti] + nm[ti] * cs[ni]) * sc_); };
        auto tilev = [&](int h_, int ti) { return pk4((acc[4 + h_][ti] + nmv[ti] * csv[h_]) * rsv[ti]); };
#define KOUT(ti) do { const u32x4 v = unit16(tile(2, ti, rs[ti]), tile(3, ti, rs[ti])); st16wt<ti * 1024>(kptr, v); *(LAS u32x4*)(kl + ti * 1024) = v; } while (0)
#define VOUT(j) do { const u32x4 v = __builtin_bit_cast(u32x4, cat(tilev(j & 1, j & 2), tilev(j & 1, (j & 2) + 1))); st16wt<j * 1024>(vptr, v); *(LAS u32x4*)(vl + j * 1024) = v; } while (0)
        KOUT(0); KOUT(1); KOUT(2); KOUT(3);
        VOUT(0); VOUT(1); VOUT(2); VOUT(3);
        SB(); BARL(); SB();
        if (PO == 3) { publish(fkv + fi(wn * 32 + wm * 16 + g), lane, tag); SB(); }
#pragma unroll
        for (int ti = 0; ti < 4; ++ti) rs[ti] *= QSCALE * LOG2E;
        qa0 = __builtin_bit_cast(bf16x8, unit16(tile(0, 0, rs[0]), tile(0, 1, rs[1])));
        qa1 = __builtin_bit_cast(bf16x8, unit16(tile(1, 0, rs[0]), tile(1, 1, rs[1])));
        qb0 = __builtin_bit_cast(bf16x8, unit16(tile(0, 2, rs[2]), tile(0, 3, rs[3])));
        qb1 = __builtin_bit_cast(bf16x8, unit16(tile(1, 2, rs[2]), tile(1, 3, rs[3])));
    }
    STAMP(3);
    const int r = lane & 31, hh = lane >> 5;
    const int kro = ((r & 4) * 4 + (r >> 3) * 4 + (r & 3)) * 64 + ((hh ^ (r >> 3)) << 4), vro = r * 64 + ((hh ^ ((r >> 2) & 3)) << 4);
    const lchar* fp[8] = {gs + kro, gs + (kro ^ 32), gs + 4096 + vro, gs + 4096 + (vro ^ 32)};
#pragma unroll
    for (int i = 0; i < 4; ++i) fp[4 + i] = fp[i] + AR1;
    auto frag = [&](int i, int o) { return ldk(fp[i + 4 * (o >= AR1)] + o % AR1); };
    const short se = (l15 & 1) ? (short)0 : (short)0x3f80, so = se ^ 0x3f80;
    bf16x8 sel = {se, se, se, se, so, so, so, so};
    int hi = 0xdddd;
    const char* op = (const char*)O + ((size_t)wn * SEQ + g * 128 + wm * 64 + l15) * 64 + (((((lg & 1) << 1) | (lg >> 1)) ^ fz(l15)) << 4);
    const lchar* wb0 = wo_wb(smem, lane, wn * 2);
    const lchar* wb1 = wo_wb(smem, lane, wn * 2 + 1);
    unsigned gof = go_off(g, lane, wm, wn);
    lchar* ol = smem + 65536 + wn * 8192 + wm * 4096 + l15 * 64 + (((((lg & 1) << 1) | (lg >> 1)) ^ fz(l15)) << 4);
    asm volatile("" : "+v"(hi), "+v"(op), "+v"(wb0), "+v"(wb1), "+v"(ol), "+v"(gof));
    f32x16 oA, oB, cA, cB, sA, sB;
    f32x4 lA = {0.f, 0.f, 0.f, 0.f}, lB = {0.f, 0.f, 0.f, 0.f};
#pragma unroll
    for (int i = 0; i < 16; ++i) { oA[i] = 0.f; oB[i] = 0.f; }
    bf16x8 kf0, kf1, vf0, vf1[2];
    f32x8 p1 = {0.f, 0.f, 0.f, 0.f, 0.f, 0.f, 0.f, 0.f};
    auto nop = [&]() {};
    int wj = 0;
    auto tilestep = [&](int par, int nx, int bar, int nw = 0) {
        auto wrd = [&](int k) {
#pragma unroll
            for (int i = 0; i < 4; ++i) if (i * 4 + k < nw) WRD1();
        };
        block(sA, sB, kf0, kf1, qb0, qb1, cB, oB, lB, vf1[par ^ 1], p1, oA, lA, vf0, sel, hi, [&]() { if (bar == 2) LGKM0(); if (bar) BAR(); },
              [&]() { kf0 = frag(0, nx); }, [&]() { __builtin_amdgcn_s_waitcnt(0xc17f); wrd(2); }, [&]() { LGKM0(); SB(); kf1 = frag(1, nx); wrd(0); });
        block(sB, sA, kf0, kf1, qa0, qa1, cA, oA, lA, vf1[par], p1, oB, lB, vf0, sel, hi, nop,
              [&]() { LGKM0(); SB(); vf1[par ^ 1] = frag(3, nx); wrd(1); }, nop, [&]() { vf0 = frag(2, nx); wrd(3); });
    };
    {
        const int own = 8192;
        asm volatile("" : "+v"(fp[0]), "+v"(fp[1]), "+v"(fp[2]), "+v"(fp[3]), "+v"(fp[4]), "+v"(fp[5]), "+v"(fp[6]), "+v"(fp[7]));
        kf0 = frag(0, own); kf1 = frag(1, own);
        vf0 = frag(2, own); vf1[0] = frag(3, own); vf1[1] = sel;
        if (ZP) asm volatile("" : "+v"(oA), "+v"(oB), "+v"(lA), "+v"(lB), "+v"(p1), "+v"(sel), "+v"(vf1[1]));
        asm volatile("" : "+v"(qa0), "+v"(qa1), "+v"(qb0), "+v"(qb1)); SB();
        if (PO == 0) publish(fkv + fi(wn * 32 + wm * 16 + g), lane, tag);
        SB();
        const f32x16 z16 = {};
        sA = MFMA32(kf0, qa0, z16); sB = MFMA32(kf0, qb0, z16);
        sA = MFMA32(kf1, qa1, sA); sB = MFMA32(kf1, qb1, sB);
        SB();
        if (PO == 1) publish(fkv + fi(wn * 32 + wm * 16 + g), lane, tag);
        SB();
        const float mA = -rowmax32(sA), mB = -rowmax32(sB);
        const f32x2 a2 = {mA, mA}, b2 = {mB, mB};
#pragma unroll
        for (int i = 0; i < 16; i += 2) {
            f32x2 x, y, t = {sA[i], sA[i + 1]};
            asm("v_mov_b64 %0, %3\n\tv_mov_b64 %1, %4\n\tv_pk_add_f32 %2, %2, %3" : "=&v"(x), "=&v"(y), "+v"(t) : "v"(a2), "v"(b2));
            cA[i] = x[0]; cA[i + 1] = x[1]; cB[i] = y[0]; cB[i + 1] = y[1]; sA[i] = t[0]; sA[i + 1] = t[1];
        }
        asm volatile("s_nop 1" : "+v"(cA), "+v"(cB), "+v"(sA));
        SB();
        if (PO == 2) publish(fkv + fi(wn * 32 + wm * 16 + g), lane, tag);
        SB();
        STAMP(5);
        STAMP(6);
    }
    auto chunk = [&](int cur, int nxt, int nw, int bt = 3) __attribute__((always_inline)) {
        tilestep(0, cur + 10240, 0, nw);
        tilestep(1, cur, 2 * (bt == 1), nw);
        tilestep(0, cur + 2048, bt == 2, nw);
        tilestep(1, nxt + 8192, nw == 16 ? 2 : bt == 3, nw & 15);
    };
#pragma unroll 1
    for (int it = 0; it < 5; ++it) {
        chunk(0, AR1, 0);
        chunk(AR1, AR2, 0);
        chunk(AR2, 0, 0);
    }
    chunk(0, AR1, HN, BT);
    asm volatile("" : "+v"(oA), "+v"(oB), "+v"(lA), "+v"(lB), "+v"(p1));
    oB = SMF32(vf1[1], p1, oB);
    lB = SMF16(sel, p1, lB);
    SB();
    STAMP(9);
    TSD(); TS(0);
    auto fin = [&](const f32x16& o, float l, u32x4& u0, u32x4& u1, bool rd) {
        const float inv = __builtin_amdgcn_rcpf(l);
        unsigned y[8];
        if (rd) { WRD1(); WRD1(); }
#pragma unroll
        for (int i = 0; i < 8; ++i) {
            const f32x2 t = (f32x2){o[2 * i], o[2 * i + 1]} * inv;
            if (rd) WRD1();
            y[i] = cvtpk(t[0], t[1]);
        }
        const u32x2 a = sw32(y[0], y[2]), b = sw32(y[1], y[3]);
        if (rd) WRD1();
        const u32x2 c = sw32(y[4], y[6]), d = sw32(y[5], y[7]);
        if (rd) WRD1();
        const u32x2 e = sw16(a[0], c[0]), f = sw16(b[0], d[0]);
        if (rd) WRD1();
        const u32x2 i = sw16(a[1], c[1]), j = sw16(b[1], d[1]);
        if (rd) WRD1();
        u0 = (u32x4){e[0], f[0], i[0], j[0]};
        u1 = (u32x4){e[1], f[1], i[1], j[1]};
    };
    if (EB == 4) { SB(); go(fo, gof, gm, tag); SB(); }
    u32x4 u0, u1;
    float la = (lane & 16) ? lA[1] : lA[0];
#pragma unroll
    for (int i = 0; i < WB; ++i) if (wj < WB) WRD1();
    if (BP == 2) { SB(); LGKM0(); BAR(); SB(); }
    fin(oA, la, u0, u1, false);
    float lb = (lane & 16) ? lB[1] : lB[0];
    if (__builtin_expect(__any(!(la <= 1073741824.0f) || !(lb <= 1073741824.0f)), 0)) {
        const char* kh = (const char*)kv + (size_t)h * SEQ * 64;
#pragma unroll 1
        for (int ps = 0; ps < 2; ++ps) {
            fin(oB, lb, u0, u1, false);
            attn_slow(kh + kro, kh + (size_t)NH * SEQ * 64 + vro, ps ? qb0 : qa0, ps ? qb1 : qa1, oB, lb, hi);
        }
    }
    TS(1);
    if (EB == 3) { SB(); go(fo, gof, gm, tag); SB(); }
    st16wt<0>(op, u0); TS(2); st16wt<1024>(op, u1);
    if (EB == 2) { SB(); go(fo, gof, gm, tag); SB(); }
    if (BP == 0) { SB(); LGKM0(); BAR(); SB(); }
    if (EB == 1) { go(fo, gof, gm, tag); SB(); }
    const u32x4 a0 = u0, a1 = u1;
    if (BP != 1) { *(LAS u32x4*)ol = u0; *(LAS u32x4*)(ol + 1024) = u1; }
    SB();
    TS(3);
    fin(oB, lb, u0, u1, false);
    TS(4);
    st16wt<2048>(op, u0); TS(5); st16wt<3072>(op, u1);
    TS(6);
    if (EB == 0) go(fo, gof, gm, tag);
    SM(0, 2); SMD();
    if (BP == 1) { SB(); LGKM0(); BAR(); SB(); *(LAS u32x4*)ol = a0; *(LAS u32x4*)(ol + 1024) = a1; }
    *(LAS u32x4*)(ol + 2048) = u0; *(LAS u32x4*)(ol + 3072) = u1;
    TS(7);
    STAMP(10);
#pragma unroll
    for (int i = 0; i < 56; ++i) if (wj < 56) WRD1();
    TS(8);
    TS(9);
    TSW(10);
}

constexpr u64 kinv() { u64 r = 0; for (int s = 0; s < 16; ++s) r |= (u64)s << (4 * ((KORD >> (4 * s)) & 15)); return r; }
DEV constexpr int wo_unit(int t) { return t >= 7 && t < 10 ? t + 9 : t; }
DEV unsigned wo_voff(int lane, int cb, int i) {
    const int k = 8 * i + (lane >> 3), pc = lane & 7;
    return (k * 1024 + cb * 64 + ((((pc >> 1) ^ fswz(k)) << 1) + (pc & 1)) * 8) * 2 - i * 1024;
}
DEV void wo_compute(lchar* smem, u32x4 (&rr)[4], WReg& wr, u16* C, bool stats, const char* fx, unsigned gm, int nl, u64 tag, int g, int cb, int lane,
                    int wid STPARG) {
    OPAQUE(lane, wid, g, cb);
    const int wm = wid >> 1, wn = wid & 1, l15 = lane & 15, lg = lane >> 4;
    const lchar* wb0 = wo_wb(smem + WQ0, lane, wn * 2);
    const lchar* wb1 = wo_wb(smem + WQ0, lane, wn * 2 + 1);
    asm volatile("" : "+v"(wb0), "+v"(wb1));
    bf16x8 xf[2][8];
    s16x4 wh[16][8];
    auto rdw = [&](int t, int j) { wh[t][j] = ldtr4(((j & 2) ? wb1 : wb0) + wo_unit(t) * 8192 - WQ0 + (j >> 2) * 4096 + (j & 1) * 512); };
#pragma unroll
    for (int j = 0; j < NRW * 8; ++j) wh[j >> 3][j & 7] = wr[j];
    asm volatile("s_cmp_gt_u32 %0, 1\n\ts_waitcnt vmcnt(5)\n\ts_cbranch_scc0 1f\n\ts_waitcnt vmcnt(4)\n1:" ::"s"(cb) : "memory", "scc");
    STAMP(5);
    STAMPW(6 + wid);
    asm volatile("" : "+v"(lane), "+v"(rr[0]), "+v"(rr[1]), "+v"(rr[2]), "+v"(rr[3]));
    const int un = ((lg & 1) << 1) | (lg >> 1), row = g * 128 + wm * 64 + l15, ch = cb * 2 + wn;
    const unsigned ocm = (ch * SEQ + row) * 64 + ((un ^ fz(l15)) << 4), orm = row * 2048 + ch * 64 + un * 16;
    const char* cp = (const char*)C + (stats ? ocm : orm);
    const unsigned cs = stats ? 1024 : 32768;
    const lchar* xb = smem + (wm * 64 + l15) * 64 + ((lg ^ fz(l15)) << 4);
    lchar* xl = smem + wn * 8192 + (wn + 1) * nl + (wm * 64 + l15) * 64 + ((un ^ fz(l15)) << 4);
    unsigned gof = go_off(g, lane, wm, wn);
    gm *= !stats ? 0 : (nl && wm && g < 15) ? 0x10001 : 1;
    asm volatile("" : "+v"(cp), "+v"(xb), "+v"(xl), "+v"(gof));
    int late = stats ? cb : 0;
    asm volatile("" : "+s"(gm), "+s"(late));
    f32x4 acc[2][4];
#pragma unroll
    for (int ti = 0; ti < 4; ++ti) {
        const u32x2 x = sw16(rr[ti][0], rr[ti][2]), y = sw16(rr[ti][1], rr[ti][3]);
#pragma unroll
        for (int ni = 0; ni < 2; ++ni) acc[ni][ti] = (f32x4){lo16(x[ni]), hi16(x[ni]), lo16(y[ni]), hi16(y[ni])};
    }
    auto rdx = [&](int t, int j) { xf[t & 1][j] = ldk(xb + ((t + 4) % 5) * 16384 + (j >> 2) * 8192 + (j & 3) * 1024); };
    auto mm = [&](int t, int kk, int j) {
        const bool alt = MW && !(t == 15 && kk == 1);
        const int ni = !alt ? (MW ? ((j >> 1) ^ j) & 1 : j & 1) : MW == 1 ? j >> 2 : ((j >> 1) ^ j) & 1;
        const int ti = (!alt || MW == 2) ? j >> 1 : ni ? 7 - j : j;
        mfma16t(acc[ni][ti], cat8(wh[t][kk * 4 + ni * 2], wh[t][kk * 4 + ni * 2 + 1]), xf[t & 1][kk * 4 + ti]);
    };
    SB();
    LGKM0(); SB();
#pragma unroll
    for (int t = 0; t < 16; ++t) {
        BAR(); SB();
        if (t == 1) SM(0, 4);
        if (EXP && t == 16 - EXP) { go(fx, gof, gm, tag); SB(); }
        if (t == 0) {
            STAMP(4);
#pragma unroll
            for (int j = 0; j < 8; ++j) rdx(0, j);
            SB();
        } else {
#pragma unroll
            for (int j = 0; j < 8; ++j) { rdx(t, j); SB(); mm(t - 1, 1, j); SB(); }
        }
        const bool more = t + 1 >= NRW && t < 15;
        if (WS) {
            __builtin_amdgcn_s_waitcnt(0xc47f); SB();
#pragma unroll
            for (int j = 0; j < 8; ++j) {
                if (more && (WS == 1 || j)) { rdw(t + 1, WS == 1 ? j : j - 1); SB(); }
                if (more && WS == 2 && j == 7) { rdw(t + 1, 7); SB(); }
                mm(t, 0, j); SB();
            }
        } else
#pragma unroll
        for (int j = 0; j < 8; ++j) { mm(t, 0, j); SB(); if (more) { rdw(t + 1, j); SB(); } }
        if (more) __builtin_amdgcn_s_waitcnt(0xc87f); else LGKM0();
        SB();
    }
    u32x4 (&ov)[4] = rr;
    auto out = [&](int ti) {
        asm volatile("" : "+v"(acc[0][ti]), "+v"(acc[1][ti]));
        ov[ti] = unit16(pk4(acc[0][ti]), pk4(acc[1][ti]));
        asm volatile("s_cmp_ge_u32 %2, %3\n\ts_cbranch_scc1 1f\n\tglobal_store_dwordx4 %0, %1, off sc1\n\ts_nop 1\n1:" ::"v"(cp + ti * cs), "v"(ov[ti]), "s"(late), "n"(PE) : "memory", "scc");
        SB();
    };
#pragma unroll
    for (int j = 0; j < 6; ++j) mm(15, 1, j);
    SB();
    out(0);
    mm(15, 1, 6); mm(15, 1, 7); SB();
    out(1); out(2); out(3);
    STAMP(2);
    if (stats) {
        if (!EXP) go(fx, gof, gm, tag);
        SM(0, 2); SMD(); SB();
#pragma unroll
        for (int ti = 0; ti < 4; ++ti) *(LAS u32x4*)(xl + ti * 1024) = ov[ti];
        SB(); BARL(); SB();
        STAMP(10);
        STAMP(11);
        STAMPW(12 + wid);
    } else {
        STAMP(10);
    }
}

DEV unsigned ln0_stats(const u32x4 (&x)[4]) {
    const bf16x2_t one2 = __builtin_bit_cast(bf16x2_t, 0x3f803f80u);
    float s[2] = {0.f, 0.f}, q[2] = {0.f, 0.f};
#pragma unroll
    for (int i = 0; i < 16; ++i) {
        const unsigned u = x[i >> 2][i & 3];
        const bf16x2_t v = __builtin_bit_cast(bf16x2_t, u);
        s[i & 1] = __builtin_amdgcn_fdot2_f32_bf16(v, one2, s[i & 1], false);
        q[i & 1] = __builtin_amdgcn_fdot2_f32_bf16(v, v, q[i & 1], false);
    }
    const f32x2 a = sw32f(s[0] + s[1], q[0] + q[1]);
    float v = dppadd<0x128>(dppadd<0x4E>(dppadd<0xB1>(a[0] + a[1])));
    const f32x2 b = sw16f(v, v);
    v = (b[0] + b[1]) * (1.0f / DM);
    const f32x2 c = sw32f(v, v);
    return cvtpk(c[0], __builtin_amdgcn_sqrtf(bfr(bfr(fmx(c[1] - c[0] * c[0], 0.f)) + 1e-5f)));
}
typedef f32x2 Raw[4];
DEV void unpk(Raw& v, u32x4 x) {
#pragma unroll
    for (int i = 0; i < 4; ++i) { v[i] = (f32x2){lo16(x[i]), hi16(x[i])}; asm volatile("" : "+v"(v[i])); }
}
DEV u32x4 ln0_apply(const Raw& v, f32x2 st) {
    u32x4 x;
#pragma unroll
    for (int i = 0; i < 4; ++i) {
        const f32x2 e = v[i] - st[0];
        f32x2 d;
        asm("v_cvt_pk_bf16_f32 %0, 0, %1" : "=v"(d[0]) : "v"(e[0]));
        asm("v_cvt_pk_bf16_f32 %0, 0, %1" : "=v"(d[1]) : "v"(e[1]));
        d *= st[1];
        x[i] = cvtpk(d[0], d[1]);
    }
    return x;
}

struct HW { unsigned wq[2][3], wo[4], vo16, lds, po, px, er, ec; int lw, pos; u64 kord; LAS float* st; const u64* go; const void* out; };
typedef u32x4 Cols[24];
template <int N> DEV void wq_issue(const HW& h, const u16* W, int t, int at, int r0) {
    const char* src = (const char*)W + (unsigned)(at * 8 + r0) * (8 * 6144);
    const unsigned l = h.lds + WQ0 + ((t + 2) % 3) * WQS + r0 * 3072;
#pragma unroll
    for (int i = 0; i < N; ++i) { glds3(src, l + i * 3072, h.wq[i & 1][0], h.wq[i & 1][1], h.wq[i & 1][2]); src += 8 * 6144; }
}
DEV void wo_issue(const HW& h, const u16* W, int n, int lim, int a, int b) {
#pragma unroll 1
    for (int j = h.lw; j < n; j += 4) {
        const int i = j + (j < lim ? a : b);
        glds4((const char*)W + (((unsigned)(h.kord >> (2 * (i & ~1))) & 15) * 2 + (i & 1)) * (32 * 2048), h.lds + wo_unit(i >> 1) * 8192 + (i & 1) * 4096, h.wo[0], h.wo[1], h.wo[2], h.wo[3]);
    }
}
DEV void fill(const void* sb, unsigned vo) {
    asm volatile("s_nop 4\n\tglobal_store_dwordx4 %0, %1, %2 sc1\n\tglobal_store_dwordx4 %0, %1, %2 offset:1024 sc1\n\t"
                 "global_store_dwordx4 %0, %1, %2 offset:2048 sc1\n\tglobal_store_dwordx4 %0, %1, %2 offset:3072 sc1\n\ts_nop 1"
                 ::"v"(vo), "v"((u32x4){~0u, ~0u, ~0u, ~0u}), "s"(sb) : "memory");
}
DEV void filled(const HW& h, const u64* fe, u64 tag) {
    asm volatile("s_mov_b64 exec, 3\n\ts_nop 3\n\tglobal_atomic_swap_x2 %0, %1, %2 sc1\n\ts_mov_b64 exec, -1\n\ts_nop 0" ::"v"(h.er), "v"(tag), "s"(fe) : "memory");
}
DEV u64 eload(const HW& h, const u64* fe) {
    u64 e;
    asm volatile("s_nop 4\n\tglobal_load_dwordx2 %0, %1, %2 sc1" : "=&v"(e) : "v"(h.ec), "s"(fe) : "memory");
    return e;
}
template <int N> DEV void echeck(const HW& h, const u64* fe, u64 e, u64 tag) {
    unsigned bud = POLLMAX;
    asm volatile("s_waitcnt vmcnt(%6)\n1:\n\tv_cmp_ne_u64 vcc, %5, %0\n\ts_sub_u32 %1, %1, 1\n\ts_cbranch_vccz 2f\n\ts_cbranch_scc1 2f\n\ts_sleep 1\n\t"
                 "global_atomic_add_x2 %0, %2, %3, %4 sc0 sc1\n\ts_waitcnt vmcnt(0)\n\ts_branch 1b\n2:"
                 : "+v"(e), "+s"(bud) : "v"(h.ec), "v"(0ull), "s"(fe), "s"(tag), "n"(N) : "memory", "scc", "vcc");
}
#define GL "global_load_lds_dwordx4 "
#define GL4(a, b, s) GL a ", " s " sc1\n\t" GL b ", " s " offset:1024 sc1\n\t" GL b ", " s " offset:2048 sc1\n\t" GL b ", " s " offset:3072 sc1\n\t"
#define XRD(c) "s_setprio 2\n\tds_read2st64_b32 v[240:241], " c " offset1:4\n\tds_read2st64_b32 v[242:243], " c " offset0:8 offset1:12\n\t" \
    "ds_read2st64_b32 v[244:245], " c " offset0:16 offset1:20\n\tds_read2st64_b32 v[246:247], " c " offset0:24 offset1:28\n\t" \
    ".if %[L]\n\tds_read_b32 v248, " c " offset:8192\n\t.endif\n\t"
#define XCMP "s_waitcnt lgkmcnt(0)\n\tv_max3_u32 v240, v240, v241, v242\n\tv_max3_u32 v243, v243, v244, v245\n\tv_max3_u32 v240, v240, v246, v247\n\t" \
    ".if %[L]\n\tv_max3_u32 v240, v240, v243, v248\n\t.else\n\tv_max_u32 v240, v240, v243\n\t.endif\n\tv_cmp_eq_u32 %[bad], -1, v240\n\ts_setprio 0"
#define XCLOB "memory", "v240", "v241", "v242", "v243", "v244", "v245", "v246", "v247", "v248"
template <bool L> DEV bool stale(unsigned c) {
    u64 bad;
    asm volatile(XRD("%[c]") XCMP : [bad] "=s"(bad) : [c] "v"(c), [L] "n"((int)L) : XCLOB);
    return bad != 0;
}
template <bool L, bool CK> DEV bool xstep(const HW& h, const void* sw, unsigned lw, const void* sx, unsigned lx, unsigned vo0, unsigned c, int wt, int t) {
    u64 bad;
    asm volatile("s_mov_b32 m0, %[lw]\n\t.if %[CK]\n\ts_nop 1\n\t.else\n\ts_cmp_eq_u32 %[wt], 0\n\ts_cbranch_scc1 5f\n\ts_waitcnt vmcnt(11+%[L])\n5:\n\t.endif\n\t"
                 "s_cmp_eq_u32 %[t], %[B1]\n\ts_cbranch_scc1 6f\n\ts_barrier\n6:\n\t" GL "%[w0], %[sw] sc1\n\t" GL "%[w1], %[sw] offset:1024 sc1\n\t" GL "%[w2], %[sw] offset:2048 sc1\n\t"
                 "s_mov_b32 m0, %[lx]\n\ts_nop 0\n\t" GL4("%[v0]", "%[va]", "%[sx]")
                 ".if %[CK]\n\ts_waitcnt vmcnt(7)\n\t" XRD("%[c]") ".endif\n\t"
                 "s_add_u32 m0, %[lx], 4096\n\ts_nop 0\n\t" GL4("%[vb]", "%[vb]", "%[sx]")
                 ".if %[L]\n\ts_add_u32 m0, %[lx], 8192\n\ts_nop 0\n\t" GL "%[vc], %[sx] sc1\n\t.endif\n\t"
                 ".if %[CK]\n\t" XCMP "\n\t.endif"
                 : [bad] "=s"(bad)
                 : [lw] "s"(lw), [sw] "s"(sw), [w0] "v"(h.wq[0][0]), [w1] "v"(h.wq[0][1]), [w2] "v"(h.wq[0][2]), [lx] "s"(lx), [sx] "s"(sx), [v0] "v"(vo0),
                   [va] "v"(h.vo16), [vb] "v"(h.vo16 + 4096), [vc] "v"(h.vo16 + 8192), [c] "v"(c), [L] "n"((int)L), [CK] "n"((int)CK), [wt] "s"(wt), [t] "s"(t), [B1] "n"(QB ? 3 : 0) : XCLOB, "scc");
    return CK && bad != 0;
}
DEV void warm(const HW& h, const u16* Wo, int lane) {
    asm volatile("s_mov_b32 m0, %2\n\ts_nop 4\n\tglobal_load_lds_dword %0, %1\n\tglobal_load_lds_dword %3, %1" ::"v"(lane * 64),
                 "s"(Wo + (size_t)blockIdx.x * 4096), "s"(h.lds + DUMP), "v"(lane * 64 + 4096) : "memory");
}
template <int ST> DEV void ld8(f32x4 (&d)[8], const char* sb, unsigned vo) {
    asm volatile("s_nop 4\n\tglobal_load_dwordx4 %0, %8, %9 offset:%10 sc1\n\tglobal_load_dwordx4 %1, %8, %9 offset:%11 sc1\n\t"
                 "global_load_dwordx4 %2, %8, %9 offset:%12 sc1\n\tglobal_load_dwordx4 %3, %8, %9 offset:%13 sc1\n\t"
                 "global_load_dwordx4 %4, %8, %9 sc1\n\tglobal_load_dwordx4 %5, %8, %9 offset:%14 sc1\n\t"
                 "global_load_dwordx4 %6, %8, %9 offset:%15 sc1\n\tglobal_load_dwordx4 %7, %8, %9 offset:%16 sc1"
                 : "=&v"(d[0]), "=&v"(d[1]), "=&v"(d[2]), "=&v"(d[3]), "=&v"(d[4]), "=&v"(d[5]), "=&v"(d[6]), "=&v"(d[7])
                 : "v"(vo), "s"(sb + 4 * ST), "n"(-4 * ST), "n"(-3 * ST), "n"(-2 * ST), "n"(-ST), "n"(ST), "n"(2 * ST), "n"(3 * ST) : "memory");
}

template <class F> DEV void colsum(Cols& d, const void* out, int lane, F next) {
    const char* dst = (const char*)out + (lane & 7) * 32 + (lane >> 4) * 16;
    bf16x8 sel;
#pragma unroll
    for (int e = 0; e < 8; ++e) sel[e] = (lane & 7) == e ? (short)0x3f80 : (short)0;
#pragma unroll
    for (int i = 0; i < 24; ++i) asm volatile("" : "+v"(d[i]));
    f32x4 c;
#pragma unroll
    for (int k = 0; k < 6; ++k) {
        if (!(k & 1)) c = (f32x4){0.f, 0.f, 0.f, 0.f};
#pragma unroll
        for (int i = 0; i < 4; ++i) c = MFMA16(sel, __builtin_bit_cast(bf16x8, d[k * 4 + i]), c);
        if (k & 1) {
#pragma unroll
            for (int e = 0; e < 4; ++e) c[e] = dppadd<0x128>(c[e]);
            if (lane < 32 && !(lane & 8)) st16wt<0>(dst + (k >> 1) * 256, __builtin_bit_cast(u32x4, c));
        }
        SB();
        next(k);
    }
}

template <bool LOCAL>
DEV void qkv_helper(lchar* smem, HW& h, Cols& cw, char* ws, int layer, const u16* X, const u16* W, const u16* W2, const u16* Wo, const char* stats, float* csum,
                    u64* fl, u64 tag, int g, int hp, int lane, int lw STPARG) {
    OPAQUE(lane, lw, g, hp);
    constexpr int NX = LOCAL ? 9 : 8, HO = LOCAL ? 16 : 0;
    const int m0 = g * 128;
    const bool halo = LOCAL && g > 0;
    const int ps = (LOCAL && layer == 0) ? 0 : h.pos;
    auto at = [&](int t) { return t - 1 + (t > ps); };
    auto first = [&](int r0) {
        BAR();
#pragma unroll 1
        for (int t = 0; t < 3; ++t) {
            wq_issue<4>(h, W, t, t, r0);
            if (t == WN - 1) BAR();
        }
    };
    auto colld = [&](Cols& d, const u16* M, int i0 = 0, int i1 = 8) {
        const unsigned vo = ((g * 64 + (lane >> 3)) * 3072 + (hp ^ CJ) * 64 + (lane & 7) * 8) * 2 + 2048;
#pragma unroll
        for (int i = i0; i < i1; ++i)
            asm volatile("s_nop 4\n\tglobal_load_dwordx4 %0, %3, %4 offset:-2048\n\tglobal_load_dwordx4 %1, %3, %4\n\t"
                         "global_load_dwordx4 %2, %3, %4 offset:2048" : "=&v"(d[i]), "=&v"(d[8 + i]), "=&v"(d[16 + i])
                         : "v"(vo), "s"((const char*)M + i * (8 * 6144)) : "memory");
    };
    f32x4 sp[4];
    float hs, hq;
    auto any4 = [](f32x4 (&d)[4]) { asm volatile("" : "=v"(d[0]), "=v"(d[1]), "=v"(d[2]), "=v"(d[3])); };
    const unsigned so = (layer - 1) * (unsigned)(MB / 2) + g * 16384 + (lane >> 4) * 4096 + (lane & 15) * 16;
    auto ld4 = [&](f32x4 (&d)[4], unsigned vo, int a, int b) {
        asm volatile("s_cmp_lg_u32 %6, %7\n\ts_cbranch_scc1 1f\n\tglobal_load_dwordx4 %0, %4, %5 sc1\n\tglobal_load_dwordx4 %1, %4, %5 offset:1024 sc1\n\t"
                     "global_load_dwordx4 %2, %4, %5 offset:2048 sc1\n\tglobal_load_dwordx4 %3, %4, %5 offset:3072 sc1\n1:"
                     : "+v"(d[0]), "+v"(d[1]), "+v"(d[2]), "+v"(d[3]) : "v"(vo), "s"(stats), "s"(a), "s"(b) : "memory", "scc");
    };
    auto add1 = [](f32x4& a, const f32x4& d, int e0) {
#pragma unroll
        for (int e = e0; e < 4; ++e) asm volatile("v_add_f32 %0, %0, %1" : "+v"(a[e]) : "v"(d[e]));
    };
    auto cols = [&](int src, int dst, auto next) {
        f32x4 sc[2][8];
        const unsigned vc = hp * (16 * 768) + (lane < 48 ? lane * 16 : 0);
        ld8<768>(sc[0], (const char*)csum + src, vc);
        next(0);
        ld8<768>(sc[1], (const char*)csum + src + 6144, vc);
        next(1);
        WAITV(8);
#pragma unroll
        for (int i = 1; i < 8; ++i) add1(sc[0][0], sc[0][i], 0);
        next(2);
        WAITV(0);
#pragma unroll
        for (int i = 0; i < 8; ++i) add1(sc[0][0], sc[1][i], 0);
        if (lane < 48) *(LAS f32x4*)(smem + dst + lane * 16) = sc[0][0];
        SB();
    };
    auto sum4 = [&]() {
#pragma unroll
        for (int i = 1; i < 4; ++i) add1(sp[0], sp[i], 0);
    };
    unsigned vo0 = (LOCAL && g == 0) ? h.vo16 + 1024 : h.vo16;
    const char* xsrc = (const char*)X + ((size_t)lw * SEQ + m0 - HO) * 64;
    unsigned ldx = h.lds + lw * (NX * 1024), xv = ldx + lane * 16 + (lane >> 4) * 4;
    auto fetch = [&](const char* p, int so) {
        glds4(p, ldx + so, vo0, h.vo16, h.vo16, h.vo16);
        glds4(p + 4096, ldx + so + 4096, h.vo16, h.vo16, h.vo16, h.vo16);
        if (LOCAL) glds1(p + 8192, ldx + so + 8192, h.vo16);
    };
    u64 v, v2;
    asm volatile("" : "=v"(v), "=v"(v2));
    if (lw < 2) {
        STAMP(0);
        const char* p1 = xsrc + (unsigned)at(1) * (2 * SEQ * 64);
        const char* p2 = xsrc + (unsigned)at(2) * (2 * SEQ * 64);
        asm volatile("" : "+s"(p1), "+s"(p2));
        if (LOCAL && layer == 0) {
            const u64* f0 = (const u64*)(ws + WS_L + (g * 16 + hp) * 256) + (lane & 31);
            asm volatile("" : "+v"(f0));
            BAR();
            if (lw == 1) STAMPW(5);
            v = ~tag;
            poll2<0>(f0, halo ? 0x3ffff : 0xffff, tag, v, v2);
            fetch(xsrc, 0);
        } else poll2<7>(h.go + (layer * 2 - 1) * 16 + lw * 8 + (lane & 3), halo ? 7 : 3, tag, v, v2);
        STAMP(1);
        if (lw == 1) STAMPW(11);
        fetch(p1, XS);
        SM(4, 1);
        fetch(p2, 2 * XS);
    } else if (!LOCAL || layer) {
        WAITB(6);
        wq_issue<4>(h, W, 2, at(2), lw * 4 - 8);
        SM(6, 15);
        WAITV(12);
    }
    if (!LOCAL || layer) {
        const unsigned fo = (lane & 15) * 64 + (((lane >> 4) ^ fz(lane & 15)) << 4);
        const lchar* xs = smem + HO * 64 + lw * 2048 + fo;
        bf16x8 y[4];
#pragma unroll
        for (int i = 0; i < 4; ++i) y[i] = ldk(xs + (i & 1) * (NX * 1024) + (i >> 1) * 1024);
        const bf16x8 ones = {0x3f80, 0x3f80, 0x3f80, 0x3f80, 0x3f80, 0x3f80, 0x3f80, 0x3f80};
        f32x4 a[4] = {};
#pragma unroll
        for (int i = 0; i < 8; ++i) {
            const bf16x8& x = y[(i & 2) + (i >> 2)];
            a[i & 3] = MFMA16((i & 1) ? x : ones, x, a[i & 3]);
        }
        SB();
        if (PE < 16)
        asm volatile("s_cmp_lt_u32 %7, %8\n\ts_cbranch_scc1 1f\n\tglobal_store_dwordx4 %0, %1, %5 sc1\n\tglobal_store_dwordx4 %0, %2, %6 sc1\n\t"
                     "global_store_dwordx4 %0, %3, %5 offset:1024 sc1\n\tglobal_store_dwordx4 %0, %4, %6 offset:1024 sc1\n\ts_nop 1\n1:"
                     ::"v"(h.px - h.vo16 + fo), "v"(y[0]), "v"(y[1]), "v"(y[2]), "v"(y[3]), "s"((const char*)X + lw * 2048), "s"((const char*)X + lw * 2048 + SEQ * 64),
                     "s"(h.pos), "n"(PE) : "memory", "scc");
        auto diag = [&](const f32x4& d) { return (lane & 2) ? ((lane & 1) ? d[3] : d[2]) : ((lane & 1) ? d[1] : d[0]); };
        asm volatile("s_mov_b64 exec, %2\n\tglobal_store_dwordx4 %0, %1, %3 sc1\n\ts_mov_b64 exec, -1\n\ts_nop 0"
                     ::"v"((layer - 1) * (unsigned)(MB / 2) + (g * 16 + h.pos) * 1024 + lw * 256 + (lane & 15) * 16), "v"((f32x4){a[0][0], diag(a[1]), a[2][0], diag(a[3])}),
                     "s"(0xf0000f0000f0000full), "s"(stats) : "memory");
        SM(6, 14);
        __builtin_amdgcn_s_setprio(HPQ);
    }
    if (lw < 2) {
        int bud = POLLMAX;
        auto redo = [&](int t, int so) {
#pragma unroll 1
            do { --bud; fetch(xsrc + (unsigned)at(t) * (2 * SEQ * 64), so); WAITV(0); } while (stale<LOCAL>(xv + so) && bud > 0);
        };
        unsigned x1 = xv + XS;
        asm volatile("" : "+v"(x1));
        if (LOCAL && layer == 0) { WAITB(2 * NX); WAITV(NX); }
        asm volatile("s_cmp_ge_u32 %0, %1\n\ts_waitcnt vmcnt(%2+5)\n\ts_cbranch_scc1 1f\n\ts_waitcnt vmcnt(%2+1)\n1:" ::"s"(h.pos), "n"(PE), "n"(NX) : "memory", "scc");
        asm volatile("" ::"v"(v), "v"(v2));
        SM(5, 13);
        if (__builtin_expect(stale<LOCAL>(x1), 0)) redo(1, XS);
        if (QB) { BAR(); SB(); }
        const char* wsrc = (const char*)W + (unsigned)lw * (8 * 6144);
        const unsigned ldw = h.lds + WQ0 + lw * 3072;
        int xo = 0, sv = 2 * XS, sw = 2 * WQS, t = 3;
        auto next = [&]() {
            sv = xo;
            xo = xo == 2 * XS ? 0 : xo + XS;
            sw = sw == 2 * WQS ? 0 : sw + WQS;
        };
#pragma unroll 1
        for (; t <= (K0 && LOCAL && layer == 0 ? 0 : NCK); ++t) {
            const unsigned a = at(t);
            if (__builtin_expect((xstep<LOCAL, true>(h, wsrc + a * (64 * 6144), ldw + sw, xsrc + a * (2 * SEQ * 64), ldx + xo, vo0, xv + sv, 0, t)), 0)) redo(t - 1, sv);
            next();
        }
        any4(sp);
        ld4(sp, so + lw * 256, t, NCK + 1);
        const int t0 = t;
#pragma unroll 1
        for (; t < 16; ++t) {
            const unsigned a = at(t);
            xstep<LOCAL, false>(h, wsrc + a * (64 * 6144), ldw + sw, xsrc + a * (2 * SEQ * 64), ldx + xo, vo0, 0, t - t0, t);
            next();
        }
        if (LOCAL) glds1(xsrc + (unsigned)ps * (2 * SEQ * 64), h.lds + HXO + lw * 1024, vo0);
        WAITB(NX + 3 + LOCAL);
        sum4();
        asm volatile("" : "=v"(hs), "=v"(hq));
    } else if (lw == 2) {
        if (LOCAL && layer == 0) {
            first(0);
            STAMPW(6);
            WAITB(24);
            WAITV(12);
        }
        { HT0(); HT1(0); }
        BAR();
        SM(6, 4);
        any4(sp);
#pragma unroll 1
        for (int t = 3; t < 16; ++t) {
            HT0();
            wq_issue<6>(h, W, t, at(t), 2);
            HT1(t - 2);
            WAITV(18);
            ld4(sp, so + 512, t, layer ? NCK + L2 : 0);
            BAR();
        }
        sum4();
        asm volatile("" : "=v"(hs), "=v"(hq));
    } else {
        if (LOCAL && layer == 0) {
            first(4);
            Cols c0;
            WAITB(24);
            u64* fr = fl + fi(FL_B + g * 16 + (hp & 3) * 4 + (hp >> 2));
            if (BR == 0) raise(fr, tag);
            colld(c0, W);
            warm(h, Wo, lane);
            WAITV(38 + (BR == 0));
            auto fill0 = [&](int k) { fill(ws + (k < 4 ? WS_O : WS_X + 4 * MB) + ((k >> 1) & 1) * (SEQ * 64) + (k & 1) * 4096, k < 4 ? h.po : h.px); };
            Cols c1;
            constexpr int NG[16] = {0, CE == 1 ? 8 : 0, 0, 0, 0, 0, 0, 0, 0, CE == 2 ? 8 : 0, CE == 3 ? 8 : 0, CE == 4 ? 8 : 0};
            int ng = 0;
            auto job = [&](int i) {
#pragma unroll
                for (int k = 0; k < 8; ++k) if (!F0 && FE + k / FN == i && (!FX || k < 4 || h.pos < NCK)) fill0(k);
                if (!F0 && i == 10) { WAITV(1 + (CE ? 3 * (NG[8] + NG[9]) : 0)); filled(h, fl + fi(FL_E), tag); }
                if (CE && NG[i]) { if (i > 8) __builtin_amdgcn_s_sleep(CS); colld(c1, W2, ng, ng + NG[i]); ng += NG[i]; }
                BAR();
            };
            job(1);
            if (BR == 1) raise(fr, tag);
            WAITV(26 + (BR < 2));
            job(2);
            if (BR == 2) raise(fr, tag);
            job(3); job(4);
            WAITV(0);
            const u64* fb = fl + fi(FL_B + g * 16 - (halo ? 2 : 0)) + min(lane, halo ? 17 : 15);
            u64 f0;
            asm volatile("global_load_dwordx2 %0, %1, off sc1" : "=&v"(f0) : "v"(fb) : "memory");
            colsum(c0, (const char*)csum + ((hp ^ CJ) * 16 + g) * 768, lane, [&](int k) {
                if (k == 1) {
                    WAITV(F0 ? 1 : 9 + (CE ? 3 * NG[5] : 0));
                    asm volatile("" : "+v"(f0));
                    if (!__all(f0 == tag)) poll(fb, lane < 18, tag, ~0u, [](u64 m) { return (unsigned)m; });
                }
                job(5 + k);
            });
            job(11);
            if (CE) {
                if (CE == 4) BAR();
                WAITV(0);
                colsum(c1, (const char*)csum + (256 + (hp ^ CJ) * 16 + g) * 768, lane, [&](int k) { if (CE == 4 ? k % 3 == 2 : k & 1) BAR(); });
            } else { BAR(); BAR(); BAR(); }
            asm volatile("" : "=v"(hs), "=v"(hq), "=v"(sp[0]));
        } else {
            f32x4 sh[4];
            u64* fs = fl + fi(FL_S + (layer - 1) * 256 + g * 16);
            u64 f0;
            if (!LOCAL && layer == 1) {
                const u64* fc = fl + fi(FL_C + hp * 16 + (lane & 15));
                asm volatile("global_load_dwordx2 %0, %1, off sc1" : "=&v"(f0) : "v"(fc) : "memory");
                BAR();
                WAITV(0);
                asm volatile("" : "+v"(f0));
                if (!__all(f0 == tag)) poll(fc, lane < 16, tag, 0xffffu, [](u64 m) { return (unsigned)m; });
                cols(256 * 768, SCB, [&](int k) { BAR(); if (k == DW - 1) raise(fs + h.pos, tag); });
            } else {
                BAR(); WAITB(0);
                if (DW == 2) BAR();
                raise(fs + h.pos, tag);
                BAR();
                if (DW == 1) BAR();
            }
#pragma unroll
            for (int i = 5; i < SF; ++i) BAR();
            fs += (halo && lane >= 16) ? (lane & 15) - 16 : lane & 15;
            asm volatile("global_load_dwordx2 %0, %1, off sc1" : "=&v"(f0) : "v"(fs) : "memory");
            BAR();
            WAITV(0);
            asm volatile("" : "+v"(f0));
            if (!__all(f0 == tag)) poll(fs, lane < 32, tag, ~0u, [](u64 m) { return (unsigned)m; });
            any4(sp);
            ld4(sp, so + 768, 0, 0);
            if (LOCAL) {
                any4(sh);
                ld4(sh, so + 768 - (halo ? 16384 : 0), 0, 0);
            }
            BAR(); BAR();
            WAITV(0);
            if (LOCAL) {
#pragma unroll
                for (int i = 1; i < 4; ++i) add1(sh[0], sh[i], 2);
                const f32x2 x = sw16f(sh[0][2], sh[0][2]), y = sw16f(sh[0][3], sh[0][3]);
                hs = x[0] + x[1]; hq = y[0] + y[1];
                asm volatile("" : "+v"(hs), "+v"(hq));
            } else asm volatile("" : "=v"(hs), "=v"(hq));
#pragma unroll
            for (int i = SF + 3; i < 15; ++i) BAR();
            sum4();
        }
    }
    { HT0(); HT1(14); }
    if (LOCAL) wo_issue(h, Wo, 8, 2, 6, 18);
    if (!LOCAL || layer) {
        const f32x2 x = sw16f(sp[0][0], sp[0][2]), y = sw16f(sp[0][1], sp[0][3]);
        const f32x2 z = sw32f(x[0] + x[1], hs), u = sw32f(y[0] + y[1], hq);
        const float mean = (z[0] + z[1]) * (1.0f / DM);
        const float r = rsd(fmx((u[0] + u[1]) * (1.0f / DM) - mean * mean, 0.f));
        if (lane < 32 || lw == 3) { h.st[0] = -bfr(mean); h.st[(SRS - SNM) / 4] = r; }
    }
    WAITV(LOCAL ? 8 : 0);
    BARL();
    { HT0(); HT1(15); }
    if (LOCAL) wo_issue(h, Wo, 12, 6, 8, 20);
    if (!LOCAL && layer == 1 && lw == 3) cols(0, SCOL, [](int k) { if (k == 1) BAR(); });
    else BAR();
    if (LOCAL) {
        wo_issue(h, Wo, 12, 6, 0, 8);
        if (layer == 0 && lw == 3) {
            if (F0) { colld(cw, W2); WAITB(28); }
            else {
            const u64 e = eload(h, fl + fi(FL_E));
            colld(cw, W2);
            WAITB(29);
            echeck<24>(h, fl + fi(FL_E), e, tag);
            }
        } else WAITB(4);
    } else BAR();
}

DEV u64 probe(const u64* f) {
    u64 v;
    asm volatile("s_mov_b64 exec, 1\n\t" FLD("%0", "%1", "%2") "\n\ts_mov_b64 exec, -1" : "=&v"(v) : "v"(f), "v"(0ull) : "memory");
    return v;
}
DEV void attn_helper(const HW& h, char* ws, int nf, const u16* kv, const u16* Wo, const u64* fe, const u64* fkv, u64 tag, int g, int hp, int lane, int lw STPARG) {
    __builtin_amdgcn_s_setprio(2);
    OPAQUE(lane, lw, g, hp);
    const int rh = lw & 1, hd = lw >> 1;
    const char* src = (const char*)kv + (unsigned)((hp * 2 + hd) * SEQ * 64 + rh * 4096);
    const unsigned ld = h.lds + AR0 + hd * 16384 + rh * 8192;
    const u64* f = fkv + fi(lw * 16 + (lane & 15));
    unsigned rdy = 0u;
    int reg = AR1;
    auto issue = [&](int c) {
        const int blk = g ^ c;
        if (!((rdy >> blk) & 1)) rdy = poll(f, lane < 16, tag, 1u << blk, [](u64 m) { return (unsigned)m; });
        glds4(src + blk * 8192, ld + reg, h.vo16, h.vo16, h.vo16, h.vo16);
        glds4(src + blk * 8192 + NH * SEQ * 64, ld + reg + 4096, h.vo16, h.vo16, h.vo16, h.vo16);
        reg = reg == AR1 ? AR2 : reg == AR2 ? 0 : AR1;
    };
    issue(1);
    STAMP(7);
#pragma unroll 1
    for (int c = 0; c < 14; ++c) {
        WAITB(0);
        issue(c + 2);
        if (c == 1 && lw == 3) warm(h, Wo, lane);
        if ((TL & 4) && c == 2 && lw == 3) touch(h.out, h.lds + DUMP, (lane & 3) * MB + blockIdx.x * 4096);
        if ((c >> 2) == (FA == 4 ? 2 : 1)) wo_issue(h, Wo, min(4, 14 - 4 * (c & 3)), 4, 4 * (c & 3), 0);
        if (c >= FA && c < FA + nf && (!FX || !(c & 1) || h.pos < NCK))
            fill(ws + (c == FA ? WS_O + 4 * MB : c == FA + 1 ? WS_X + 8 * MB : c == FA + 2 ? WS_O : WS_X + 12 * MB) + hd * (SEQ * 64) + rh * 4096, (c & 1) ? h.px : h.po);
        if (c == FA + 4 && lw == 3) filled(h, fe, tag);
    }
    if (BT < 3 && lw == 3) echeck<0>(h, fe, eload(h, fe), tag);
    WAITB(0);
    wo_issue(h, Wo, 8, 6, 14, 24);
    if (BT == 3 && lw == 3) echeck<0>(h, fe, eload(h, fe), tag);
    BAR();
}

#define TM0
#define TM1(t)
#define CNT(n)
DEV void wo_helper(lchar* smem, HW& h, Cols& cw, const char* csum, const char* stats, int nl, const u16* O, const u16* Wo, int nw, const u16* Wn, u64* fc, const u64* fo, u64 tag, int g, int lane, int lw STPARG) {
    int dummy = 0;
    OPAQUE(lane, lw, g, dummy);
    const int p = lw & 1, rh = lw >> 1;
    const char* xsrc = (const char*)O + ((size_t)p * SEQ + g * 128 + rh * 64) * 64;
    const unsigned ldx = h.lds + p * 8192 + rh * 4096, c0 = ldx + lane * 16 + (lane >> 4) * 4;
    const u64* f = fo + p * 8 + rh;
    STAMP(0);
    unsigned rdy;
    u64 v, v2;
    u64 pa, pb;
    if (PF) pa = probe(f);
    wo_issue(h, Wo, nw, 10, 20, 0);
    __builtin_amdgcn_s_setprio(0);
    if (SX && nw) __builtin_amdgcn_s_sleep(SX);
    if (!PF) { __builtin_amdgcn_s_sleep(S1); pa = probe(f); }
    asm volatile("s_sleep %7\n\ts_mov_b64 exec, 1\n\t" FLD("%1", "%4", "%5") "\n\ts_mov_b32 %3, 1\n\ts_barrier\n\t" FLD("%2", "%4", "%5") "\n\t"
                 ".if %9\n\ts_cmp_lg_u32 %10, 0\n\ts_waitcnt vmcnt(10)\n\ts_cbranch_scc1 2f\n\t.endif\n\t"
                 "s_waitcnt vmcnt(2)\n2:\n\tv_cmp_ne_u64 vcc, %6, %0\n\ts_cbranch_vccz 1f\n\t"
                 "s_waitcnt vmcnt(1)\n\tv_cmp_ne_u64 vcc, %6, %1\n\ts_cbranch_vccz 1f\n\t"
                 ".if %8\n\ts_cmp_ge_u32 %10, %8\n\ts_cbranch_scc1 3f\n\ts_waitcnt vmcnt(0)\n\tv_cmp_ne_u64 vcc, %6, %2\n\ts_cbranch_vccz 1f\n\t" FLD("%2", "%4", "%5") "\n3:\n\t.endif\n\t"
                 "s_mov_b32 %3, 0\n\ts_barrier\n1:\n\ts_mov_b64 exec, -1"
                 : "+v"(pa), "=&v"(pb), "=&v"(v), "=&s"(rdy) : "v"(f), "v"(0ull), "s"(tag), "n"(S2), "n"(X3), "n"(PF), "s"(nw) : "memory", "scc", "vcc");
    auto issue = [&]() {
#pragma unroll 1
        for (int t = 0; t < 3; ++t) {
            glds4(xsrc + (unsigned)(t + (t >= h.pos)) * (2 * SEQ * 64), ldx + t * 16384, h.vo16, h.vo16, h.vo16, h.vo16);
            if (t == 0) { STAMP(1); SM(4, 1); SM(5, 5); SM(6, 6); SM(7, 7); }
        }
    };
    if (!CE && CW == 2 && fc) colsum(cw, csum, lane, [](int) {});
    if (rdy) issue();
    if (!CE && CW == 1 && fc) colsum(cw, csum, lane, [](int) {});
    asm volatile("s_cmp_lg_u32 %1, 0\n\ts_cbranch_scc0 1f\n\ts_barrier\n\ts_branch 2f\n1:\n\ts_cmp_lg_u64 %2, 0\n\ts_waitcnt vmcnt(3)\n\ts_cbranch_scc1 2f\n\ts_waitcnt vmcnt(0)\n2:"
                 : "+v"(v) : "s"(rdy), "s"(!CE && CW == 1 ? fc : nullptr) : "memory", "scc");
    poll2<0>(f, rdy ^ 1, tag, v, v2);
    if (!rdy) issue();
    __builtin_amdgcn_s_setprio(HPW);
    if (!CE && CW == 0 && fc) colsum(cw, csum, lane, [](int) {});
    unsigned vo = h.vo16 + (3 + (3 >= h.pos)) * (2 * SEQ * 64), sa, sb, sm;
    int bud = POLLMAX;
    u64 ret;
    unsigned tv = 0, cn = 0, np = 0;
    const char* wn = (const char*)Wn + lw * (8 * 6144);
    const unsigned wa = h.pos * 0x60000, wd = (h.pos == 0) * 0x60000 - wa - 0x30000;
    unsigned w0 = h.wq[lw == 3][0] + wa, w1 = h.wq[lw == 3][1] + wa, w2 = h.wq[lw == 3][2] + wa;
    asm volatile(
        ".macro WRD t\n.if (\\t %% 5) == 0\n"
        "ds_read2st64_b32 v[240:241], %[c4] offset1:4\nds_read2st64_b32 v[242:243], %[c4] offset0:8 offset1:12\n.else\n"
        "ds_read2st64_b32 v[240:241], %[c0] offset0:(\\t %% 5)*64-64 offset1:(\\t %% 5)*64-60\n"
        "ds_read2st64_b32 v[242:243], %[c0] offset0:(\\t %% 5)*64-56 offset1:(\\t %% 5)*64-52\n.endif\n.endm\n"
        ".macro WCMP\ns_waitcnt lgkmcnt(0)\nv_max3_u32 v240, v240, v241, v242\nv_max_u32 v240, v240, v243\nv_cmp_eq_u32 vcc, -1, v240\n.endm\n"
        ".macro WTS t, x\nWCMP\n" TM1("(\\t+63)&63") "s_cbranch_vccnz .Lwr%=_\\x\\t\n.Lwb%=_\\x\\t:\n.endm\n"
        ".macro WST t\n" GL "%[vo], %[src] sc1\n" GL "%[vo], %[src] offset:1024 sc1\n"
        "s_waitcnt vmcnt(10)\nWRD \\t\n"
        GL "%[vo], %[src] offset:2048 sc1\n" GL "%[vo], %[src] offset:3072 sc1\n"
        "s_add_u32 m0, %[ldx], ((\\t+3) %% 5)*16384\ns_cmp_eq_u32 %[pos], \\t+3\ns_cselect_b32 %[sa], 2, 1\nv_lshl_add_u32 %[vo], %[sa], 18, %[vo]\n"
        "WTS \\t, a\n"
        ".endm\n"
        ".macro WTL t, n, x\ns_waitcnt vmcnt(\\n)\nWRD \\t\nWTS \\t, \\x\ns_barrier\n" TM0 ".endm\n"
        ".macro WWN o, d\ns_add_u32 m0, %[wl], \\o\ns_nop 0\n" GL "%[w0], %[wn] sc1\n" GL "%[w1], %[wn] offset:1024 sc1\n" GL "%[w2], %[wn] offset:2048 sc1\n"
        "v_add_u32 %[w0], \\d, %[w0]\nv_add_u32 %[w1], \\d, %[w1]\nv_add_u32 %[w2], \\d, %[w2]\n.endm\n"
        ".macro WCR t, x\n.Lwr%=_\\x\\t:\ns_mov_b32 %[sa], ((\\t+4) %% 5)*16384\ns_mov_b32 %[sb], \\t-1\ns_call_b64 %[ret], .Lwretry%=\ns_branch .Lwb%=_\\x\\t\n.endm\n"
        "s_add_u32 m0, %[ldx], 49152\ns_nop 0\n" TM0
        ".irp t,1,2,3,4,5,6,7,8,9,10,11\nWST \\t\n.if \\t == 4\ns_cmp_eq_u64 %[fc], 0\ns_cbranch_scc1 3f\ns_mov_b64 exec, 1\n"
        "global_atomic_swap_x2 %[v0], %[tg], %[fc] sc1\ns_mov_b64 exec, -1\n3:\n.endif\ns_barrier\n" TM0 ".endr\n"
        "WST 12\ns_cmp_eq_u64 %[Wn], 0\ns_barrier\n" TM0 "s_cbranch_scc1 .Lwlast%=\n"
        "WWN 2*24576, 0x30000\nWTL 13, 11, a\nWWN 2*24576+12288, %[wd]\nWTL 14, 10, a\nWWN 0, 0x30000\nWTL 15, 9, a\nWWN 12288, 0\n"
        "s_branch .Lwend%=\n"
        ".irp t,1,2,3,4,5,6,7,8,9,10,11,12,13,14,15\nWCR \\t, a\n.endr\n"
        ".irp t,13,14,15\nWCR \\t, b\n.endr\n"
        ".Lwretry%=:\ns_cmp_ge_u32 %[sb], %[pos]\ns_addc_u32 %[sb], %[sb], 0\ns_mov_b32 %[sm], m0\ns_add_u32 m0, %[ldx], %[sa]\n"
        "v_lshl_add_u32 v244, %[sb], 18, %[v0]\nv_add_u32 v245, %[sa], %[c0]\n"
        "1:\n" GL "v244, %[src] sc1\n" GL "v244, %[src] offset:1024 sc1\n" GL "v244, %[src] offset:2048 sc1\n" GL "v244, %[src] offset:3072 sc1\n"
        "s_waitcnt vmcnt(0)\nds_read2st64_b32 v[240:241], v245 offset1:4\nds_read2st64_b32 v[242:243], v245 offset0:8 offset1:12\n"
        "WCMP\n" CNT("0x10000") "s_sub_i32 %[bud], %[bud], 1\ns_cmp_lt_i32 %[bud], 0\ns_cbranch_scc1 2f\ns_cbranch_vccnz 1b\n"
        "2:\ns_mov_b32 m0, %[sm]\ns_setpc_b64 %[ret]\n"
        ".Lwlast%=:\nWTL 13, 8, b\nWTL 14, 4, b\nWTL 15, 0, b\n"
        ".Lwend%=:\n"
        ".purgem WRD\n.purgem WCMP\n.purgem WTS\n.purgem WST\n.purgem WTL\n.purgem WWN\n.purgem WCR"
        : [vo] "+v"(vo), [bud] "+s"(bud), [sa] "=&s"(sa), [sb] "=&s"(sb), [sm] "=&s"(sm), [ret] "=&s"(ret),
          [w0] "+v"(w0), [w1] "+v"(w1), [w2] "+v"(w2)
        : [src] "s"(xsrc), [ldx] "s"(ldx), [c0] "v"(c0), [c4] "v"(c0 + 65536), [v0] "v"(h.vo16), [pos] "s"(h.pos), [wd] "s"(wd), [fc] "s"(fc), [tg] "v"(tag),
          [wn] "s"(wn), [Wn] "s"(Wn), [wl] "s"(h.lds + WQ0 + lw * 3072)
        : "memory", "scc", "vcc", "v240", "v241", "v242", "v243", "v244", "v245"
        );
    asm volatile("" ::"v"(v), "v"(v2), "v"(pb));
}

__global__ __launch_bounds__(512) void mega(const u16* xin, char* ws, const u16* wqa, const u16* __restrict__ woa,
                                            const u16* __restrict__ wqb, const u16* __restrict__ wob, u16* __restrict__ out) {
    __shared__ __attribute__((aligned(16))) char smem_[LDS_BYTES];
    u32x4 a01;
    {
        u64 pc;
        unsigned t, v;
        asm volatile("s_load_dwordx4 %0, %5, 0\n\ts_getpc_b64 %2\n1:\n\ts_mov_b32 m0, %8\n\ts_lshr_b32 %3, %6, 3\n\ts_mulk_i32 %3, 28*64\n\t"
                     "v_min_u32 %4, 27, %7\n\tv_lshl_add_u32 %4, %4, 6, %3\n\tv_min_u32 %4, .Lmega_last-1b, %4\n\tglobal_load_lds_dword %4, %2\n\t"
                     "s_load_dwordx2 %1, %5, 16"
                     : "=&s"(a01), "=&s"(wqa), "=&s"(pc), "=&s"(t), "=&v"(v)
                     : "s"(__builtin_amdgcn_kernarg_segment_ptr()), "s"(blockIdx.x), "v"(threadIdx.x), "n"(DUMP) : "memory", "scc");
    }
    SB();
    lchar* smem = (lchar*)smem_;
    const int bid = blockIdx.x, xcd = bid & 7, idx = bid >> 3;
    const int lane = threadIdx.x & 63, wid = __builtin_amdgcn_readfirstlane(threadIdx.x >> 6);
    const int rg = (bid & 4) * 32 + idx * 4 + (bid & 3), row = rg * 8 + (wid & 3) * 2 + ((lane >> 2) & 1);
    u32x4 x[4];
    u64 e;
    {
        unsigned xo = row * 2048 + (lane >> 3) * 64 + (lane & 3) * 16, eo = WS_FL + fi(FL_EP) * 8;
        asm volatile("" : "+v"(xo), "+v"(eo));
        asm volatile("s_waitcnt lgkmcnt(0)" : "+s"(a01), "+s"(wqa));
        xin = (const u16*)(((u64)a01[1] << 32) | a01[0]);
        ws = (char*)(((u64)a01[3] << 32) | a01[2]);
        asm volatile("s_cmp_gt_u32 %9, 3\n\ts_cbranch_scc1 1f\n\t"
                     "global_load_dwordx4 %0, %5, %6" NTX "\n\tglobal_load_dwordx4 %1, %5, %6 offset:512" NTX "\n\tglobal_load_dwordx4 %2, %5, %6 offset:1024" NTX "\n\t"
                     "global_load_dwordx4 %3, %5, %6 offset:1536" NTX "\n1:\n\tglobal_load_dwordx2 %4, %7, %8 sc1"
                     : "=&v"(x[0]), "=&v"(x[1]), "=&v"(x[2]), "=&v"(x[3]), "=&v"(e)
                     : "v"(xo), "s"(xin), "v"(eo), "s"(ws), "s"(wid) : "memory", "scc");
    }
    SB();
    const int g = (xcd >> 2) * 8 + (idx >> 2), hp = (xcd & 3) * 4 + (idx & 3);
    const int pos = (int)(kinv() >> (4 * hp)) & 15, g2 = g, cb = pos;
    u64* fl = (u64*)(ws + WS_FL);
    auto mktag = [](u64 e) {
        e += 1;
        return ((u64)(unsigned)__builtin_amdgcn_readfirstlane((int)(e >> 32)) << 32) | (unsigned)__builtin_amdgcn_readfirstlane((int)e);
    };
    const unsigned lds0 = (unsigned)(uintptr_t)smem;
    LAS unsigned* cnt = (LAS unsigned*)(smem + SNM + 1152);
    *cnt = 0u;
    BARL();
    u64 tag;
    if (wid < 4) {
        unsigned d = ((lane >> 3) * SEQ + row) * 64 + (((lane & 3) ^ fz(row)) << 4);
        unsigned lo_ = (g * 16 + (lane & 31)) * 256 + (rg & 15) * 8 + (lane & 16), lm = ((rg & 15) >= 14 && g < 15) ? ~0u : 0xffffu;
        asm volatile("" : "+v"(d), "+v"(lo_));
        asm volatile("" : "+s"(lm));
        WAITV(0);
        asm volatile("" : "+v"(x[0]), "+v"(x[1]), "+v"(x[2]), "+v"(x[3]), "+v"(e));
        if (wid == 0) STAMP(14);
        const unsigned s0 = ln0_stats(x);
        const f32x2 st = {lo16(s0), __builtin_amdgcn_rcpf(hi16(s0))};
        u32x4 y[4];
        auto put = [&](int i) { asm volatile("global_store_dwordx4 %0, %1, %2 sc1\n\ts_nop 1" ::"v"(d), "v"(i < 4 ? y[i] : x[i - 4]), "s"(ws + (i < 4 ? WS_X : WS_R - 4 * MB) + i * MB) : "memory"); };
#pragma unroll
        for (int i = 0; i < 4; ++i) {
            Raw v;
            unpk(v, x[i]);
            y[i] = ln0_apply(v, st);
            if (SA < 2 || i < SA - 1) put(i);
            SB();
        }
        tag = mktag(e);
        if (wid == 0) STAMP(15);
        WAITV(SA == 1 ? 3 : 0);
        if (LG) {
            unsigned n = 0;
            if (lane == 0) n = __hip_atomic_fetch_add(cnt, 1u, __ATOMIC_RELAXED, __HIP_MEMORY_SCOPE_WORKGROUP);
            n = __builtin_amdgcn_readfirstlane(n);
            go(ws + WS_L, lo_, n == 3 ? lm : 0, tag);
            if (n == 3) STAMPW(3);
        } else
        if (lane == 0 && __hip_atomic_fetch_add(cnt, 1u, __ATOMIC_RELAXED, __HIP_MEMORY_SCOPE_WORKGROUP) == 3)
        {
            stflag(fl + fi(FL_L + rg), tag);
            STAMPW(3);
        }
        if (SA >= 2) {
            BAR();
#pragma unroll
            for (int i = SA - 1; i < (RC && RT < 0 ? 8 : 4); ++i) {
                put(i);
            }
            if (F0) {
                if (F0 > 1) __builtin_amdgcn_s_sleep(F0);
                const unsigned o = ((pos * 2 + (wid & 1)) * SEQ + g * 128 + (wid >> 1) * 64) * 64 + lane * 16;
                fill(ws + WS_O, o);
                if (pos < NCK) fill(ws + WS_X + 4 * MB, o);
            }
        }
    } else {
        if (TD) WAITV(0);
        if (wid > 4) touch((const char*)wqa + (wid - 5) * 2048, lds0 + DUMP, (idx + ((lane >> 3) & (TW - 1)) * 32) * 6144 + (xcd & 3) * 512 + (lane & 7) * 64);
        else {
            const unsigned f = lane < 7 ? WS_GO + (g * 16 + pos) * 1024 + lane * 128
                             : (LG && lane >= 9) ? WS_L + (g * 16 + hp) * 256 + (lane & 1) * 128
                                        : WS_FL + (lane < 9 ? FL_C + bid * 32 + (lane & 1) * 16 : FL_L + (g & 7) * 16 + (lane & 1) * 128) * 8;
            asm volatile("s_mov_b64 exec, %3\n\tglobal_atomic_add_x2 %0, %1, %2 sc1\n\ts_mov_b64 exec, -1"
                         ::"v"(lane < 11 ? f : (unsigned)(WS_X + (lane - 11) * MB + rg * 512)), "v"(0ull), "s"(ws), "n"((TL & 1) ? 0x7fff : 0x7ff) : "memory");
        }
        auto tc = [&]() {
            const unsigned v = (g * 64 + (lane >> 1)) * 6144 + (hp ^ CJ) * 128 + (lane & 1) * 64;
            const char* b = (const char*)wqa + (wid == 4 ? 0 : wid - 5) * 2048;
            touch(b, lds0 + DUMP, wid == 4 ? 0 : v);
            touch(b + 32 * 6144, lds0 + DUMP, wid == 4 ? 0 : v);
        };
        if (TC == 1) tc();
        unsigned lo, hi;
        asm volatile("s_waitcnt vmcnt(%4)\n\tv_readfirstlane_b32 %0, %2\n\tv_readfirstlane_b32 %1, %3" : "=&s"(lo), "=s"(hi) : "v"((unsigned)e), "v"((unsigned)(e >> 32)), "n"(TC == 1 ? 3 : 1) : "memory");
        if (T2 && wid > 4) {
            if (T2 & 4) WAITV(0);
#pragma unroll
            for (int i = 1; i <= (T2 & 3); ++i)
                touch((const char*)wqa + (wid - 5) * 2048 + i * 256 * 6144, lds0 + DUMP, (idx + (lane >> 3) * 32) * 6144 + (xcd & 3) * 512 + (lane & 7) * 64);
        }
        if (TC == 2) tc();
        if (TC == 3) { WAITV(0); tc(); }
        tag = (((u64)hi << 32) | lo) + 1;
    }
    if (wid < 4) {
        if (SA < 2) BAR();
        if (wid == 0) STAMP(9);
        __builtin_amdgcn_s_setprio(1);
        u32x4 res[4];
        {
            const unsigned ro = RC ? ((cb * 2 + (wid & 1)) * SEQ + g2 * 128 + (wid >> 1) * 64 + (lane & 15)) * 64 + ((((lane & 16) << 1) + ((lane & 32) >> 1)) ^ (fz(lane & 15) << 4))
                                   : (g2 * 128 + (wid >> 1) * 64 + (lane & 15)) * 2048 + (cb * 2 + (wid & 1)) * 64 + ((lane & 16) << 1) + ((lane & 32) >> 1);
#pragma unroll
            for (int i = 0; i < 4; ++i) res[i] = (u32x4){ro, ro, ro, ro};
            if (RI == 9 || RT >= 0) for (int i = 0; i < 4; ++i) res[i] = x[i];
            if (!PB) { BAR(); BAR(); }
        }
#pragma unroll 1
        for (int layer = 0; layer < 4; ++layer) {
            const char* fo = ws + WS_GO + layer * 256;
            const unsigned gm = __builtin_amdgcn_readfirstlane(pos == 0 ? 0xfffe : pos == 1);
            u16* ob = (u16*)(ws + WS_O + pos * (2 * SEQ * 64));
            WReg wr;
            if (layer & 1) qb_compute(smem, (u16*)(ws + WS_KV + (layer >> 1) * 8 * MB), wr, ob + 2 * MB, fo, gm, fl + fi(FL_KV + (layer >> 1) * 1024 + hp * 64),
                                      tag, g, hp, lane, wid STPPASS(layer * 32));
            else qa_compute(smem, layer, RC ? (const u16*)(ws + WS_R) : xin, cb, res, wr, ob, fo, gm, tag, g, hp, lane, wid STPPASS(layer * 32));
            const bool last = layer == 3;
            wo_compute(smem, res, wr,
                       last ? out : (u16*)(ws + WS_X + (layer + 1) * 4 * MB), !last,
                       fo + 128, gm, (layer & 1) * 1024, tag, g2, cb, lane, wid STPPASS(layer * 32 + 16));
        }
        asm volatile(".Lmega_last:");
        if (bid == 0 && threadIdx.x == 0) stflag(fl + fi(FL_EP), tag);
    } else {
        const int lw = wid - 4;
        HW h;
        h.lds = lds0;
        h.out = out;
        h.lw = lw;
        h.pos = pos;
        h.go = (const u64*)(ws + WS_GO + (unsigned)(g * 16 + pos) * 1024);
        h.st = (LAS float*)(smem + SNM + (lane < 32 ? 64 + lw * 128 + lane * 4 : (lane & 15) * 4));
        asm volatile("" : "+v"(h.st));
        h.kord = ((KORD & ((1ull << (4 * pos)) - 1)) << 4) | hp | (KORD & ~((16ull << (4 * pos)) - 1));
        h.vo16 = lane * 16;
        h.po = (pos * 2 * SEQ + g * 128) * 64 + lane * 16;
        h.px = (cb * 2 * SEQ + g2 * 128) * 64 + lane * 16;
        h.er = fi(lane ? 256 + g2 * 16 + cb : g * 16 + hp) * 8;
        h.ec = fi((lane < 16 ? g2 : lane < 32 || g == 0 ? 16 + g : 15 + g) * 16 + (lane & 15)) * 8;
#pragma unroll
        for (int i = 0; i < 6; ++i) {
            const int k = (lane >> 3) + (((i / 3) ^ (lw == 1)) << 3), pc = lane & 7;
            h.wq[i / 3][i % 3] = (lane >> 3) * 6144 + (i % 3) * 1024 + hp * 128 + ((((pc >> 1) ^ fswz(k)) << 1) + (pc & 1)) * 16;
        }
#pragma unroll
        for (int i = 0; i < 4; ++i) h.wo[i] = wo_voff(lane, cb, i);
#pragma unroll 1
        for (int layer = 0; layer < 4; ++layer) {
            if (OQ) asm volatile("" : "+s"(wqa), "+s"(wqb), "+s"(ws));
            const u16* X = (const u16*)(ws + WS_X + layer * 4 * MB);
            const char* st = ws + WS_ST;
            const u16* wo = (layer & 1) ? wob : woa;
            Cols cw;
            const u64* fe = fl + fi(FL_E + ((layer + 1) >> 1) * 512);
            if (layer & 1) {
                qkv_helper<false>(smem, h, cw, ws, layer, X, wqb, wqb, wo, st, (float*)(ws + WS_CS), fl, tag, g, hp, lane, lw STPPASS(layer * 32));
                attn_helper(h, ws, layer == 1 ? 4 : 1, (const u16*)(ws + WS_KV + (layer >> 1) * 8 * MB), wo, fe, fl + fi(FL_KV + (layer >> 1) * 1024 + hp * 64), tag, g, hp, lane,
                            lw STPPASS(layer * 32));
            } else {
                qkv_helper<true>(smem, h, cw, ws, layer, X, wqa, wqb, wo, st, (float*)(ws + WS_CS), fl, tag, g, hp, lane, lw STPPASS(layer * 32));
            }
            wo_helper(smem, h, cw, ws + WS_CS + (256 + (hp ^ CJ) * 16 + g) * 768, st + layer * (MB / 2), (layer & 1) * 1024, (const u16*)(ws + WS_O + (layer & 1) * 4 * MB), wo, (layer & 1) * 10, layer == 3 ? nullptr : (layer & 1) ? wqa : wqb, (layer == 0 && lw == 3) ? fl + fi(FL_C + (hp ^ CJ) * 16 + g) : nullptr,
                      h.go + layer * 32, tag, g2, lane, lw STPPASS(layer * 32 + 16));
        }
    }
}

extern "C" void kernel_launch(void* const* d_in, const int* in_sizes, int n_in, void* d_out, int out_size, void* d_ws,
                              size_t ws_size, hipStream_t stream) {
    mega<<<256, 512, 0, stream>>>((const u16*)d_in[0], (char*)d_ws, (const u16*)d_in[1], (const u16*)d_in[2], (const u16*)d_in[4],
                                  (const u16*)d_in[5], (u16*)d_out);
}
```
